```python
import torch

def _softmax(x, axis=-1):
    return torch.softmax(x, dim=axis)
def _gelu(x):
    return torch.nn.functional.gelu(x, approximate="tanh")
def _silu(x):
    return torch.nn.functional.silu(x)
def _sigmoid(x):
    return torch.sigmoid(x)
def _layernorm(x, axis=-1, eps=1e-5):
    m = x.mean(dim=axis, keepdim=True)
    v = x.var(dim=axis, keepdim=True, unbiased=False)
    return (x - m) / torch.sqrt(v + eps)
def _scatter_add(tgt, idx, src, axis=0):
    return tgt.clone().index_add_(axis, idx, src)
def _segment_sum(src, seg, n):
    out = torch.zeros((n,) + src.shape[1:], dtype=src.dtype)
    return out.index_add_(0, seg, src)
def _pad(x, pw):
    flat = tuple(v for pair in reversed(pw) for v in pair)
    return torch.nn.functional.pad(x, flat)

kernel_name = 'concat_heads__linear_kernel_hedgehog__compressed_kv_cla__prompt'

def setup_inputs():

    _w = (lambda a: (lambda t: t.to(torch.bfloat16) if t.is_floating_point() else t)(torch.as_tensor(a)))
    def _A_setup():
        B, S, D, H = 1, 2048, 512, 32
        Dh = D // H
        return {
            "x": _w((lambda *s: torch.randn(*s, dtype=torch.bfloat16) * 0.4)(B, S, D)),
            "wqkv": _w((lambda *s: torch.randn(*s, dtype=torch.bfloat16) * 0.4)(D, 3 * D)),
            "w_phi": _w((lambda *s: torch.randn(*s, dtype=torch.bfloat16) * 0.4)(H, Dh, Dh) * (Dh ** -0.5)),
            "wo": _w((lambda *s: torch.randn(*s, dtype=torch.bfloat16) * 0.05)(D, D)),
        }
    _A_in = _A_setup()

    def _B_setup():
        B, H, Sq, Sk, D, M = 1, 32, 2048, 2048, 128, 512
        Dc = 4 * D
        return {
            "x": _w((lambda *s: torch.randn(*s, dtype=torch.bfloat16) * 0.8)(B, Sq, M)),
            "c_kv": _w((lambda *s: torch.randn(*s, dtype=torch.bfloat16) * 0.4)(B, Sk, Dc)),
            "w_q": _w((lambda *s: torch.randn(*s, dtype=torch.bfloat16) * 6.4)(M, H, D) * (M ** -0.5)),
            "w_uk": _w((lambda *s: torch.randn(*s, dtype=torch.bfloat16) * 6.4)(Dc, H, D) * (Dc ** -0.5)),
            "w_uv": _w((lambda *s: torch.randn(*s, dtype=torch.bfloat16) * 0.4)(Dc, H, D) * (Dc ** -0.5)),
            "w_o": _w((lambda *s: torch.randn(*s, dtype=torch.bfloat16) * 0.4)(H, D, M) * ((H * D) ** -0.5)),
        }
    _B_in = _B_setup()

    return {
        "A_x": _A_in["x"],
        "A_wqkv": _A_in["wqkv"],
        "A_w_phi": _A_in["w_phi"],
        "A_wo": _A_in["wo"],
        "B_x": _B_in["x"],
        "B_c_kv": _B_in["c_kv"],
        "B_w_q": _B_in["w_q"],
        "B_w_uk": _B_in["w_uk"],
        "B_w_uv": _B_in["w_uv"],
        "B_w_o": _B_in["w_o"],
    }

def reference(A_x, A_wqkv, A_w_phi, A_wo, B_x, B_c_kv, B_w_q, B_w_uk, B_w_uv, B_w_o):
    def _A_ref(x, wqkv, w_phi, wo):
        B, S, D = x.shape
        H = 32; Dh = D // H
        qkv = (lambda x, *d: x.permute(*d))((x @ wqkv).reshape(B, S, 3, H, Dh), 2, 0, 3, 1, 4)
        q, k, v = qkv[0], qkv[1], qkv[2]
        pq = torch.einsum("bhsd,hdf->bhsf", q, w_phi)
        pk = torch.einsum("bhsd,hdf->bhsf", k, w_phi)
        phi_q = torch.concatenate([_softmax(pq, axis=-1), _softmax(-pq, axis=-1)], axis=-1)
        phi_k = torch.concatenate([_softmax(pk, axis=-1), _softmax(-pk, axis=-1)], axis=-1)
        kv = torch.einsum("bhsf,bhsd->bhfd", phi_k, v)
        z  = torch.einsum("bhsf->bhf", phi_k)
        num = torch.einsum("bhqf,bhfd->bhqd", phi_q, kv)
        den = torch.einsum("bhqf,bhf->bhq", phi_q, z)[..., None] + 1e-6
        o = num / den
        return (lambda x, *d: x.permute(*d))(o, 0, 2, 1, 3).reshape(B, S, D) @ wo

    def _B_ref(x, c_kv, w_q, w_uk, w_uv, w_o):
        B, Sq, M = x.shape[0], x.shape[1], x.shape[2]
        Sk = c_kv.shape[1]
        H, D = w_q.shape[1], w_q.shape[2]
        q = torch.einsum("bqm,mhd->bhqd", x, w_q)
        k = torch.einsum("bkc,chd->bhkd", c_kv, w_uk)
        v = torch.einsum("bkc,chd->bhkd", c_kv, w_uv)
        scores = torch.einsum("bhqd,bhkd->bhqk", q, k) * (D ** -0.5)
        off = Sk - Sq
        qi = torch.arange(Sq)[:, None]
        kj = torch.arange(Sk)[None, :]
        cmask = kj <= (qi + off)
        scores = torch.where(cmask[None, None], scores, -1e9)
        p = _softmax(scores, axis=-1)
        ctx = torch.einsum("bhqk,bhkd->bhqd", p, v)
        return torch.einsum("bhqd,hdm->bqm", ctx, w_o)

    a = _A_ref(A_x, A_wqkv, A_w_phi, A_wo)
    b = _B_ref(B_x, B_c_kv, B_w_q, B_w_uk, B_w_uv, B_w_o)
    return torch.concatenate([a, b], axis=-1)
```

```cpp
#include <hip/hip_runtime.h>
#include <hip/hip_bf16.h>
typedef unsigned short u16;
__device__ __forceinline__ float bf2f(u16 x){ return __uint_as_float(((unsigned)x)<<16); }
__device__ __forceinline__ u16 f2bf(float f){ unsigned u=__float_as_uint(f); return (u16)((u+0x7FFF+((u>>16)&1))>>16); }

__global__ void gemm_naive(const u16* A,const u16* B,u16* C,int M,int N,int K,int ldc,float scale){
  __shared__ float As[32][33], Bs[32][33];
  int tx=threadIdx.x&31, ty=threadIdx.x>>5;
  int row0=blockIdx.y*32, col0=blockIdx.x*32;
  float acc[4]={0,0,0,0};
  for(int k0=0;k0<K;k0+=32){
    for(int i=0;i<4;i++){ int r=ty*4+i; As[r][tx]=bf2f(A[(size_t)(row0+r)*K+k0+tx]); Bs[r][tx]=bf2f(B[(size_t)(k0+r)*N+col0+tx]); }
    __syncthreads();
    for(int k=0;k<32;k++){ float b=Bs[k][tx]; for(int i=0;i<4;i++) acc[i]+=As[ty*4+i][k]*b; }
    __syncthreads();
  }
  for(int i=0;i<4;i++) C[(size_t)(row0+ty*4+i)*ldc+col0+tx]=f2bf(acc[i]*scale);
}
__global__ void feat(const u16* qkv,const u16* wphi,float* phi){
  int idx=blockIdx.x*blockDim.x+threadIdx.x;
  int h=idx&31, which=(idx>>5)&1, s=idx>>6;
  float x[16]; for(int d=0;d<16;d++) x[d]=bf2f(qkv[(size_t)s*1536+which*512+h*16+d]);
  float p[16]; float mx=-1e30f, mn=1e30f;
  for(int f=0;f<16;f++){ float a=0; for(int d=0;d<16;d++) a+=x[d]*bf2f(wphi[h*256+d*16+f]); p[f]=a; mx=fmaxf(mx,a); mn=fminf(mn,a);}
  float s1=0,s2=0; for(int f=0;f<16;f++){ s1+=__expf(p[f]-mx); s2+=__expf(mn-p[f]); }
  float* o=phi+(size_t)idx*32;
  for(int f=0;f<16;f++){ o[f]=__expf(p[f]-mx)/s1; o[16+f]=__expf(mn-p[f])/s2; }
}
__global__ void kvsum(const u16* qkv,const float* phi,float* kvz){
  int h=blockIdx.x; int t=threadIdx.x; if(t>=32*17) return; int f=t/17, d=t%17;
  float a=0; for(int s=0;s<2048;s++){ float pk=phi[((size_t)s*64+32+h)*32+f]; float v=d<16?bf2f(qkv[(size_t)s*1536+1024+h*16+d]):1.f; a+=pk*v; }
  kvz[(h*32+f)*17+d]=a;
}
__global__ void lin_o(const float* phi,const float* kvz,u16* o){
  int idx=blockIdx.x*blockDim.x+threadIdx.x; int d=idx&15,h=(idx>>4)&31,s=idx>>9;
  float num=0,den=0; for(int f=0;f<32;f++){ float p=phi[((size_t)s*64+h)*32+f]; num+=p*kvz[(h*32+f)*17+d]; den+=p*kvz[(h*32+f)*17+16]; }
  o[idx]=f2bf(num/(den+1e-6f));
}
__global__ __launch_bounds__(256) void attn_naive(const u16* q,const u16* k,const u16* v,u16* ctx){
  __shared__ float sc[4][2048]; __shared__ float qs[4][128];
  int w=threadIdx.x>>6, lane=threadIdx.x&63; int row=blockIdx.x*4+w, h=blockIdx.y;
  qs[w][lane]=bf2f(q[(size_t)row*4096+h*128+lane]); qs[w][lane+64]=bf2f(q[(size_t)row*4096+h*128+lane+64]);
  __syncthreads();
  float mx=-1e30f;
  for(int j=lane;j<=row;j+=64){ float a=0; const uint4* kp=(const uint4*)(k+(size_t)j*4096+h*128);
    for(int c=0;c<16;c++){ uint4 u=kp[c]; unsigned uu[4]={u.x,u.y,u.z,u.w}; for(int e=0;e<4;e++){ a+=qs[w][c*8+e*2]*__uint_as_float(uu[e]<<16)+qs[w][c*8+e*2+1]*__uint_as_float(uu[e]&0xffff0000u);} }
    a*=0.08838834764831845f; sc[w][j]=a; mx=fmaxf(mx,a); }
  for(int o=32;o;o>>=1) mx=fmaxf(mx,__shfl_xor(mx,o));
  float sum=0; for(int j=lane;j<=row;j+=64){ float p=__expf(sc[w][j]-mx); sc[w][j]=p; sum+=p; }
  for(int o=32;o;o>>=1) sum+=__shfl_xor(sum,o);
  __syncthreads();
  float a0=0,a1=0; for(int j=0;j<=row;j++){ float p=sc[w][j]; unsigned u=*(const unsigned*)(v+(size_t)j*4096+h*128+lane*2); a0+=p*__uint_as_float(u<<16); a1+=p*__uint_as_float(u&0xffff0000u); }
  ctx[(size_t)row*4096+h*128+lane*2]=f2bf(a0/sum); ctx[(size_t)row*4096+h*128+lane*2+1]=f2bf(a1/sum);
}
extern "C" void kernel_launch(void* const* d_in, const int* in_sizes, int n_in, void* d_out, int out_size, void* d_ws, size_t ws_size, hipStream_t stream) {
  const u16 *Ax=(const u16*)d_in[0],*Awqkv=(const u16*)d_in[1],*Awphi=(const u16*)d_in[2],*Awo=(const u16*)d_in[3];
  const u16 *Bx=(const u16*)d_in[4],*Bc=(const u16*)d_in[5],*Bwq=(const u16*)d_in[6],*Bwk=(const u16*)d_in[7],*Bwv=(const u16*)d_in[8],*Bwo=(const u16*)d_in[9];
  u16* out=(u16*)d_out; char* ws=(char*)d_ws;
  u16* qkv=(u16*)ws; ws+=2048*1536*2;
  float* phi=(float*)ws; ws+=2048*64*32*4;
  float* kvz=(float*)ws; ws+=32*32*17*4+1024;
  u16* o=(u16*)ws; ws+=2048*512*2;
  u16* q=(u16*)ws; ws+=2048*4096*2; u16* k=(u16*)ws; ws+=2048*4096*2; u16* v=(u16*)ws; ws+=2048*4096*2; u16* ctx=(u16*)ws;
  gemm_naive<<<dim3(1536/32,64),256,0,stream>>>(Ax,Awqkv,qkv,2048,1536,512,1536,1.f);
  feat<<<2048*64/256,256,0,stream>>>(qkv,Awphi,phi);
  kvsum<<<32,576,0,stream>>>(qkv,phi,kvz);
  lin_o<<<2048*512/256,256,0,stream>>>(phi,kvz,o);
  gemm_naive<<<dim3(16,64),256,0,stream>>>(o,Awo,out,2048,512,512,1024,1.f);
  gemm_naive<<<dim3(128,64),256,0,stream>>>(Bx,Bwq,q,2048,4096,512,4096,1.f);
  gemm_naive<<<dim3(128,64),256,0,stream>>>(Bc,Bwk,k,2048,4096,512,4096,1.f);
  gemm_naive<<<dim3(128,64),256,0,stream>>>(Bc,Bwv,v,2048,4096,512,4096,1.f);
  attn_naive<<<dim3(512,32),256,0,stream>>>(q,k,v,ctx);
  gemm_naive<<<dim3(16,64),256,0,stream>>>(ctx,Bwo,out+512,2048,512,4096,1024,1.f);
}
```

```cpp
#include <hip/hip_runtime.h>

__global__ __launch_bounds__(256, 1) void fused_fwd(const void* p0, const void* p1, const void* p2, const void* p3, const void* p4, const void* p5,
    const void* p6, const void* p7, const void* p8, const void* p9, void* out, void* ws) {
  __shared__ __attribute__((aligned(16))) char lds[163840];
  unsigned w = __builtin_amdgcn_readfirstlane(threadIdx.x >> 6);
  asm volatile("" :: "s"((unsigned)(size_t)lds));
  asm volatile(
    "s_load_dwordx16 s[36:51], %0, 0\n"
    "s_load_dwordx8 s[52:59], %0, 64\n"
    "s_mov_b32 s8, %1\n"
    "s_mov_b32 s9, %2\n"
    "v_mbcnt_lo_u32_b32 v255, -1, 0\n"
    "v_mbcnt_hi_u32_b32 v255, -1, v255\n"
    "s_getpc_b64 s[14:15]\n"
    "L_pc10_%=:\n"
    "v_lshlrev_b32 v250, 7, v255\n"
    "v_lshl_add_u32 v250, s8, 13, v250\n"
    "v_min_u32 v250, L_end_%=-L_pc10_%=-4, v250\n"
    "global_load_dword a234, v250, s[14:15]\n"
    "s_and_b32 s10, s9, 7\n"
    "s_lshr_b32 s11, s9, 3\n"
    "s_lshr_b32 s14, s11, 3\n"
    "s_lshl_b32 s12, s10, 2\n"
    "s_add_u32 s12, s12, s14\n"
    "s_and_b32 s13, s11, 7\n"
    "v_lshrrev_b32 v250, 4, v255\n"
    "v_or_b32 v250, 0, v250\n"
    "v_and_b32 v236, 7, v255\n"
    "v_xor_b32 v250, v250, v236\n"
    "v_lshrrev_b32 v236, 3, v255\n"
    "v_mul_u32_u24 v236, 1024, v236\n"
    "v_lshl_add_u32 v236, v250, 4, v236\n"
    "v_xor_b32 v237, 64, v236\n"
    "v_add_u32 v237, 8192, v237\n"
    "v_add_u32 v238, 16384, v236\n"
    "v_add_u32 v239, 16384, v237\n"
    "v_add_u32 v240, 32768, v236\n"
    "v_add_u32 v241, 32768, v237\n"
    "v_add_u32 v242, 49152, v236\n"
    "v_add_u32 v243, 49152, v237\n"
    "v_lshrrev_b32 v250, 4, v255\n"
    "v_mul_u32_u24 v244, 8192, v250\n"
    "v_or_b32 v250, 0, v250\n"
    "v_lshlrev_b32 v250, 1, v250\n"
    "v_and_b32 v251, 15, v255\n"
    "v_xor_b32 v250, v250, v251\n"
    "v_lshl_add_u32 v244, v250, 4, v244\n"
    "v_xor_b32 v246, 128, v244\n"
    "v_add_u32 v245, 32768, v244\n"
    "v_add_u32 v246, 65536, v246\n"
    "v_add_u32 v247, 32768, v246\n"
    "s_lshl_b32 s16, s13, 18\n"
    "s_lshl_b32 s15, s8, 16\n"
    "s_add_u32 s16, s16, s15\n"
    "s_lshl_b32 s17, s8, 17\n"
    "s_lshl_b32 s18, s8, 13\n"
    "s_lshl_b32 s19, s8, 12\n"
    "s_add_u32 s19, s19, 32768\n"
    "s_waitcnt lgkmcnt(0)\n"
    "s_mov_b32 s60, s46\n"
    "s_and_b32 s61, s47, 0xffff\n"
    "s_mov_b32 s62, 0x200000\n"
    "s_mov_b32 s63, 0x20000\n"
    "s_lshl_b32 s15, s12, 8\n"
    "s_add_u32 s64, s50, s15\n"
    "s_addc_u32 s65, s51, 0\n"
    "s_and_b32 s65, s65, 0xffff\n"
    "s_mov_b32 s66, 0x400000\n"
    "s_mov_b32 s67, 0x20000\n"
    "s_sub_u32 s66, s66, s15\n"
    "s_add_u32 s68, s52, s15\n"
    "s_addc_u32 s69, s53, 0\n"
    "s_and_b32 s69, s69, 0xffff\n"
    "s_mov_b32 s70, 0x400000\n"
    "s_mov_b32 s71, 0x20000\n"
    "s_sub_u32 s70, s70, s15\n"
    "s_add_u32 m0, s18, 0\n"
    "s_nop 0\n"
    "buffer_load_dwordx4 v236, s[60:63], s16 offen lds\n"
    "s_add_u32 m0, s18, 1024\n"
    "s_nop 0\n"
    "buffer_load_dwordx4 v237, s[60:63], s16 offen lds\n"
    "s_add_u32 m0, s18, 2048\n"
    "s_nop 0\n"
    "buffer_load_dwordx4 v238, s[60:63], s16 offen lds\n"
    "s_add_u32 m0, s18, 3072\n"
    "s_nop 0\n"
    "buffer_load_dwordx4 v239, s[60:63], s16 offen lds\n"
    "s_add_u32 m0, s18, 4096\n"
    "s_nop 0\n"
    "buffer_load_dwordx4 v240, s[60:63], s16 offen lds\n"
    "s_add_u32 m0, s18, 5120\n"
    "s_nop 0\n"
    "buffer_load_dwordx4 v241, s[60:63], s16 offen lds\n"
    "s_add_u32 m0, s18, 6144\n"
    "s_nop 0\n"
    "buffer_load_dwordx4 v242, s[60:63], s16 offen lds\n"
    "s_add_u32 m0, s18, 7168\n"
    "s_nop 0\n"
    "buffer_load_dwordx4 v243, s[60:63], s16 offen lds\n"
    "s_add_u32 m0, s19, 0\n"
    "s_nop 0\n"
    "buffer_load_dwordx4 v244, s[64:67], s17 offen lds\n"
    "s_add_u32 m0, s19, 1024\n"
    "s_nop 0\n"
    "buffer_load_dwordx4 v245, s[64:67], s17 offen lds\n"
    "s_add_u32 m0, s19, 2048\n"
    "s_nop 0\n"
    "buffer_load_dwordx4 v246, s[64:67], s17 offen lds\n"
    "s_add_u32 m0, s19, 3072\n"
    "s_nop 0\n"
    "buffer_load_dwordx4 v247, s[64:67], s17 offen lds\n"
    "s_add_u32 m0, s19, 16384\n"
    "s_nop 0\n"
    "buffer_load_dwordx4 v244, s[68:71], s17 offen lds\n"
    "s_add_u32 m0, s19, 17408\n"
    "s_nop 0\n"
    "buffer_load_dwordx4 v245, s[68:71], s17 offen lds\n"
    "s_add_u32 m0, s19, 18432\n"
    "s_nop 0\n"
    "buffer_load_dwordx4 v246, s[68:71], s17 offen lds\n"
    "s_add_u32 m0, s19, 19456\n"
    "s_nop 0\n"
    "buffer_load_dwordx4 v247, s[68:71], s17 offen lds\n"
    "s_add_u32 s16, s16, 128\n"
    "s_add_u32 s17, s17, 524288\n"
    "s_add_u32 s14, s58, 0x3400000\n"
    "s_addc_u32 s15, s59, 0\n"
    "s_load_dwordx2 s[28:29], s[14:15], 0\n"
    "s_lshl_b32 s14, s11, 2\n"
    "s_add_u32 s14, s14, s8\n"
    "v_min_u32 v250, 23, v255\n"
    "v_cmp_gt_u32 vcc, 16, v250\n"
    "v_cmp_gt_u32 s[20:21], 20, v250\n"
    "s_lshl_b32 s15, s14, 14\n"
    "v_lshlrev_b32 v251, 10, v250\n"
    "v_add_u32 v251, s15, v251\n"
    "v_add_u32 v251, 256, v251\n"
    "s_lshr_b32 s15, s14, 1\n"
    "s_lshl_b32 s15, s15, 13\n"
    "s_and_b32 s22, s14, 1\n"
    "s_lshl_b32 s22, s22, 9\n"
    "s_add_u32 s15, s15, s22\n"
    "s_lshl_b32 s22, s10, 10\n"
    "s_add_u32 s15, s15, s22\n"
    "s_add_u32 s15, s15, 1048576\n"
    "v_and_b32 v250, 3, v250\n"
    "v_lshlrev_b32 v250, 7, v250\n"
    "v_add_u32 v250, s15, v250\n"
    "v_cndmask_b32 v250, v250, v251, vcc\n"
    "v_mov_b32 v252, s52\n"
    "v_mov_b32 v251, s50\n"
    "v_cndmask_b32 v252, v252, v251, s[20:21]\n"
    "v_mov_b32 v251, s46\n"
    "v_cndmask_b32 v252, v252, v251, vcc\n"
    "v_mov_b32 v253, s53\n"
    "v_mov_b32 v251, s51\n"
    "v_cndmask_b32 v253, v253, v251, s[20:21]\n"
    "v_mov_b32 v251, s47\n"
    "v_cndmask_b32 v253, v253, v251, vcc\n"
    "v_mov_b32 v254, 524288\n"
    "v_mov_b32 v251, 128\n"
    "v_cndmask_b32 v254, v254, v251, vcc\n"
    "v_add_co_u32 v252, vcc, v252, v250\n"
    "s_nop 1\n"
    "v_addc_co_u32 v253, vcc, 0, v253, vcc\n"
    "s_and_b32 s15, s8, 1\n"
    "s_lshl_b32 s15, s15, 14\n"
    "v_lshrrev_b32 v250, 4, v255\n"
    "v_or_b32 v250, 0, v250\n"
    "v_bfe_u32 v224, v255, 1, 3\n"
    "v_xor_b32 v250, v250, v224\n"
    "v_and_b32 v224, 15, v255\n"
    "v_lshlrev_b32 v224, 7, v224\n"
    "v_lshl_add_u32 v224, v250, 4, v224\n"
    "v_add_u32 v224, s15, v224\n"
    "v_xor_b32 v225, 64, v224\n"
    "v_add_u32 v226, 65536, v224\n"
    "v_add_u32 v227, 65536, v225\n"
    "s_lshr_b32 s15, s8, 1\n"
    "s_lshl_b32 s15, s15, 2\n"
    "v_bfe_u32 v250, v255, 2, 3\n"
    "s_add_u32 s14, s15, 0\n"
    "v_xor_b32 v250, s14, v250\n"
    "v_lshrrev_b32 v228, 4, v255\n"
    "v_lshlrev_b32 v228, 3, v228\n"
    "v_bfe_u32 v251, v255, 2, 2\n"
    "v_add_u32 v228, v228, v251\n"
    "v_lshlrev_b32 v228, 8, v228\n"
    "v_lshl_add_u32 v228, v250, 5, v228\n"
    "v_and_b32 v250, 3, v255\n"
    "v_lshl_add_u32 v228, v250, 3, v228\n"
    "v_add_u32 v228, 32768, v228\n"
    "v_xor_b32 v229, 32, v228\n"
    "v_xor_b32 v230, 64, v228\n"
    "v_xor_b32 v231, 96, v228\n"
    "v_add_u32 v232, 65536, v228\n"
    "v_add_u32 v233, 65536, v229\n"
    "v_add_u32 v234, 65536, v230\n"
    "v_add_u32 v235, 65536, v231\n"
    "v_lshlrev_b32 v248, 4, v255\n"
    "s_mul_i32 s15, s12, 1082368\n"
    "s_add_u32 s72, s58, s15\n"
    "s_addc_u32 s73, s59, 0\n"
    "s_add_u32 s72, s72, 0x6000000\n"
    "s_addc_u32 s73, s73, 0\n"
    "s_and_b32 s73, s73, 0xffff\n"
    "s_mov_b32 s74, 0x108400\n"
    "s_mov_b32 s75, 0x20000\n"
    "s_mul_i32 s15, s13, 65536\n"
    "s_and_b32 s14, s8, 1\n"
    "s_mul_i32 s14, s14, 32768\n"
    "s_add_u32 s15, s15, s14\n"
    "s_lshr_b32 s14, s8, 1\n"
    "s_lshl_b32 s26, s14, 11\n"
    "s_add_u32 s26, s26, s15\n"
    "s_lshl_b32 s27, s14, 12\n"
    "s_add_u32 s27, s27, s15\n"
    "s_add_u32 s27, s27, 524288\n"
    "s_lshr_b32 s14, s10, 1\n"
    "s_lshl_b32 s14, s14, 2\n"
    "s_and_b32 s15, s11, 3\n"
    "s_add_u32 s33, s14, s15\n"
    "s_and_b32 s14, s10, 1\n"
    "s_lshl_b32 s14, s14, 3\n"
    "s_lshr_b32 s15, s11, 2\n"
    "s_add_u32 s34, s14, s15\n"
    "s_waitcnt lgkmcnt(0)\n"
    "s_add_u32 s28, s28, 1\n"
    "s_addc_u32 s29, s29, 0\n"
    "s_add_u32 m0, s18, 65536\n"
    "s_nop 0\n"
    "buffer_load_dwordx4 v236, s[60:63], s16 offen lds\n"
    "s_add_u32 m0, s18, 66560\n"
    "s_nop 0\n"
    "buffer_load_dwordx4 v237, s[60:63], s16 offen lds\n"
    "s_add_u32 m0, s18, 67584\n"
    "s_nop 0\n"
    "buffer_load_dwordx4 v238, s[60:63], s16 offen lds\n"
    "s_add_u32 m0, s18, 68608\n"
    "s_nop 0\n"
    "buffer_load_dwordx4 v239, s[60:63], s16 offen lds\n"
    "s_add_u32 m0, s18, 69632\n"
    "s_nop 0\n"
    "buffer_load_dwordx4 v240, s[60:63], s16 offen lds\n"
    "s_add_u32 m0, s18, 70656\n"
    "s_nop 0\n"
    "buffer_load_dwordx4 v241, s[60:63], s16 offen lds\n"
    "s_add_u32 m0, s18, 71680\n"
    "s_nop 0\n"
    "buffer_load_dwordx4 v242, s[60:63], s16 offen lds\n"
    "s_add_u32 m0, s18, 72704\n"
    "s_nop 0\n"
    "buffer_load_dwordx4 v243, s[60:63], s16 offen lds\n"
    "s_add_u32 m0, s19, 65536\n"
    "s_nop 0\n"
    "buffer_load_dwordx4 v244, s[64:67], s17 offen lds\n"
    "s_add_u32 m0, s19, 66560\n"
    "s_nop 0\n"
    "buffer_load_dwordx4 v245, s[64:67], s17 offen lds\n"
    "s_add_u32 m0, s19, 67584\n"
    "s_nop 0\n"
    "buffer_load_dwordx4 v246, s[64:67], s17 offen lds\n"
    "s_add_u32 m0, s19, 68608\n"
    "s_nop 0\n"
    "buffer_load_dwordx4 v247, s[64:67], s17 offen lds\n"
    "s_add_u32 m0, s19, 81920\n"
    "s_nop 0\n"
    "buffer_load_dwordx4 v244, s[68:71], s17 offen lds\n"
    "s_add_u32 m0, s19, 82944\n"
    "s_nop 0\n"
    "buffer_load_dwordx4 v245, s[68:71], s17 offen lds\n"
    "s_add_u32 m0, s19, 83968\n"
    "s_nop 0\n"
    "buffer_load_dwordx4 v246, s[68:71], s17 offen lds\n"
    "s_add_u32 m0, s19, 84992\n"
    "s_nop 0\n"
    "buffer_load_dwordx4 v247, s[68:71], s17 offen lds\n"
    "s_waitcnt vmcnt(16)\n"
    "s_barrier\n"
    "ds_read_b64_tr_b16 a[48:49], v228 offset:16384\n"
    "ds_read_b64_tr_b16 a[50:51], v228 offset:17408\n"
    "ds_read_b64_tr_b16 a[52:53], v229 offset:16384\n"
    "ds_read_b64_tr_b16 a[54:55], v229 offset:17408\n"
    "ds_read_b128 a[0:3], v224 offset:0\n"
    "ds_read_b128 a[4:7], v224 offset:2048\n"
    "ds_read_b128 a[8:11], v224 offset:4096\n"
    "ds_read_b128 a[12:15], v224 offset:6144\n"
    "ds_read_b128 a[16:19], v224 offset:8192\n"
    "ds_read_b128 a[20:23], v224 offset:10240\n"
    "ds_read_b128 a[24:27], v224 offset:12288\n"
    "ds_read_b128 a[28:31], v224 offset:14336\n"
    "ds_read_b64_tr_b16 a[56:57], v230 offset:16384\n"
    "ds_read_b64_tr_b16 a[58:59], v230 offset:17408\n"
    "ds_read_b64_tr_b16 a[60:61], v231 offset:16384\n"
    "ds_read_b64_tr_b16 a[62:63], v231 offset:17408\n"
    "ds_read_b64_tr_b16 a[32:33], v228 offset:0\n"
    "ds_read_b64_tr_b16 a[34:35], v228 offset:1024\n"
    "ds_read_b64_tr_b16 a[36:37], v229 offset:0\n"
    "ds_read_b64_tr_b16 a[38:39], v229 offset:1024\n"
    "ds_read_b64_tr_b16 a[40:41], v230 offset:0\n"
    "ds_read_b64_tr_b16 a[42:43], v230 offset:1024\n"
    "ds_read_b64_tr_b16 a[44:45], v231 offset:0\n"
    "ds_read_b64_tr_b16 a[46:47], v231 offset:1024\n"
    "s_waitcnt lgkmcnt(15)\n"
    "v_mfma_f32_16x16x32_bf16 v[128:131], a[0:3], a[48:51], 0\n"
    "ds_read_b64_tr_b16 a[112:113], v228 offset:24576\n"
    "s_add_u32 s16, s16, 128\n"
    "s_add_u32 s17, s17, 524288\n"
    "v_mfma_f32_16x16x32_bf16 v[136:139], a[0:3], a[52:55], 0\n"
    "global_load_dword a221, v[252:253], off\n"
    "v_mfma_f32_16x16x32_bf16 v[132:135], a[4:7], a[48:51], 0\n"
    "ds_read_b64_tr_b16 a[114:115], v228 offset:25600\n"
    "v_add_co_u32 v252, vcc, v252, v254\n"
    "s_nop 1\n"
    "v_mfma_f32_16x16x32_bf16 v[140:143], a[4:7], a[52:55], 0\n"
    "v_addc_co_u32 v253, vcc, 0, v253, vcc\n"
    "v_mfma_f32_16x16x32_bf16 v[160:163], a[8:11], a[48:51], 0\n"
    "ds_read_b64_tr_b16 a[116:117], v229 offset:24576\n"
    "v_mfma_f32_16x16x32_bf16 v[168:171], a[8:11], a[52:55], 0\n"
    "global_load_dword a222, v[252:253], off\n"
    "v_mfma_f32_16x16x32_bf16 v[164:167], a[12:15], a[48:51], 0\n"
    "ds_read_b64_tr_b16 a[118:119], v229 offset:25600\n"
    "v_add_co_u32 v252, vcc, v252, v254\n"
    "s_nop 1\n"
    "v_mfma_f32_16x16x32_bf16 v[172:175], a[12:15], a[52:55], 0\n"
    "v_addc_co_u32 v253, vcc, 0, v253, vcc\n"
    "v_mfma_f32_16x16x32_bf16 v[192:195], a[16:19], a[48:51], 0\n"
    "ds_read_b128 a[64:67], v225 offset:0\n"
    "v_mfma_f32_16x16x32_bf16 v[200:203], a[16:19], a[52:55], 0\n"
    "s_waitcnt lgkmcnt(15)\n"
    "v_mfma_f32_16x16x32_bf16 v[196:199], a[20:23], a[48:51], 0\n"
    "ds_read_b128 a[68:71], v225 offset:2048\n"
    "v_mfma_f32_16x16x32_bf16 v[204:207], a[20:23], a[52:55], 0\n"
    "v_mfma_f32_16x16x32_bf16 a[128:131], a[24:27], a[48:51], 0\n"
    "ds_read_b128 a[72:75], v225 offset:4096\n"
    "v_mfma_f32_16x16x32_bf16 a[136:139], a[24:27], a[52:55], 0\n"
    "v_mfma_f32_16x16x32_bf16 a[132:135], a[28:31], a[48:51], 0\n"
    "ds_read_b128 a[76:79], v225 offset:6144\n"
    "v_mfma_f32_16x16x32_bf16 a[140:143], a[28:31], a[52:55], 0\n"
    "v_mfma_f32_16x16x32_bf16 a[148:151], a[28:31], a[56:59], 0\n"
    "s_waitcnt lgkmcnt(13)\n"
    "ds_read_b128 a[80:83], v225 offset:8192\n"
    "v_mfma_f32_16x16x32_bf16 a[156:159], a[28:31], a[60:63], 0\n"
    "v_mfma_f32_16x16x32_bf16 a[144:147], a[24:27], a[56:59], 0\n"
    "ds_read_b128 a[84:87], v225 offset:10240\n"
    "v_mfma_f32_16x16x32_bf16 a[152:155], a[24:27], a[60:63], 0\n"
    "v_mfma_f32_16x16x32_bf16 v[212:215], a[20:23], a[56:59], 0\n"
    "ds_read_b128 a[88:91], v225 offset:12288\n"
    "v_mfma_f32_16x16x32_bf16 v[220:223], a[20:23], a[60:63], 0\n"
    "v_mfma_f32_16x16x32_bf16 v[208:211], a[16:19], a[56:59], 0\n"
    "ds_read_b128 a[92:95], v225 offset:14336\n"
    "v_mfma_f32_16x16x32_bf16 v[216:219], a[16:19], a[60:63], 0\n"
    "v_mfma_f32_16x16x32_bf16 v[180:183], a[12:15], a[56:59], 0\n"
    "ds_read_b64_tr_b16 a[120:121], v230 offset:24576\n"
    "v_mfma_f32_16x16x32_bf16 v[188:191], a[12:15], a[60:63], 0\n"
    "v_mfma_f32_16x16x32_bf16 v[176:179], a[8:11], a[56:59], 0\n"
    "ds_read_b64_tr_b16 a[122:123], v230 offset:25600\n"
    "v_mfma_f32_16x16x32_bf16 v[184:187], a[8:11], a[60:63], 0\n"
    "v_mfma_f32_16x16x32_bf16 v[148:151], a[4:7], a[56:59], 0\n"
    "ds_read_b64_tr_b16 a[124:125], v231 offset:24576\n"
    "v_mfma_f32_16x16x32_bf16 v[156:159], a[4:7], a[60:63], 0\n"
    "v_mfma_f32_16x16x32_bf16 v[144:147], a[0:3], a[56:59], 0\n"
    "ds_read_b64_tr_b16 a[126:127], v231 offset:25600\n"
    "v_mfma_f32_16x16x32_bf16 v[152:155], a[0:3], a[60:63], 0\n"
    "v_mfma_f32_16x16x32_bf16 v[0:3], a[32:35], a[0:3], 0\n"
    "s_waitcnt lgkmcnt(14)\n"
    "ds_read_b64_tr_b16 a[96:97], v228 offset:8192\n"
    "v_mfma_f32_16x16x32_bf16 v[4:7], a[36:39], a[0:3], 0\n"
    "v_mfma_f32_16x16x32_bf16 v[16:19], a[32:35], a[4:7], 0\n"
    "ds_read_b64_tr_b16 a[98:99], v228 offset:9216\n"
    "v_mfma_f32_16x16x32_bf16 v[20:23], a[36:39], a[4:7], 0\n"
    "v_mfma_f32_16x16x32_bf16 v[32:35], a[32:35], a[8:11], 0\n"
    "ds_read_b64_tr_b16 a[100:101], v229 offset:8192\n"
    "v_mfma_f32_16x16x32_bf16 v[36:39], a[36:39], a[8:11], 0\n"
    "v_mfma_f32_16x16x32_bf16 v[48:51], a[32:35], a[12:15], 0\n"
    "ds_read_b64_tr_b16 a[102:103], v229 offset:9216\n"
    "v_mfma_f32_16x16x32_bf16 v[52:55], a[36:39], a[12:15], 0\n"
    "v_mfma_f32_16x16x32_bf16 v[64:67], a[32:35], a[16:19], 0\n"
    "ds_read_b64_tr_b16 a[104:105], v230 offset:8192\n"
    "v_mfma_f32_16x16x32_bf16 v[68:71], a[36:39], a[16:19], 0\n"
    "v_mfma_f32_16x16x32_bf16 v[80:83], a[32:35], a[20:23], 0\n"
    "ds_read_b64_tr_b16 a[106:107], v230 offset:9216\n"
    "v_mfma_f32_16x16x32_bf16 v[84:87], a[36:39], a[20:23], 0\n"
    "v_mfma_f32_16x16x32_bf16 v[96:99], a[32:35], a[24:27], 0\n"
    "ds_read_b64_tr_b16 a[108:109], v231 offset:8192\n"
    "v_mfma_f32_16x16x32_bf16 v[100:103], a[36:39], a[24:27], 0\n"
    "v_mfma_f32_16x16x32_bf16 v[112:115], a[32:35], a[28:31], 0\n"
    "ds_read_b64_tr_b16 a[110:111], v231 offset:9216\n"
    "v_mfma_f32_16x16x32_bf16 v[116:119], a[36:39], a[28:31], 0\n"
    "v_mfma_f32_16x16x32_bf16 v[120:123], a[40:43], a[28:31], 0\n"
    "v_mfma_f32_16x16x32_bf16 v[124:127], a[44:47], a[28:31], 0\n"
    "v_mfma_f32_16x16x32_bf16 v[104:107], a[40:43], a[24:27], 0\n"
    "v_mfma_f32_16x16x32_bf16 v[108:111], a[44:47], a[24:27], 0\n"
    "v_mfma_f32_16x16x32_bf16 v[88:91], a[40:43], a[20:23], 0\n"
    "v_mfma_f32_16x16x32_bf16 v[92:95], a[44:47], a[20:23], 0\n"
    "v_mfma_f32_16x16x32_bf16 v[72:75], a[40:43], a[16:19], 0\n"
    "v_mfma_f32_16x16x32_bf16 v[76:79], a[44:47], a[16:19], 0\n"
    "v_mfma_f32_16x16x32_bf16 v[56:59], a[40:43], a[12:15], 0\n"
    "v_mfma_f32_16x16x32_bf16 v[60:63], a[44:47], a[12:15], 0\n"
    "v_mfma_f32_16x16x32_bf16 v[40:43], a[40:43], a[8:11], 0\n"
    "v_mfma_f32_16x16x32_bf16 v[44:47], a[44:47], a[8:11], 0\n"
    "v_mfma_f32_16x16x32_bf16 v[24:27], a[40:43], a[4:7], 0\n"
    "v_mfma_f32_16x16x32_bf16 v[28:31], a[44:47], a[4:7], 0\n"
    "v_mfma_f32_16x16x32_bf16 v[8:11], a[40:43], a[0:3], 0\n"
    "v_mfma_f32_16x16x32_bf16 v[12:15], a[44:47], a[0:3], 0\n"
    "s_waitcnt lgkmcnt(15)\n"
    "v_mfma_f32_16x16x32_bf16 v[128:131], a[64:67], a[112:115], v[128:131]\n"
    "v_mfma_f32_16x16x32_bf16 v[136:139], a[64:67], a[116:119], v[136:139]\n"
    "v_mfma_f32_16x16x32_bf16 v[132:135], a[68:71], a[112:115], v[132:135]\n"
    "v_mfma_f32_16x16x32_bf16 v[140:143], a[68:71], a[116:119], v[140:143]\n"
    "s_waitcnt vmcnt(2) lgkmcnt(0)\n"
    "s_barrier\n"
    "v_mfma_f32_16x16x32_bf16 v[160:163], a[72:75], a[112:115], v[160:163]\n"
    "ds_read_b64_tr_b16 a[48:49], v232 offset:16384\n"
    "s_add_u32 m0, s18, 0\n"
    "v_mfma_f32_16x16x32_bf16 v[168:171], a[72:75], a[116:119], v[168:171]\n"
    "buffer_load_dwordx4 v236, s[60:63], s16 offen lds\n"
    "v_mfma_f32_16x16x32_bf16 v[164:167], a[76:79], a[112:115], v[164:167]\n"
    "ds_read_b64_tr_b16 a[50:51], v232 offset:17408\n"
    "s_add_u32 m0, s18, 1024\n"
    "v_mfma_f32_16x16x32_bf16 v[172:175], a[76:79], a[116:119], v[172:175]\n"
    "buffer_load_dwordx4 v237, s[60:63], s16 offen lds\n"
    "v_mfma_f32_16x16x32_bf16 v[192:195], a[80:83], a[112:115], v[192:195]\n"
    "ds_read_b64_tr_b16 a[52:53], v233 offset:16384\n"
    "v_mfma_f32_16x16x32_bf16 v[200:203], a[80:83], a[116:119], v[200:203]\n"
    "v_mfma_f32_16x16x32_bf16 v[196:199], a[84:87], a[112:115], v[196:199]\n"
    "ds_read_b64_tr_b16 a[54:55], v233 offset:17408\n"
    "s_add_u32 m0, s18, 2048\n"
    "v_mfma_f32_16x16x32_bf16 v[204:207], a[84:87], a[116:119], v[204:207]\n"
    "buffer_load_dwordx4 v238, s[60:63], s16 offen lds\n"
    "v_mfma_f32_16x16x32_bf16 a[128:131], a[88:91], a[112:115], a[128:131]\n"
    "ds_read_b128 a[0:3], v226 offset:0\n"
    "v_mfma_f32_16x16x32_bf16 a[136:139], a[88:91], a[116:119], a[136:139]\n"
    "v_mfma_f32_16x16x32_bf16 a[132:135], a[92:95], a[112:115], a[132:135]\n"
    "ds_read_b128 a[4:7], v226 offset:2048\n"
    "s_add_u32 m0, s18, 3072\n"
    "v_mfma_f32_16x16x32_bf16 a[140:143], a[92:95], a[116:119], a[140:143]\n"
    "buffer_load_dwordx4 v239, s[60:63], s16 offen lds\n"
    "v_mfma_f32_16x16x32_bf16 a[148:151], a[92:95], a[120:123], a[148:151]\n"
    "ds_read_b128 a[8:11], v226 offset:4096\n"
    "v_mfma_f32_16x16x32_bf16 a[156:159], a[92:95], a[124:127], a[156:159]\n"
    "v_mfma_f32_16x16x32_bf16 a[144:147], a[88:91], a[120:123], a[144:147]\n"
    "ds_read_b128 a[12:15], v226 offset:6144\n"
    "s_add_u32 m0, s18, 4096\n"
    "v_mfma_f32_16x16x32_bf16 a[152:155], a[88:91], a[124:127], a[152:155]\n"
    "buffer_load_dwordx4 v240, s[60:63], s16 offen lds\n"
    "v_mfma_f32_16x16x32_bf16 v[212:215], a[84:87], a[120:123], v[212:215]\n"
    "ds_read_b128 a[16:19], v226 offset:8192\n"
    "v_mfma_f32_16x16x32_bf16 v[220:223], a[84:87], a[124:127], v[220:223]\n"
    "v_mfma_f32_16x16x32_bf16 v[208:211], a[80:83], a[120:123], v[208:211]\n"
    "ds_read_b128 a[20:23], v226 offset:10240\n"
    "s_add_u32 m0, s18, 5120\n"
    "v_mfma_f32_16x16x32_bf16 v[216:219], a[80:83], a[124:127], v[216:219]\n"
    "buffer_load_dwordx4 v241, s[60:63], s16 offen lds\n"
    "v_mfma_f32_16x16x32_bf16 v[180:183], a[76:79], a[120:123], v[180:183]\n"
    "ds_read_b128 a[24:27], v226 offset:12288\n"
    "v_mfma_f32_16x16x32_bf16 v[188:191], a[76:79], a[124:127], v[188:191]\n"
    "v_mfma_f32_16x16x32_bf16 v[176:179], a[72:75], a[120:123], v[176:179]\n"
    "ds_read_b128 a[28:31], v226 offset:14336\n"
    "s_add_u32 m0, s18, 6144\n"
    "v_mfma_f32_16x16x32_bf16 v[184:187], a[72:75], a[124:127], v[184:187]\n"
    "buffer_load_dwordx4 v242, s[60:63], s16 offen lds\n"
    "v_mfma_f32_16x16x32_bf16 v[148:151], a[68:71], a[120:123], v[148:151]\n"
    "ds_read_b64_tr_b16 a[56:57], v234 offset:16384\n"
    "v_mfma_f32_16x16x32_bf16 v[156:159], a[68:71], a[124:127], v[156:159]\n"
    "v_mfma_f32_16x16x32_bf16 v[144:147], a[64:67], a[120:123], v[144:147]\n"
    "ds_read_b64_tr_b16 a[58:59], v234 offset:17408\n"
    "s_add_u32 m0, s18, 7168\n"
    "v_mfma_f32_16x16x32_bf16 v[152:155], a[64:67], a[124:127], v[152:155]\n"
    "buffer_load_dwordx4 v243, s[60:63], s16 offen lds\n"
    "v_mfma_f32_16x16x32_bf16 v[0:3], a[96:99], a[64:67], v[0:3]\n"
    "ds_read_b64_tr_b16 a[60:61], v235 offset:16384\n"
    "v_mfma_f32_16x16x32_bf16 v[4:7], a[100:103], a[64:67], v[4:7]\n"
    "v_mfma_f32_16x16x32_bf16 v[16:19], a[96:99], a[68:71], v[16:19]\n"
    "ds_read_b64_tr_b16 a[62:63], v235 offset:17408\n"
    "s_add_u32 m0, s19, 0\n"
    "v_mfma_f32_16x16x32_bf16 v[20:23], a[100:103], a[68:71], v[20:23]\n"
    "buffer_load_dwordx4 v244, s[64:67], s17 offen lds\n"
    "v_mfma_f32_16x16x32_bf16 v[32:35], a[96:99], a[72:75], v[32:35]\n"
    "ds_read_b64_tr_b16 a[32:33], v232 offset:0\n"
    "s_add_u32 m0, s19, 1024\n"
    "v_mfma_f32_16x16x32_bf16 v[36:39], a[100:103], a[72:75], v[36:39]\n"
    "buffer_load_dwordx4 v245, s[64:67], s17 offen lds\n"
    "v_mfma_f32_16x16x32_bf16 v[48:51], a[96:99], a[76:79], v[48:51]\n"
    "ds_read_b64_tr_b16 a[34:35], v232 offset:1024\n"
    "v_mfma_f32_16x16x32_bf16 v[52:55], a[100:103], a[76:79], v[52:55]\n"
    "v_mfma_f32_16x16x32_bf16 v[64:67], a[96:99], a[80:83], v[64:67]\n"
    "ds_read_b64_tr_b16 a[36:37], v233 offset:0\n"
    "s_add_u32 m0, s19, 2048\n"
    "v_mfma_f32_16x16x32_bf16 v[68:71], a[100:103], a[80:83], v[68:71]\n"
    "buffer_load_dwordx4 v246, s[64:67], s17 offen lds\n"
    "v_mfma_f32_16x16x32_bf16 v[80:83], a[96:99], a[84:87], v[80:83]\n"
    "ds_read_b64_tr_b16 a[38:39], v233 offset:1024\n"
    "v_mfma_f32_16x16x32_bf16 v[84:87], a[100:103], a[84:87], v[84:87]\n"
    "v_mfma_f32_16x16x32_bf16 v[96:99], a[96:99], a[88:91], v[96:99]\n"
    "ds_read_b64_tr_b16 a[40:41], v234 offset:0\n"
    "s_add_u32 m0, s19, 3072\n"
    "v_mfma_f32_16x16x32_bf16 v[100:103], a[100:103], a[88:91], v[100:103]\n"
    "buffer_load_dwordx4 v247, s[64:67], s17 offen lds\n"
    "v_mfma_f32_16x16x32_bf16 v[112:115], a[96:99], a[92:95], v[112:115]\n"
    "ds_read_b64_tr_b16 a[42:43], v234 offset:1024\n"
    "v_mfma_f32_16x16x32_bf16 v[116:119], a[100:103], a[92:95], v[116:119]\n"
    "v_mfma_f32_16x16x32_bf16 v[120:123], a[104:107], a[92:95], v[120:123]\n"
    "ds_read_b64_tr_b16 a[44:45], v235 offset:0\n"
    "s_add_u32 m0, s19, 16384\n"
    "v_mfma_f32_16x16x32_bf16 v[124:127], a[108:111], a[92:95], v[124:127]\n"
    "buffer_load_dwordx4 v244, s[68:71], s17 offen lds\n"
    "v_mfma_f32_16x16x32_bf16 v[104:107], a[104:107], a[88:91], v[104:107]\n"
    "ds_read_b64_tr_b16 a[46:47], v235 offset:1024\n"
    "v_mfma_f32_16x16x32_bf16 v[108:111], a[108:111], a[88:91], v[108:111]\n"
    "s_add_u32 m0, s19, 17408\n"
    "v_mfma_f32_16x16x32_bf16 v[88:91], a[104:107], a[84:87], v[88:91]\n"
    "buffer_load_dwordx4 v245, s[68:71], s17 offen lds\n"
    "v_mfma_f32_16x16x32_bf16 v[92:95], a[108:111], a[84:87], v[92:95]\n"
    "v_mfma_f32_16x16x32_bf16 v[72:75], a[104:107], a[80:83], v[72:75]\n"
    "v_mfma_f32_16x16x32_bf16 v[76:79], a[108:111], a[80:83], v[76:79]\n"
    "s_add_u32 m0, s19, 18432\n"
    "v_mfma_f32_16x16x32_bf16 v[56:59], a[104:107], a[76:79], v[56:59]\n"
    "buffer_load_dwordx4 v246, s[68:71], s17 offen lds\n"
    "v_mfma_f32_16x16x32_bf16 v[60:63], a[108:111], a[76:79], v[60:63]\n"
    "v_mfma_f32_16x16x32_bf16 v[40:43], a[104:107], a[72:75], v[40:43]\n"
    "v_mfma_f32_16x16x32_bf16 v[44:47], a[108:111], a[72:75], v[44:47]\n"
    "s_add_u32 m0, s19, 19456\n"
    "v_mfma_f32_16x16x32_bf16 v[24:27], a[104:107], a[68:71], v[24:27]\n"
    "buffer_load_dwordx4 v247, s[68:71], s17 offen lds\n"
    "v_mfma_f32_16x16x32_bf16 v[28:31], a[108:111], a[68:71], v[28:31]\n"
    "s_add_u32 s16, s16, 128\n"
    "s_add_u32 s17, s17, 524288\n"
    "v_mfma_f32_16x16x32_bf16 v[8:11], a[104:107], a[64:67], v[8:11]\n"
    "v_mfma_f32_16x16x32_bf16 v[12:15], a[108:111], a[64:67], v[12:15]\n"
    "s_waitcnt lgkmcnt(15)\n"
    "v_mfma_f32_16x16x32_bf16 v[128:131], a[0:3], a[48:51], v[128:131]\n"
    "ds_read_b64_tr_b16 a[112:113], v232 offset:24576\n"
    "v_mfma_f32_16x16x32_bf16 v[136:139], a[0:3], a[52:55], v[136:139]\n"
    "global_load_dword a223, v[252:253], off\n"
    "v_mfma_f32_16x16x32_bf16 v[132:135], a[4:7], a[48:51], v[132:135]\n"
    "ds_read_b64_tr_b16 a[114:115], v232 offset:25600\n"
    "v_add_co_u32 v252, vcc, v252, v254\n"
    "s_nop 1\n"
    "v_mfma_f32_16x16x32_bf16 v[140:143], a[4:7], a[52:55], v[140:143]\n"
    "v_addc_co_u32 v253, vcc, 0, v253, vcc\n"
    "v_mfma_f32_16x16x32_bf16 v[160:163], a[8:11], a[48:51], v[160:163]\n"
    "ds_read_b64_tr_b16 a[116:117], v233 offset:24576\n"
    "v_mfma_f32_16x16x32_bf16 v[168:171], a[8:11], a[52:55], v[168:171]\n"
    "v_mfma_f32_16x16x32_bf16 v[164:167], a[12:15], a[48:51], v[164:167]\n"
    "ds_read_b64_tr_b16 a[118:119], v233 offset:25600\n"
    "v_mfma_f32_16x16x32_bf16 v[172:175], a[12:15], a[52:55], v[172:175]\n"
    "v_mfma_f32_16x16x32_bf16 v[192:195], a[16:19], a[48:51], v[192:195]\n"
    "ds_read_b128 a[64:67], v227 offset:0\n"
    "v_mfma_f32_16x16x32_bf16 v[200:203], a[16:19], a[52:55], v[200:203]\n"
    "s_waitcnt lgkmcnt(15)\n"
    "v_mfma_f32_16x16x32_bf16 v[196:199], a[20:23], a[48:51], v[196:199]\n"
    "ds_read_b128 a[68:71], v227 offset:2048\n"
    "v_mfma_f32_16x16x32_bf16 v[204:207], a[20:23], a[52:55], v[204:207]\n"
    "v_mfma_f32_16x16x32_bf16 a[128:131], a[24:27], a[48:51], a[128:131]\n"
    "ds_read_b128 a[72:75], v227 offset:4096\n"
    "v_mfma_f32_16x16x32_bf16 a[136:139], a[24:27], a[52:55], a[136:139]\n"
    "v_mfma_f32_16x16x32_bf16 a[132:135], a[28:31], a[48:51], a[132:135]\n"
    "ds_read_b128 a[76:79], v227 offset:6144\n"
    "v_mfma_f32_16x16x32_bf16 a[140:143], a[28:31], a[52:55], a[140:143]\n"
    "v_mfma_f32_16x16x32_bf16 a[148:151], a[28:31], a[56:59], a[148:151]\n"
    "s_waitcnt lgkmcnt(13)\n"
    "ds_read_b128 a[80:83], v227 offset:8192\n"
    "v_mfma_f32_16x16x32_bf16 a[156:159], a[28:31], a[60:63], a[156:159]\n"
    "v_mfma_f32_16x16x32_bf16 a[144:147], a[24:27], a[56:59], a[144:147]\n"
    "ds_read_b128 a[84:87], v227 offset:10240\n"
    "v_mfma_f32_16x16x32_bf16 a[152:155], a[24:27], a[60:63], a[152:155]\n"
    "v_mfma_f32_16x16x32_bf16 v[212:215], a[20:23], a[56:59], v[212:215]\n"
    "ds_read_b128 a[88:91], v227 offset:12288\n"
    "v_mfma_f32_16x16x32_bf16 v[220:223], a[20:23], a[60:63], v[220:223]\n"
    "v_mfma_f32_16x16x32_bf16 v[208:211], a[16:19], a[56:59], v[208:211]\n"
    "ds_read_b128 a[92:95], v227 offset:14336\n"
    "v_mfma_f32_16x16x32_bf16 v[216:219], a[16:19], a[60:63], v[216:219]\n"
    "v_mfma_f32_16x16x32_bf16 v[180:183], a[12:15], a[56:59], v[180:183]\n"
    "ds_read_b64_tr_b16 a[120:121], v234 offset:24576\n"
    "v_mfma_f32_16x16x32_bf16 v[188:191], a[12:15], a[60:63], v[188:191]\n"
    "v_mfma_f32_16x16x32_bf16 v[176:179], a[8:11], a[56:59], v[176:179]\n"
    "ds_read_b64_tr_b16 a[122:123], v234 offset:25600\n"
    "v_mfma_f32_16x16x32_bf16 v[184:187], a[8:11], a[60:63], v[184:187]\n"
    "v_mfma_f32_16x16x32_bf16 v[148:151], a[4:7], a[56:59], v[148:151]\n"
    "ds_read_b64_tr_b16 a[124:125], v235 offset:24576\n"
    "v_mfma_f32_16x16x32_bf16 v[156:159], a[4:7], a[60:63], v[156:159]\n"
    "v_mfma_f32_16x16x32_bf16 v[144:147], a[0:3], a[56:59], v[144:147]\n"
    "ds_read_b64_tr_b16 a[126:127], v235 offset:25600\n"
    "v_mfma_f32_16x16x32_bf16 v[152:155], a[0:3], a[60:63], v[152:155]\n"
    "v_mfma_f32_16x16x32_bf16 v[0:3], a[32:35], a[0:3], v[0:3]\n"
    "s_waitcnt lgkmcnt(14)\n"
    "ds_read_b64_tr_b16 a[96:97], v232 offset:8192\n"
    "v_mfma_f32_16x16x32_bf16 v[4:7], a[36:39], a[0:3], v[4:7]\n"
    "v_mfma_f32_16x16x32_bf16 v[16:19], a[32:35], a[4:7], v[16:19]\n"
    "ds_read_b64_tr_b16 a[98:99], v232 offset:9216\n"
    "v_mfma_f32_16x16x32_bf16 v[20:23], a[36:39], a[4:7], v[20:23]\n"
    "v_mfma_f32_16x16x32_bf16 v[32:35], a[32:35], a[8:11], v[32:35]\n"
    "ds_read_b64_tr_b16 a[100:101], v233 offset:8192\n"
    "v_mfma_f32_16x16x32_bf16 v[36:39], a[36:39], a[8:11], v[36:39]\n"
    "v_mfma_f32_16x16x32_bf16 v[48:51], a[32:35], a[12:15], v[48:51]\n"
    "ds_read_b64_tr_b16 a[102:103], v233 offset:9216\n"
    "v_mfma_f32_16x16x32_bf16 v[52:55], a[36:39], a[12:15], v[52:55]\n"
    "v_mfma_f32_16x16x32_bf16 v[64:67], a[32:35], a[16:19], v[64:67]\n"
    "ds_read_b64_tr_b16 a[104:105], v234 offset:8192\n"
    "v_mfma_f32_16x16x32_bf16 v[68:71], a[36:39], a[16:19], v[68:71]\n"
    "v_mfma_f32_16x16x32_bf16 v[80:83], a[32:35], a[20:23], v[80:83]\n"
    "ds_read_b64_tr_b16 a[106:107], v234 offset:9216\n"
    "v_mfma_f32_16x16x32_bf16 v[84:87], a[36:39], a[20:23], v[84:87]\n"
    "v_mfma_f32_16x16x32_bf16 v[96:99], a[32:35], a[24:27], v[96:99]\n"
    "ds_read_b64_tr_b16 a[108:109], v235 offset:8192\n"
    "v_mfma_f32_16x16x32_bf16 v[100:103], a[36:39], a[24:27], v[100:103]\n"
    "v_mfma_f32_16x16x32_bf16 v[112:115], a[32:35], a[28:31], v[112:115]\n"
    "ds_read_b64_tr_b16 a[110:111], v235 offset:9216\n"
    "v_mfma_f32_16x16x32_bf16 v[116:119], a[36:39], a[28:31], v[116:119]\n"
    "v_mfma_f32_16x16x32_bf16 v[120:123], a[40:43], a[28:31], v[120:123]\n"
    "v_mfma_f32_16x16x32_bf16 v[124:127], a[44:47], a[28:31], v[124:127]\n"
    "v_mfma_f32_16x16x32_bf16 v[104:107], a[40:43], a[24:27], v[104:107]\n"
    "v_mfma_f32_16x16x32_bf16 v[108:111], a[44:47], a[24:27], v[108:111]\n"
    "v_mfma_f32_16x16x32_bf16 v[88:91], a[40:43], a[20:23], v[88:91]\n"
    "v_mfma_f32_16x16x32_bf16 v[92:95], a[44:47], a[20:23], v[92:95]\n"
    "v_mfma_f32_16x16x32_bf16 v[72:75], a[40:43], a[16:19], v[72:75]\n"
    "v_mfma_f32_16x16x32_bf16 v[76:79], a[44:47], a[16:19], v[76:79]\n"
    "v_mfma_f32_16x16x32_bf16 v[56:59], a[40:43], a[12:15], v[56:59]\n"
    "v_mfma_f32_16x16x32_bf16 v[60:63], a[44:47], a[12:15], v[60:63]\n"
    "v_mfma_f32_16x16x32_bf16 v[40:43], a[40:43], a[8:11], v[40:43]\n"
    "v_mfma_f32_16x16x32_bf16 v[44:47], a[44:47], a[8:11], v[44:47]\n"
    "v_mfma_f32_16x16x32_bf16 v[24:27], a[40:43], a[4:7], v[24:27]\n"
    "v_mfma_f32_16x16x32_bf16 v[28:31], a[44:47], a[4:7], v[28:31]\n"
    "v_mfma_f32_16x16x32_bf16 v[8:11], a[40:43], a[0:3], v[8:11]\n"
    "v_mfma_f32_16x16x32_bf16 v[12:15], a[44:47], a[0:3], v[12:15]\n"
    "s_waitcnt lgkmcnt(15)\n"
    "v_mfma_f32_16x16x32_bf16 v[128:131], a[64:67], a[112:115], v[128:131]\n"
    "v_mfma_f32_16x16x32_bf16 v[136:139], a[64:67], a[116:119], v[136:139]\n"
    "v_mfma_f32_16x16x32_bf16 v[132:135], a[68:71], a[112:115], v[132:135]\n"
    "v_mfma_f32_16x16x32_bf16 v[140:143], a[68:71], a[116:119], v[140:143]\n"
    "s_waitcnt vmcnt(1) lgkmcnt(0)\n"
    "s_barrier\n"
    "v_mfma_f32_16x16x32_bf16 v[160:163], a[72:75], a[112:115], v[160:163]\n"
    "ds_read_b64_tr_b16 a[48:49], v228 offset:16384\n"
    "s_add_u32 m0, s18, 65536\n"
    "v_mfma_f32_16x16x32_bf16 v[168:171], a[72:75], a[116:119], v[168:171]\n"
    "buffer_load_dwordx4 v236, s[60:63], s16 offen lds\n"
    "v_mfma_f32_16x16x32_bf16 v[164:167], a[76:79], a[112:115], v[164:167]\n"
    "ds_read_b64_tr_b16 a[50:51], v228 offset:17408\n"
    "s_add_u32 m0, s18, 66560\n"
    "v_mfma_f32_16x16x32_bf16 v[172:175], a[76:79], a[116:119], v[172:175]\n"
    "buffer_load_dwordx4 v237, s[60:63], s16 offen lds\n"
    "v_mfma_f32_16x16x32_bf16 v[192:195], a[80:83], a[112:115], v[192:195]\n"
    "ds_read_b64_tr_b16 a[52:53], v229 offset:16384\n"
    "v_mfma_f32_16x16x32_bf16 v[200:203], a[80:83], a[116:119], v[200:203]\n"
    "v_mfma_f32_16x16x32_bf16 v[196:199], a[84:87], a[112:115], v[196:199]\n"
    "ds_read_b64_tr_b16 a[54:55], v229 offset:17408\n"
    "s_add_u32 m0, s18, 67584\n"
    "v_mfma_f32_16x16x32_bf16 v[204:207], a[84:87], a[116:119], v[204:207]\n"
    "buffer_load_dwordx4 v238, s[60:63], s16 offen lds\n"
    "v_mfma_f32_16x16x32_bf16 a[128:131], a[88:91], a[112:115], a[128:131]\n"
    "ds_read_b128 a[0:3], v224 offset:0\n"
    "v_mfma_f32_16x16x32_bf16 a[136:139], a[88:91], a[116:119], a[136:139]\n"
    "v_mfma_f32_16x16x32_bf16 a[132:135], a[92:95], a[112:115], a[132:135]\n"
    "ds_read_b128 a[4:7], v224 offset:2048\n"
    "s_add_u32 m0, s18, 68608\n"
    "v_mfma_f32_16x16x32_bf16 a[140:143], a[92:95], a[116:119], a[140:143]\n"
    "buffer_load_dwordx4 v239, s[60:63], s16 offen lds\n"
    "v_mfma_f32_16x16x32_bf16 a[148:151], a[92:95], a[120:123], a[148:151]\n"
    "ds_read_b128 a[8:11], v224 offset:4096\n"
    "v_mfma_f32_16x16x32_bf16 a[156:159], a[92:95], a[124:127], a[156:159]\n"
    "v_mfma_f32_16x16x32_bf16 a[144:147], a[88:91], a[120:123], a[144:147]\n"
    "ds_read_b128 a[12:15], v224 offset:6144\n"
    "s_add_u32 m0, s18, 69632\n"
    "v_mfma_f32_16x16x32_bf16 a[152:155], a[88:91], a[124:127], a[152:155]\n"
    "buffer_load_dwordx4 v240, s[60:63], s16 offen lds\n"
    "v_mfma_f32_16x16x32_bf16 v[212:215], a[84:87], a[120:123], v[212:215]\n"
    "ds_read_b128 a[16:19], v224 offset:8192\n"
    "v_mfma_f32_16x16x32_bf16 v[220:223], a[84:87], a[124:127], v[220:223]\n"
    "v_mfma_f32_16x16x32_bf16 v[208:211], a[80:83], a[120:123], v[208:211]\n"
    "ds_read_b128 a[20:23], v224 offset:10240\n"
    "s_add_u32 m0, s18, 70656\n"
    "v_mfma_f32_16x16x32_bf16 v[216:219], a[80:83], a[124:127], v[216:219]\n"
    "buffer_load_dwordx4 v241, s[60:63], s16 offen lds\n"
    "v_mfma_f32_16x16x32_bf16 v[180:183], a[76:79], a[120:123], v[180:183]\n"
    "ds_read_b128 a[24:27], v224 offset:12288\n"
    "v_mfma_f32_16x16x32_bf16 v[188:191], a[76:79], a[124:127], v[188:191]\n"
    "v_mfma_f32_16x16x32_bf16 v[176:179], a[72:75], a[120:123], v[176:179]\n"
    "ds_read_b128 a[28:31], v224 offset:14336\n"
    "s_add_u32 m0, s18, 71680\n"
    "v_mfma_f32_16x16x32_bf16 v[184:187], a[72:75], a[124:127], v[184:187]\n"
    "buffer_load_dwordx4 v242, s[60:63], s16 offen lds\n"
    "v_mfma_f32_16x16x32_bf16 v[148:151], a[68:71], a[120:123], v[148:151]\n"
    "ds_read_b64_tr_b16 a[56:57], v230 offset:16384\n"
    "v_mfma_f32_16x16x32_bf16 v[156:159], a[68:71], a[124:127], v[156:159]\n"
    "v_mfma_f32_16x16x32_bf16 v[144:147], a[64:67], a[120:123], v[144:147]\n"
    "ds_read_b64_tr_b16 a[58:59], v230 offset:17408\n"
    "s_add_u32 m0, s18, 72704\n"
    "v_mfma_f32_16x16x32_bf16 v[152:155], a[64:67], a[124:127], v[152:155]\n"
    "buffer_load_dwordx4 v243, s[60:63], s16 offen lds\n"
    "v_mfma_f32_16x16x32_bf16 v[0:3], a[96:99], a[64:67], v[0:3]\n"
    "ds_read_b64_tr_b16 a[60:61], v231 offset:16384\n"
    "v_mfma_f32_16x16x32_bf16 v[4:7], a[100:103], a[64:67], v[4:7]\n"
    "v_mfma_f32_16x16x32_bf16 v[16:19], a[96:99], a[68:71], v[16:19]\n"
    "ds_read_b64_tr_b16 a[62:63], v231 offset:17408\n"
    "s_add_u32 m0, s19, 65536\n"
    "v_mfma_f32_16x16x32_bf16 v[20:23], a[100:103], a[68:71], v[20:23]\n"
    "buffer_load_dwordx4 v244, s[64:67], s17 offen lds\n"
    "v_mfma_f32_16x16x32_bf16 v[32:35], a[96:99], a[72:75], v[32:35]\n"
    "ds_read_b64_tr_b16 a[32:33], v228 offset:0\n"
    "s_add_u32 m0, s19, 66560\n"
    "v_mfma_f32_16x16x32_bf16 v[36:39], a[100:103], a[72:75], v[36:39]\n"
    "buffer_load_dwordx4 v245, s[64:67], s17 offen lds\n"
    "v_mfma_f32_16x16x32_bf16 v[48:51], a[96:99], a[76:79], v[48:51]\n"
    "ds_read_b64_tr_b16 a[34:35], v228 offset:1024\n"
    "v_mfma_f32_16x16x32_bf16 v[52:55], a[100:103], a[76:79], v[52:55]\n"
    "v_mfma_f32_16x16x32_bf16 v[64:67], a[96:99], a[80:83], v[64:67]\n"
    "ds_read_b64_tr_b16 a[36:37], v229 offset:0\n"
    "s_add_u32 m0, s19, 67584\n"
    "v_mfma_f32_16x16x32_bf16 v[68:71], a[100:103], a[80:83], v[68:71]\n"
    "buffer_load_dwordx4 v246, s[64:67], s17 offen lds\n"
    "v_mfma_f32_16x16x32_bf16 v[80:83], a[96:99], a[84:87], v[80:83]\n"
    "ds_read_b64_tr_b16 a[38:39], v229 offset:1024\n"
    "v_mfma_f32_16x16x32_bf16 v[84:87], a[100:103], a[84:87], v[84:87]\n"
    "v_mfma_f32_16x16x32_bf16 v[96:99], a[96:99], a[88:91], v[96:99]\n"
    "ds_read_b64_tr_b16 a[40:41], v230 offset:0\n"
    "s_add_u32 m0, s19, 68608\n"
    "v_mfma_f32_16x16x32_bf16 v[100:103], a[100:103], a[88:91], v[100:103]\n"
    "buffer_load_dwordx4 v247, s[64:67], s17 offen lds\n"
    "v_mfma_f32_16x16x32_bf16 v[112:115], a[96:99], a[92:95], v[112:115]\n"
    "ds_read_b64_tr_b16 a[42:43], v230 offset:1024\n"
    "v_mfma_f32_16x16x32_bf16 v[116:119], a[100:103], a[92:95], v[116:119]\n"
    "v_mfma_f32_16x16x32_bf16 v[120:123], a[104:107], a[92:95], v[120:123]\n"
    "ds_read_b64_tr_b16 a[44:45], v231 offset:0\n"
    "s_add_u32 m0, s19, 81920\n"
    "v_mfma_f32_16x16x32_bf16 v[124:127], a[108:111], a[92:95], v[124:127]\n"
    "buffer_load_dwordx4 v244, s[68:71], s17 offen lds\n"
    "v_mfma_f32_16x16x32_bf16 v[104:107], a[104:107], a[88:91], v[104:107]\n"
    "ds_read_b64_tr_b16 a[46:47], v231 offset:1024\n"
    "v_mfma_f32_16x16x32_bf16 v[108:111], a[108:111], a[88:91], v[108:111]\n"
    "s_add_u32 m0, s19, 82944\n"
    "v_mfma_f32_16x16x32_bf16 v[88:91], a[104:107], a[84:87], v[88:91]\n"
    "buffer_load_dwordx4 v245, s[68:71], s17 offen lds\n"
    "v_mfma_f32_16x16x32_bf16 v[92:95], a[108:111], a[84:87], v[92:95]\n"
    "v_mfma_f32_16x16x32_bf16 v[72:75], a[104:107], a[80:83], v[72:75]\n"
    "v_mfma_f32_16x16x32_bf16 v[76:79], a[108:111], a[80:83], v[76:79]\n"
    "s_add_u32 m0, s19, 83968\n"
    "v_mfma_f32_16x16x32_bf16 v[56:59], a[104:107], a[76:79], v[56:59]\n"
    "buffer_load_dwordx4 v246, s[68:71], s17 offen lds\n"
    "v_mfma_f32_16x16x32_bf16 v[60:63], a[108:111], a[76:79], v[60:63]\n"
    "v_mfma_f32_16x16x32_bf16 v[40:43], a[104:107], a[72:75], v[40:43]\n"
    "v_mfma_f32_16x16x32_bf16 v[44:47], a[108:111], a[72:75], v[44:47]\n"
    "s_add_u32 m0, s19, 84992\n"
    "v_mfma_f32_16x16x32_bf16 v[24:27], a[104:107], a[68:71], v[24:27]\n"
    "buffer_load_dwordx4 v247, s[68:71], s17 offen lds\n"
    "v_mfma_f32_16x16x32_bf16 v[28:31], a[108:111], a[68:71], v[28:31]\n"
    "s_add_u32 s16, s16, 128\n"
    "s_add_u32 s17, s17, 524288\n"
    "v_mfma_f32_16x16x32_bf16 v[8:11], a[104:107], a[64:67], v[8:11]\n"
    "v_mfma_f32_16x16x32_bf16 v[12:15], a[108:111], a[64:67], v[12:15]\n"
    "s_waitcnt lgkmcnt(15)\n"
    "v_mfma_f32_16x16x32_bf16 v[128:131], a[0:3], a[48:51], v[128:131]\n"
    "ds_read_b64_tr_b16 a[112:113], v228 offset:24576\n"
    "v_mfma_f32_16x16x32_bf16 v[136:139], a[0:3], a[52:55], v[136:139]\n"
    "global_load_dword a224, v[252:253], off\n"
    "v_mfma_f32_16x16x32_bf16 v[132:135], a[4:7], a[48:51], v[132:135]\n"
    "ds_read_b64_tr_b16 a[114:115], v228 offset:25600\n"
    "v_add_co_u32 v252, vcc, v252, v254\n"
    "s_nop 1\n"
    "v_mfma_f32_16x16x32_bf16 v[140:143], a[4:7], a[52:55], v[140:143]\n"
    "v_addc_co_u32 v253, vcc, 0, v253, vcc\n"
    "s_lshl_b32 s14, s9, 2\n"
    "s_add_u32 s14, s14, s8\n"
    "s_mul_i32 s14, s14, 2048\n"
    "s_add_u32 s14, s14, 0\n"
    "v_min_u32 v250, 15, v255\n"
    "v_mfma_f32_16x16x32_bf16 v[160:163], a[8:11], a[48:51], v[160:163]\n"
    "ds_read_b64_tr_b16 a[116:117], v229 offset:24576\n"
    "v_lshlrev_b32 v250, 7, v250\n"
    "v_mfma_f32_16x16x32_bf16 v[168:171], a[8:11], a[52:55], v[168:171]\n"
    "v_add_u32 v250, s14, v250\n"
    "v_mfma_f32_16x16x32_bf16 v[164:167], a[12:15], a[48:51], v[164:167]\n"
    "ds_read_b64_tr_b16 a[118:119], v229 offset:25600\n"
    "v_mfma_f32_16x16x32_bf16 v[172:175], a[12:15], a[52:55], v[172:175]\n"
    "global_load_dword a214, v250, s[36:37]\n"
    "v_mfma_f32_16x16x32_bf16 v[192:195], a[16:19], a[48:51], v[192:195]\n"
    "ds_read_b128 a[64:67], v225 offset:0\n"
    "s_lshl_b32 s14, s9, 2\n"
    "s_add_u32 s14, s14, s8\n"
    "s_mul_i32 s14, s14, 1536\n"
    "s_add_u32 s14, s14, 0\n"
    "v_min_u32 v250, 11, v255\n"
    "v_mfma_f32_16x16x32_bf16 v[200:203], a[16:19], a[52:55], v[200:203]\n"
    "s_waitcnt lgkmcnt(15)\n"
    "v_lshlrev_b32 v250, 7, v250\n"
    "v_add_u32 v250, s14, v250\n"
    "v_mfma_f32_16x16x32_bf16 v[196:199], a[20:23], a[48:51], v[196:199]\n"
    "ds_read_b128 a[68:71], v225 offset:2048\n"
    "v_mfma_f32_16x16x32_bf16 v[204:207], a[20:23], a[52:55], v[204:207]\n"
    "global_load_dword a215, v250, s[38:39]\n"
    "v_mfma_f32_16x16x32_bf16 a[128:131], a[24:27], a[48:51], a[128:131]\n"
    "ds_read_b128 a[72:75], v225 offset:4096\n"
    "v_mfma_f32_16x16x32_bf16 a[136:139], a[24:27], a[52:55], a[136:139]\n"
    "v_mfma_f32_16x16x32_bf16 a[132:135], a[28:31], a[48:51], a[132:135]\n"
    "ds_read_b128 a[76:79], v225 offset:6144\n"
    "v_mfma_f32_16x16x32_bf16 a[140:143], a[28:31], a[52:55], a[140:143]\n"
    "v_mfma_f32_16x16x32_bf16 a[148:151], a[28:31], a[56:59], a[148:151]\n"
    "s_waitcnt lgkmcnt(13)\n"
    "ds_read_b128 a[80:83], v225 offset:8192\n"
    "v_mfma_f32_16x16x32_bf16 a[156:159], a[28:31], a[60:63], a[156:159]\n"
    "v_mfma_f32_16x16x32_bf16 a[144:147], a[24:27], a[56:59], a[144:147]\n"
    "ds_read_b128 a[84:87], v225 offset:10240\n"
    "v_mfma_f32_16x16x32_bf16 a[152:155], a[24:27], a[60:63], a[152:155]\n"
    "v_mfma_f32_16x16x32_bf16 v[212:215], a[20:23], a[56:59], v[212:215]\n"
    "ds_read_b128 a[88:91], v225 offset:12288\n"
    "v_mfma_f32_16x16x32_bf16 v[220:223], a[20:23], a[60:63], v[220:223]\n"
    "v_mfma_f32_16x16x32_bf16 v[208:211], a[16:19], a[56:59], v[208:211]\n"
    "ds_read_b128 a[92:95], v225 offset:14336\n"
    "v_mfma_f32_16x16x32_bf16 v[216:219], a[16:19], a[60:63], v[216:219]\n"
    "v_mfma_f32_16x16x32_bf16 v[180:183], a[12:15], a[56:59], v[180:183]\n"
    "ds_read_b64_tr_b16 a[120:121], v230 offset:24576\n"
    "v_mfma_f32_16x16x32_bf16 v[188:191], a[12:15], a[60:63], v[188:191]\n"
    "v_mfma_f32_16x16x32_bf16 v[176:179], a[8:11], a[56:59], v[176:179]\n"
    "ds_read_b64_tr_b16 a[122:123], v230 offset:25600\n"
    "v_mfma_f32_16x16x32_bf16 v[184:187], a[8:11], a[60:63], v[184:187]\n"
    "v_mfma_f32_16x16x32_bf16 v[148:151], a[4:7], a[56:59], v[148:151]\n"
    "ds_read_b64_tr_b16 a[124:125], v231 offset:24576\n"
    "v_mfma_f32_16x16x32_bf16 v[156:159], a[4:7], a[60:63], v[156:159]\n"
    "v_mfma_f32_16x16x32_bf16 v[144:147], a[0:3], a[56:59], v[144:147]\n"
    "ds_read_b64_tr_b16 a[126:127], v231 offset:25600\n"
    "v_mfma_f32_16x16x32_bf16 v[152:155], a[0:3], a[60:63], v[152:155]\n"
    "v_mfma_f32_16x16x32_bf16 v[0:3], a[32:35], a[0:3], v[0:3]\n"
    "s_waitcnt lgkmcnt(14)\n"
    "ds_read_b64_tr_b16 a[96:97], v228 offset:8192\n"
    "v_mfma_f32_16x16x32_bf16 v[4:7], a[36:39], a[0:3], v[4:7]\n"
    "v_mfma_f32_16x16x32_bf16 v[16:19], a[32:35], a[4:7], v[16:19]\n"
    "ds_read_b64_tr_b16 a[98:99], v228 offset:9216\n"
    "v_mfma_f32_16x16x32_bf16 v[20:23], a[36:39], a[4:7], v[20:23]\n"
    "v_mfma_f32_16x16x32_bf16 v[32:35], a[32:35], a[8:11], v[32:35]\n"
    "ds_read_b64_tr_b16 a[100:101], v229 offset:8192\n"
    "v_mfma_f32_16x16x32_bf16 v[36:39], a[36:39], a[8:11], v[36:39]\n"
    "v_mfma_f32_16x16x32_bf16 v[48:51], a[32:35], a[12:15], v[48:51]\n"
    "ds_read_b64_tr_b16 a[102:103], v229 offset:9216\n"
    "v_mfma_f32_16x16x32_bf16 v[52:55], a[36:39], a[12:15], v[52:55]\n"
    "v_mfma_f32_16x16x32_bf16 v[64:67], a[32:35], a[16:19], v[64:67]\n"
    "ds_read_b64_tr_b16 a[104:105], v230 offset:8192\n"
    "v_mfma_f32_16x16x32_bf16 v[68:71], a[36:39], a[16:19], v[68:71]\n"
    "v_mfma_f32_16x16x32_bf16 v[80:83], a[32:35], a[20:23], v[80:83]\n"
    "ds_read_b64_tr_b16 a[106:107], v230 offset:9216\n"
    "v_mfma_f32_16x16x32_bf16 v[84:87], a[36:39], a[20:23], v[84:87]\n"
    "v_mfma_f32_16x16x32_bf16 v[96:99], a[32:35], a[24:27], v[96:99]\n"
    "ds_read_b64_tr_b16 a[108:109], v231 offset:8192\n"
    "v_mfma_f32_16x16x32_bf16 v[100:103], a[36:39], a[24:27], v[100:103]\n"
    "v_mfma_f32_16x16x32_bf16 v[112:115], a[32:35], a[28:31], v[112:115]\n"
    "ds_read_b64_tr_b16 a[110:111], v231 offset:9216\n"
    "v_mfma_f32_16x16x32_bf16 v[116:119], a[36:39], a[28:31], v[116:119]\n"
    "v_mfma_f32_16x16x32_bf16 v[120:123], a[40:43], a[28:31], v[120:123]\n"
    "v_mfma_f32_16x16x32_bf16 v[124:127], a[44:47], a[28:31], v[124:127]\n"
    "v_mfma_f32_16x16x32_bf16 v[104:107], a[40:43], a[24:27], v[104:107]\n"
    "v_mfma_f32_16x16x32_bf16 v[108:111], a[44:47], a[24:27], v[108:111]\n"
    "v_mfma_f32_16x16x32_bf16 v[88:91], a[40:43], a[20:23], v[88:91]\n"
    "v_mfma_f32_16x16x32_bf16 v[92:95], a[44:47], a[20:23], v[92:95]\n"
    "v_mfma_f32_16x16x32_bf16 v[72:75], a[40:43], a[16:19], v[72:75]\n"
    "v_mfma_f32_16x16x32_bf16 v[76:79], a[44:47], a[16:19], v[76:79]\n"
    "v_mfma_f32_16x16x32_bf16 v[56:59], a[40:43], a[12:15], v[56:59]\n"
    "v_mfma_f32_16x16x32_bf16 v[60:63], a[44:47], a[12:15], v[60:63]\n"
    "v_mfma_f32_16x16x32_bf16 v[40:43], a[40:43], a[8:11], v[40:43]\n"
    "v_mfma_f32_16x16x32_bf16 v[44:47], a[44:47], a[8:11], v[44:47]\n"
    "v_mfma_f32_16x16x32_bf16 v[24:27], a[40:43], a[4:7], v[24:27]\n"
    "v_mfma_f32_16x16x32_bf16 v[28:31], a[44:47], a[4:7], v[28:31]\n"
    "v_mfma_f32_16x16x32_bf16 v[8:11], a[40:43], a[0:3], v[8:11]\n"
    "v_mfma_f32_16x16x32_bf16 v[12:15], a[44:47], a[0:3], v[12:15]\n"
    "s_waitcnt lgkmcnt(15)\n"
    "v_mfma_f32_16x16x32_bf16 v[128:131], a[64:67], a[112:115], v[128:131]\n"
    "v_mfma_f32_16x16x32_bf16 v[136:139], a[64:67], a[116:119], v[136:139]\n"
    "v_mfma_f32_16x16x32_bf16 v[132:135], a[68:71], a[112:115], v[132:135]\n"
    "v_mfma_f32_16x16x32_bf16 v[140:143], a[68:71], a[116:119], v[140:143]\n"
    "s_waitcnt vmcnt(3) lgkmcnt(0)\n"
    "s_barrier\n"
    "v_mfma_f32_16x16x32_bf16 v[160:163], a[72:75], a[112:115], v[160:163]\n"
    "ds_read_b64_tr_b16 a[48:49], v232 offset:16384\n"
    "s_add_u32 m0, s18, 0\n"
    "v_mfma_f32_16x16x32_bf16 v[168:171], a[72:75], a[116:119], v[168:171]\n"
    "buffer_load_dwordx4 v236, s[60:63], s16 offen lds\n"
    "v_mfma_f32_16x16x32_bf16 v[164:167], a[76:79], a[112:115], v[164:167]\n"
    "ds_read_b64_tr_b16 a[50:51], v232 offset:17408\n"
    "s_add_u32 m0, s18, 1024\n"
    "v_mfma_f32_16x16x32_bf16 v[172:175], a[76:79], a[116:119], v[172:175]\n"
    "buffer_load_dwordx4 v237, s[60:63], s16 offen lds\n"
    "v_mfma_f32_16x16x32_bf16 v[192:195], a[80:83], a[112:115], v[192:195]\n"
    "ds_read_b64_tr_b16 a[52:53], v233 offset:16384\n"
    "v_mfma_f32_16x16x32_bf16 v[200:203], a[80:83], a[116:119], v[200:203]\n"
    "v_mfma_f32_16x16x32_bf16 v[196:199], a[84:87], a[112:115], v[196:199]\n"
    "ds_read_b64_tr_b16 a[54:55], v233 offset:17408\n"
    "s_add_u32 m0, s18, 2048\n"
    "v_mfma_f32_16x16x32_bf16 v[204:207], a[84:87], a[116:119], v[204:207]\n"
    "buffer_load_dwordx4 v238, s[60:63], s16 offen lds\n"
    "v_mfma_f32_16x16x32_bf16 a[128:131], a[88:91], a[112:115], a[128:131]\n"
    "ds_read_b128 a[0:3], v226 offset:0\n"
    "v_mfma_f32_16x16x32_bf16 a[136:139], a[88:91], a[116:119], a[136:139]\n"
    "v_mfma_f32_16x16x32_bf16 a[132:135], a[92:95], a[112:115], a[132:135]\n"
    "ds_read_b128 a[4:7], v226 offset:2048\n"
    "s_add_u32 m0, s18, 3072\n"
    "v_mfma_f32_16x16x32_bf16 a[140:143], a[92:95], a[116:119], a[140:143]\n"
    "buffer_load_dwordx4 v239, s[60:63], s16 offen lds\n"
    "v_mfma_f32_16x16x32_bf16 a[148:151], a[92:95], a[120:123], a[148:151]\n"
    "ds_read_b128 a[8:11], v226 offset:4096\n"
    "v_mfma_f32_16x16x32_bf16 a[156:159], a[92:95], a[124:127], a[156:159]\n"
    "v_mfma_f32_16x16x32_bf16 a[144:147], a[88:91], a[120:123], a[144:147]\n"
    "ds_read_b128 a[12:15], v226 offset:6144\n"
    "s_add_u32 m0, s18, 4096\n"
    "v_mfma_f32_16x16x32_bf16 a[152:155], a[88:91], a[124:127], a[152:155]\n"
    "buffer_load_dwordx4 v240, s[60:63], s16 offen lds\n"
    "v_mfma_f32_16x16x32_bf16 v[212:215], a[84:87], a[120:123], v[212:215]\n"
    "ds_read_b128 a[16:19], v226 offset:8192\n"
    "v_mfma_f32_16x16x32_bf16 v[220:223], a[84:87], a[124:127], v[220:223]\n"
    "v_mfma_f32_16x16x32_bf16 v[208:211], a[80:83], a[120:123], v[208:211]\n"
    "ds_read_b128 a[20:23], v226 offset:10240\n"
    "s_add_u32 m0, s18, 5120\n"
    "v_mfma_f32_16x16x32_bf16 v[216:219], a[80:83], a[124:127], v[216:219]\n"
    "buffer_load_dwordx4 v241, s[60:63], s16 offen lds\n"
    "v_mfma_f32_16x16x32_bf16 v[180:183], a[76:79], a[120:123], v[180:183]\n"
    "ds_read_b128 a[24:27], v226 offset:12288\n"
    "v_mfma_f32_16x16x32_bf16 v[188:191], a[76:79], a[124:127], v[188:191]\n"
    "v_mfma_f32_16x16x32_bf16 v[176:179], a[72:75], a[120:123], v[176:179]\n"
    "ds_read_b128 a[28:31], v226 offset:14336\n"
    "s_add_u32 m0, s18, 6144\n"
    "v_mfma_f32_16x16x32_bf16 v[184:187], a[72:75], a[124:127], v[184:187]\n"
    "buffer_load_dwordx4 v242, s[60:63], s16 offen lds\n"
    "v_mfma_f32_16x16x32_bf16 v[148:151], a[68:71], a[120:123], v[148:151]\n"
    "ds_read_b64_tr_b16 a[56:57], v234 offset:16384\n"
    "v_mfma_f32_16x16x32_bf16 v[156:159], a[68:71], a[124:127], v[156:159]\n"
    "v_mfma_f32_16x16x32_bf16 v[144:147], a[64:67], a[120:123], v[144:147]\n"
    "ds_read_b64_tr_b16 a[58:59], v234 offset:17408\n"
    "s_add_u32 m0, s18, 7168\n"
    "v_mfma_f32_16x16x32_bf16 v[152:155], a[64:67], a[124:127], v[152:155]\n"
    "buffer_load_dwordx4 v243, s[60:63], s16 offen lds\n"
    "v_mfma_f32_16x16x32_bf16 v[0:3], a[96:99], a[64:67], v[0:3]\n"
    "ds_read_b64_tr_b16 a[60:61], v235 offset:16384\n"
    "v_mfma_f32_16x16x32_bf16 v[4:7], a[100:103], a[64:67], v[4:7]\n"
    "v_mfma_f32_16x16x32_bf16 v[16:19], a[96:99], a[68:71], v[16:19]\n"
    "ds_read_b64_tr_b16 a[62:63], v235 offset:17408\n"
    "s_add_u32 m0, s19, 0\n"
    "v_mfma_f32_16x16x32_bf16 v[20:23], a[100:103], a[68:71], v[20:23]\n"
    "buffer_load_dwordx4 v244, s[64:67], s17 offen lds\n"
    "v_mfma_f32_16x16x32_bf16 v[32:35], a[96:99], a[72:75], v[32:35]\n"
    "ds_read_b64_tr_b16 a[32:33], v232 offset:0\n"
    "s_add_u32 m0, s19, 1024\n"
    "v_mfma_f32_16x16x32_bf16 v[36:39], a[100:103], a[72:75], v[36:39]\n"
    "buffer_load_dwordx4 v245, s[64:67], s17 offen lds\n"
    "v_mfma_f32_16x16x32_bf16 v[48:51], a[96:99], a[76:79], v[48:51]\n"
    "ds_read_b64_tr_b16 a[34:35], v232 offset:1024\n"
    "v_mfma_f32_16x16x32_bf16 v[52:55], a[100:103], a[76:79], v[52:55]\n"
    "v_mfma_f32_16x16x32_bf16 v[64:67], a[96:99], a[80:83], v[64:67]\n"
    "ds_read_b64_tr_b16 a[36:37], v233 offset:0\n"
    "s_add_u32 m0, s19, 2048\n"
    "v_mfma_f32_16x16x32_bf16 v[68:71], a[100:103], a[80:83], v[68:71]\n"
    "buffer_load_dwordx4 v246, s[64:67], s17 offen lds\n"
    "v_mfma_f32_16x16x32_bf16 v[80:83], a[96:99], a[84:87], v[80:83]\n"
    "ds_read_b64_tr_b16 a[38:39], v233 offset:1024\n"
    "v_mfma_f32_16x16x32_bf16 v[84:87], a[100:103], a[84:87], v[84:87]\n"
    "v_mfma_f32_16x16x32_bf16 v[96:99], a[96:99], a[88:91], v[96:99]\n"
    "ds_read_b64_tr_b16 a[40:41], v234 offset:0\n"
    "s_add_u32 m0, s19, 3072\n"
    "v_mfma_f32_16x16x32_bf16 v[100:103], a[100:103], a[88:91], v[100:103]\n"
    "buffer_load_dwordx4 v247, s[64:67], s17 offen lds\n"
    "v_mfma_f32_16x16x32_bf16 v[112:115], a[96:99], a[92:95], v[112:115]\n"
    "ds_read_b64_tr_b16 a[42:43], v234 offset:1024\n"
    "v_mfma_f32_16x16x32_bf16 v[116:119], a[100:103], a[92:95], v[116:119]\n"
    "v_mfma_f32_16x16x32_bf16 v[120:123], a[104:107], a[92:95], v[120:123]\n"
    "ds_read_b64_tr_b16 a[44:45], v235 offset:0\n"
    "s_add_u32 m0, s19, 16384\n"
    "v_mfma_f32_16x16x32_bf16 v[124:127], a[108:111], a[92:95], v[124:127]\n"
    "buffer_load_dwordx4 v244, s[68:71], s17 offen lds\n"
    "v_mfma_f32_16x16x32_bf16 v[104:107], a[104:107], a[88:91], v[104:107]\n"
    "ds_read_b64_tr_b16 a[46:47], v235 offset:1024\n"
    "v_mfma_f32_16x16x32_bf16 v[108:111], a[108:111], a[88:91], v[108:111]\n"
    "s_add_u32 m0, s19, 17408\n"
    "v_mfma_f32_16x16x32_bf16 v[88:91], a[104:107], a[84:87], v[88:91]\n"
    "buffer_load_dwordx4 v245, s[68:71], s17 offen lds\n"
    "v_mfma_f32_16x16x32_bf16 v[92:95], a[108:111], a[84:87], v[92:95]\n"
    "v_mfma_f32_16x16x32_bf16 v[72:75], a[104:107], a[80:83], v[72:75]\n"
    "v_mfma_f32_16x16x32_bf16 v[76:79], a[108:111], a[80:83], v[76:79]\n"
    "s_add_u32 m0, s19, 18432\n"
    "v_mfma_f32_16x16x32_bf16 v[56:59], a[104:107], a[76:79], v[56:59]\n"
    "buffer_load_dwordx4 v246, s[68:71], s17 offen lds\n"
    "v_mfma_f32_16x16x32_bf16 v[60:63], a[108:111], a[76:79], v[60:63]\n"
    "v_mfma_f32_16x16x32_bf16 v[40:43], a[104:107], a[72:75], v[40:43]\n"
    "v_mfma_f32_16x16x32_bf16 v[44:47], a[108:111], a[72:75], v[44:47]\n"
    "s_add_u32 m0, s19, 19456\n"
    "v_mfma_f32_16x16x32_bf16 v[24:27], a[104:107], a[68:71], v[24:27]\n"
    "buffer_load_dwordx4 v247, s[68:71], s17 offen lds\n"
    "v_mfma_f32_16x16x32_bf16 v[28:31], a[108:111], a[68:71], v[28:31]\n"
    "s_add_u32 s16, s16, 128\n"
    "s_add_u32 s17, s17, 524288\n"
    "v_mfma_f32_16x16x32_bf16 v[8:11], a[104:107], a[64:67], v[8:11]\n"
    "v_mfma_f32_16x16x32_bf16 v[12:15], a[108:111], a[64:67], v[12:15]\n"
    "s_waitcnt lgkmcnt(15)\n"
    "v_mfma_f32_16x16x32_bf16 v[128:131], a[0:3], a[48:51], v[128:131]\n"
    "ds_read_b64_tr_b16 a[112:113], v232 offset:24576\n"
    "v_mfma_f32_16x16x32_bf16 v[136:139], a[0:3], a[52:55], v[136:139]\n"
    "global_load_dword a225, v[252:253], off\n"
    "v_mfma_f32_16x16x32_bf16 v[132:135], a[4:7], a[48:51], v[132:135]\n"
    "ds_read_b64_tr_b16 a[114:115], v232 offset:25600\n"
    "v_add_co_u32 v252, vcc, v252, v254\n"
    "s_nop 1\n"
    "v_mfma_f32_16x16x32_bf16 v[140:143], a[4:7], a[52:55], v[140:143]\n"
    "v_addc_co_u32 v253, vcc, 0, v253, vcc\n"
    "s_lshl_b32 s14, s9, 2\n"
    "s_add_u32 s14, s14, s8\n"
    "s_mul_i32 s14, s14, 2048\n"
    "s_add_u32 s14, s14, 0\n"
    "v_min_u32 v250, 15, v255\n"
    "v_mfma_f32_16x16x32_bf16 v[160:163], a[8:11], a[48:51], v[160:163]\n"
    "ds_read_b64_tr_b16 a[116:117], v233 offset:24576\n"
    "v_lshlrev_b32 v250, 7, v250\n"
    "v_mfma_f32_16x16x32_bf16 v[168:171], a[8:11], a[52:55], v[168:171]\n"
    "v_add_u32 v250, s14, v250\n"
    "v_mfma_f32_16x16x32_bf16 v[164:167], a[12:15], a[48:51], v[164:167]\n"
    "ds_read_b64_tr_b16 a[118:119], v233 offset:25600\n"
    "v_mfma_f32_16x16x32_bf16 v[172:175], a[12:15], a[52:55], v[172:175]\n"
    "global_load_dword a216, v250, s[44:45]\n"
    "v_mfma_f32_16x16x32_bf16 v[192:195], a[16:19], a[48:51], v[192:195]\n"
    "ds_read_b128 a[64:67], v227 offset:0\n"
    "v_mfma_f32_16x16x32_bf16 v[200:203], a[16:19], a[52:55], v[200:203]\n"
    "s_waitcnt lgkmcnt(15)\n"
    "v_mfma_f32_16x16x32_bf16 v[196:199], a[20:23], a[48:51], v[196:199]\n"
    "ds_read_b128 a[68:71], v227 offset:2048\n"
    "v_mfma_f32_16x16x32_bf16 v[204:207], a[20:23], a[52:55], v[204:207]\n"
    "v_mfma_f32_16x16x32_bf16 a[128:131], a[24:27], a[48:51], a[128:131]\n"
    "ds_read_b128 a[72:75], v227 offset:4096\n"
    "v_mfma_f32_16x16x32_bf16 a[136:139], a[24:27], a[52:55], a[136:139]\n"
    "v_mfma_f32_16x16x32_bf16 a[132:135], a[28:31], a[48:51], a[132:135]\n"
    "ds_read_b128 a[76:79], v227 offset:6144\n"
    "v_mfma_f32_16x16x32_bf16 a[140:143], a[28:31], a[52:55], a[140:143]\n"
    "v_mfma_f32_16x16x32_bf16 a[148:151], a[28:31], a[56:59], a[148:151]\n"
    "s_waitcnt lgkmcnt(13)\n"
    "ds_read_b128 a[80:83], v227 offset:8192\n"
    "v_mfma_f32_16x16x32_bf16 a[156:159], a[28:31], a[60:63], a[156:159]\n"
    "v_mfma_f32_16x16x32_bf16 a[144:147], a[24:27], a[56:59], a[144:147]\n"
    "ds_read_b128 a[84:87], v227 offset:10240\n"
    "v_mfma_f32_16x16x32_bf16 a[152:155], a[24:27], a[60:63], a[152:155]\n"
    "v_mfma_f32_16x16x32_bf16 v[212:215], a[20:23], a[56:59], v[212:215]\n"
    "ds_read_b128 a[88:91], v227 offset:12288\n"
    "v_mfma_f32_16x16x32_bf16 v[220:223], a[20:23], a[60:63], v[220:223]\n"
    "v_mfma_f32_16x16x32_bf16 v[208:211], a[16:19], a[56:59], v[208:211]\n"
    "ds_read_b128 a[92:95], v227 offset:14336\n"
    "v_mfma_f32_16x16x32_bf16 v[216:219], a[16:19], a[60:63], v[216:219]\n"
    "v_mfma_f32_16x16x32_bf16 v[180:183], a[12:15], a[56:59], v[180:183]\n"
    "ds_read_b64_tr_b16 a[120:121], v234 offset:24576\n"
    "v_mfma_f32_16x16x32_bf16 v[188:191], a[12:15], a[60:63], v[188:191]\n"
    "v_mfma_f32_16x16x32_bf16 v[176:179], a[8:11], a[56:59], v[176:179]\n"
    "ds_read_b64_tr_b16 a[122:123], v234 offset:25600\n"
    "v_mfma_f32_16x16x32_bf16 v[184:187], a[8:11], a[60:63], v[184:187]\n"
    "v_mfma_f32_16x16x32_bf16 v[148:151], a[4:7], a[56:59], v[148:151]\n"
    "ds_read_b64_tr_b16 a[124:125], v235 offset:24576\n"
    "v_mfma_f32_16x16x32_bf16 v[156:159], a[4:7], a[60:63], v[156:159]\n"
    "v_mfma_f32_16x16x32_bf16 v[144:147], a[0:3], a[56:59], v[144:147]\n"
    "ds_read_b64_tr_b16 a[126:127], v235 offset:25600\n"
    "v_mfma_f32_16x16x32_bf16 v[152:155], a[0:3], a[60:63], v[152:155]\n"
    "v_mfma_f32_16x16x32_bf16 v[0:3], a[32:35], a[0:3], v[0:3]\n"
    "s_waitcnt lgkmcnt(14)\n"
    "ds_read_b64_tr_b16 a[96:97], v232 offset:8192\n"
    "v_mfma_f32_16x16x32_bf16 v[4:7], a[36:39], a[0:3], v[4:7]\n"
    "v_mfma_f32_16x16x32_bf16 v[16:19], a[32:35], a[4:7], v[16:19]\n"
    "ds_read_b64_tr_b16 a[98:99], v232 offset:9216\n"
    "v_mfma_f32_16x16x32_bf16 v[20:23], a[36:39], a[4:7], v[20:23]\n"
    "v_mfma_f32_16x16x32_bf16 v[32:35], a[32:35], a[8:11], v[32:35]\n"
    "ds_read_b64_tr_b16 a[100:101], v233 offset:8192\n"
    "v_mfma_f32_16x16x32_bf16 v[36:39], a[36:39], a[8:11], v[36:39]\n"
    "v_mfma_f32_16x16x32_bf16 v[48:51], a[32:35], a[12:15], v[48:51]\n"
    "ds_read_b64_tr_b16 a[102:103], v233 offset:9216\n"
    "v_mfma_f32_16x16x32_bf16 v[52:55], a[36:39], a[12:15], v[52:55]\n"
    "v_mfma_f32_16x16x32_bf16 v[64:67], a[32:35], a[16:19], v[64:67]\n"
    "ds_read_b64_tr_b16 a[104:105], v234 offset:8192\n"
    "v_mfma_f32_16x16x32_bf16 v[68:71], a[36:39], a[16:19], v[68:71]\n"
    "v_mfma_f32_16x16x32_bf16 v[80:83], a[32:35], a[20:23], v[80:83]\n"
    "ds_read_b64_tr_b16 a[106:107], v234 offset:9216\n"
    "v_mfma_f32_16x16x32_bf16 v[84:87], a[36:39], a[20:23], v[84:87]\n"
    "v_mfma_f32_16x16x32_bf16 v[96:99], a[32:35], a[24:27], v[96:99]\n"
    "ds_read_b64_tr_b16 a[108:109], v235 offset:8192\n"
    "v_mfma_f32_16x16x32_bf16 v[100:103], a[36:39], a[24:27], v[100:103]\n"
    "v_mfma_f32_16x16x32_bf16 v[112:115], a[32:35], a[28:31], v[112:115]\n"
    "ds_read_b64_tr_b16 a[110:111], v235 offset:9216\n"
    "v_mfma_f32_16x16x32_bf16 v[116:119], a[36:39], a[28:31], v[116:119]\n"
    "v_mfma_f32_16x16x32_bf16 v[120:123], a[40:43], a[28:31], v[120:123]\n"
    "v_mfma_f32_16x16x32_bf16 v[124:127], a[44:47], a[28:31], v[124:127]\n"
    "v_mfma_f32_16x16x32_bf16 v[104:107], a[40:43], a[24:27], v[104:107]\n"
    "v_mfma_f32_16x16x32_bf16 v[108:111], a[44:47], a[24:27], v[108:111]\n"
    "v_mfma_f32_16x16x32_bf16 v[88:91], a[40:43], a[20:23], v[88:91]\n"
    "v_mfma_f32_16x16x32_bf16 v[92:95], a[44:47], a[20:23], v[92:95]\n"
    "v_mfma_f32_16x16x32_bf16 v[72:75], a[40:43], a[16:19], v[72:75]\n"
    "v_mfma_f32_16x16x32_bf16 v[76:79], a[44:47], a[16:19], v[76:79]\n"
    "v_mfma_f32_16x16x32_bf16 v[56:59], a[40:43], a[12:15], v[56:59]\n"
    "v_mfma_f32_16x16x32_bf16 v[60:63], a[44:47], a[12:15], v[60:63]\n"
    "v_mfma_f32_16x16x32_bf16 v[40:43], a[40:43], a[8:11], v[40:43]\n"
    "v_mfma_f32_16x16x32_bf16 v[44:47], a[44:47], a[8:11], v[44:47]\n"
    "v_mfma_f32_16x16x32_bf16 v[24:27], a[40:43], a[4:7], v[24:27]\n"
    "v_mfma_f32_16x16x32_bf16 v[28:31], a[44:47], a[4:7], v[28:31]\n"
    "v_mfma_f32_16x16x32_bf16 v[8:11], a[40:43], a[0:3], v[8:11]\n"
    "v_mfma_f32_16x16x32_bf16 v[12:15], a[44:47], a[0:3], v[12:15]\n"
    "s_waitcnt lgkmcnt(15)\n"
    "v_mfma_f32_16x16x32_bf16 v[128:131], a[64:67], a[112:115], v[128:131]\n"
    "v_mfma_f32_16x16x32_bf16 v[136:139], a[64:67], a[116:119], v[136:139]\n"
    "v_mfma_f32_16x16x32_bf16 v[132:135], a[68:71], a[112:115], v[132:135]\n"
    "v_mfma_f32_16x16x32_bf16 v[140:143], a[68:71], a[116:119], v[140:143]\n"
    "s_waitcnt vmcnt(2) lgkmcnt(0)\n"
    "s_barrier\n"
    "v_mfma_f32_16x16x32_bf16 v[160:163], a[72:75], a[112:115], v[160:163]\n"
    "ds_read_b64_tr_b16 a[48:49], v228 offset:16384\n"
    "s_add_u32 m0, s18, 65536\n"
    "v_mfma_f32_16x16x32_bf16 v[168:171], a[72:75], a[116:119], v[168:171]\n"
    "buffer_load_dwordx4 v236, s[60:63], s16 offen lds\n"
    "v_mfma_f32_16x16x32_bf16 v[164:167], a[76:79], a[112:115], v[164:167]\n"
    "ds_read_b64_tr_b16 a[50:51], v228 offset:17408\n"
    "s_add_u32 m0, s18, 66560\n"
    "v_mfma_f32_16x16x32_bf16 v[172:175], a[76:79], a[116:119], v[172:175]\n"
    "buffer_load_dwordx4 v237, s[60:63], s16 offen lds\n"
    "v_mfma_f32_16x16x32_bf16 v[192:195], a[80:83], a[112:115], v[192:195]\n"
    "ds_read_b64_tr_b16 a[52:53], v229 offset:16384\n"
    "v_mfma_f32_16x16x32_bf16 v[200:203], a[80:83], a[116:119], v[200:203]\n"
    "v_mfma_f32_16x16x32_bf16 v[196:199], a[84:87], a[112:115], v[196:199]\n"
    "ds_read_b64_tr_b16 a[54:55], v229 offset:17408\n"
    "s_add_u32 m0, s18, 67584\n"
    "v_mfma_f32_16x16x32_bf16 v[204:207], a[84:87], a[116:119], v[204:207]\n"
    "buffer_load_dwordx4 v238, s[60:63], s16 offen lds\n"
    "v_mfma_f32_16x16x32_bf16 a[128:131], a[88:91], a[112:115], a[128:131]\n"
    "ds_read_b128 a[0:3], v224 offset:0\n"
    "v_mfma_f32_16x16x32_bf16 a[136:139], a[88:91], a[116:119], a[136:139]\n"
    "v_mfma_f32_16x16x32_bf16 a[132:135], a[92:95], a[112:115], a[132:135]\n"
    "ds_read_b128 a[4:7], v224 offset:2048\n"
    "s_add_u32 m0, s18, 68608\n"
    "v_mfma_f32_16x16x32_bf16 a[140:143], a[92:95], a[116:119], a[140:143]\n"
    "buffer_load_dwordx4 v239, s[60:63], s16 offen lds\n"
    "v_mfma_f32_16x16x32_bf16 a[148:151], a[92:95], a[120:123], a[148:151]\n"
    "ds_read_b128 a[8:11], v224 offset:4096\n"
    "v_mfma_f32_16x16x32_bf16 a[156:159], a[92:95], a[124:127], a[156:159]\n"
    "v_mfma_f32_16x16x32_bf16 a[144:147], a[88:91], a[120:123], a[144:147]\n"
    "ds_read_b128 a[12:15], v224 offset:6144\n"
    "s_add_u32 m0, s18, 69632\n"
    "v_mfma_f32_16x16x32_bf16 a[152:155], a[88:91], a[124:127], a[152:155]\n"
    "buffer_load_dwordx4 v240, s[60:63], s16 offen lds\n"
    "v_mfma_f32_16x16x32_bf16 v[212:215], a[84:87], a[120:123], v[212:215]\n"
    "ds_read_b128 a[16:19], v224 offset:8192\n"
    "v_mfma_f32_16x16x32_bf16 v[220:223], a[84:87], a[124:127], v[220:223]\n"
    "v_mfma_f32_16x16x32_bf16 v[208:211], a[80:83], a[120:123], v[208:211]\n"
    "ds_read_b128 a[20:23], v224 offset:10240\n"
    "s_add_u32 m0, s18, 70656\n"
    "v_mfma_f32_16x16x32_bf16 v[216:219], a[80:83], a[124:127], v[216:219]\n"
    "buffer_load_dwordx4 v241, s[60:63], s16 offen lds\n"
    "v_mfma_f32_16x16x32_bf16 v[180:183], a[76:79], a[120:123], v[180:183]\n"
    "ds_read_b128 a[24:27], v224 offset:12288\n"
    "v_mfma_f32_16x16x32_bf16 v[188:191], a[76:79], a[124:127], v[188:191]\n"
    "v_mfma_f32_16x16x32_bf16 v[176:179], a[72:75], a[120:123], v[176:179]\n"
    "ds_read_b128 a[28:31], v224 offset:14336\n"
    "s_add_u32 m0, s18, 71680\n"
    "v_mfma_f32_16x16x32_bf16 v[184:187], a[72:75], a[124:127], v[184:187]\n"
    "buffer_load_dwordx4 v242, s[60:63], s16 offen lds\n"
    "v_mfma_f32_16x16x32_bf16 v[148:151], a[68:71], a[120:123], v[148:151]\n"
    "ds_read_b64_tr_b16 a[56:57], v230 offset:16384\n"
    "v_mfma_f32_16x16x32_bf16 v[156:159], a[68:71], a[124:127], v[156:159]\n"
    "v_mfma_f32_16x16x32_bf16 v[144:147], a[64:67], a[120:123], v[144:147]\n"
    "ds_read_b64_tr_b16 a[58:59], v230 offset:17408\n"
    "s_add_u32 m0, s18, 72704\n"
    "v_mfma_f32_16x16x32_bf16 v[152:155], a[64:67], a[124:127], v[152:155]\n"
    "buffer_load_dwordx4 v243, s[60:63], s16 offen lds\n"
    "v_mfma_f32_16x16x32_bf16 v[0:3], a[96:99], a[64:67], v[0:3]\n"
    "ds_read_b64_tr_b16 a[60:61], v231 offset:16384\n"
    "v_mfma_f32_16x16x32_bf16 v[4:7], a[100:103], a[64:67], v[4:7]\n"
    "v_mfma_f32_16x16x32_bf16 v[16:19], a[96:99], a[68:71], v[16:19]\n"
    "ds_read_b64_tr_b16 a[62:63], v231 offset:17408\n"
    "s_add_u32 m0, s19, 65536\n"
    "v_mfma_f32_16x16x32_bf16 v[20:23], a[100:103], a[68:71], v[20:23]\n"
    "buffer_load_dwordx4 v244, s[64:67], s17 offen lds\n"
    "v_mfma_f32_16x16x32_bf16 v[32:35], a[96:99], a[72:75], v[32:35]\n"
    "ds_read_b64_tr_b16 a[32:33], v228 offset:0\n"
    "s_add_u32 m0, s19, 66560\n"
    "v_mfma_f32_16x16x32_bf16 v[36:39], a[100:103], a[72:75], v[36:39]\n"
    "buffer_load_dwordx4 v245, s[64:67], s17 offen lds\n"
    "v_mfma_f32_16x16x32_bf16 v[48:51], a[96:99], a[76:79], v[48:51]\n"
    "ds_read_b64_tr_b16 a[34:35], v228 offset:1024\n"
    "v_mfma_f32_16x16x32_bf16 v[52:55], a[100:103], a[76:79], v[52:55]\n"
    "v_mfma_f32_16x16x32_bf16 v[64:67], a[96:99], a[80:83], v[64:67]\n"
    "ds_read_b64_tr_b16 a[36:37], v229 offset:0\n"
    "s_add_u32 m0, s19, 67584\n"
    "v_mfma_f32_16x16x32_bf16 v[68:71], a[100:103], a[80:83], v[68:71]\n"
    "buffer_load_dwordx4 v246, s[64:67], s17 offen lds\n"
    "v_mfma_f32_16x16x32_bf16 v[80:83], a[96:99], a[84:87], v[80:83]\n"
    "ds_read_b64_tr_b16 a[38:39], v229 offset:1024\n"
    "v_mfma_f32_16x16x32_bf16 v[84:87], a[100:103], a[84:87], v[84:87]\n"
    "v_mfma_f32_16x16x32_bf16 v[96:99], a[96:99], a[88:91], v[96:99]\n"
    "ds_read_b64_tr_b16 a[40:41], v230 offset:0\n"
    "s_add_u32 m0, s19, 68608\n"
    "v_mfma_f32_16x16x32_bf16 v[100:103], a[100:103], a[88:91], v[100:103]\n"
    "buffer_load_dwordx4 v247, s[64:67], s17 offen lds\n"
    "v_mfma_f32_16x16x32_bf16 v[112:115], a[96:99], a[92:95], v[112:115]\n"
    "ds_read_b64_tr_b16 a[42:43], v230 offset:1024\n"
    "v_mfma_f32_16x16x32_bf16 v[116:119], a[100:103], a[92:95], v[116:119]\n"
    "v_mfma_f32_16x16x32_bf16 v[120:123], a[104:107], a[92:95], v[120:123]\n"
    "ds_read_b64_tr_b16 a[44:45], v231 offset:0\n"
    "s_add_u32 m0, s19, 81920\n"
    "v_mfma_f32_16x16x32_bf16 v[124:127], a[108:111], a[92:95], v[124:127]\n"
    "buffer_load_dwordx4 v244, s[68:71], s17 offen lds\n"
    "v_mfma_f32_16x16x32_bf16 v[104:107], a[104:107], a[88:91], v[104:107]\n"
    "ds_read_b64_tr_b16 a[46:47], v231 offset:1024\n"
    "v_mfma_f32_16x16x32_bf16 v[108:111], a[108:111], a[88:91], v[108:111]\n"
    "s_add_u32 m0, s19, 82944\n"
    "v_mfma_f32_16x16x32_bf16 v[88:91], a[104:107], a[84:87], v[88:91]\n"
    "buffer_load_dwordx4 v245, s[68:71], s17 offen lds\n"
    "v_mfma_f32_16x16x32_bf16 v[92:95], a[108:111], a[84:87], v[92:95]\n"
    "v_mfma_f32_16x16x32_bf16 v[72:75], a[104:107], a[80:83], v[72:75]\n"
    "v_mfma_f32_16x16x32_bf16 v[76:79], a[108:111], a[80:83], v[76:79]\n"
    "s_add_u32 m0, s19, 83968\n"
    "v_mfma_f32_16x16x32_bf16 v[56:59], a[104:107], a[76:79], v[56:59]\n"
    "buffer_load_dwordx4 v246, s[68:71], s17 offen lds\n"
    "v_mfma_f32_16x16x32_bf16 v[60:63], a[108:111], a[76:79], v[60:63]\n"
    "v_mfma_f32_16x16x32_bf16 v[40:43], a[104:107], a[72:75], v[40:43]\n"
    "v_mfma_f32_16x16x32_bf16 v[44:47], a[108:111], a[72:75], v[44:47]\n"
    "s_add_u32 m0, s19, 84992\n"
    "v_mfma_f32_16x16x32_bf16 v[24:27], a[104:107], a[68:71], v[24:27]\n"
    "buffer_load_dwordx4 v247, s[68:71], s17 offen lds\n"
    "v_mfma_f32_16x16x32_bf16 v[28:31], a[108:111], a[68:71], v[28:31]\n"
    "s_add_u32 s16, s16, 128\n"
    "s_add_u32 s17, s17, 524288\n"
    "v_mfma_f32_16x16x32_bf16 v[8:11], a[104:107], a[64:67], v[8:11]\n"
    "v_mfma_f32_16x16x32_bf16 v[12:15], a[108:111], a[64:67], v[12:15]\n"
    "s_waitcnt lgkmcnt(15)\n"
    "v_mfma_f32_16x16x32_bf16 v[128:131], a[0:3], a[48:51], v[128:131]\n"
    "ds_read_b64_tr_b16 a[112:113], v228 offset:24576\n"
    "v_mfma_f32_16x16x32_bf16 v[136:139], a[0:3], a[52:55], v[136:139]\n"
    "global_load_dword a226, v[252:253], off\n"
    "v_mfma_f32_16x16x32_bf16 v[132:135], a[4:7], a[48:51], v[132:135]\n"
    "ds_read_b64_tr_b16 a[114:115], v228 offset:25600\n"
    "v_add_co_u32 v252, vcc, v252, v254\n"
    "s_nop 1\n"
    "v_mfma_f32_16x16x32_bf16 v[140:143], a[4:7], a[52:55], v[140:143]\n"
    "v_addc_co_u32 v253, vcc, 0, v253, vcc\n"
    "s_lshl_b32 s14, s9, 2\n"
    "s_add_u32 s14, s14, s8\n"
    "s_mul_i32 s14, s14, 2048\n"
    "s_add_u32 s14, s14, 0\n"
    "v_min_u32 v250, 15, v255\n"
    "v_mfma_f32_16x16x32_bf16 v[160:163], a[8:11], a[48:51], v[160:163]\n"
    "ds_read_b64_tr_b16 a[116:117], v229 offset:24576\n"
    "v_lshlrev_b32 v250, 7, v250\n"
    "v_mfma_f32_16x16x32_bf16 v[168:171], a[8:11], a[52:55], v[168:171]\n"
    "v_add_u32 v250, s14, v250\n"
    "v_mfma_f32_16x16x32_bf16 v[164:167], a[12:15], a[48:51], v[164:167]\n"
    "ds_read_b64_tr_b16 a[118:119], v229 offset:25600\n"
    "v_mfma_f32_16x16x32_bf16 v[172:175], a[12:15], a[52:55], v[172:175]\n"
    "global_load_dword a217, v250, s[48:49]\n"
    "v_mfma_f32_16x16x32_bf16 v[192:195], a[16:19], a[48:51], v[192:195]\n"
    "ds_read_b128 a[64:67], v225 offset:0\n"
    "v_mfma_f32_16x16x32_bf16 v[200:203], a[16:19], a[52:55], v[200:203]\n"
    "s_waitcnt lgkmcnt(15)\n"
    "v_mfma_f32_16x16x32_bf16 v[196:199], a[20:23], a[48:51], v[196:199]\n"
    "ds_read_b128 a[68:71], v225 offset:2048\n"
    "v_mfma_f32_16x16x32_bf16 v[204:207], a[20:23], a[52:55], v[204:207]\n"
    "v_mfma_f32_16x16x32_bf16 a[128:131], a[24:27], a[48:51], a[128:131]\n"
    "ds_read_b128 a[72:75], v225 offset:4096\n"
    "v_mfma_f32_16x16x32_bf16 a[136:139], a[24:27], a[52:55], a[136:139]\n"
    "v_mfma_f32_16x16x32_bf16 a[132:135], a[28:31], a[48:51], a[132:135]\n"
    "ds_read_b128 a[76:79], v225 offset:6144\n"
    "v_mfma_f32_16x16x32_bf16 a[140:143], a[28:31], a[52:55], a[140:143]\n"
    "v_mfma_f32_16x16x32_bf16 a[148:151], a[28:31], a[56:59], a[148:151]\n"
    "s_waitcnt lgkmcnt(13)\n"
    "ds_read_b128 a[80:83], v225 offset:8192\n"
    "v_mfma_f32_16x16x32_bf16 a[156:159], a[28:31], a[60:63], a[156:159]\n"
    "v_mfma_f32_16x16x32_bf16 a[144:147], a[24:27], a[56:59], a[144:147]\n"
    "ds_read_b128 a[84:87], v225 offset:10240\n"
    "v_mfma_f32_16x16x32_bf16 a[152:155], a[24:27], a[60:63], a[152:155]\n"
    "v_mfma_f32_16x16x32_bf16 v[212:215], a[20:23], a[56:59], v[212:215]\n"
    "ds_read_b128 a[88:91], v225 offset:12288\n"
    "v_mfma_f32_16x16x32_bf16 v[220:223], a[20:23], a[60:63], v[220:223]\n"
    "v_mfma_f32_16x16x32_bf16 v[208:211], a[16:19], a[56:59], v[208:211]\n"
    "ds_read_b128 a[92:95], v225 offset:14336\n"
    "v_mfma_f32_16x16x32_bf16 v[216:219], a[16:19], a[60:63], v[216:219]\n"
    "v_mfma_f32_16x16x32_bf16 v[180:183], a[12:15], a[56:59], v[180:183]\n"
    "ds_read_b64_tr_b16 a[120:121], v230 offset:24576\n"
    "v_mfma_f32_16x16x32_bf16 v[188:191], a[12:15], a[60:63], v[188:191]\n"
    "v_mfma_f32_16x16x32_bf16 v[176:179], a[8:11], a[56:59], v[176:179]\n"
    "ds_read_b64_tr_b16 a[122:123], v230 offset:25600\n"
    "v_mfma_f32_16x16x32_bf16 v[184:187], a[8:11], a[60:63], v[184:187]\n"
    "v_mfma_f32_16x16x32_bf16 v[148:151], a[4:7], a[56:59], v[148:151]\n"
    "ds_read_b64_tr_b16 a[124:125], v231 offset:24576\n"
    "v_mfma_f32_16x16x32_bf16 v[156:159], a[4:7], a[60:63], v[156:159]\n"
    "v_mfma_f32_16x16x32_bf16 v[144:147], a[0:3], a[56:59], v[144:147]\n"
    "ds_read_b64_tr_b16 a[126:127], v231 offset:25600\n"
    "v_mfma_f32_16x16x32_bf16 v[152:155], a[0:3], a[60:63], v[152:155]\n"
    "v_mfma_f32_16x16x32_bf16 v[0:3], a[32:35], a[0:3], v[0:3]\n"
    "s_waitcnt lgkmcnt(14)\n"
    "ds_read_b64_tr_b16 a[96:97], v228 offset:8192\n"
    "v_mfma_f32_16x16x32_bf16 v[4:7], a[36:39], a[0:3], v[4:7]\n"
    "v_mfma_f32_16x16x32_bf16 v[16:19], a[32:35], a[4:7], v[16:19]\n"
    "ds_read_b64_tr_b16 a[98:99], v228 offset:9216\n"
    "v_mfma_f32_16x16x32_bf16 v[20:23], a[36:39], a[4:7], v[20:23]\n"
    "v_mfma_f32_16x16x32_bf16 v[32:35], a[32:35], a[8:11], v[32:35]\n"
    "ds_read_b64_tr_b16 a[100:101], v229 offset:8192\n"
    "v_mfma_f32_16x16x32_bf16 v[36:39], a[36:39], a[8:11], v[36:39]\n"
    "v_mfma_f32_16x16x32_bf16 v[48:51], a[32:35], a[12:15], v[48:51]\n"
    "ds_read_b64_tr_b16 a[102:103], v229 offset:9216\n"
    "v_mfma_f32_16x16x32_bf16 v[52:55], a[36:39], a[12:15], v[52:55]\n"
    "v_mfma_f32_16x16x32_bf16 v[64:67], a[32:35], a[16:19], v[64:67]\n"
    "ds_read_b64_tr_b16 a[104:105], v230 offset:8192\n"
    "v_mfma_f32_16x16x32_bf16 v[68:71], a[36:39], a[16:19], v[68:71]\n"
    "v_mfma_f32_16x16x32_bf16 v[80:83], a[32:35], a[20:23], v[80:83]\n"
    "ds_read_b64_tr_b16 a[106:107], v230 offset:9216\n"
    "v_mfma_f32_16x16x32_bf16 v[84:87], a[36:39], a[20:23], v[84:87]\n"
    "v_mfma_f32_16x16x32_bf16 v[96:99], a[32:35], a[24:27], v[96:99]\n"
    "ds_read_b64_tr_b16 a[108:109], v231 offset:8192\n"
    "v_mfma_f32_16x16x32_bf16 v[100:103], a[36:39], a[24:27], v[100:103]\n"
    "v_mfma_f32_16x16x32_bf16 v[112:115], a[32:35], a[28:31], v[112:115]\n"
    "ds_read_b64_tr_b16 a[110:111], v231 offset:9216\n"
    "v_mfma_f32_16x16x32_bf16 v[116:119], a[36:39], a[28:31], v[116:119]\n"
    "v_mfma_f32_16x16x32_bf16 v[120:123], a[40:43], a[28:31], v[120:123]\n"
    "v_mfma_f32_16x16x32_bf16 v[124:127], a[44:47], a[28:31], v[124:127]\n"
    "v_mfma_f32_16x16x32_bf16 v[104:107], a[40:43], a[24:27], v[104:107]\n"
    "v_mfma_f32_16x16x32_bf16 v[108:111], a[44:47], a[24:27], v[108:111]\n"
    "v_mfma_f32_16x16x32_bf16 v[88:91], a[40:43], a[20:23], v[88:91]\n"
    "v_mfma_f32_16x16x32_bf16 v[92:95], a[44:47], a[20:23], v[92:95]\n"
    "v_mfma_f32_16x16x32_bf16 v[72:75], a[40:43], a[16:19], v[72:75]\n"
    "v_mfma_f32_16x16x32_bf16 v[76:79], a[44:47], a[16:19], v[76:79]\n"
    "v_mfma_f32_16x16x32_bf16 v[56:59], a[40:43], a[12:15], v[56:59]\n"
    "v_mfma_f32_16x16x32_bf16 v[60:63], a[44:47], a[12:15], v[60:63]\n"
    "v_mfma_f32_16x16x32_bf16 v[40:43], a[40:43], a[8:11], v[40:43]\n"
    "v_mfma_f32_16x16x32_bf16 v[44:47], a[44:47], a[8:11], v[44:47]\n"
    "v_mfma_f32_16x16x32_bf16 v[24:27], a[40:43], a[4:7], v[24:27]\n"
    "v_mfma_f32_16x16x32_bf16 v[28:31], a[44:47], a[4:7], v[28:31]\n"
    "v_mfma_f32_16x16x32_bf16 v[8:11], a[40:43], a[0:3], v[8:11]\n"
    "v_mfma_f32_16x16x32_bf16 v[12:15], a[44:47], a[0:3], v[12:15]\n"
    "s_waitcnt lgkmcnt(15)\n"
    "v_mfma_f32_16x16x32_bf16 v[128:131], a[64:67], a[112:115], v[128:131]\n"
    "v_mfma_f32_16x16x32_bf16 v[136:139], a[64:67], a[116:119], v[136:139]\n"
    "v_mfma_f32_16x16x32_bf16 v[132:135], a[68:71], a[112:115], v[132:135]\n"
    "v_mfma_f32_16x16x32_bf16 v[140:143], a[68:71], a[116:119], v[140:143]\n"
    "s_waitcnt vmcnt(2) lgkmcnt(0)\n"
    "s_barrier\n"
    "v_mfma_f32_16x16x32_bf16 v[160:163], a[72:75], a[112:115], v[160:163]\n"
    "ds_read_b64_tr_b16 a[48:49], v232 offset:16384\n"
    "s_add_u32 m0, s18, 0\n"
    "v_mfma_f32_16x16x32_bf16 v[168:171], a[72:75], a[116:119], v[168:171]\n"
    "buffer_load_dwordx4 v236, s[60:63], s16 offen lds\n"
    "v_mfma_f32_16x16x32_bf16 v[164:167], a[76:79], a[112:115], v[164:167]\n"
    "ds_read_b64_tr_b16 a[50:51], v232 offset:17408\n"
    "s_add_u32 m0, s18, 1024\n"
    "v_mfma_f32_16x16x32_bf16 v[172:175], a[76:79], a[116:119], v[172:175]\n"
    "buffer_load_dwordx4 v237, s[60:63], s16 offen lds\n"
    "v_mfma_f32_16x16x32_bf16 v[192:195], a[80:83], a[112:115], v[192:195]\n"
    "ds_read_b64_tr_b16 a[52:53], v233 offset:16384\n"
    "v_mfma_f32_16x16x32_bf16 v[200:203], a[80:83], a[116:119], v[200:203]\n"
    "v_mfma_f32_16x16x32_bf16 v[196:199], a[84:87], a[112:115], v[196:199]\n"
    "ds_read_b64_tr_b16 a[54:55], v233 offset:17408\n"
    "s_add_u32 m0, s18, 2048\n"
    "v_mfma_f32_16x16x32_bf16 v[204:207], a[84:87], a[116:119], v[204:207]\n"
    "buffer_load_dwordx4 v238, s[60:63], s16 offen lds\n"
    "v_mfma_f32_16x16x32_bf16 a[128:131], a[88:91], a[112:115], a[128:131]\n"
    "ds_read_b128 a[0:3], v226 offset:0\n"
    "v_mfma_f32_16x16x32_bf16 a[136:139], a[88:91], a[116:119], a[136:139]\n"
    "v_mfma_f32_16x16x32_bf16 a[132:135], a[92:95], a[112:115], a[132:135]\n"
    "ds_read_b128 a[4:7], v226 offset:2048\n"
    "s_add_u32 m0, s18, 3072\n"
    "v_mfma_f32_16x16x32_bf16 a[140:143], a[92:95], a[116:119], a[140:143]\n"
    "buffer_load_dwordx4 v239, s[60:63], s16 offen lds\n"
    "v_mfma_f32_16x16x32_bf16 a[148:151], a[92:95], a[120:123], a[148:151]\n"
    "ds_read_b128 a[8:11], v226 offset:4096\n"
    "v_mfma_f32_16x16x32_bf16 a[156:159], a[92:95], a[124:127], a[156:159]\n"
    "v_mfma_f32_16x16x32_bf16 a[144:147], a[88:91], a[120:123], a[144:147]\n"
    "ds_read_b128 a[12:15], v226 offset:6144\n"
    "s_add_u32 m0, s18, 4096\n"
    "v_mfma_f32_16x16x32_bf16 a[152:155], a[88:91], a[124:127], a[152:155]\n"
    "buffer_load_dwordx4 v240, s[60:63], s16 offen lds\n"
    "v_mfma_f32_16x16x32_bf16 v[212:215], a[84:87], a[120:123], v[212:215]\n"
    "ds_read_b128 a[16:19], v226 offset:8192\n"
    "v_mfma_f32_16x16x32_bf16 v[220:223], a[84:87], a[124:127], v[220:223]\n"
    "v_mfma_f32_16x16x32_bf16 v[208:211], a[80:83], a[120:123], v[208:211]\n"
    "ds_read_b128 a[20:23], v226 offset:10240\n"
    "s_add_u32 m0, s18, 5120\n"
    "v_mfma_f32_16x16x32_bf16 v[216:219], a[80:83], a[124:127], v[216:219]\n"
    "buffer_load_dwordx4 v241, s[60:63], s16 offen lds\n"
    "v_mfma_f32_16x16x32_bf16 v[180:183], a[76:79], a[120:123], v[180:183]\n"
    "ds_read_b128 a[24:27], v226 offset:12288\n"
    "v_mfma_f32_16x16x32_bf16 v[188:191], a[76:79], a[124:127], v[188:191]\n"
    "v_mfma_f32_16x16x32_bf16 v[176:179], a[72:75], a[120:123], v[176:179]\n"
    "ds_read_b128 a[28:31], v226 offset:14336\n"
    "s_add_u32 m0, s18, 6144\n"
    "v_mfma_f32_16x16x32_bf16 v[184:187], a[72:75], a[124:127], v[184:187]\n"
    "buffer_load_dwordx4 v242, s[60:63], s16 offen lds\n"
    "v_mfma_f32_16x16x32_bf16 v[148:151], a[68:71], a[120:123], v[148:151]\n"
    "ds_read_b64_tr_b16 a[56:57], v234 offset:16384\n"
    "v_mfma_f32_16x16x32_bf16 v[156:159], a[68:71], a[124:127], v[156:159]\n"
    "v_mfma_f32_16x16x32_bf16 v[144:147], a[64:67], a[120:123], v[144:147]\n"
    "ds_read_b64_tr_b16 a[58:59], v234 offset:17408\n"
    "s_add_u32 m0, s18, 7168\n"
    "v_mfma_f32_16x16x32_bf16 v[152:155], a[64:67], a[124:127], v[152:155]\n"
    "buffer_load_dwordx4 v243, s[60:63], s16 offen lds\n"
    "v_mfma_f32_16x16x32_bf16 v[0:3], a[96:99], a[64:67], v[0:3]\n"
    "ds_read_b64_tr_b16 a[60:61], v235 offset:16384\n"
    "v_mfma_f32_16x16x32_bf16 v[4:7], a[100:103], a[64:67], v[4:7]\n"
    "v_mfma_f32_16x16x32_bf16 v[16:19], a[96:99], a[68:71], v[16:19]\n"
    "ds_read_b64_tr_b16 a[62:63], v235 offset:17408\n"
    "s_add_u32 m0, s19, 0\n"
    "v_mfma_f32_16x16x32_bf16 v[20:23], a[100:103], a[68:71], v[20:23]\n"
    "buffer_load_dwordx4 v244, s[64:67], s17 offen lds\n"
    "v_mfma_f32_16x16x32_bf16 v[32:35], a[96:99], a[72:75], v[32:35]\n"
    "ds_read_b64_tr_b16 a[32:33], v232 offset:0\n"
    "s_add_u32 m0, s19, 1024\n"
    "v_mfma_f32_16x16x32_bf16 v[36:39], a[100:103], a[72:75], v[36:39]\n"
    "buffer_load_dwordx4 v245, s[64:67], s17 offen lds\n"
    "v_mfma_f32_16x16x32_bf16 v[48:51], a[96:99], a[76:79], v[48:51]\n"
    "ds_read_b64_tr_b16 a[34:35], v232 offset:1024\n"
    "v_mfma_f32_16x16x32_bf16 v[52:55], a[100:103], a[76:79], v[52:55]\n"
    "v_mfma_f32_16x16x32_bf16 v[64:67], a[96:99], a[80:83], v[64:67]\n"
    "ds_read_b64_tr_b16 a[36:37], v233 offset:0\n"
    "s_add_u32 m0, s19, 2048\n"
    "v_mfma_f32_16x16x32_bf16 v[68:71], a[100:103], a[80:83], v[68:71]\n"
    "buffer_load_dwordx4 v246, s[64:67], s17 offen lds\n"
    "v_mfma_f32_16x16x32_bf16 v[80:83], a[96:99], a[84:87], v[80:83]\n"
    "ds_read_b64_tr_b16 a[38:39], v233 offset:1024\n"
    "v_mfma_f32_16x16x32_bf16 v[84:87], a[100:103], a[84:87], v[84:87]\n"
    "v_mfma_f32_16x16x32_bf16 v[96:99], a[96:99], a[88:91], v[96:99]\n"
    "ds_read_b64_tr_b16 a[40:41], v234 offset:0\n"
    "s_add_u32 m0, s19, 3072\n"
    "v_mfma_f32_16x16x32_bf16 v[100:103], a[100:103], a[88:91], v[100:103]\n"
    "buffer_load_dwordx4 v247, s[64:67], s17 offen lds\n"
    "v_mfma_f32_16x16x32_bf16 v[112:115], a[96:99], a[92:95], v[112:115]\n"
    "ds_read_b64_tr_b16 a[42:43], v234 offset:1024\n"
    "v_mfma_f32_16x16x32_bf16 v[116:119], a[100:103], a[92:95], v[116:119]\n"
    "v_mfma_f32_16x16x32_bf16 v[120:123], a[104:107], a[92:95], v[120:123]\n"
    "ds_read_b64_tr_b16 a[44:45], v235 offset:0\n"
    "s_add_u32 m0, s19, 16384\n"
    "v_mfma_f32_16x16x32_bf16 v[124:127], a[108:111], a[92:95], v[124:127]\n"
    "buffer_load_dwordx4 v244, s[68:71], s17 offen lds\n"
    "v_mfma_f32_16x16x32_bf16 v[104:107], a[104:107], a[88:91], v[104:107]\n"
    "ds_read_b64_tr_b16 a[46:47], v235 offset:1024\n"
    "v_mfma_f32_16x16x32_bf16 v[108:111], a[108:111], a[88:91], v[108:111]\n"
    "s_add_u32 m0, s19, 17408\n"
    "v_mfma_f32_16x16x32_bf16 v[88:91], a[104:107], a[84:87], v[88:91]\n"
    "buffer_load_dwordx4 v245, s[68:71], s17 offen lds\n"
    "v_mfma_f32_16x16x32_bf16 v[92:95], a[108:111], a[84:87], v[92:95]\n"
    "v_mfma_f32_16x16x32_bf16 v[72:75], a[104:107], a[80:83], v[72:75]\n"
    "v_mfma_f32_16x16x32_bf16 v[76:79], a[108:111], a[80:83], v[76:79]\n"
    "s_add_u32 m0, s19, 18432\n"
    "v_mfma_f32_16x16x32_bf16 v[56:59], a[104:107], a[76:79], v[56:59]\n"
    "buffer_load_dwordx4 v246, s[68:71], s17 offen lds\n"
    "v_mfma_f32_16x16x32_bf16 v[60:63], a[108:111], a[76:79], v[60:63]\n"
    "v_mfma_f32_16x16x32_bf16 v[40:43], a[104:107], a[72:75], v[40:43]\n"
    "v_mfma_f32_16x16x32_bf16 v[44:47], a[108:111], a[72:75], v[44:47]\n"
    "s_add_u32 m0, s19, 19456\n"
    "v_mfma_f32_16x16x32_bf16 v[24:27], a[104:107], a[68:71], v[24:27]\n"
    "buffer_load_dwordx4 v247, s[68:71], s17 offen lds\n"
    "v_mfma_f32_16x16x32_bf16 v[28:31], a[108:111], a[68:71], v[28:31]\n"
    "s_add_u32 s16, s16, 128\n"
    "s_add_u32 s17, s17, 524288\n"
    "v_mfma_f32_16x16x32_bf16 v[8:11], a[104:107], a[64:67], v[8:11]\n"
    "v_mfma_f32_16x16x32_bf16 v[12:15], a[108:111], a[64:67], v[12:15]\n"
    "s_waitcnt lgkmcnt(15)\n"
    "v_mfma_f32_16x16x32_bf16 v[128:131], a[0:3], a[48:51], v[128:131]\n"
    "ds_read_b64_tr_b16 a[112:113], v232 offset:24576\n"
    "s_lshl_b32 s14, s9, 2\n"
    "s_add_u32 s14, s14, s8\n"
    "s_mul_i32 s14, s14, 2048\n"
    "s_add_u32 s14, s14, 2097152\n"
    "v_min_u32 v250, 15, v255\n"
    "v_mfma_f32_16x16x32_bf16 v[136:139], a[0:3], a[52:55], v[136:139]\n"
    "v_lshlrev_b32 v250, 7, v250\n"
    "v_add_u32 v250, s14, v250\n"
    "v_mfma_f32_16x16x32_bf16 v[132:135], a[4:7], a[48:51], v[132:135]\n"
    "ds_read_b64_tr_b16 a[114:115], v232 offset:25600\n"
    "v_mfma_f32_16x16x32_bf16 v[140:143], a[4:7], a[52:55], v[140:143]\n"
    "global_load_dword a218, v250, s[48:49]\n"
    "v_mfma_f32_16x16x32_bf16 v[160:163], a[8:11], a[48:51], v[160:163]\n"
    "ds_read_b64_tr_b16 a[116:117], v233 offset:24576\n"
    "s_getpc_b64 s[14:15]\n"
    "L_pc1_%=:\n"
    "v_lshlrev_b32 v250, 7, v255\n"
    "v_mfma_f32_16x16x32_bf16 v[168:171], a[8:11], a[52:55], v[168:171]\n"
    "v_lshl_add_u32 v250, s8, 13, v250\n"
    "v_min_u32 v250, L_end_%=-L_a1_%=-4, v250\n"
    "s_add_u32 s14, s14, L_a1_%=-L_pc1_%=\n"
    "s_addc_u32 s15, s15, 0\n"
    "v_mfma_f32_16x16x32_bf16 v[164:167], a[12:15], a[48:51], v[164:167]\n"
    "ds_read_b64_tr_b16 a[118:119], v233 offset:25600\n"
    "v_mfma_f32_16x16x32_bf16 v[172:175], a[12:15], a[52:55], v[172:175]\n"
    "global_load_dword a232, v250, s[14:15]\n"
    "v_mfma_f32_16x16x32_bf16 v[192:195], a[16:19], a[48:51], v[192:195]\n"
    "ds_read_b128 a[64:67], v227 offset:0\n"
    "v_mfma_f32_16x16x32_bf16 v[200:203], a[16:19], a[52:55], v[200:203]\n"
    "s_waitcnt lgkmcnt(15)\n"
    "v_mfma_f32_16x16x32_bf16 v[196:199], a[20:23], a[48:51], v[196:199]\n"
    "ds_read_b128 a[68:71], v227 offset:2048\n"
    "v_mfma_f32_16x16x32_bf16 v[204:207], a[20:23], a[52:55], v[204:207]\n"
    "v_mfma_f32_16x16x32_bf16 a[128:131], a[24:27], a[48:51], a[128:131]\n"
    "ds_read_b128 a[72:75], v227 offset:4096\n"
    "v_mfma_f32_16x16x32_bf16 a[136:139], a[24:27], a[52:55], a[136:139]\n"
    "v_mfma_f32_16x16x32_bf16 a[132:135], a[28:31], a[48:51], a[132:135]\n"
    "ds_read_b128 a[76:79], v227 offset:6144\n"
    "v_mfma_f32_16x16x32_bf16 a[140:143], a[28:31], a[52:55], a[140:143]\n"
    "v_mfma_f32_16x16x32_bf16 a[148:151], a[28:31], a[56:59], a[148:151]\n"
    "s_waitcnt lgkmcnt(13)\n"
    "ds_read_b128 a[80:83], v227 offset:8192\n"
    "v_mfma_f32_16x16x32_bf16 a[156:159], a[28:31], a[60:63], a[156:159]\n"
    "v_mfma_f32_16x16x32_bf16 a[144:147], a[24:27], a[56:59], a[144:147]\n"
    "ds_read_b128 a[84:87], v227 offset:10240\n"
    "v_mfma_f32_16x16x32_bf16 a[152:155], a[24:27], a[60:63], a[152:155]\n"
    "v_mfma_f32_16x16x32_bf16 v[212:215], a[20:23], a[56:59], v[212:215]\n"
    "ds_read_b128 a[88:91], v227 offset:12288\n"
    "v_mfma_f32_16x16x32_bf16 v[220:223], a[20:23], a[60:63], v[220:223]\n"
    "v_mfma_f32_16x16x32_bf16 v[208:211], a[16:19], a[56:59], v[208:211]\n"
    "ds_read_b128 a[92:95], v227 offset:14336\n"
    "v_mfma_f32_16x16x32_bf16 v[216:219], a[16:19], a[60:63], v[216:219]\n"
    "v_mfma_f32_16x16x32_bf16 v[180:183], a[12:15], a[56:59], v[180:183]\n"
    "ds_read_b64_tr_b16 a[120:121], v234 offset:24576\n"
    "v_mfma_f32_16x16x32_bf16 v[188:191], a[12:15], a[60:63], v[188:191]\n"
    "v_mfma_f32_16x16x32_bf16 v[176:179], a[8:11], a[56:59], v[176:179]\n"
    "ds_read_b64_tr_b16 a[122:123], v234 offset:25600\n"
    "v_mfma_f32_16x16x32_bf16 v[184:187], a[8:11], a[60:63], v[184:187]\n"
    "v_mfma_f32_16x16x32_bf16 v[148:151], a[4:7], a[56:59], v[148:151]\n"
    "ds_read_b64_tr_b16 a[124:125], v235 offset:24576\n"
    "v_mfma_f32_16x16x32_bf16 v[156:159], a[4:7], a[60:63], v[156:159]\n"
    "v_mfma_f32_16x16x32_bf16 v[144:147], a[0:3], a[56:59], v[144:147]\n"
    "ds_read_b64_tr_b16 a[126:127], v235 offset:25600\n"
    "v_mfma_f32_16x16x32_bf16 v[152:155], a[0:3], a[60:63], v[152:155]\n"
    "v_mfma_f32_16x16x32_bf16 v[0:3], a[32:35], a[0:3], v[0:3]\n"
    "s_waitcnt lgkmcnt(14)\n"
    "ds_read_b64_tr_b16 a[96:97], v232 offset:8192\n"
    "v_mfma_f32_16x16x32_bf16 v[4:7], a[36:39], a[0:3], v[4:7]\n"
    "v_mfma_f32_16x16x32_bf16 v[16:19], a[32:35], a[4:7], v[16:19]\n"
    "ds_read_b64_tr_b16 a[98:99], v232 offset:9216\n"
    "v_mfma_f32_16x16x32_bf16 v[20:23], a[36:39], a[4:7], v[20:23]\n"
    "v_mfma_f32_16x16x32_bf16 v[32:35], a[32:35], a[8:11], v[32:35]\n"
    "ds_read_b64_tr_b16 a[100:101], v233 offset:8192\n"
    "v_mfma_f32_16x16x32_bf16 v[36:39], a[36:39], a[8:11], v[36:39]\n"
    "v_mfma_f32_16x16x32_bf16 v[48:51], a[32:35], a[12:15], v[48:51]\n"
    "ds_read_b64_tr_b16 a[102:103], v233 offset:9216\n"
    "v_mfma_f32_16x16x32_bf16 v[52:55], a[36:39], a[12:15], v[52:55]\n"
    "v_mfma_f32_16x16x32_bf16 v[64:67], a[32:35], a[16:19], v[64:67]\n"
    "ds_read_b64_tr_b16 a[104:105], v234 offset:8192\n"
    "v_mfma_f32_16x16x32_bf16 v[68:71], a[36:39], a[16:19], v[68:71]\n"
    "v_mfma_f32_16x16x32_bf16 v[80:83], a[32:35], a[20:23], v[80:83]\n"
    "ds_read_b64_tr_b16 a[106:107], v234 offset:9216\n"
    "v_mfma_f32_16x16x32_bf16 v[84:87], a[36:39], a[20:23], v[84:87]\n"
    "v_mfma_f32_16x16x32_bf16 v[96:99], a[32:35], a[24:27], v[96:99]\n"
    "ds_read_b64_tr_b16 a[108:109], v235 offset:8192\n"
    "v_mfma_f32_16x16x32_bf16 v[100:103], a[36:39], a[24:27], v[100:103]\n"
    "v_mfma_f32_16x16x32_bf16 v[112:115], a[32:35], a[28:31], v[112:115]\n"
    "ds_read_b64_tr_b16 a[110:111], v235 offset:9216\n"
    "v_mfma_f32_16x16x32_bf16 v[116:119], a[36:39], a[28:31], v[116:119]\n"
    "v_mfma_f32_16x16x32_bf16 v[120:123], a[40:43], a[28:31], v[120:123]\n"
    "v_mfma_f32_16x16x32_bf16 v[124:127], a[44:47], a[28:31], v[124:127]\n"
    "v_mfma_f32_16x16x32_bf16 v[104:107], a[40:43], a[24:27], v[104:107]\n"
    "v_mfma_f32_16x16x32_bf16 v[108:111], a[44:47], a[24:27], v[108:111]\n"
    "v_mfma_f32_16x16x32_bf16 v[88:91], a[40:43], a[20:23], v[88:91]\n"
    "v_mfma_f32_16x16x32_bf16 v[92:95], a[44:47], a[20:23], v[92:95]\n"
    "v_mfma_f32_16x16x32_bf16 v[72:75], a[40:43], a[16:19], v[72:75]\n"
    "v_mfma_f32_16x16x32_bf16 v[76:79], a[44:47], a[16:19], v[76:79]\n"
    "v_mfma_f32_16x16x32_bf16 v[56:59], a[40:43], a[12:15], v[56:59]\n"
    "v_mfma_f32_16x16x32_bf16 v[60:63], a[44:47], a[12:15], v[60:63]\n"
    "v_mfma_f32_16x16x32_bf16 v[40:43], a[40:43], a[8:11], v[40:43]\n"
    "v_mfma_f32_16x16x32_bf16 v[44:47], a[44:47], a[8:11], v[44:47]\n"
    "v_mfma_f32_16x16x32_bf16 v[24:27], a[40:43], a[4:7], v[24:27]\n"
    "v_mfma_f32_16x16x32_bf16 v[28:31], a[44:47], a[4:7], v[28:31]\n"
    "v_mfma_f32_16x16x32_bf16 v[8:11], a[40:43], a[0:3], v[8:11]\n"
    "v_mfma_f32_16x16x32_bf16 v[12:15], a[44:47], a[0:3], v[12:15]\n"
    "s_waitcnt lgkmcnt(15)\n"
    "v_mfma_f32_16x16x32_bf16 v[128:131], a[64:67], a[112:115], v[128:131]\n"
    "v_mfma_f32_16x16x32_bf16 v[136:139], a[64:67], a[116:119], v[136:139]\n"
    "v_mfma_f32_16x16x32_bf16 v[132:135], a[68:71], a[112:115], v[132:135]\n"
    "v_mfma_f32_16x16x32_bf16 v[140:143], a[68:71], a[116:119], v[140:143]\n"
    "s_waitcnt vmcnt(2) lgkmcnt(0)\n"
    "s_barrier\n"
    "v_mfma_f32_16x16x32_bf16 v[160:163], a[72:75], a[112:115], v[160:163]\n"
    "ds_read_b64_tr_b16 a[48:49], v228 offset:16384\n"
    "s_add_u32 m0, s18, 65536\n"
    "v_mfma_f32_16x16x32_bf16 v[168:171], a[72:75], a[116:119], v[168:171]\n"
    "buffer_load_dwordx4 v236, s[60:63], s16 offen lds\n"
    "v_mfma_f32_16x16x32_bf16 v[164:167], a[76:79], a[112:115], v[164:167]\n"
    "ds_read_b64_tr_b16 a[50:51], v228 offset:17408\n"
    "s_add_u32 m0, s18, 66560\n"
    "v_mfma_f32_16x16x32_bf16 v[172:175], a[76:79], a[116:119], v[172:175]\n"
    "buffer_load_dwordx4 v237, s[60:63], s16 offen lds\n"
    "v_mfma_f32_16x16x32_bf16 v[192:195], a[80:83], a[112:115], v[192:195]\n"
    "ds_read_b64_tr_b16 a[52:53], v229 offset:16384\n"
    "v_mfma_f32_16x16x32_bf16 v[200:203], a[80:83], a[116:119], v[200:203]\n"
    "v_mfma_f32_16x16x32_bf16 v[196:199], a[84:87], a[112:115], v[196:199]\n"
    "ds_read_b64_tr_b16 a[54:55], v229 offset:17408\n"
    "s_add_u32 m0, s18, 67584\n"
    "v_mfma_f32_16x16x32_bf16 v[204:207], a[84:87], a[116:119], v[204:207]\n"
    "buffer_load_dwordx4 v238, s[60:63], s16 offen lds\n"
    "v_mfma_f32_16x16x32_bf16 a[128:131], a[88:91], a[112:115], a[128:131]\n"
    "ds_read_b128 a[0:3], v224 offset:0\n"
    "v_mfma_f32_16x16x32_bf16 a[136:139], a[88:91], a[116:119], a[136:139]\n"
    "v_mfma_f32_16x16x32_bf16 a[132:135], a[92:95], a[112:115], a[132:135]\n"
    "ds_read_b128 a[4:7], v224 offset:2048\n"
    "s_add_u32 m0, s18, 68608\n"
    "v_mfma_f32_16x16x32_bf16 a[140:143], a[92:95], a[116:119], a[140:143]\n"
    "buffer_load_dwordx4 v239, s[60:63], s16 offen lds\n"
    "v_mfma_f32_16x16x32_bf16 a[148:151], a[92:95], a[120:123], a[148:151]\n"
    "ds_read_b128 a[8:11], v224 offset:4096\n"
    "v_mfma_f32_16x16x32_bf16 a[156:159], a[92:95], a[124:127], a[156:159]\n"
    "v_mfma_f32_16x16x32_bf16 a[144:147], a[88:91], a[120:123], a[144:147]\n"
    "ds_read_b128 a[12:15], v224 offset:6144\n"
    "s_add_u32 m0, s18, 69632\n"
    "v_mfma_f32_16x16x32_bf16 a[152:155], a[88:91], a[124:127], a[152:155]\n"
    "buffer_load_dwordx4 v240, s[60:63], s16 offen lds\n"
    "v_mfma_f32_16x16x32_bf16 v[212:215], a[84:87], a[120:123], v[212:215]\n"
    "ds_read_b128 a[16:19], v224 offset:8192\n"
    "v_mfma_f32_16x16x32_bf16 v[220:223], a[84:87], a[124:127], v[220:223]\n"
    "v_mfma_f32_16x16x32_bf16 v[208:211], a[80:83], a[120:123], v[208:211]\n"
    "ds_read_b128 a[20:23], v224 offset:10240\n"
    "s_add_u32 m0, s18, 70656\n"
    "v_mfma_f32_16x16x32_bf16 v[216:219], a[80:83], a[124:127], v[216:219]\n"
    "buffer_load_dwordx4 v241, s[60:63], s16 offen lds\n"
    "v_mfma_f32_16x16x32_bf16 v[180:183], a[76:79], a[120:123], v[180:183]\n"
    "ds_read_b128 a[24:27], v224 offset:12288\n"
    "v_mfma_f32_16x16x32_bf16 v[188:191], a[76:79], a[124:127], v[188:191]\n"
    "v_mfma_f32_16x16x32_bf16 v[176:179], a[72:75], a[120:123], v[176:179]\n"
    "ds_read_b128 a[28:31], v224 offset:14336\n"
    "s_add_u32 m0, s18, 71680\n"
    "v_mfma_f32_16x16x32_bf16 v[184:187], a[72:75], a[124:127], v[184:187]\n"
    "buffer_load_dwordx4 v242, s[60:63], s16 offen lds\n"
    "v_mfma_f32_16x16x32_bf16 v[148:151], a[68:71], a[120:123], v[148:151]\n"
    "ds_read_b64_tr_b16 a[56:57], v230 offset:16384\n"
    "v_mfma_f32_16x16x32_bf16 v[156:159], a[68:71], a[124:127], v[156:159]\n"
    "v_mfma_f32_16x16x32_bf16 v[144:147], a[64:67], a[120:123], v[144:147]\n"
    "ds_read_b64_tr_b16 a[58:59], v230 offset:17408\n"
    "s_add_u32 m0, s18, 72704\n"
    "v_mfma_f32_16x16x32_bf16 v[152:155], a[64:67], a[124:127], v[152:155]\n"
    "buffer_load_dwordx4 v243, s[60:63], s16 offen lds\n"
    "v_mfma_f32_16x16x32_bf16 v[0:3], a[96:99], a[64:67], v[0:3]\n"
    "ds_read_b64_tr_b16 a[60:61], v231 offset:16384\n"
    "v_mfma_f32_16x16x32_bf16 v[4:7], a[100:103], a[64:67], v[4:7]\n"
    "v_mfma_f32_16x16x32_bf16 v[16:19], a[96:99], a[68:71], v[16:19]\n"
    "ds_read_b64_tr_b16 a[62:63], v231 offset:17408\n"
    "s_add_u32 m0, s19, 65536\n"
    "v_mfma_f32_16x16x32_bf16 v[20:23], a[100:103], a[68:71], v[20:23]\n"
    "buffer_load_dwordx4 v244, s[64:67], s17 offen lds\n"
    "v_mfma_f32_16x16x32_bf16 v[32:35], a[96:99], a[72:75], v[32:35]\n"
    "ds_read_b64_tr_b16 a[32:33], v228 offset:0\n"
    "s_add_u32 m0, s19, 66560\n"
    "v_mfma_f32_16x16x32_bf16 v[36:39], a[100:103], a[72:75], v[36:39]\n"
    "buffer_load_dwordx4 v245, s[64:67], s17 offen lds\n"
    "v_mfma_f32_16x16x32_bf16 v[48:51], a[96:99], a[76:79], v[48:51]\n"
    "ds_read_b64_tr_b16 a[34:35], v228 offset:1024\n"
    "v_mfma_f32_16x16x32_bf16 v[52:55], a[100:103], a[76:79], v[52:55]\n"
    "v_mfma_f32_16x16x32_bf16 v[64:67], a[96:99], a[80:83], v[64:67]\n"
    "ds_read_b64_tr_b16 a[36:37], v229 offset:0\n"
    "s_add_u32 m0, s19, 67584\n"
    "v_mfma_f32_16x16x32_bf16 v[68:71], a[100:103], a[80:83], v[68:71]\n"
    "buffer_load_dwordx4 v246, s[64:67], s17 offen lds\n"
    "v_mfma_f32_16x16x32_bf16 v[80:83], a[96:99], a[84:87], v[80:83]\n"
    "ds_read_b64_tr_b16 a[38:39], v229 offset:1024\n"
    "v_mfma_f32_16x16x32_bf16 v[84:87], a[100:103], a[84:87], v[84:87]\n"
    "v_mfma_f32_16x16x32_bf16 v[96:99], a[96:99], a[88:91], v[96:99]\n"
    "ds_read_b64_tr_b16 a[40:41], v230 offset:0\n"
    "s_add_u32 m0, s19, 68608\n"
    "v_mfma_f32_16x16x32_bf16 v[100:103], a[100:103], a[88:91], v[100:103]\n"
    "buffer_load_dwordx4 v247, s[64:67], s17 offen lds\n"
    "v_mfma_f32_16x16x32_bf16 v[112:115], a[96:99], a[92:95], v[112:115]\n"
    "ds_read_b64_tr_b16 a[42:43], v230 offset:1024\n"
    "v_mfma_f32_16x16x32_bf16 v[116:119], a[100:103], a[92:95], v[116:119]\n"
    "v_mfma_f32_16x16x32_bf16 v[120:123], a[104:107], a[92:95], v[120:123]\n"
    "ds_read_b64_tr_b16 a[44:45], v231 offset:0\n"
    "s_add_u32 m0, s19, 81920\n"
    "v_mfma_f32_16x16x32_bf16 v[124:127], a[108:111], a[92:95], v[124:127]\n"
    "buffer_load_dwordx4 v244, s[68:71], s17 offen lds\n"
    "v_mfma_f32_16x16x32_bf16 v[104:107], a[104:107], a[88:91], v[104:107]\n"
    "ds_read_b64_tr_b16 a[46:47], v231 offset:1024\n"
    "v_mfma_f32_16x16x32_bf16 v[108:111], a[108:111], a[88:91], v[108:111]\n"
    "s_add_u32 m0, s19, 82944\n"
    "v_mfma_f32_16x16x32_bf16 v[88:91], a[104:107], a[84:87], v[88:91]\n"
    "buffer_load_dwordx4 v245, s[68:71], s17 offen lds\n"
    "v_mfma_f32_16x16x32_bf16 v[92:95], a[108:111], a[84:87], v[92:95]\n"
    "v_mfma_f32_16x16x32_bf16 v[72:75], a[104:107], a[80:83], v[72:75]\n"
    "v_mfma_f32_16x16x32_bf16 v[76:79], a[108:111], a[80:83], v[76:79]\n"
    "s_add_u32 m0, s19, 83968\n"
    "v_mfma_f32_16x16x32_bf16 v[56:59], a[104:107], a[76:79], v[56:59]\n"
    "buffer_load_dwordx4 v246, s[68:71], s17 offen lds\n"
    "v_mfma_f32_16x16x32_bf16 v[60:63], a[108:111], a[76:79], v[60:63]\n"
    "v_mfma_f32_16x16x32_bf16 v[40:43], a[104:107], a[72:75], v[40:43]\n"
    "v_mfma_f32_16x16x32_bf16 v[44:47], a[108:111], a[72:75], v[44:47]\n"
    "s_add_u32 m0, s19, 84992\n"
    "v_mfma_f32_16x16x32_bf16 v[24:27], a[104:107], a[68:71], v[24:27]\n"
    "buffer_load_dwordx4 v247, s[68:71], s17 offen lds\n"
    "v_mfma_f32_16x16x32_bf16 v[28:31], a[108:111], a[68:71], v[28:31]\n"
    "s_add_u32 s16, s16, 128\n"
    "s_add_u32 s17, s17, 524288\n"
    "v_mfma_f32_16x16x32_bf16 v[8:11], a[104:107], a[64:67], v[8:11]\n"
    "v_mfma_f32_16x16x32_bf16 v[12:15], a[108:111], a[64:67], v[12:15]\n"
    "s_waitcnt lgkmcnt(15)\n"
    "v_mfma_f32_16x16x32_bf16 v[128:131], a[0:3], a[48:51], v[128:131]\n"
    "ds_read_b64_tr_b16 a[112:113], v228 offset:24576\n"
    "s_mov_b32 s60, s36\n"
    "s_and_b32 s61, s37, 0xffff\n"
    "s_mov_b32 s62, 0x200000\n"
    "s_mov_b32 s63, 0x20000\n"
    "s_lshl_b32 s15, s34, 6\n"
    "s_add_u32 s64, s38, s15\n"
    "s_addc_u32 s65, s39, 0\n"
    "s_and_b32 s65, s65, 0xffff\n"
    "s_mov_b32 s66, 0x180000\n"
    "s_mov_b32 s67, 0x20000\n"
    "s_sub_u32 s66, s66, s15\n"
    "s_lshl_b32 s15, s34, 10\n"
    "s_add_u32 s68, s40, s15\n"
    "s_addc_u32 s69, s41, 0\n"
    "s_and_b32 s69, s69, 0xffff\n"
    "s_mov_b32 s70, 0x400\n"
    "s_mov_b32 s71, 0x20000\n"
    "v_lshrrev_b32 v249, 5, v255\n"
    "v_mfma_f32_16x16x32_bf16 v[136:139], a[0:3], a[52:55], v[136:139]\n"
    "v_lshlrev_b32 v249, 1, v249\n"
    "v_and_b32 v250, 3, v255\n"
    "v_mfma_f32_16x16x32_bf16 v[132:135], a[4:7], a[48:51], v[132:135]\n"
    "ds_read_b64_tr_b16 a[114:115], v228 offset:25600\n"
    "v_xor_b32 v249, v249, v250\n"
    "v_mfma_f32_16x16x32_bf16 v[140:143], a[4:7], a[52:55], v[140:143]\n"
    "v_lshrrev_b32 v250, 2, v255\n"
    "v_mul_u32_u24 v240, 3072, v250\n"
    "v_mfma_f32_16x16x32_bf16 v[160:163], a[8:11], a[48:51], v[160:163]\n"
    "ds_read_b64_tr_b16 a[116:117], v229 offset:24576\n"
    "v_lshl_add_u32 v240, v249, 4, v240\n"
    "v_mfma_f32_16x16x32_bf16 v[168:171], a[8:11], a[52:55], v[168:171]\n"
    "v_add_u32 v241, 1024, v240\n"
    "v_add_u32 v242, 2048, v240\n"
    "v_mfma_f32_16x16x32_bf16 v[164:167], a[12:15], a[48:51], v[164:167]\n"
    "ds_read_b64_tr_b16 a[118:119], v229 offset:25600\n"
    "v_lshlrev_b32 v252, 4, v255\n"
    "s_lshl_b32 s16, s33, 17\n"
    "s_lshl_b32 s15, s8, 15\n"
    "s_add_u32 s16, s16, s15\n"
    "s_mul_i32 s17, s8, 49152\n"
    "s_lshl_b32 s18, s8, 12\n"
    "s_add_u32 s18, s18, 12288\n"
    "s_lshl_b32 s19, s8, 10\n"
    "v_mfma_f32_16x16x32_bf16 v[172:175], a[12:15], a[52:55], v[172:175]\n"
    "v_lshrrev_b32 v249, 4, v255\n"
    "v_or_b32 v249, 0, v249\n"
    "v_mfma_f32_16x16x32_bf16 v[192:195], a[16:19], a[48:51], v[192:195]\n"
    "ds_read_b128 a[64:67], v225 offset:0\n"
    "v_bfe_u32 v244, v255, 1, 3\n"
    "v_mfma_f32_16x16x32_bf16 v[200:203], a[16:19], a[52:55], v[200:203]\n"
    "s_waitcnt lgkmcnt(15)\n"
    "v_xor_b32 v249, v249, v244\n"
    "v_and_b32 v244, 15, v255\n"
    "v_mfma_f32_16x16x32_bf16 v[196:199], a[20:23], a[48:51], v[196:199]\n"
    "ds_read_b128 a[68:71], v225 offset:2048\n"
    "v_lshlrev_b32 v244, 7, v244\n"
    "v_mfma_f32_16x16x32_bf16 v[204:207], a[20:23], a[52:55], v[204:207]\n"
    "v_lshl_add_u32 v244, v249, 4, v244\n"
    "v_add_u32 v244, s18, v244\n"
    "v_mfma_f32_16x16x32_bf16 a[128:131], a[24:27], a[48:51], a[128:131]\n"
    "ds_read_b128 a[72:75], v225 offset:4096\n"
    "v_xor_b32 v245, 64, v244\n"
    "v_mfma_f32_16x16x32_bf16 a[136:139], a[24:27], a[52:55], a[136:139]\n"
    "v_lshrrev_b32 v249, 4, v255\n"
    "v_bfe_u32 v250, v255, 2, 2\n"
    "v_mfma_f32_16x16x32_bf16 a[132:135], a[28:31], a[48:51], a[132:135]\n"
    "ds_read_b128 a[76:79], v225 offset:6144\n"
    "v_lshl_add_u32 v250, v249, 3, v250\n"
    "v_mfma_f32_16x16x32_bf16 a[140:143], a[28:31], a[52:55], a[140:143]\n"
    "v_lshlrev_b32 v250, 6, v250\n"
    "v_and_b32 v251, 3, v255\n"
    "v_mfma_f32_16x16x32_bf16 a[148:151], a[28:31], a[56:59], a[148:151]\n"
    "s_waitcnt lgkmcnt(13)\n"
    "ds_read_b128 a[80:83], v225 offset:8192\n"
    "v_lshl_add_u32 v250, v251, 3, v250\n"
    "v_mfma_f32_16x16x32_bf16 a[156:159], a[28:31], a[60:63], a[156:159]\n"
    "v_and_b32 v249, 1, v249\n"
    "v_lshl_add_u32 v246, v249, 5, v250\n"
    "v_mfma_f32_16x16x32_bf16 a[144:147], a[24:27], a[56:59], a[144:147]\n"
    "ds_read_b128 a[84:87], v225 offset:10240\n"
    "v_xor_b32 v247, 32, v246\n"
    "v_mfma_f32_16x16x32_bf16 a[152:155], a[24:27], a[60:63], a[152:155]\n"
    "v_mfma_f32_16x16x32_bf16 v[212:215], a[20:23], a[56:59], v[212:215]\n"
    "ds_read_b128 a[88:91], v225 offset:12288\n"
    "v_mfma_f32_16x16x32_bf16 v[220:223], a[20:23], a[60:63], v[220:223]\n"
    "v_mfma_f32_16x16x32_bf16 v[208:211], a[16:19], a[56:59], v[208:211]\n"
    "ds_read_b128 a[92:95], v225 offset:14336\n"
    "v_mfma_f32_16x16x32_bf16 v[216:219], a[16:19], a[60:63], v[216:219]\n"
    "v_mfma_f32_16x16x32_bf16 v[180:183], a[12:15], a[56:59], v[180:183]\n"
    "ds_read_b64_tr_b16 a[120:121], v230 offset:24576\n"
    "v_mfma_f32_16x16x32_bf16 v[188:191], a[12:15], a[60:63], v[188:191]\n"
    "v_mfma_f32_16x16x32_bf16 v[176:179], a[8:11], a[56:59], v[176:179]\n"
    "ds_read_b64_tr_b16 a[122:123], v230 offset:25600\n"
    "v_mfma_f32_16x16x32_bf16 v[184:187], a[8:11], a[60:63], v[184:187]\n"
    "v_mfma_f32_16x16x32_bf16 v[148:151], a[4:7], a[56:59], v[148:151]\n"
    "ds_read_b64_tr_b16 a[124:125], v231 offset:24576\n"
    "v_mfma_f32_16x16x32_bf16 v[156:159], a[4:7], a[60:63], v[156:159]\n"
    "v_mfma_f32_16x16x32_bf16 v[144:147], a[0:3], a[56:59], v[144:147]\n"
    "ds_read_b64_tr_b16 a[126:127], v231 offset:25600\n"
    "v_mfma_f32_16x16x32_bf16 v[152:155], a[0:3], a[60:63], v[152:155]\n"
    "v_mfma_f32_16x16x32_bf16 v[0:3], a[32:35], a[0:3], v[0:3]\n"
    "s_waitcnt lgkmcnt(14)\n"
    "ds_read_b64_tr_b16 a[96:97], v228 offset:8192\n"
    "v_mfma_f32_16x16x32_bf16 v[4:7], a[36:39], a[0:3], v[4:7]\n"
    "v_mfma_f32_16x16x32_bf16 v[16:19], a[32:35], a[4:7], v[16:19]\n"
    "ds_read_b64_tr_b16 a[98:99], v228 offset:9216\n"
    "v_mfma_f32_16x16x32_bf16 v[20:23], a[36:39], a[4:7], v[20:23]\n"
    "v_mfma_f32_16x16x32_bf16 v[32:35], a[32:35], a[8:11], v[32:35]\n"
    "ds_read_b64_tr_b16 a[100:101], v229 offset:8192\n"
    "v_mfma_f32_16x16x32_bf16 v[36:39], a[36:39], a[8:11], v[36:39]\n"
    "v_mfma_f32_16x16x32_bf16 v[48:51], a[32:35], a[12:15], v[48:51]\n"
    "ds_read_b64_tr_b16 a[102:103], v229 offset:9216\n"
    "v_mfma_f32_16x16x32_bf16 v[52:55], a[36:39], a[12:15], v[52:55]\n"
    "v_mfma_f32_16x16x32_bf16 v[64:67], a[32:35], a[16:19], v[64:67]\n"
    "ds_read_b64_tr_b16 a[104:105], v230 offset:8192\n"
    "v_mfma_f32_16x16x32_bf16 v[68:71], a[36:39], a[16:19], v[68:71]\n"
    "v_mfma_f32_16x16x32_bf16 v[80:83], a[32:35], a[20:23], v[80:83]\n"
    "ds_read_b64_tr_b16 a[106:107], v230 offset:9216\n"
    "v_mfma_f32_16x16x32_bf16 v[84:87], a[36:39], a[20:23], v[84:87]\n"
    "v_mfma_f32_16x16x32_bf16 v[96:99], a[32:35], a[24:27], v[96:99]\n"
    "ds_read_b64_tr_b16 a[108:109], v231 offset:8192\n"
    "v_mfma_f32_16x16x32_bf16 v[100:103], a[36:39], a[24:27], v[100:103]\n"
    "v_mfma_f32_16x16x32_bf16 v[112:115], a[32:35], a[28:31], v[112:115]\n"
    "ds_read_b64_tr_b16 a[110:111], v231 offset:9216\n"
    "v_mfma_f32_16x16x32_bf16 v[116:119], a[36:39], a[28:31], v[116:119]\n"
    "v_mfma_f32_16x16x32_bf16 v[120:123], a[40:43], a[28:31], v[120:123]\n"
    "v_mfma_f32_16x16x32_bf16 v[124:127], a[44:47], a[28:31], v[124:127]\n"
    "v_mfma_f32_16x16x32_bf16 v[104:107], a[40:43], a[24:27], v[104:107]\n"
    "v_mfma_f32_16x16x32_bf16 v[108:111], a[44:47], a[24:27], v[108:111]\n"
    "v_mfma_f32_16x16x32_bf16 v[88:91], a[40:43], a[20:23], v[88:91]\n"
    "v_mfma_f32_16x16x32_bf16 v[92:95], a[44:47], a[20:23], v[92:95]\n"
    "v_mfma_f32_16x16x32_bf16 v[72:75], a[40:43], a[16:19], v[72:75]\n"
    "v_mfma_f32_16x16x32_bf16 v[76:79], a[44:47], a[16:19], v[76:79]\n"
    "v_mfma_f32_16x16x32_bf16 v[56:59], a[40:43], a[12:15], v[56:59]\n"
    "v_mfma_f32_16x16x32_bf16 v[60:63], a[44:47], a[12:15], v[60:63]\n"
    "v_mfma_f32_16x16x32_bf16 v[40:43], a[40:43], a[8:11], v[40:43]\n"
    "v_mfma_f32_16x16x32_bf16 v[44:47], a[44:47], a[8:11], v[44:47]\n"
    "v_mfma_f32_16x16x32_bf16 v[24:27], a[40:43], a[4:7], v[24:27]\n"
    "v_mfma_f32_16x16x32_bf16 v[28:31], a[44:47], a[4:7], v[28:31]\n"
    "v_mfma_f32_16x16x32_bf16 v[8:11], a[40:43], a[0:3], v[8:11]\n"
    "v_mfma_f32_16x16x32_bf16 v[12:15], a[44:47], a[0:3], v[12:15]\n"
    "s_waitcnt lgkmcnt(15)\n"
    "v_mfma_f32_16x16x32_bf16 v[128:131], a[64:67], a[112:115], v[128:131]\n"
    "v_mfma_f32_16x16x32_bf16 v[136:139], a[64:67], a[116:119], v[136:139]\n"
    "v_mfma_f32_16x16x32_bf16 v[132:135], a[68:71], a[112:115], v[132:135]\n"
    "v_mfma_f32_16x16x32_bf16 v[140:143], a[68:71], a[116:119], v[140:143]\n"
    "s_waitcnt vmcnt(0) lgkmcnt(0)\n"
    "s_barrier\n"
    "v_mfma_f32_16x16x32_bf16 v[160:163], a[72:75], a[112:115], v[160:163]\n"
    "ds_read_b64_tr_b16 a[48:49], v232 offset:16384\n"
    "s_add_u32 m0, s19, 0\n"
    "v_mfma_f32_16x16x32_bf16 v[168:171], a[72:75], a[116:119], v[168:171]\n"
    "buffer_load_dwordx4 v240, s[64:67], s17 offen lds\n"
    "v_mfma_f32_16x16x32_bf16 v[164:167], a[76:79], a[112:115], v[164:167]\n"
    "ds_read_b64_tr_b16 a[50:51], v232 offset:17408\n"
    "v_mfma_f32_16x16x32_bf16 v[172:175], a[76:79], a[116:119], v[172:175]\n"
    "v_mfma_f32_16x16x32_bf16 v[192:195], a[80:83], a[112:115], v[192:195]\n"
    "ds_read_b64_tr_b16 a[52:53], v233 offset:16384\n"
    "s_add_u32 m0, s19, 4096\n"
    "v_mfma_f32_16x16x32_bf16 v[200:203], a[80:83], a[116:119], v[200:203]\n"
    "buffer_load_dwordx4 v241, s[64:67], s17 offen lds\n"
    "v_mfma_f32_16x16x32_bf16 v[196:199], a[84:87], a[112:115], v[196:199]\n"
    "ds_read_b64_tr_b16 a[54:55], v233 offset:17408\n"
    "v_mfma_f32_16x16x32_bf16 v[204:207], a[84:87], a[116:119], v[204:207]\n"
    "v_mfma_f32_16x16x32_bf16 a[128:131], a[88:91], a[112:115], a[128:131]\n"
    "ds_read_b128 a[0:3], v226 offset:0\n"
    "s_add_u32 m0, s19, 8192\n"
    "v_mfma_f32_16x16x32_bf16 a[136:139], a[88:91], a[116:119], a[136:139]\n"
    "buffer_load_dwordx4 v242, s[64:67], s17 offen lds\n"
    "v_mfma_f32_16x16x32_bf16 a[132:135], a[92:95], a[112:115], a[132:135]\n"
    "ds_read_b128 a[4:7], v226 offset:2048\n"
    "v_mfma_f32_16x16x32_bf16 a[140:143], a[92:95], a[116:119], a[140:143]\n"
    "v_mfma_f32_16x16x32_bf16 a[148:151], a[92:95], a[120:123], a[148:151]\n"
    "ds_read_b128 a[8:11], v226 offset:4096\n"
    "s_add_u32 m0, s18, 0\n"
    "v_mfma_f32_16x16x32_bf16 a[156:159], a[92:95], a[124:127], a[156:159]\n"
    "buffer_load_dwordx4 v236, s[60:63], s16 offen lds\n"
    "v_mfma_f32_16x16x32_bf16 a[144:147], a[88:91], a[120:123], a[144:147]\n"
    "ds_read_b128 a[12:15], v226 offset:6144\n"
    "v_mfma_f32_16x16x32_bf16 a[152:155], a[88:91], a[124:127], a[152:155]\n"
    "v_mfma_f32_16x16x32_bf16 v[212:215], a[84:87], a[120:123], v[212:215]\n"
    "ds_read_b128 a[16:19], v226 offset:8192\n"
    "s_add_u32 m0, s18, 1024\n"
    "v_mfma_f32_16x16x32_bf16 v[220:223], a[84:87], a[124:127], v[220:223]\n"
    "buffer_load_dwordx4 v237, s[60:63], s16 offen lds\n"
    "v_mfma_f32_16x16x32_bf16 v[208:211], a[80:83], a[120:123], v[208:211]\n"
    "ds_read_b128 a[20:23], v226 offset:10240\n"
    "v_mfma_f32_16x16x32_bf16 v[216:219], a[80:83], a[124:127], v[216:219]\n"
    "v_mfma_f32_16x16x32_bf16 v[180:183], a[76:79], a[120:123], v[180:183]\n"
    "ds_read_b128 a[24:27], v226 offset:12288\n"
    "s_add_u32 m0, s18, 2048\n"
    "v_mfma_f32_16x16x32_bf16 v[188:191], a[76:79], a[124:127], v[188:191]\n"
    "buffer_load_dwordx4 v238, s[60:63], s16 offen lds\n"
    "v_mfma_f32_16x16x32_bf16 v[176:179], a[72:75], a[120:123], v[176:179]\n"
    "ds_read_b128 a[28:31], v226 offset:14336\n"
    "v_mfma_f32_16x16x32_bf16 v[184:187], a[72:75], a[124:127], v[184:187]\n"
    "v_mfma_f32_16x16x32_bf16 v[148:151], a[68:71], a[120:123], v[148:151]\n"
    "ds_read_b64_tr_b16 a[56:57], v234 offset:16384\n"
    "s_add_u32 m0, s18, 3072\n"
    "v_mfma_f32_16x16x32_bf16 v[156:159], a[68:71], a[124:127], v[156:159]\n"
    "buffer_load_dwordx4 v239, s[60:63], s16 offen lds\n"
    "v_mfma_f32_16x16x32_bf16 v[144:147], a[64:67], a[120:123], v[144:147]\n"
    "ds_read_b64_tr_b16 a[58:59], v234 offset:17408\n"
    "s_add_u32 s16, s16, 128\n"
    "s_add_u32 s17, s17, 196608\n"
    "v_mfma_f32_16x16x32_bf16 v[152:155], a[64:67], a[124:127], v[152:155]\n"
    "v_mfma_f32_16x16x32_bf16 v[0:3], a[96:99], a[64:67], v[0:3]\n"
    "ds_read_b64_tr_b16 a[60:61], v235 offset:16384\n"
    "s_add_u32 m0, s19, 28672\n"
    "v_mfma_f32_16x16x32_bf16 v[4:7], a[100:103], a[64:67], v[4:7]\n"
    "buffer_load_dwordx4 v240, s[64:67], s17 offen lds\n"
    "v_mfma_f32_16x16x32_bf16 v[16:19], a[96:99], a[68:71], v[16:19]\n"
    "ds_read_b64_tr_b16 a[62:63], v235 offset:17408\n"
    "v_mfma_f32_16x16x32_bf16 v[20:23], a[100:103], a[68:71], v[20:23]\n"
    "v_mfma_f32_16x16x32_bf16 v[32:35], a[96:99], a[72:75], v[32:35]\n"
    "ds_read_b64_tr_b16 a[32:33], v232 offset:0\n"
    "s_add_u32 m0, s19, 32768\n"
    "v_mfma_f32_16x16x32_bf16 v[36:39], a[100:103], a[72:75], v[36:39]\n"
    "buffer_load_dwordx4 v241, s[64:67], s17 offen lds\n"
    "v_mfma_f32_16x16x32_bf16 v[48:51], a[96:99], a[76:79], v[48:51]\n"
    "ds_read_b64_tr_b16 a[34:35], v232 offset:1024\n"
    "v_mfma_f32_16x16x32_bf16 v[52:55], a[100:103], a[76:79], v[52:55]\n"
    "v_mfma_f32_16x16x32_bf16 v[64:67], a[96:99], a[80:83], v[64:67]\n"
    "ds_read_b64_tr_b16 a[36:37], v233 offset:0\n"
    "s_add_u32 m0, s19, 36864\n"
    "v_mfma_f32_16x16x32_bf16 v[68:71], a[100:103], a[80:83], v[68:71]\n"
    "buffer_load_dwordx4 v242, s[64:67], s17 offen lds\n"
    "v_mfma_f32_16x16x32_bf16 v[80:83], a[96:99], a[84:87], v[80:83]\n"
    "ds_read_b64_tr_b16 a[38:39], v233 offset:1024\n"
    "v_mfma_f32_16x16x32_bf16 v[84:87], a[100:103], a[84:87], v[84:87]\n"
    "v_mfma_f32_16x16x32_bf16 v[96:99], a[96:99], a[88:91], v[96:99]\n"
    "ds_read_b64_tr_b16 a[40:41], v234 offset:0\n"
    "s_add_u32 m0, s18, 28672\n"
    "v_mfma_f32_16x16x32_bf16 v[100:103], a[100:103], a[88:91], v[100:103]\n"
    "buffer_load_dwordx4 v236, s[60:63], s16 offen lds\n"
    "v_mfma_f32_16x16x32_bf16 v[112:115], a[96:99], a[92:95], v[112:115]\n"
    "ds_read_b64_tr_b16 a[42:43], v234 offset:1024\n"
    "v_mfma_f32_16x16x32_bf16 v[116:119], a[100:103], a[92:95], v[116:119]\n"
    "v_mfma_f32_16x16x32_bf16 v[120:123], a[104:107], a[92:95], v[120:123]\n"
    "ds_read_b64_tr_b16 a[44:45], v235 offset:0\n"
    "s_add_u32 m0, s18, 29696\n"
    "v_mfma_f32_16x16x32_bf16 v[124:127], a[108:111], a[92:95], v[124:127]\n"
    "buffer_load_dwordx4 v237, s[60:63], s16 offen lds\n"
    "v_mfma_f32_16x16x32_bf16 v[104:107], a[104:107], a[88:91], v[104:107]\n"
    "ds_read_b64_tr_b16 a[46:47], v235 offset:1024\n"
    "v_mfma_f32_16x16x32_bf16 v[108:111], a[108:111], a[88:91], v[108:111]\n"
    "s_add_u32 m0, s18, 30720\n"
    "v_mfma_f32_16x16x32_bf16 v[88:91], a[104:107], a[84:87], v[88:91]\n"
    "buffer_load_dwordx4 v238, s[60:63], s16 offen lds\n"
    "v_mfma_f32_16x16x32_bf16 v[92:95], a[108:111], a[84:87], v[92:95]\n"
    "v_mfma_f32_16x16x32_bf16 v[72:75], a[104:107], a[80:83], v[72:75]\n"
    "v_mfma_f32_16x16x32_bf16 v[76:79], a[108:111], a[80:83], v[76:79]\n"
    "s_add_u32 m0, s18, 31744\n"
    "v_mfma_f32_16x16x32_bf16 v[56:59], a[104:107], a[76:79], v[56:59]\n"
    "buffer_load_dwordx4 v239, s[60:63], s16 offen lds\n"
    "v_mfma_f32_16x16x32_bf16 v[60:63], a[108:111], a[76:79], v[60:63]\n"
    "s_add_u32 s16, s16, 128\n"
    "s_add_u32 s17, s17, 196608\n"
    "v_mfma_f32_16x16x32_bf16 v[40:43], a[104:107], a[72:75], v[40:43]\n"
    "v_mfma_f32_16x16x32_bf16 v[44:47], a[108:111], a[72:75], v[44:47]\n"
    "s_add_u32 m0, s19, 159744\n"
    "v_mfma_f32_16x16x32_bf16 v[24:27], a[104:107], a[68:71], v[24:27]\n"
    "buffer_load_dwordx4 v252, s[68:71], 0 offen lds\n"
    "v_mfma_f32_16x16x32_bf16 v[28:31], a[108:111], a[68:71], v[28:31]\n"
    "v_mfma_f32_16x16x32_bf16 v[8:11], a[104:107], a[64:67], v[8:11]\n"
    "v_mfma_f32_16x16x32_bf16 v[12:15], a[108:111], a[64:67], v[12:15]\n"
    "s_waitcnt lgkmcnt(15)\n"
    "v_mfma_f32_16x16x32_bf16 v[128:131], a[0:3], a[48:51], v[128:131]\n"
    "ds_read_b64_tr_b16 a[112:113], v232 offset:24576\n"
    "s_add_u32 m0, s19, 131072\n"
    "v_mfma_f32_16x16x32_bf16 v[136:139], a[0:3], a[52:55], v[136:139]\n"
    "buffer_load_dwordx4 v240, s[64:67], s17 offen lds\n"
    "v_mfma_f32_16x16x32_bf16 v[132:135], a[4:7], a[48:51], v[132:135]\n"
    "ds_read_b64_tr_b16 a[114:115], v232 offset:25600\n"
    "v_mfma_f32_16x16x32_bf16 v[140:143], a[4:7], a[52:55], v[140:143]\n"
    "v_mfma_f32_16x16x32_bf16 v[160:163], a[8:11], a[48:51], v[160:163]\n"
    "ds_read_b64_tr_b16 a[116:117], v233 offset:24576\n"
    "v_mfma_f32_16x16x32_bf16 v[168:171], a[8:11], a[52:55], v[168:171]\n"
    "v_mfma_f32_16x16x32_bf16 v[164:167], a[12:15], a[48:51], v[164:167]\n"
    "ds_read_b64_tr_b16 a[118:119], v233 offset:25600\n"
    "v_mfma_f32_16x16x32_bf16 v[172:175], a[12:15], a[52:55], v[172:175]\n"
    "v_mfma_f32_16x16x32_bf16 v[192:195], a[16:19], a[48:51], v[192:195]\n"
    "ds_read_b128 a[64:67], v227 offset:0\n"
    "s_add_u32 m0, s19, 135168\n"
    "v_mfma_f32_16x16x32_bf16 v[200:203], a[16:19], a[52:55], v[200:203]\n"
    "s_waitcnt lgkmcnt(15)\n"
    "buffer_load_dwordx4 v241, s[64:67], s17 offen lds\n"
    "v_mfma_f32_16x16x32_bf16 v[196:199], a[20:23], a[48:51], v[196:199]\n"
    "ds_read_b128 a[68:71], v227 offset:2048\n"
    "v_mfma_f32_16x16x32_bf16 v[204:207], a[20:23], a[52:55], v[204:207]\n"
    "v_mfma_f32_16x16x32_bf16 a[128:131], a[24:27], a[48:51], a[128:131]\n"
    "ds_read_b128 a[72:75], v227 offset:4096\n"
    "v_mfma_f32_16x16x32_bf16 a[136:139], a[24:27], a[52:55], a[136:139]\n"
    "v_mfma_f32_16x16x32_bf16 a[132:135], a[28:31], a[48:51], a[132:135]\n"
    "ds_read_b128 a[76:79], v227 offset:6144\n"
    "v_mfma_f32_16x16x32_bf16 a[140:143], a[28:31], a[52:55], a[140:143]\n"
    "v_mfma_f32_16x16x32_bf16 a[148:151], a[28:31], a[56:59], a[148:151]\n"
    "s_waitcnt lgkmcnt(13)\n"
    "ds_read_b128 a[80:83], v227 offset:8192\n"
    "v_mfma_f32_16x16x32_bf16 a[156:159], a[28:31], a[60:63], a[156:159]\n"
    "v_mfma_f32_16x16x32_bf16 a[144:147], a[24:27], a[56:59], a[144:147]\n"
    "ds_read_b128 a[84:87], v227 offset:10240\n"
    "s_add_u32 m0, s19, 139264\n"
    "v_mfma_f32_16x16x32_bf16 a[152:155], a[24:27], a[60:63], a[152:155]\n"
    "buffer_load_dwordx4 v242, s[64:67], s17 offen lds\n"
    "v_mfma_f32_16x16x32_bf16 v[212:215], a[20:23], a[56:59], v[212:215]\n"
    "ds_read_b128 a[88:91], v227 offset:12288\n"
    "v_mfma_f32_16x16x32_bf16 v[220:223], a[20:23], a[60:63], v[220:223]\n"
    "v_mfma_f32_16x16x32_bf16 v[208:211], a[16:19], a[56:59], v[208:211]\n"
    "ds_read_b128 a[92:95], v227 offset:14336\n"
    "v_mfma_f32_16x16x32_bf16 v[216:219], a[16:19], a[60:63], v[216:219]\n"
    "v_mfma_f32_16x16x32_bf16 v[180:183], a[12:15], a[56:59], v[180:183]\n"
    "ds_read_b64_tr_b16 a[120:121], v234 offset:24576\n"
    "v_mfma_f32_16x16x32_bf16 v[188:191], a[12:15], a[60:63], v[188:191]\n"
    "v_mfma_f32_16x16x32_bf16 v[176:179], a[8:11], a[56:59], v[176:179]\n"
    "ds_read_b64_tr_b16 a[122:123], v234 offset:25600\n"
    "s_add_u32 m0, s18, 131072\n"
    "v_mfma_f32_16x16x32_bf16 v[184:187], a[8:11], a[60:63], v[184:187]\n"
    "buffer_load_dwordx4 v236, s[60:63], s16 offen lds\n"
    "v_mfma_f32_16x16x32_bf16 v[148:151], a[4:7], a[56:59], v[148:151]\n"
    "ds_read_b64_tr_b16 a[124:125], v235 offset:24576\n"
    "v_mfma_f32_16x16x32_bf16 v[156:159], a[4:7], a[60:63], v[156:159]\n"
    "v_mfma_f32_16x16x32_bf16 v[144:147], a[0:3], a[56:59], v[144:147]\n"
    "ds_read_b64_tr_b16 a[126:127], v235 offset:25600\n"
    "v_mfma_f32_16x16x32_bf16 v[152:155], a[0:3], a[60:63], v[152:155]\n"
    "v_mfma_f32_16x16x32_bf16 v[0:3], a[32:35], a[0:3], v[0:3]\n"
    "s_waitcnt lgkmcnt(14)\n"
    "ds_read_b64_tr_b16 a[96:97], v232 offset:8192\n"
    "v_mfma_f32_16x16x32_bf16 v[4:7], a[36:39], a[0:3], v[4:7]\n"
    "v_mfma_f32_16x16x32_bf16 v[16:19], a[32:35], a[4:7], v[16:19]\n"
    "ds_read_b64_tr_b16 a[98:99], v232 offset:9216\n"
    "v_mfma_f32_16x16x32_bf16 v[20:23], a[36:39], a[4:7], v[20:23]\n"
    "v_mfma_f32_16x16x32_bf16 v[32:35], a[32:35], a[8:11], v[32:35]\n"
    "ds_read_b64_tr_b16 a[100:101], v233 offset:8192\n"
    "s_add_u32 m0, s18, 132096\n"
    "v_mfma_f32_16x16x32_bf16 v[36:39], a[36:39], a[8:11], v[36:39]\n"
    "buffer_load_dwordx4 v237, s[60:63], s16 offen lds\n"
    "v_mfma_f32_16x16x32_bf16 v[48:51], a[32:35], a[12:15], v[48:51]\n"
    "ds_read_b64_tr_b16 a[102:103], v233 offset:9216\n"
    "v_mfma_f32_16x16x32_bf16 v[52:55], a[36:39], a[12:15], v[52:55]\n"
    "v_mfma_f32_16x16x32_bf16 v[64:67], a[32:35], a[16:19], v[64:67]\n"
    "ds_read_b64_tr_b16 a[104:105], v234 offset:8192\n"
    "v_mfma_f32_16x16x32_bf16 v[68:71], a[36:39], a[16:19], v[68:71]\n"
    "v_mfma_f32_16x16x32_bf16 v[80:83], a[32:35], a[20:23], v[80:83]\n"
    "ds_read_b64_tr_b16 a[106:107], v234 offset:9216\n"
    "v_mfma_f32_16x16x32_bf16 v[84:87], a[36:39], a[20:23], v[84:87]\n"
    "v_mfma_f32_16x16x32_bf16 v[96:99], a[32:35], a[24:27], v[96:99]\n"
    "ds_read_b64_tr_b16 a[108:109], v235 offset:8192\n"
    "s_add_u32 m0, s18, 133120\n"
    "v_mfma_f32_16x16x32_bf16 v[100:103], a[36:39], a[24:27], v[100:103]\n"
    "buffer_load_dwordx4 v238, s[60:63], s16 offen lds\n"
    "v_mfma_f32_16x16x32_bf16 v[112:115], a[32:35], a[28:31], v[112:115]\n"
    "ds_read_b64_tr_b16 a[110:111], v235 offset:9216\n"
    "v_mfma_f32_16x16x32_bf16 v[116:119], a[36:39], a[28:31], v[116:119]\n"
    "v_mfma_f32_16x16x32_bf16 v[120:123], a[40:43], a[28:31], v[120:123]\n"
    "v_mfma_f32_16x16x32_bf16 v[124:127], a[44:47], a[28:31], v[124:127]\n"
    "v_mfma_f32_16x16x32_bf16 v[104:107], a[40:43], a[24:27], v[104:107]\n"
    "v_mfma_f32_16x16x32_bf16 v[108:111], a[44:47], a[24:27], v[108:111]\n"
    "v_mfma_f32_16x16x32_bf16 v[88:91], a[40:43], a[20:23], v[88:91]\n"
    "v_mfma_f32_16x16x32_bf16 v[92:95], a[44:47], a[20:23], v[92:95]\n"
    "s_add_u32 m0, s18, 134144\n"
    "v_mfma_f32_16x16x32_bf16 v[72:75], a[40:43], a[16:19], v[72:75]\n"
    "buffer_load_dwordx4 v239, s[60:63], s16 offen lds\n"
    "v_mfma_f32_16x16x32_bf16 v[76:79], a[44:47], a[16:19], v[76:79]\n"
    "s_add_u32 s16, s16, 128\n"
    "s_add_u32 s17, s17, 196608\n"
    "v_mfma_f32_16x16x32_bf16 v[56:59], a[40:43], a[12:15], v[56:59]\n"
    "v_mfma_f32_16x16x32_bf16 v[60:63], a[44:47], a[12:15], v[60:63]\n"
    "v_mfma_f32_16x16x32_bf16 v[40:43], a[40:43], a[8:11], v[40:43]\n"
    "v_mfma_f32_16x16x32_bf16 v[44:47], a[44:47], a[8:11], v[44:47]\n"
    "v_mfma_f32_16x16x32_bf16 v[24:27], a[40:43], a[4:7], v[24:27]\n"
    "v_mfma_f32_16x16x32_bf16 v[28:31], a[44:47], a[4:7], v[28:31]\n"
    "v_mfma_f32_16x16x32_bf16 v[8:11], a[40:43], a[0:3], v[8:11]\n"
    "v_mfma_f32_16x16x32_bf16 v[12:15], a[44:47], a[0:3], v[12:15]\n"
    "s_waitcnt lgkmcnt(15)\n"
    "v_mfma_f32_16x16x32_bf16 v[128:131], a[64:67], a[112:115], v[128:131]\n"
    "v_mfma_f32_16x16x32_bf16 v[136:139], a[64:67], a[116:119], v[136:139]\n"
    "v_mfma_f32_16x16x32_bf16 v[132:135], a[68:71], a[112:115], v[132:135]\n"
    "v_mfma_f32_16x16x32_bf16 v[140:143], a[68:71], a[116:119], v[140:143]\n"
    "s_waitcnt vmcnt(8) lgkmcnt(0)\n"
    "s_barrier\n"
    "v_mfma_f32_16x16x32_bf16 v[160:163], a[72:75], a[112:115], v[160:163]\n"
    "ds_read_b128 a[0:3], v244 offset:0\n"
    "s_add_u32 m0, s19, 65536\n"
    "v_mfma_f32_16x16x32_bf16 v[168:171], a[72:75], a[116:119], v[168:171]\n"
    "buffer_load_dwordx4 v240, s[64:67], s17 offen lds\n"
    "v_mfma_f32_16x16x32_bf16 v[164:167], a[76:79], a[112:115], v[164:167]\n"
    "ds_read_b128 a[4:7], v244 offset:2048\n"
    "v_mfma_f32_16x16x32_bf16 v[172:175], a[76:79], a[116:119], v[172:175]\n"
    "v_mfma_f32_16x16x32_bf16 v[192:195], a[80:83], a[112:115], v[192:195]\n"
    "ds_read_b64_tr_b16 a[8:9], v246 offset:0\n"
    "s_add_u32 m0, s19, 69632\n"
    "v_mfma_f32_16x16x32_bf16 v[200:203], a[80:83], a[116:119], v[200:203]\n"
    "buffer_load_dwordx4 v241, s[64:67], s17 offen lds\n"
    "v_mfma_f32_16x16x32_bf16 v[196:199], a[84:87], a[112:115], v[196:199]\n"
    "ds_read_b64_tr_b16 a[10:11], v246 offset:256\n"
    "v_mfma_f32_16x16x32_bf16 v[204:207], a[84:87], a[116:119], v[204:207]\n"
    "v_mfma_f32_16x16x32_bf16 a[128:131], a[88:91], a[112:115], a[128:131]\n"
    "ds_read_b64_tr_b16 a[12:13], v247 offset:0\n"
    "s_add_u32 m0, s19, 73728\n"
    "v_mfma_f32_16x16x32_bf16 a[136:139], a[88:91], a[116:119], a[136:139]\n"
    "buffer_load_dwordx4 v242, s[64:67], s17 offen lds\n"
    "v_mfma_f32_16x16x32_bf16 a[132:135], a[92:95], a[112:115], a[132:135]\n"
    "ds_read_b64_tr_b16 a[14:15], v247 offset:256\n"
    "v_mfma_f32_16x16x32_bf16 a[140:143], a[92:95], a[116:119], a[140:143]\n"
    "v_mfma_f32_16x16x32_bf16 a[148:151], a[92:95], a[120:123], a[148:151]\n"
    "ds_read_b64_tr_b16 a[16:17], v246 offset:4096\n"
    "s_add_u32 m0, s18, 65536\n"
    "v_mfma_f32_16x16x32_bf16 a[156:159], a[92:95], a[124:127], a[156:159]\n"
    "buffer_load_dwordx4 v236, s[60:63], s16 offen lds\n"
    "v_mfma_f32_16x16x32_bf16 a[144:147], a[88:91], a[120:123], a[144:147]\n"
    "ds_read_b64_tr_b16 a[18:19], v246 offset:4352\n"
    "v_mfma_f32_16x16x32_bf16 a[152:155], a[88:91], a[124:127], a[152:155]\n"
    "v_mfma_f32_16x16x32_bf16 v[212:215], a[84:87], a[120:123], v[212:215]\n"
    "ds_read_b64_tr_b16 a[20:21], v247 offset:4096\n"
    "s_add_u32 m0, s18, 66560\n"
    "v_mfma_f32_16x16x32_bf16 v[220:223], a[84:87], a[124:127], v[220:223]\n"
    "buffer_load_dwordx4 v237, s[60:63], s16 offen lds\n"
    "v_mfma_f32_16x16x32_bf16 v[208:211], a[80:83], a[120:123], v[208:211]\n"
    "ds_read_b64_tr_b16 a[22:23], v247 offset:4352\n"
    "v_mfma_f32_16x16x32_bf16 v[216:219], a[80:83], a[124:127], v[216:219]\n"
    "v_mfma_f32_16x16x32_bf16 v[180:183], a[76:79], a[120:123], v[180:183]\n"
    "ds_read_b64_tr_b16 a[24:25], v246 offset:8192\n"
    "s_add_u32 m0, s18, 67584\n"
    "v_mfma_f32_16x16x32_bf16 v[188:191], a[76:79], a[124:127], v[188:191]\n"
    "buffer_load_dwordx4 v238, s[60:63], s16 offen lds\n"
    "v_mfma_f32_16x16x32_bf16 v[176:179], a[72:75], a[120:123], v[176:179]\n"
    "ds_read_b64_tr_b16 a[26:27], v246 offset:8448\n"
    "v_mfma_f32_16x16x32_bf16 v[184:187], a[72:75], a[124:127], v[184:187]\n"
    "v_mfma_f32_16x16x32_bf16 v[148:151], a[68:71], a[120:123], v[148:151]\n"
    "ds_read_b64_tr_b16 a[28:29], v247 offset:8192\n"
    "s_add_u32 m0, s18, 68608\n"
    "v_mfma_f32_16x16x32_bf16 v[156:159], a[68:71], a[124:127], v[156:159]\n"
    "buffer_load_dwordx4 v239, s[60:63], s16 offen lds\n"
    "v_mfma_f32_16x16x32_bf16 v[144:147], a[64:67], a[120:123], v[144:147]\n"
    "ds_read_b64_tr_b16 a[30:31], v247 offset:8448\n"
    "s_add_u32 s16, s16, 128\n"
    "s_add_u32 s17, s17, 196608\n"
    "v_mfma_f32_16x16x32_bf16 v[152:155], a[64:67], a[124:127], v[152:155]\n"
    "v_mfma_f32_16x16x32_bf16 v[0:3], a[96:99], a[64:67], v[0:3]\n"
    "v_mfma_f32_16x16x32_bf16 v[4:7], a[100:103], a[64:67], v[4:7]\n"
    "s_add_u32 m0, s19, 94208\n"
    "v_mfma_f32_16x16x32_bf16 v[16:19], a[96:99], a[68:71], v[16:19]\n"
    "buffer_load_dwordx4 v240, s[64:67], s17 offen lds\n"
    "v_mfma_f32_16x16x32_bf16 v[20:23], a[100:103], a[68:71], v[20:23]\n"
    "v_mfma_f32_16x16x32_bf16 v[32:35], a[96:99], a[72:75], v[32:35]\n"
    "v_mfma_f32_16x16x32_bf16 v[36:39], a[100:103], a[72:75], v[36:39]\n"
    "s_add_u32 m0, s19, 98304\n"
    "v_mfma_f32_16x16x32_bf16 v[48:51], a[96:99], a[76:79], v[48:51]\n"
    "buffer_load_dwordx4 v241, s[64:67], s17 offen lds\n"
    "v_mfma_f32_16x16x32_bf16 v[52:55], a[100:103], a[76:79], v[52:55]\n"
    "v_mfma_f32_16x16x32_bf16 v[64:67], a[96:99], a[80:83], v[64:67]\n"
    "v_mfma_f32_16x16x32_bf16 v[68:71], a[100:103], a[80:83], v[68:71]\n"
    "s_add_u32 m0, s19, 102400\n"
    "v_mfma_f32_16x16x32_bf16 v[80:83], a[96:99], a[84:87], v[80:83]\n"
    "buffer_load_dwordx4 v242, s[64:67], s17 offen lds\n"
    "v_mfma_f32_16x16x32_bf16 v[84:87], a[100:103], a[84:87], v[84:87]\n"
    "v_mfma_f32_16x16x32_bf16 v[96:99], a[96:99], a[88:91], v[96:99]\n"
    "v_mfma_f32_16x16x32_bf16 v[100:103], a[100:103], a[88:91], v[100:103]\n"
    "s_add_u32 m0, s18, 94208\n"
    "v_mfma_f32_16x16x32_bf16 v[112:115], a[96:99], a[92:95], v[112:115]\n"
    "buffer_load_dwordx4 v236, s[60:63], s16 offen lds\n"
    "v_mfma_f32_16x16x32_bf16 v[116:119], a[100:103], a[92:95], v[116:119]\n"
    "v_mfma_f32_16x16x32_bf16 v[120:123], a[104:107], a[92:95], v[120:123]\n"
    "v_mfma_f32_16x16x32_bf16 v[124:127], a[108:111], a[92:95], v[124:127]\n"
    "v_mfma_f32_16x16x32_bf16 v[104:107], a[104:107], a[88:91], v[104:107]\n"
    "s_add_u32 m0, s18, 95232\n"
    "v_mfma_f32_16x16x32_bf16 v[108:111], a[108:111], a[88:91], v[108:111]\n"
    "buffer_load_dwordx4 v237, s[60:63], s16 offen lds\n"
    "v_mfma_f32_16x16x32_bf16 v[88:91], a[104:107], a[84:87], v[88:91]\n"
    "v_mfma_f32_16x16x32_bf16 v[92:95], a[108:111], a[84:87], v[92:95]\n"
    "v_mfma_f32_16x16x32_bf16 v[72:75], a[104:107], a[80:83], v[72:75]\n"
    "s_add_u32 m0, s18, 96256\n"
    "v_mfma_f32_16x16x32_bf16 v[76:79], a[108:111], a[80:83], v[76:79]\n"
    "buffer_load_dwordx4 v238, s[60:63], s16 offen lds\n"
    "v_mfma_f32_16x16x32_bf16 v[56:59], a[104:107], a[76:79], v[56:59]\n"
    "v_mfma_f32_16x16x32_bf16 v[60:63], a[108:111], a[76:79], v[60:63]\n"
    "v_mfma_f32_16x16x32_bf16 v[40:43], a[104:107], a[72:75], v[40:43]\n"
    "s_add_u32 m0, s18, 97280\n"
    "v_mfma_f32_16x16x32_bf16 v[44:47], a[108:111], a[72:75], v[44:47]\n"
    "buffer_load_dwordx4 v239, s[60:63], s16 offen lds\n"
    "v_mfma_f32_16x16x32_bf16 v[24:27], a[104:107], a[68:71], v[24:27]\n"
    "s_add_u32 s16, s16, 128\n"
    "s_add_u32 s17, s17, 196608\n"
    "v_add_u32 v224, 65536, v244\n"
    "v_add_u32 v225, 65536, v245\n"
    "v_mfma_f32_16x16x32_bf16 v[28:31], a[108:111], a[68:71], v[28:31]\n"
    "v_add_u32 v226, 65536, v246\n"
    "v_add_u32 v227, 65536, v247\n"
    "v_mfma_f32_16x16x32_bf16 v[8:11], a[104:107], a[64:67], v[8:11]\n"
    "v_add_u32 v228, 131072, v244\n"
    "v_add_u32 v229, 131072, v245\n"
    "v_mfma_f32_16x16x32_bf16 v[12:15], a[108:111], a[64:67], v[12:15]\n"
    "v_add_u32 v230, 131072, v246\n"
    "v_add_u32 v231, 131072, v247\n"
    "L_a1_%=:\n"
    "s_waitcnt lgkmcnt(7)\n"
    "v_mfma_f32_16x16x32_bf16 a[160:163], a[8:11], a[0:3], 0\n"
    "ds_read_b128 a[32:35], v245 offset:0\n"
    "ds_read_b128 a[36:39], v245 offset:2048\n"
    "v_cvt_pk_bf16_f32 v128, v128, v129\n"
    "v_mfma_f32_16x16x32_bf16 a[168:171], a[8:11], a[4:7], 0\n"
    "ds_read_b64_tr_b16 a[40:41], v246 offset:2048\n"
    "v_cvt_pk_bf16_f32 v129, v130, v131\n"
    "v_mfma_f32_16x16x32_bf16 a[164:167], a[12:15], a[0:3], 0\n"
    "ds_read_b64_tr_b16 a[42:43], v246 offset:2304\n"
    "v_cvt_pk_bf16_f32 v130, v132, v133\n"
    "v_mfma_f32_16x16x32_bf16 a[172:175], a[12:15], a[4:7], 0\n"
    "s_waitcnt lgkmcnt(7)\n"
    "ds_read_b64_tr_b16 a[44:45], v247 offset:2048\n"
    "v_cvt_pk_bf16_f32 v131, v134, v135\n"
    "v_cvt_pk_bf16_f32 v136, v136, v137\n"
    "v_mfma_f32_16x16x32_bf16 a[176:179], a[16:19], a[0:3], 0\n"
    "ds_read_b64_tr_b16 a[46:47], v247 offset:2304\n"
    "v_cvt_pk_bf16_f32 v137, v138, v139\n"
    "v_mfma_f32_16x16x32_bf16 a[184:187], a[16:19], a[4:7], 0\n"
    "ds_read_b64_tr_b16 a[48:49], v246 offset:6144\n"
    "v_cvt_pk_bf16_f32 v138, v140, v141\n"
    "v_mfma_f32_16x16x32_bf16 a[180:183], a[20:23], a[0:3], 0\n"
    "ds_read_b64_tr_b16 a[50:51], v246 offset:6400\n"
    "ds_read_b64_tr_b16 a[52:53], v247 offset:6144\n"
    "v_cvt_pk_bf16_f32 v139, v142, v143\n"
    "v_mfma_f32_16x16x32_bf16 a[188:191], a[20:23], a[4:7], 0\n"
    "s_waitcnt lgkmcnt(8)\n"
    "ds_read_b64_tr_b16 a[54:55], v247 offset:6400\n"
    "v_cvt_pk_bf16_f32 v144, v144, v145\n"
    "v_mfma_f32_16x16x32_bf16 a[192:195], a[0:3], a[24:27], 0\n"
    "ds_read_b64_tr_b16 a[56:57], v246 offset:10240\n"
    "v_cvt_pk_bf16_f32 v145, v146, v147\n"
    "v_mfma_f32_16x16x32_bf16 a[196:199], a[4:7], a[24:27], 0\n"
    "ds_read_b64_tr_b16 a[58:59], v246 offset:10496\n"
    "v_cvt_pk_bf16_f32 v146, v148, v149\n"
    "v_mfma_f32_16x16x32_bf16 a[200:203], a[0:3], a[28:31], 0\n"
    "ds_read_b64_tr_b16 a[60:61], v247 offset:10240\n"
    "v_cvt_pk_bf16_f32 v147, v150, v151\n"
    "v_mfma_f32_16x16x32_bf16 a[204:207], a[4:7], a[28:31], 0\n"
    "s_waitcnt lgkmcnt(6)\n"
    "ds_read_b64_tr_b16 a[62:63], v247 offset:10496\n"
    "v_cvt_pk_bf16_f32 v152, v152, v153\n"
    "v_cvt_pk_bf16_f32 v153, v154, v155\n"
    "v_mfma_f32_16x16x32_bf16 a[160:163], a[40:43], a[32:35], a[160:163]\n"
    "v_cvt_pk_bf16_f32 v154, v156, v157\n"
    "v_mfma_f32_16x16x32_bf16 a[168:171], a[40:43], a[36:39], a[168:171]\n"
    "v_cvt_pk_bf16_f32 v155, v158, v159\n"
    "v_cvt_pk_bf16_f32 v160, v160, v161\n"
    "v_mfma_f32_16x16x32_bf16 a[164:167], a[44:47], a[32:35], a[164:167]\n"
    "v_cvt_pk_bf16_f32 v161, v162, v163\n"
    "v_cvt_pk_bf16_f32 v162, v164, v165\n"
    "v_mfma_f32_16x16x32_bf16 a[172:175], a[44:47], a[36:39], a[172:175]\n"
    "s_waitcnt vmcnt(14) lgkmcnt(0)\n"
    "s_barrier\n"
    "ds_read_b128 a[0:3], v244 offset:28672\n"
    "s_add_u32 m0, s19, 0\n"
    "ds_read_b128 a[4:7], v244 offset:30720\n"
    "v_cvt_pk_bf16_f32 v163, v166, v167\n"
    "v_mfma_f32_16x16x32_bf16 a[176:179], a[48:51], a[32:35], a[176:179]\n"
    "ds_read_b64_tr_b16 a[8:9], v246 offset:28672\n"
    "buffer_load_dwordx4 v240, s[64:67], s17 offen lds\n"
    "v_mfma_f32_16x16x32_bf16 a[184:187], a[48:51], a[36:39], a[184:187]\n"
    "ds_read_b64_tr_b16 a[10:11], v246 offset:28928\n"
    "ds_read_b64_tr_b16 a[12:13], v247 offset:28672\n"
    "v_mfma_f32_16x16x32_bf16 a[180:183], a[52:55], a[32:35], a[180:183]\n"
    "ds_read_b64_tr_b16 a[14:15], v247 offset:28928\n"
    "s_add_u32 m0, s19, 4096\n"
    "ds_read_b64_tr_b16 a[16:17], v246 offset:32768\n"
    "v_cvt_pk_bf16_f32 v168, v168, v169\n"
    "v_mfma_f32_16x16x32_bf16 a[188:191], a[52:55], a[36:39], a[188:191]\n"
    "ds_read_b64_tr_b16 a[18:19], v246 offset:33024\n"
    "buffer_load_dwordx4 v241, s[64:67], s17 offen lds\n"
    "v_mfma_f32_16x16x32_bf16 a[192:195], a[32:35], a[56:59], a[192:195]\n"
    "ds_read_b64_tr_b16 a[20:21], v247 offset:32768\n"
    "ds_read_b64_tr_b16 a[22:23], v247 offset:33024\n"
    "v_mfma_f32_16x16x32_bf16 a[196:199], a[36:39], a[56:59], a[196:199]\n"
    "ds_read_b64_tr_b16 a[24:25], v246 offset:36864\n"
    "s_add_u32 m0, s19, 8192\n"
    "ds_read_b64_tr_b16 a[26:27], v246 offset:37120\n"
    "v_cvt_pk_bf16_f32 v169, v170, v171\n"
    "v_mfma_f32_16x16x32_bf16 a[200:203], a[32:35], a[60:63], a[200:203]\n"
    "buffer_load_dwordx4 v242, s[64:67], s17 offen lds\n"
    "v_cvt_pk_bf16_f32 v170, v172, v173\n"
    "v_mfma_f32_16x16x32_bf16 a[204:207], a[36:39], a[60:63], a[204:207]\n"
    "s_waitcnt lgkmcnt(5)\n"
    "ds_read_b64_tr_b16 a[28:29], v247 offset:36864\n"
    "ds_read_b64_tr_b16 a[30:31], v247 offset:37120\n"
    "v_mfma_f32_16x16x32_bf16 a[160:163], a[8:11], a[0:3], a[160:163]\n"
    "ds_read_b128 a[32:35], v245 offset:28672\n"
    "s_add_u32 m0, s18, 0\n"
    "ds_read_b128 a[36:39], v245 offset:30720\n"
    "v_cvt_pk_bf16_f32 v171, v174, v175\n"
    "v_mfma_f32_16x16x32_bf16 a[168:171], a[8:11], a[4:7], a[168:171]\n"
    "buffer_load_dwordx4 v236, s[60:63], s16 offen lds\n"
    "v_mfma_f32_16x16x32_bf16 a[164:167], a[12:15], a[0:3], a[164:167]\n"
    "ds_read_b64_tr_b16 a[40:41], v246 offset:30720\n"
    "ds_read_b64_tr_b16 a[42:43], v246 offset:30976\n"
    "v_cvt_pk_bf16_f32 v176, v176, v177\n"
    "v_mfma_f32_16x16x32_bf16 a[172:175], a[12:15], a[4:7], a[172:175]\n"
    "s_waitcnt lgkmcnt(7)\n"
    "ds_read_b64_tr_b16 a[44:45], v247 offset:30720\n"
    "v_cvt_pk_bf16_f32 v177, v178, v179\n"
    "v_mfma_f32_16x16x32_bf16 a[176:179], a[16:19], a[0:3], a[176:179]\n"
    "ds_read_b64_tr_b16 a[46:47], v247 offset:30976\n"
    "s_add_u32 m0, s18, 1024\n"
    "ds_read_b64_tr_b16 a[48:49], v246 offset:34816\n"
    "v_cvt_pk_bf16_f32 v178, v180, v181\n"
    "v_mfma_f32_16x16x32_bf16 a[184:187], a[16:19], a[4:7], a[184:187]\n"
    "buffer_load_dwordx4 v237, s[60:63], s16 offen lds\n"
    "v_mfma_f32_16x16x32_bf16 a[180:183], a[20:23], a[0:3], a[180:183]\n"
    "ds_read_b64_tr_b16 a[50:51], v246 offset:35072\n"
    "ds_read_b64_tr_b16 a[52:53], v247 offset:34816\n"
    "v_cvt_pk_bf16_f32 v179, v182, v183\n"
    "v_mfma_f32_16x16x32_bf16 a[188:191], a[20:23], a[4:7], a[188:191]\n"
    "s_waitcnt lgkmcnt(8)\n"
    "ds_read_b64_tr_b16 a[54:55], v247 offset:35072\n"
    "s_add_u32 m0, s18, 2048\n"
    "v_cvt_pk_bf16_f32 v184, v184, v185\n"
    "v_mfma_f32_16x16x32_bf16 a[192:195], a[0:3], a[24:27], a[192:195]\n"
    "ds_read_b64_tr_b16 a[56:57], v246 offset:38912\n"
    "buffer_load_dwordx4 v238, s[60:63], s16 offen lds\n"
    "v_mfma_f32_16x16x32_bf16 a[196:199], a[4:7], a[24:27], a[196:199]\n"
    "ds_read_b64_tr_b16 a[58:59], v246 offset:39168\n"
    "v_cvt_pk_bf16_f32 v185, v186, v187\n"
    "v_mfma_f32_16x16x32_bf16 a[200:203], a[0:3], a[28:31], a[200:203]\n"
    "ds_read_b64_tr_b16 a[60:61], v247 offset:38912\n"
    "s_add_u32 m0, s18, 3072\n"
    "ds_read_b64_tr_b16 a[62:63], v247 offset:39168\n"
    "v_cvt_pk_bf16_f32 v186, v188, v189\n"
    "v_mfma_f32_16x16x32_bf16 a[204:207], a[4:7], a[28:31], a[204:207]\n"
    "s_waitcnt lgkmcnt(7)\n"
    "buffer_load_dwordx4 v239, s[60:63], s16 offen lds\n"
    "v_cvt_pk_bf16_f32 v187, v190, v191\n"
    "v_mfma_f32_16x16x32_bf16 a[160:163], a[40:43], a[32:35], a[160:163]\n"
    "s_add_u32 s16, s16, 128\n"
    "s_add_u32 s17, s17, 196608\n"
    "v_cvt_pk_bf16_f32 v0, v0, v1\n"
    "v_accvgpr_read_b32 v249, a128\n"
    "v_mfma_f32_16x16x32_bf16 a[168:171], a[40:43], a[36:39], a[168:171]\n"
    "v_cvt_pk_bf16_f32 v1, v2, v3\n"
    "v_accvgpr_read_b32 v250, a129\n"
    "v_mfma_f32_16x16x32_bf16 a[164:167], a[44:47], a[32:35], a[164:167]\n"
    "v_cvt_pk_bf16_f32 v2, v4, v5\n"
    "v_cvt_pk_bf16_f32 v132, v249, v250\n"
    "v_mfma_f32_16x16x32_bf16 a[172:175], a[44:47], a[36:39], a[172:175]\n"
    "s_waitcnt vmcnt(14) lgkmcnt(0)\n"
    "s_barrier\n"
    "ds_read_b128 a[0:3], v228 offset:0\n"
    "s_add_u32 m0, s19, 28672\n"
    "ds_read_b128 a[4:7], v228 offset:2048\n"
    "v_accvgpr_read_b32 v249, a130\n"
    "v_mfma_f32_16x16x32_bf16 a[176:179], a[48:51], a[32:35], a[176:179]\n"
    "ds_read_b64_tr_b16 a[8:9], v230 offset:0\n"
    "buffer_load_dwordx4 v240, s[64:67], s17 offen lds\n"
    "v_mfma_f32_16x16x32_bf16 a[184:187], a[48:51], a[36:39], a[184:187]\n"
    "ds_read_b64_tr_b16 a[10:11], v230 offset:256\n"
    "ds_read_b64_tr_b16 a[12:13], v231 offset:0\n"
    "v_mfma_f32_16x16x32_bf16 a[180:183], a[52:55], a[32:35], a[180:183]\n"
    "ds_read_b64_tr_b16 a[14:15], v231 offset:256\n"
    "s_add_u32 m0, s19, 32768\n"
    "ds_read_b64_tr_b16 a[16:17], v230 offset:4096\n"
    "v_cvt_pk_bf16_f32 v3, v6, v7\n"
    "s_add_u32 s25, s26, 0\n"
    "v_mfma_f32_16x16x32_bf16 a[188:191], a[52:55], a[36:39], a[188:191]\n"
    "ds_read_b64_tr_b16 a[18:19], v230 offset:4352\n"
    "buffer_load_dwordx4 v241, s[64:67], s17 offen lds\n"
    "v_mfma_f32_16x16x32_bf16 a[192:195], a[32:35], a[56:59], a[192:195]\n"
    "ds_read_b64_tr_b16 a[20:21], v231 offset:4096\n"
    "ds_read_b64_tr_b16 a[22:23], v231 offset:4352\n"
    "v_accvgpr_read_b32 v250, a131\n"
    "v_mfma_f32_16x16x32_bf16 a[196:199], a[36:39], a[56:59], a[196:199]\n"
    "ds_read_b64_tr_b16 a[24:25], v230 offset:8192\n"
    "s_add_u32 m0, s19, 36864\n"
    "ds_read_b64_tr_b16 a[26:27], v230 offset:8448\n"
    "v_cvt_pk_bf16_f32 v133, v249, v250\n"
    "v_mfma_f32_16x16x32_bf16 a[200:203], a[32:35], a[60:63], a[200:203]\n"
    "buffer_load_dwordx4 v242, s[64:67], s17 offen lds\n"
    "v_mfma_f32_16x16x32_bf16 a[204:207], a[36:39], a[60:63], a[204:207]\n"
    "s_waitcnt lgkmcnt(5)\n"
    "ds_read_b64_tr_b16 a[28:29], v231 offset:8192\n"
    "ds_read_b64_tr_b16 a[30:31], v231 offset:8448\n"
    "v_accvgpr_read_b32 v249, a132\n"
    "v_mfma_f32_16x16x32_bf16 a[160:163], a[8:11], a[0:3], a[160:163]\n"
    "ds_read_b128 a[32:35], v229 offset:0\n"
    "s_add_u32 m0, s18, 28672\n"
    "ds_read_b128 a[36:39], v229 offset:2048\n"
    "v_mfma_f32_16x16x32_bf16 a[168:171], a[8:11], a[4:7], a[168:171]\n"
    "buffer_load_dwordx4 v236, s[60:63], s16 offen lds\n"
    "v_accvgpr_read_b32 v250, a133\n"
    "v_mfma_f32_16x16x32_bf16 a[164:167], a[12:15], a[0:3], a[164:167]\n"
    "ds_read_b64_tr_b16 a[40:41], v230 offset:2048\n"
    "ds_read_b64_tr_b16 a[42:43], v230 offset:2304\n"
    "v_cvt_pk_bf16_f32 v134, v249, v250\n"
    "v_mfma_f32_16x16x32_bf16 a[172:175], a[12:15], a[4:7], a[172:175]\n"
    "s_waitcnt lgkmcnt(7)\n"
    "ds_read_b64_tr_b16 a[44:45], v231 offset:2048\n"
    "buffer_store_dwordx4 v[0:3], v248, s[72:75], s25 offen offset:0 sc1\n"
    "v_mfma_f32_16x16x32_bf16 a[176:179], a[16:19], a[0:3], a[176:179]\n"
    "ds_read_b64_tr_b16 a[46:47], v231 offset:2304\n"
    "s_add_u32 m0, s18, 29696\n"
    "ds_read_b64_tr_b16 a[48:49], v230 offset:6144\n"
    "v_mfma_f32_16x16x32_bf16 a[184:187], a[16:19], a[4:7], a[184:187]\n"
    "buffer_load_dwordx4 v237, s[60:63], s16 offen lds\n"
    "v_cvt_pk_bf16_f32 v8, v8, v9\n"
    "v_mfma_f32_16x16x32_bf16 a[180:183], a[20:23], a[0:3], a[180:183]\n"
    "ds_read_b64_tr_b16 a[50:51], v230 offset:6400\n"
    "ds_read_b64_tr_b16 a[52:53], v231 offset:6144\n"
    "v_accvgpr_read_b32 v249, a134\n"
    "v_mfma_f32_16x16x32_bf16 a[188:191], a[20:23], a[4:7], a[188:191]\n"
    "s_waitcnt lgkmcnt(8)\n"
    "ds_read_b64_tr_b16 a[54:55], v231 offset:6400\n"
    "s_add_u32 m0, s18, 30720\n"
    "v_accvgpr_read_b32 v250, a135\n"
    "v_mfma_f32_16x16x32_bf16 a[192:195], a[0:3], a[24:27], a[192:195]\n"
    "ds_read_b64_tr_b16 a[56:57], v230 offset:10240\n"
    "buffer_load_dwordx4 v238, s[60:63], s16 offen lds\n"
    "v_mfma_f32_16x16x32_bf16 a[196:199], a[4:7], a[24:27], a[196:199]\n"
    "ds_read_b64_tr_b16 a[58:59], v230 offset:10496\n"
    "v_cvt_pk_bf16_f32 v9, v10, v11\n"
    "v_cvt_pk_bf16_f32 v135, v249, v250\n"
    "v_mfma_f32_16x16x32_bf16 a[200:203], a[0:3], a[28:31], a[200:203]\n"
    "ds_read_b64_tr_b16 a[60:61], v231 offset:10240\n"
    "s_add_u32 m0, s18, 31744\n"
    "ds_read_b64_tr_b16 a[62:63], v231 offset:10496\n"
    "v_mfma_f32_16x16x32_bf16 a[204:207], a[4:7], a[28:31], a[204:207]\n"
    "s_waitcnt lgkmcnt(7)\n"
    "buffer_load_dwordx4 v239, s[60:63], s16 offen lds\n"
    "v_accvgpr_read_b32 v249, a136\n"
    "v_mfma_f32_16x16x32_bf16 a[160:163], a[40:43], a[32:35], a[160:163]\n"
    "s_add_u32 s16, s16, 128\n"
    "s_add_u32 s17, s17, 196608\n"
    "v_cvt_pk_bf16_f32 v10, v12, v13\n"
    "v_accvgpr_read_b32 v250, a137\n"
    "v_mfma_f32_16x16x32_bf16 a[168:171], a[40:43], a[36:39], a[168:171]\n"
    "v_cvt_pk_bf16_f32 v11, v14, v15\n"
    "s_add_u32 s25, s26, 0\n"
    "v_cvt_pk_bf16_f32 v140, v249, v250\n"
    "v_mfma_f32_16x16x32_bf16 a[164:167], a[44:47], a[32:35], a[164:167]\n"
    "buffer_store_dwordx4 v[8:11], v248, s[72:75], s25 offen offset:1024 sc1\n"
    "v_accvgpr_read_b32 v249, a138\n"
    "v_mfma_f32_16x16x32_bf16 a[172:175], a[44:47], a[36:39], a[172:175]\n"
    "s_waitcnt vmcnt(16) lgkmcnt(0)\n"
    "s_barrier\n"
    "ds_read_b128 a[0:3], v224 offset:0\n"
    "s_add_u32 m0, s19, 131072\n"
    "ds_read_b128 a[4:7], v224 offset:2048\n"
    "v_mfma_f32_16x16x32_bf16 a[176:179], a[48:51], a[32:35], a[176:179]\n"
    "ds_read_b64_tr_b16 a[8:9], v226 offset:0\n"
    "buffer_load_dwordx4 v240, s[64:67], s17 offen lds\n"
    "v_mfma_f32_16x16x32_bf16 a[184:187], a[48:51], a[36:39], a[184:187]\n"
    "ds_read_b64_tr_b16 a[10:11], v226 offset:256\n"
    "ds_read_b64_tr_b16 a[12:13], v227 offset:0\n"
    "v_accvgpr_read_b32 v250, a139\n"
    "v_mfma_f32_16x16x32_bf16 a[180:183], a[52:55], a[32:35], a[180:183]\n"
    "ds_read_b64_tr_b16 a[14:15], v227 offset:256\n"
    "s_add_u32 m0, s19, 135168\n"
    "ds_read_b64_tr_b16 a[16:17], v226 offset:4096\n"
    "v_cvt_pk_bf16_f32 v141, v249, v250\n"
    "v_mfma_f32_16x16x32_bf16 a[188:191], a[52:55], a[36:39], a[188:191]\n"
    "ds_read_b64_tr_b16 a[18:19], v226 offset:4352\n"
    "buffer_load_dwordx4 v241, s[64:67], s17 offen lds\n"
    "v_mfma_f32_16x16x32_bf16 a[192:195], a[32:35], a[56:59], a[192:195]\n"
    "ds_read_b64_tr_b16 a[20:21], v227 offset:4096\n"
    "ds_read_b64_tr_b16 a[22:23], v227 offset:4352\n"
    "v_mfma_f32_16x16x32_bf16 a[196:199], a[36:39], a[56:59], a[196:199]\n"
    "ds_read_b64_tr_b16 a[24:25], v226 offset:8192\n"
    "s_add_u32 m0, s19, 139264\n"
    "ds_read_b64_tr_b16 a[26:27], v226 offset:8448\n"
    "v_accvgpr_read_b32 v249, a140\n"
    "v_mfma_f32_16x16x32_bf16 a[200:203], a[32:35], a[60:63], a[200:203]\n"
    "buffer_load_dwordx4 v242, s[64:67], s17 offen lds\n"
    "v_mfma_f32_16x16x32_bf16 a[204:207], a[36:39], a[60:63], a[204:207]\n"
    "s_waitcnt lgkmcnt(5)\n"
    "ds_read_b64_tr_b16 a[28:29], v227 offset:8192\n"
    "ds_read_b64_tr_b16 a[30:31], v227 offset:8448\n"
    "v_cvt_pk_bf16_f32 v16, v16, v17\n"
    "v_mfma_f32_16x16x32_bf16 a[160:163], a[8:11], a[0:3], a[160:163]\n"
    "ds_read_b128 a[32:35], v225 offset:0\n"
    "s_add_u32 m0, s18, 131072\n"
    "ds_read_b128 a[36:39], v225 offset:2048\n"
    "v_accvgpr_read_b32 v250, a141\n"
    "v_mfma_f32_16x16x32_bf16 a[168:171], a[8:11], a[4:7], a[168:171]\n"
    "buffer_load_dwordx4 v236, s[60:63], s16 offen lds\n"
    "v_cvt_pk_bf16_f32 v142, v249, v250\n"
    "v_mfma_f32_16x16x32_bf16 a[164:167], a[12:15], a[0:3], a[164:167]\n"
    "ds_read_b64_tr_b16 a[40:41], v226 offset:2048\n"
    "ds_read_b64_tr_b16 a[42:43], v226 offset:2304\n"
    "v_mfma_f32_16x16x32_bf16 a[172:175], a[12:15], a[4:7], a[172:175]\n"
    "s_waitcnt lgkmcnt(7)\n"
    "ds_read_b64_tr_b16 a[44:45], v227 offset:2048\n"
    "v_cvt_pk_bf16_f32 v17, v18, v19\n"
    "v_accvgpr_read_b32 v249, a142\n"
    "v_mfma_f32_16x16x32_bf16 a[176:179], a[16:19], a[0:3], a[176:179]\n"
    "ds_read_b64_tr_b16 a[46:47], v227 offset:2304\n"
    "s_add_u32 m0, s18, 132096\n"
    "ds_read_b64_tr_b16 a[48:49], v226 offset:6144\n"
    "v_mfma_f32_16x16x32_bf16 a[184:187], a[16:19], a[4:7], a[184:187]\n"
    "buffer_load_dwordx4 v237, s[60:63], s16 offen lds\n"
    "v_cvt_pk_bf16_f32 v18, v20, v21\n"
    "v_mfma_f32_16x16x32_bf16 a[180:183], a[20:23], a[0:3], a[180:183]\n"
    "ds_read_b64_tr_b16 a[50:51], v226 offset:6400\n"
    "ds_read_b64_tr_b16 a[52:53], v227 offset:6144\n"
    "v_accvgpr_read_b32 v250, a143\n"
    "v_mfma_f32_16x16x32_bf16 a[188:191], a[20:23], a[4:7], a[188:191]\n"
    "s_waitcnt lgkmcnt(8)\n"
    "ds_read_b64_tr_b16 a[54:55], v227 offset:6400\n"
    "s_add_u32 m0, s18, 133120\n"
    "v_cvt_pk_bf16_f32 v143, v249, v250\n"
    "v_mfma_f32_16x16x32_bf16 a[192:195], a[0:3], a[24:27], a[192:195]\n"
    "ds_read_b64_tr_b16 a[56:57], v226 offset:10240\n"
    "buffer_load_dwordx4 v238, s[60:63], s16 offen lds\n"
    "v_mfma_f32_16x16x32_bf16 a[196:199], a[4:7], a[24:27], a[196:199]\n"
    "ds_read_b64_tr_b16 a[58:59], v226 offset:10496\n"
    "v_cvt_pk_bf16_f32 v19, v22, v23\n"
    "s_add_u32 s25, s26, 4096\n"
    "v_accvgpr_read_b32 v249, a144\n"
    "v_mfma_f32_16x16x32_bf16 a[200:203], a[0:3], a[28:31], a[200:203]\n"
    "ds_read_b64_tr_b16 a[60:61], v227 offset:10240\n"
    "s_add_u32 m0, s18, 134144\n"
    "ds_read_b64_tr_b16 a[62:63], v227 offset:10496\n"
    "v_mfma_f32_16x16x32_bf16 a[204:207], a[4:7], a[28:31], a[204:207]\n"
    "s_waitcnt lgkmcnt(7)\n"
    "buffer_load_dwordx4 v239, s[60:63], s16 offen lds\n"
    "v_accvgpr_read_b32 v250, a145\n"
    "v_mfma_f32_16x16x32_bf16 a[160:163], a[40:43], a[32:35], a[160:163]\n"
    "ds_read_b128 a[0:3], v224 offset:28672\n"
    "s_add_u32 s16, s16, 128\n"
    "s_add_u32 s17, s17, 196608\n"
    "ds_read_b128 a[4:7], v224 offset:30720\n"
    "v_cvt_pk_bf16_f32 v148, v249, v250\n"
    "v_mfma_f32_16x16x32_bf16 a[168:171], a[40:43], a[36:39], a[168:171]\n"
    "ds_read_b64_tr_b16 a[8:9], v226 offset:28672\n"
    "buffer_store_dwordx4 v[16:19], v248, s[72:75], s25 offen offset:0 sc1\n"
    "v_mfma_f32_16x16x32_bf16 a[164:167], a[44:47], a[32:35], a[164:167]\n"
    "ds_read_b64_tr_b16 a[10:11], v226 offset:28928\n"
    "v_accvgpr_read_b32 v249, a146\n"
    "v_mfma_f32_16x16x32_bf16 a[172:175], a[44:47], a[36:39], a[172:175]\n"
    "s_waitcnt lgkmcnt(7)\n"
    "ds_read_b64_tr_b16 a[12:13], v227 offset:28672\n"
    "v_accvgpr_read_b32 v250, a147\n"
    "v_mfma_f32_16x16x32_bf16 a[176:179], a[48:51], a[32:35], a[176:179]\n"
    "ds_read_b64_tr_b16 a[14:15], v227 offset:28928\n"
    "v_cvt_pk_bf16_f32 v24, v24, v25\n"
    "v_cvt_pk_bf16_f32 v149, v249, v250\n"
    "v_mfma_f32_16x16x32_bf16 a[184:187], a[48:51], a[36:39], a[184:187]\n"
    "ds_read_b64_tr_b16 a[16:17], v226 offset:32768\n"
    "v_accvgpr_read_b32 v249, a148\n"
    "v_mfma_f32_16x16x32_bf16 a[180:183], a[52:55], a[32:35], a[180:183]\n"
    "ds_read_b64_tr_b16 a[18:19], v226 offset:33024\n"
    "ds_read_b64_tr_b16 a[20:21], v227 offset:32768\n"
    "v_cvt_pk_bf16_f32 v25, v26, v27\n"
    "v_mfma_f32_16x16x32_bf16 a[188:191], a[52:55], a[36:39], a[188:191]\n"
    "s_waitcnt lgkmcnt(8)\n"
    "ds_read_b64_tr_b16 a[22:23], v227 offset:33024\n"
    "v_accvgpr_read_b32 v250, a149\n"
    "v_mfma_f32_16x16x32_bf16 a[192:195], a[32:35], a[56:59], a[192:195]\n"
    "ds_read_b64_tr_b16 a[24:25], v226 offset:36864\n"
    "v_cvt_pk_bf16_f32 v26, v28, v29\n"
    "v_cvt_pk_bf16_f32 v150, v249, v250\n"
    "v_mfma_f32_16x16x32_bf16 a[196:199], a[36:39], a[56:59], a[196:199]\n"
    "ds_read_b64_tr_b16 a[26:27], v226 offset:37120\n"
    "v_accvgpr_read_b32 v249, a150\n"
    "v_mfma_f32_16x16x32_bf16 a[200:203], a[32:35], a[60:63], a[200:203]\n"
    "ds_read_b64_tr_b16 a[28:29], v227 offset:36864\n"
    "v_cvt_pk_bf16_f32 v27, v30, v31\n"
    "s_add_u32 s25, s26, 4096\n"
    "v_mfma_f32_16x16x32_bf16 a[204:207], a[36:39], a[60:63], a[204:207]\n"
    "s_waitcnt lgkmcnt(6)\n"
    "ds_read_b64_tr_b16 a[30:31], v227 offset:37120\n"
    "v_accvgpr_read_b32 v250, a151\n"
    "v_cvt_pk_bf16_f32 v151, v249, v250\n"
    "v_mfma_f32_16x16x32_bf16 a[160:163], a[8:11], a[0:3], a[160:163]\n"
    "ds_read_b128 a[32:35], v225 offset:28672\n"
    "ds_read_b128 a[36:39], v225 offset:30720\n"
    "v_mfma_f32_16x16x32_bf16 a[168:171], a[8:11], a[4:7], a[168:171]\n"
    "ds_read_b64_tr_b16 a[40:41], v226 offset:30720\n"
    "buffer_store_dwordx4 v[24:27], v248, s[72:75], s25 offen offset:1024 sc1\n"
    "v_mfma_f32_16x16x32_bf16 a[164:167], a[12:15], a[0:3], a[164:167]\n"
    "ds_read_b64_tr_b16 a[42:43], v226 offset:30976\n"
    "v_accvgpr_read_b32 v249, a152\n"
    "v_mfma_f32_16x16x32_bf16 a[172:175], a[12:15], a[4:7], a[172:175]\n"
    "s_waitcnt lgkmcnt(7)\n"
    "ds_read_b64_tr_b16 a[44:45], v227 offset:30720\n"
    "v_accvgpr_read_b32 v250, a153\n"
    "v_cvt_pk_bf16_f32 v156, v249, v250\n"
    "v_mfma_f32_16x16x32_bf16 a[176:179], a[16:19], a[0:3], a[176:179]\n"
    "ds_read_b64_tr_b16 a[46:47], v227 offset:30976\n"
    "v_cvt_pk_bf16_f32 v32, v32, v33\n"
    "v_mfma_f32_16x16x32_bf16 a[184:187], a[16:19], a[4:7], a[184:187]\n"
    "ds_read_b64_tr_b16 a[48:49], v226 offset:34816\n"
    "v_accvgpr_read_b32 v249, a154\n"
    "v_mfma_f32_16x16x32_bf16 a[180:183], a[20:23], a[0:3], a[180:183]\n"
    "ds_read_b64_tr_b16 a[50:51], v226 offset:35072\n"
    "ds_read_b64_tr_b16 a[52:53], v227 offset:34816\n"
    "v_accvgpr_read_b32 v250, a155\n"
    "v_mfma_f32_16x16x32_bf16 a[188:191], a[20:23], a[4:7], a[188:191]\n"
    "s_waitcnt lgkmcnt(8)\n"
    "ds_read_b64_tr_b16 a[54:55], v227 offset:35072\n"
    "v_cvt_pk_bf16_f32 v33, v34, v35\n"
    "v_cvt_pk_bf16_f32 v157, v249, v250\n"
    "v_mfma_f32_16x16x32_bf16 a[192:195], a[0:3], a[24:27], a[192:195]\n"
    "ds_read_b64_tr_b16 a[56:57], v226 offset:38912\n"
    "v_accvgpr_read_b32 v249, a156\n"
    "v_mfma_f32_16x16x32_bf16 a[196:199], a[4:7], a[24:27], a[196:199]\n"
    "ds_read_b64_tr_b16 a[58:59], v226 offset:39168\n"
    "v_cvt_pk_bf16_f32 v34, v36, v37\n"
    "v_mfma_f32_16x16x32_bf16 a[200:203], a[0:3], a[28:31], a[200:203]\n"
    "ds_read_b64_tr_b16 a[60:61], v227 offset:38912\n"
    "v_accvgpr_read_b32 v250, a157\n"
    "v_cvt_pk_bf16_f32 v158, v249, v250\n"
    "v_mfma_f32_16x16x32_bf16 a[204:207], a[4:7], a[28:31], a[204:207]\n"
    "s_waitcnt lgkmcnt(6)\n"
    "ds_read_b64_tr_b16 a[62:63], v227 offset:39168\n"
    "v_cvt_pk_bf16_f32 v35, v38, v39\n"
    "s_add_u32 s25, s26, 8192\n"
    "v_mfma_f32_16x16x32_bf16 a[160:163], a[40:43], a[32:35], a[160:163]\n"
    "buffer_store_dwordx4 v[32:35], v248, s[72:75], s25 offen offset:0 sc1\n"
    "v_accvgpr_read_b32 v249, a158\n"
    "v_mfma_f32_16x16x32_bf16 a[168:171], a[40:43], a[36:39], a[168:171]\n"
    "v_accvgpr_read_b32 v250, a159\n"
    "v_cvt_pk_bf16_f32 v159, v249, v250\n"
    "v_mfma_f32_16x16x32_bf16 a[164:167], a[44:47], a[32:35], a[164:167]\n"
    "v_cvt_pk_bf16_f32 v164, v96, v97\n"
    "v_mfma_f32_16x16x32_bf16 a[172:175], a[44:47], a[36:39], a[172:175]\n"
    "s_waitcnt vmcnt(11)\n"
    "s_barrier\n"
    "ds_read_b128 a[0:3], v244 offset:0\n"
    "ds_read_b128 a[4:7], v244 offset:2048\n"
    "v_cvt_pk_bf16_f32 v40, v40, v41\n"
    "s_waitcnt lgkmcnt(5)\n"
    "v_mfma_f32_16x16x32_bf16 a[176:179], a[48:51], a[32:35], a[176:179]\n"
    "ds_read_b64_tr_b16 a[8:9], v246 offset:0\n"
    "ds_read_b64_tr_b16 a[10:11], v246 offset:256\n"
    "v_cvt_pk_bf16_f32 v165, v98, v99\n"
    "v_mfma_f32_16x16x32_bf16 a[184:187], a[48:51], a[36:39], a[184:187]\n"
    "ds_read_b64_tr_b16 a[12:13], v247 offset:0\n"
    "v_cvt_pk_bf16_f32 v166, v100, v101\n"
    "v_mfma_f32_16x16x32_bf16 a[180:183], a[52:55], a[32:35], a[180:183]\n"
    "ds_read_b64_tr_b16 a[14:15], v247 offset:256\n"
    "ds_read_b64_tr_b16 a[16:17], v246 offset:4096\n"
    "v_cvt_pk_bf16_f32 v41, v42, v43\n"
    "v_mfma_f32_16x16x32_bf16 a[188:191], a[52:55], a[36:39], a[188:191]\n"
    "s_waitcnt lgkmcnt(6)\n"
    "ds_read_b64_tr_b16 a[18:19], v246 offset:4352\n"
    "v_cvt_pk_bf16_f32 v167, v102, v103\n"
    "v_mfma_f32_16x16x32_bf16 a[192:195], a[32:35], a[56:59], a[192:195]\n"
    "ds_read_b64_tr_b16 a[20:21], v247 offset:4096\n"
    "ds_read_b64_tr_b16 a[22:23], v247 offset:4352\n"
    "v_cvt_pk_bf16_f32 v42, v44, v45\n"
    "v_mfma_f32_16x16x32_bf16 a[196:199], a[36:39], a[56:59], a[196:199]\n"
    "ds_read_b64_tr_b16 a[24:25], v246 offset:8192\n"
    "v_cvt_pk_bf16_f32 v172, v104, v105\n"
    "v_cvt_pk_bf16_f32 v173, v106, v107\n"
    "v_mfma_f32_16x16x32_bf16 a[200:203], a[32:35], a[60:63], a[200:203]\n"
    "ds_read_b64_tr_b16 a[26:27], v246 offset:8448\n"
    "ds_read_b64_tr_b16 a[28:29], v247 offset:8192\n"
    "v_mfma_f32_16x16x32_bf16 a[204:207], a[36:39], a[60:63], a[204:207]\n"
    "s_waitcnt lgkmcnt(6)\n"
    "ds_read_b64_tr_b16 a[30:31], v247 offset:8448\n"
    "v_cvt_pk_bf16_f32 v43, v46, v47\n"
    "s_add_u32 s25, s26, 8192\n"
    "v_cvt_pk_bf16_f32 v174, v108, v109\n"
    "v_mfma_f32_16x16x32_bf16 a[160:163], a[8:11], a[0:3], a[160:163]\n"
    "ds_read_b128 a[32:35], v245 offset:0\n"
    "ds_read_b128 a[36:39], v245 offset:2048\n"
    "v_mfma_f32_16x16x32_bf16 a[168:171], a[8:11], a[4:7], a[168:171]\n"
    "ds_read_b64_tr_b16 a[40:41], v246 offset:2048\n"
    "buffer_store_dwordx4 v[40:43], v248, s[72:75], s25 offen offset:1024 sc1\n"
    "v_mfma_f32_16x16x32_bf16 a[164:167], a[12:15], a[0:3], a[164:167]\n"
    "ds_read_b64_tr_b16 a[42:43], v246 offset:2304\n"
    "v_cvt_pk_bf16_f32 v175, v110, v111\n"
    "v_cvt_pk_bf16_f32 v180, v112, v113\n"
    "v_mfma_f32_16x16x32_bf16 a[172:175], a[12:15], a[4:7], a[172:175]\n"
    "s_waitcnt lgkmcnt(7)\n"
    "ds_read_b64_tr_b16 a[44:45], v247 offset:2048\n"
    "v_cvt_pk_bf16_f32 v181, v114, v115\n"
    "v_mfma_f32_16x16x32_bf16 a[176:179], a[16:19], a[0:3], a[176:179]\n"
    "ds_read_b64_tr_b16 a[46:47], v247 offset:2304\n"
    "v_cvt_pk_bf16_f32 v182, v116, v117\n"
    "v_mfma_f32_16x16x32_bf16 a[184:187], a[16:19], a[4:7], a[184:187]\n"
    "ds_read_b64_tr_b16 a[48:49], v246 offset:6144\n"
    "v_cvt_pk_bf16_f32 v48, v48, v49\n"
    "v_cvt_pk_bf16_f32 v183, v118, v119\n"
    "v_mfma_f32_16x16x32_bf16 a[180:183], a[20:23], a[0:3], a[180:183]\n"
    "ds_read_b64_tr_b16 a[50:51], v246 offset:6400\n"
    "ds_read_b64_tr_b16 a[52:53], v247 offset:6144\n"
    "v_mfma_f32_16x16x32_bf16 a[188:191], a[20:23], a[4:7], a[188:191]\n"
    "s_waitcnt lgkmcnt(8)\n"
    "ds_read_b64_tr_b16 a[54:55], v247 offset:6400\n"
    "v_cvt_pk_bf16_f32 v49, v50, v51\n"
    "v_cvt_pk_bf16_f32 v188, v120, v121\n"
    "v_mfma_f32_16x16x32_bf16 a[192:195], a[0:3], a[24:27], a[192:195]\n"
    "ds_read_b64_tr_b16 a[56:57], v246 offset:10240\n"
    "v_cvt_pk_bf16_f32 v189, v122, v123\n"
    "v_mfma_f32_16x16x32_bf16 a[196:199], a[4:7], a[24:27], a[196:199]\n"
    "ds_read_b64_tr_b16 a[58:59], v246 offset:10496\n"
    "v_cvt_pk_bf16_f32 v50, v52, v53\n"
    "v_cvt_pk_bf16_f32 v190, v124, v125\n"
    "v_mfma_f32_16x16x32_bf16 a[200:203], a[0:3], a[28:31], a[200:203]\n"
    "ds_read_b64_tr_b16 a[60:61], v247 offset:10240\n"
    "v_cvt_pk_bf16_f32 v191, v126, v127\n"
    "v_mfma_f32_16x16x32_bf16 a[204:207], a[4:7], a[28:31], a[204:207]\n"
    "s_waitcnt lgkmcnt(6)\n"
    "ds_read_b64_tr_b16 a[62:63], v247 offset:10496\n"
    "v_cvt_pk_bf16_f32 v51, v54, v55\n"
    "s_add_u32 s25, s26, 12288\n"
    "v_mfma_f32_16x16x32_bf16 a[160:163], a[40:43], a[32:35], a[160:163]\n"
    "ds_read_b128 a[0:3], v244 offset:28672\n"
    "ds_read_b128 a[4:7], v244 offset:30720\n"
    "s_mov_b32 s60, s44\n"
    "s_and_b32 s61, s45, 0xffff\n"
    "s_mov_b32 s62, 0x200000\n"
    "s_mov_b32 s63, 0x20000\n"
    "s_lshl_b32 s15, s12, 8\n"
    "s_add_u32 s64, s48, s15\n"
    "s_addc_u32 s65, s49, 0\n"
    "s_and_b32 s65, s65, 0xffff\n"
    "s_mov_b32 s66, 0x400000\n"
    "s_mov_b32 s67, 0x20000\n"
    "s_sub_u32 s66, s66, s15\n"
    "v_lshrrev_b32 v249, 4, v255\n"
    "v_mfma_f32_16x16x32_bf16 a[168:171], a[40:43], a[36:39], a[168:171]\n"
    "ds_read_b64_tr_b16 a[8:9], v246 offset:28672\n"
    "v_mul_u32_u24 v232, 8192, v249\n"
    "v_or_b32 v249, 0, v249\n"
    "v_mfma_f32_16x16x32_bf16 a[164:167], a[44:47], a[32:35], a[164:167]\n"
    "ds_read_b64_tr_b16 a[10:11], v246 offset:28928\n"
    "buffer_store_dwordx4 v[48:51], v248, s[72:75], s25 offen offset:0 sc1\n"
    "v_mfma_f32_16x16x32_bf16 a[172:175], a[44:47], a[36:39], a[172:175]\n"
    "s_waitcnt lgkmcnt(7)\n"
    "ds_read_b64_tr_b16 a[12:13], v247 offset:28672\n"
    "v_lshlrev_b32 v249, 1, v249\n"
    "v_and_b32 v250, 15, v255\n"
    "v_mfma_f32_16x16x32_bf16 a[176:179], a[48:51], a[32:35], a[176:179]\n"
    "ds_read_b64_tr_b16 a[14:15], v247 offset:28928\n"
    "v_xor_b32 v249, v249, v250\n"
    "v_lshl_add_u32 v232, v249, 4, v232\n"
    "v_mfma_f32_16x16x32_bf16 a[184:187], a[48:51], a[36:39], a[184:187]\n"
    "ds_read_b64_tr_b16 a[16:17], v246 offset:32768\n"
    "v_cvt_pk_bf16_f32 v56, v56, v57\n"
    "v_xor_b32 v234, 128, v232\n"
    "v_mfma_f32_16x16x32_bf16 a[180:183], a[52:55], a[32:35], a[180:183]\n"
    "ds_read_b64_tr_b16 a[18:19], v246 offset:33024\n"
    "ds_read_b64_tr_b16 a[20:21], v247 offset:32768\n"
    "v_add_u32 v233, 32768, v232\n"
    "v_mfma_f32_16x16x32_bf16 a[188:191], a[52:55], a[36:39], a[188:191]\n"
    "s_waitcnt lgkmcnt(8)\n"
    "ds_read_b64_tr_b16 a[22:23], v247 offset:33024\n"
    "v_add_u32 v234, 65536, v234\n"
    "v_add_u32 v235, 32768, v234\n"
    "s_lshl_b32 s15, s8, 14\n"
    "s_sub_u32 s14, 114688, s15\n"
    "s_lshl_b32 s20, s13, 17\n"
    "s_sub_u32 s30, 1966080, s20\n"
    "s_add_u32 s21, s20, s14\n"
    "s_add_u32 s31, s30, s14\n"
    "s_add_u32 s20, s20, s15\n"
    "s_add_u32 s30, s30, s15\n"
    "s_lshl_b32 s22, s8, 17\n"
    "s_lshl_b32 s23, s8, 13\n"
    "s_add_u32 s23, s23, 16384\n"
    "s_lshl_b32 s24, s8, 12\n"
    "v_mfma_f32_16x16x32_bf16 a[192:195], a[32:35], a[56:59], a[192:195]\n"
    "ds_read_b64_tr_b16 a[24:25], v246 offset:36864\n"
    "v_cvt_pk_bf16_f32 v57, v58, v59\n"
    "v_mfma_f32_16x16x32_bf16 a[196:199], a[36:39], a[56:59], a[196:199]\n"
    "ds_read_b64_tr_b16 a[26:27], v246 offset:37120\n"
    "v_mfma_f32_16x16x32_bf16 a[200:203], a[32:35], a[60:63], a[200:203]\n"
    "ds_read_b64_tr_b16 a[28:29], v247 offset:36864\n"
    "v_cvt_pk_bf16_f32 v58, v60, v61\n"
    "v_mfma_f32_16x16x32_bf16 a[204:207], a[36:39], a[60:63], a[204:207]\n"
    "s_waitcnt lgkmcnt(6)\n"
    "ds_read_b64_tr_b16 a[30:31], v247 offset:37120\n"
    "v_mfma_f32_16x16x32_bf16 a[160:163], a[8:11], a[0:3], a[160:163]\n"
    "ds_read_b128 a[32:35], v245 offset:28672\n"
    "ds_read_b128 a[36:39], v245 offset:30720\n"
    "v_cvt_pk_bf16_f32 v59, v62, v63\n"
    "s_add_u32 s25, s26, 12288\n"
    "v_mfma_f32_16x16x32_bf16 a[168:171], a[8:11], a[4:7], a[168:171]\n"
    "ds_read_b64_tr_b16 a[40:41], v246 offset:30720\n"
    "v_mfma_f32_16x16x32_bf16 a[164:167], a[12:15], a[0:3], a[164:167]\n"
    "ds_read_b64_tr_b16 a[42:43], v246 offset:30976\n"
    "buffer_store_dwordx4 v[56:59], v248, s[72:75], s25 offen offset:1024 sc1\n"
    "v_mfma_f32_16x16x32_bf16 a[172:175], a[12:15], a[4:7], a[172:175]\n"
    "s_waitcnt lgkmcnt(7)\n"
    "ds_read_b64_tr_b16 a[44:45], v247 offset:30720\n"
    "v_mfma_f32_16x16x32_bf16 a[176:179], a[16:19], a[0:3], a[176:179]\n"
    "ds_read_b64_tr_b16 a[46:47], v247 offset:30976\n"
    "v_mfma_f32_16x16x32_bf16 a[184:187], a[16:19], a[4:7], a[184:187]\n"
    "ds_read_b64_tr_b16 a[48:49], v246 offset:34816\n"
    "v_mfma_f32_16x16x32_bf16 a[180:183], a[20:23], a[0:3], a[180:183]\n"
    "ds_read_b64_tr_b16 a[50:51], v246 offset:35072\n"
    "ds_read_b64_tr_b16 a[52:53], v247 offset:34816\n"
    "v_cvt_pk_bf16_f32 v64, v64, v65\n"
    "v_mfma_f32_16x16x32_bf16 a[188:191], a[20:23], a[4:7], a[188:191]\n"
    "s_waitcnt lgkmcnt(8)\n"
    "ds_read_b64_tr_b16 a[54:55], v247 offset:35072\n"
    "v_mfma_f32_16x16x32_bf16 a[192:195], a[0:3], a[24:27], a[192:195]\n"
    "ds_read_b64_tr_b16 a[56:57], v246 offset:38912\n"
    "v_cvt_pk_bf16_f32 v65, v66, v67\n"
    "v_mfma_f32_16x16x32_bf16 a[196:199], a[4:7], a[24:27], a[196:199]\n"
    "ds_read_b64_tr_b16 a[58:59], v246 offset:39168\n"
    "v_mfma_f32_16x16x32_bf16 a[200:203], a[0:3], a[28:31], a[200:203]\n"
    "ds_read_b64_tr_b16 a[60:61], v247 offset:38912\n"
    "v_cvt_pk_bf16_f32 v66, v68, v69\n"
    "v_mfma_f32_16x16x32_bf16 a[204:207], a[4:7], a[28:31], a[204:207]\n"
    "s_waitcnt lgkmcnt(6)\n"
    "ds_read_b64_tr_b16 a[62:63], v247 offset:39168\n"
    "v_mfma_f32_16x16x32_bf16 a[160:163], a[40:43], a[32:35], a[160:163]\n"
    "v_cvt_pk_bf16_f32 v67, v70, v71\n"
    "s_add_u32 s25, s26, 16384\n"
    "v_mfma_f32_16x16x32_bf16 a[168:171], a[40:43], a[36:39], a[168:171]\n"
    "buffer_store_dwordx4 v[64:67], v248, s[72:75], s25 offen offset:0 sc1\n"
    "v_mfma_f32_16x16x32_bf16 a[164:167], a[44:47], a[32:35], a[164:167]\n"
    "v_mfma_f32_16x16x32_bf16 a[172:175], a[44:47], a[36:39], a[172:175]\n"
    "s_waitcnt vmcnt(7)\n"
    "s_barrier\n"
    "ds_read_b128 a[0:3], v228 offset:0\n"
    "ds_read_b128 a[4:7], v228 offset:2048\n"
    "s_waitcnt lgkmcnt(5)\n"
    "v_mfma_f32_16x16x32_bf16 a[176:179], a[48:51], a[32:35], a[176:179]\n"
    "ds_read_b64_tr_b16 a[8:9], v230 offset:0\n"
    "ds_read_b64_tr_b16 a[10:11], v230 offset:256\n"
    "v_mfma_f32_16x16x32_bf16 a[184:187], a[48:51], a[36:39], a[184:187]\n"
    "ds_read_b64_tr_b16 a[12:13], v231 offset:0\n"
    "v_cvt_pk_bf16_f32 v72, v72, v73\n"
    "v_mfma_f32_16x16x32_bf16 a[180:183], a[52:55], a[32:35], a[180:183]\n"
    "ds_read_b64_tr_b16 a[14:15], v231 offset:256\n"
    "ds_read_b64_tr_b16 a[16:17], v230 offset:4096\n"
    "v_mfma_f32_16x16x32_bf16 a[188:191], a[52:55], a[36:39], a[188:191]\n"
    "s_waitcnt lgkmcnt(6)\n"
    "ds_read_b64_tr_b16 a[18:19], v230 offset:4352\n"
    "v_cvt_pk_bf16_f32 v73, v74, v75\n"
    "v_mfma_f32_16x16x32_bf16 a[192:195], a[32:35], a[56:59], a[192:195]\n"
    "ds_read_b64_tr_b16 a[20:21], v231 offset:4096\n"
    "ds_read_b64_tr_b16 a[22:23], v231 offset:4352\n"
    "v_mfma_f32_16x16x32_bf16 a[196:199], a[36:39], a[56:59], a[196:199]\n"
    "ds_read_b64_tr_b16 a[24:25], v230 offset:8192\n"
    "v_mfma_f32_16x16x32_bf16 a[200:203], a[32:35], a[60:63], a[200:203]\n"
    "ds_read_b64_tr_b16 a[26:27], v230 offset:8448\n"
    "ds_read_b64_tr_b16 a[28:29], v231 offset:8192\n"
    "v_cvt_pk_bf16_f32 v74, v76, v77\n"
    "v_mfma_f32_16x16x32_bf16 a[204:207], a[36:39], a[60:63], a[204:207]\n"
    "s_waitcnt lgkmcnt(6)\n"
    "ds_read_b64_tr_b16 a[30:31], v231 offset:8448\n"
    "v_mfma_f32_16x16x32_bf16 a[160:163], a[8:11], a[0:3], a[160:163]\n"
    "ds_read_b128 a[32:35], v229 offset:0\n"
    "ds_read_b128 a[36:39], v229 offset:2048\n"
    "v_mfma_f32_16x16x32_bf16 a[168:171], a[8:11], a[4:7], a[168:171]\n"
    "ds_read_b64_tr_b16 a[40:41], v230 offset:2048\n"
    "v_cvt_pk_bf16_f32 v75, v78, v79\n"
    "s_add_u32 s25, s26, 16384\n"
    "v_mfma_f32_16x16x32_bf16 a[164:167], a[12:15], a[0:3], a[164:167]\n"
    "ds_read_b64_tr_b16 a[42:43], v230 offset:2304\n"
    "v_mfma_f32_16x16x32_bf16 a[172:175], a[12:15], a[4:7], a[172:175]\n"
    "s_waitcnt lgkmcnt(7)\n"
    "ds_read_b64_tr_b16 a[44:45], v231 offset:2048\n"
    "buffer_store_dwordx4 v[72:75], v248, s[72:75], s25 offen offset:1024 sc1\n"
    "v_mfma_f32_16x16x32_bf16 a[176:179], a[16:19], a[0:3], a[176:179]\n"
    "ds_read_b64_tr_b16 a[46:47], v231 offset:2304\n"
    "v_mfma_f32_16x16x32_bf16 a[184:187], a[16:19], a[4:7], a[184:187]\n"
    "ds_read_b64_tr_b16 a[48:49], v230 offset:6144\n"
    "v_mfma_f32_16x16x32_bf16 a[180:183], a[20:23], a[0:3], a[180:183]\n"
    "ds_read_b64_tr_b16 a[50:51], v230 offset:6400\n"
    "ds_read_b64_tr_b16 a[52:53], v231 offset:6144\n"
    "v_mfma_f32_16x16x32_bf16 a[188:191], a[20:23], a[4:7], a[188:191]\n"
    "s_waitcnt lgkmcnt(8)\n"
    "ds_read_b64_tr_b16 a[54:55], v231 offset:6400\n"
    "v_cvt_pk_bf16_f32 v80, v80, v81\n"
    "v_mfma_f32_16x16x32_bf16 a[192:195], a[0:3], a[24:27], a[192:195]\n"
    "ds_read_b64_tr_b16 a[56:57], v230 offset:10240\n"
    "v_mfma_f32_16x16x32_bf16 a[196:199], a[4:7], a[24:27], a[196:199]\n"
    "ds_read_b64_tr_b16 a[58:59], v230 offset:10496\n"
    "v_cvt_pk_bf16_f32 v81, v82, v83\n"
    "v_mfma_f32_16x16x32_bf16 a[200:203], a[0:3], a[28:31], a[200:203]\n"
    "ds_read_b64_tr_b16 a[60:61], v231 offset:10240\n"
    "v_mfma_f32_16x16x32_bf16 a[204:207], a[4:7], a[28:31], a[204:207]\n"
    "s_waitcnt lgkmcnt(6)\n"
    "ds_read_b64_tr_b16 a[62:63], v231 offset:10496\n"
    "v_cvt_pk_bf16_f32 v82, v84, v85\n"
    "v_mfma_f32_16x16x32_bf16 a[160:163], a[40:43], a[32:35], a[160:163]\n"
    "v_cvt_pk_bf16_f32 v83, v86, v87\n"
    "s_add_u32 s25, s26, 20480\n"
    "v_mfma_f32_16x16x32_bf16 a[168:171], a[40:43], a[36:39], a[168:171]\n"
    "buffer_store_dwordx4 v[80:83], v248, s[72:75], s25 offen offset:0 sc1\n"
    "v_mfma_f32_16x16x32_bf16 a[164:167], a[44:47], a[32:35], a[164:167]\n"
    "v_mfma_f32_16x16x32_bf16 a[172:175], a[44:47], a[36:39], a[172:175]\n"
    "s_waitcnt lgkmcnt(3)\n"
    "v_mfma_f32_16x16x32_bf16 a[176:179], a[48:51], a[32:35], a[176:179]\n"
    "v_cvt_pk_bf16_f32 v88, v88, v89\n"
    "v_mfma_f32_16x16x32_bf16 a[184:187], a[48:51], a[36:39], a[184:187]\n"
    "v_cvt_pk_bf16_f32 v89, v90, v91\n"
    "v_mfma_f32_16x16x32_bf16 a[180:183], a[52:55], a[32:35], a[180:183]\n"
    "v_cvt_pk_bf16_f32 v90, v92, v93\n"
    "v_mfma_f32_16x16x32_bf16 a[188:191], a[52:55], a[36:39], a[188:191]\n"
    "s_waitcnt lgkmcnt(0)\n"
    "v_mfma_f32_16x16x32_bf16 a[192:195], a[32:35], a[56:59], a[192:195]\n"
    "v_cvt_pk_bf16_f32 v91, v94, v95\n"
    "s_add_u32 s25, s26, 20480\n"
    "v_mfma_f32_16x16x32_bf16 a[196:199], a[36:39], a[56:59], a[196:199]\n"
    "buffer_store_dwordx4 v[88:91], v248, s[72:75], s25 offen offset:1024 sc1\n"
    "v_mfma_f32_16x16x32_bf16 a[200:203], a[32:35], a[60:63], a[200:203]\n"
    "v_mfma_f32_16x16x32_bf16 a[204:207], a[36:39], a[60:63], a[204:207]\n"
    "L_a1e_%=:\n"
    "v_lshlrev_b32 v120, 3, v255\n"
    "s_add_u32 s25, s27, 0\n"
    "buffer_store_dwordx4 v[128:131], v248, s[72:75], s25 offen offset:0 sc1\n"
    "s_add_u32 s14, s19, 159744\n"
    "v_add_u32 v120, s14, v120\n"
    "s_waitcnt vmcnt(55)\n"
    "ds_read_b64_tr_b16 v[24:25], v120\n"
    "ds_read_b64_tr_b16 v[30:31], v120 offset:512\n"
    "v_accvgpr_read_b32 v36, a160\n"
    "v_accvgpr_read_b32 v37, a161\n"
    "v_accvgpr_read_b32 v38, a162\n"
    "v_accvgpr_read_b32 v39, a163\n"
    "v_accvgpr_read_b32 v40, a164\n"
    "v_accvgpr_read_b32 v41, a165\n"
    "v_accvgpr_read_b32 v42, a166\n"
    "v_accvgpr_read_b32 v43, a167\n"
    "v_accvgpr_read_b32 v44, a168\n"
    "v_accvgpr_read_b32 v45, a169\n"
    "v_accvgpr_read_b32 v46, a170\n"
    "v_accvgpr_read_b32 v47, a171\n"
    "v_accvgpr_read_b32 v48, a172\n"
    "v_accvgpr_read_b32 v49, a173\n"
    "v_accvgpr_read_b32 v50, a174\n"
    "v_accvgpr_read_b32 v51, a175\n"
    "v_accvgpr_read_b32 v52, a176\n"
    "v_accvgpr_read_b32 v53, a177\n"
    "v_accvgpr_read_b32 v54, a178\n"
    "v_accvgpr_read_b32 v55, a179\n"
    "v_accvgpr_read_b32 v56, a180\n"
    "v_accvgpr_read_b32 v57, a181\n"
    "v_accvgpr_read_b32 v58, a182\n"
    "v_accvgpr_read_b32 v59, a183\n"
    "v_accvgpr_read_b32 v60, a184\n"
    "v_accvgpr_read_b32 v61, a185\n"
    "v_accvgpr_read_b32 v62, a186\n"
    "v_accvgpr_read_b32 v63, a187\n"
    "v_accvgpr_read_b32 v64, a188\n"
    "v_accvgpr_read_b32 v65, a189\n"
    "v_accvgpr_read_b32 v66, a190\n"
    "v_accvgpr_read_b32 v67, a191\n"
    "v_accvgpr_read_b32 v68, a192\n"
    "v_accvgpr_read_b32 v69, a193\n"
    "v_accvgpr_read_b32 v70, a194\n"
    "v_accvgpr_read_b32 v71, a195\n"
    "v_accvgpr_read_b32 v72, a196\n"
    "v_accvgpr_read_b32 v73, a197\n"
    "v_accvgpr_read_b32 v74, a198\n"
    "v_accvgpr_read_b32 v75, a199\n"
    "v_accvgpr_read_b32 v76, a200\n"
    "v_accvgpr_read_b32 v77, a201\n"
    "v_accvgpr_read_b32 v78, a202\n"
    "v_accvgpr_read_b32 v79, a203\n"
    "v_accvgpr_read_b32 v80, a204\n"
    "s_add_u32 s25, s27, 0\n"
    "buffer_store_dwordx4 v[136:139], v248, s[72:75], s25 offen offset:1024 sc1\n"
    "v_accvgpr_read_b32 v81, a205\n"
    "v_accvgpr_read_b32 v82, a206\n"
    "v_accvgpr_read_b32 v83, a207\n"
    "v_mov_b32 v26, 0\n"
    "v_mov_b32 v27, 0\n"
    "v_mov_b32 v28, 0\n"
    "v_mov_b32 v29, 0\n"
    "v_mov_b32 v32, 0x3f803f80\n"
    "v_mov_b32 v33, 0x3f803f80\n"
    "v_mov_b32 v34, 0x3f803f80\n"
    "v_mov_b32 v35, 0x3f803f80\n"
    "v_cvt_pk_bf16_f32 v0, v36, v37\n"
    "v_cvt_pk_bf16_f32 v1, v38, v39\n"
    "v_cvt_pk_bf16_f32 v2, v40, v41\n"
    "v_cvt_pk_bf16_f32 v3, v42, v43\n"
    "v_cvt_pk_bf16_f32 v4, v44, v45\n"
    "v_cvt_pk_bf16_f32 v5, v46, v47\n"
    "v_cvt_pk_bf16_f32 v6, v48, v49\n"
    "v_cvt_pk_bf16_f32 v7, v50, v51\n"
    "v_cvt_pk_bf16_f32 v8, v52, v53\n"
    "v_cvt_pk_bf16_f32 v9, v54, v55\n"
    "v_cvt_pk_bf16_f32 v10, v56, v57\n"
    "v_cvt_pk_bf16_f32 v11, v58, v59\n"
    "v_cvt_pk_bf16_f32 v12, v60, v61\n"
    "v_cvt_pk_bf16_f32 v13, v62, v63\n"
    "v_cvt_pk_bf16_f32 v14, v64, v65\n"
    "v_cvt_pk_bf16_f32 v15, v66, v67\n"
    "v_cvt_pk_bf16_f32 v16, v68, v69\n"
    "v_cvt_pk_bf16_f32 v17, v70, v71\n"
    "v_cvt_pk_bf16_f32 v18, v72, v73\n"
    "v_cvt_pk_bf16_f32 v19, v74, v75\n"
    "v_cvt_pk_bf16_f32 v20, v76, v77\n"
    "v_cvt_pk_bf16_f32 v21, v78, v79\n"
    "v_cvt_pk_bf16_f32 v22, v80, v81\n"
    "v_cvt_pk_bf16_f32 v23, v82, v83\n"
    "s_waitcnt lgkmcnt(0)\n"
    "s_barrier\n"
    "v_mfma_f32_16x16x32_bf16 v[36:39], v[24:27], v[0:3], 0\n"
    "s_add_u32 m0, s24, 0\n"
    "s_nop 0\n"
    "buffer_load_dwordx4 v232, s[64:67], s22 offen lds\n"
    "v_mfma_f32_16x16x32_bf16 v[40:43], v[24:27], v[4:7], 0\n"
    "v_mfma_f32_16x16x32_bf16 v[44:47], v[28:31], v[0:3], 0\n"
    "v_mfma_f32_16x16x32_bf16 v[48:51], v[28:31], v[4:7], 0\n"
    "v_mfma_f32_16x16x32_bf16 v[52:55], v[24:27], v[8:11], 0\n"
    "v_mfma_f32_16x16x32_bf16 v[56:59], v[24:27], v[12:15], 0\n"
    "v_mfma_f32_16x16x32_bf16 v[60:63], v[28:31], v[8:11], 0\n"
    "v_mfma_f32_16x16x32_bf16 v[64:67], v[28:31], v[12:15], 0\n"
    "v_lshrrev_b32 v120, 4, v255\n"
    "v_or_b32 v120, 0, v120\n"
    "s_add_u32 s25, s27, 0\n"
    "buffer_store_dwordx4 v[144:147], v248, s[72:75], s25 offen offset:2048 sc1\n"
    "v_bfe_u32 v249, v255, 1, 3\n"
    "s_add_u32 m0, s24, 1024\n"
    "s_nop 0\n"
    "buffer_load_dwordx4 v233, s[64:67], s22 offen lds\n"
    "v_xor_b32 v120, v120, v249\n"
    "v_and_b32 v249, 15, v255\n"
    "v_lshlrev_b32 v249, 7, v249\n"
    "v_lshl_add_u32 v249, v120, 4, v249\n"
    "v_add_u32 v249, s23, v249\n"
    "v_xor_b32 v250, 64, v249\n"
    "v_add_u32 v253, 98304, v249\n"
    "v_add_u32 v254, 98304, v250\n"
    "v_bfe_u32 v120, v255, 2, 3\n"
    "v_xor_b32 v120, 0, v120\n"
    "v_lshrrev_b32 v224, 4, v255\n"
    "s_add_u32 m0, s24, 2048\n"
    "s_nop 0\n"
    "buffer_load_dwordx4 v234, s[64:67], s22 offen lds\n"
    "v_lshlrev_b32 v224, 3, v224\n"
    "v_bfe_u32 v121, v255, 2, 2\n"
    "v_add_u32 v224, v224, v121\n"
    "v_lshlrev_b32 v224, 8, v224\n"
    "v_lshl_add_u32 v224, v120, 5, v224\n"
    "v_and_b32 v120, 3, v255\n"
    "v_lshl_add_u32 v224, v120, 3, v224\n"
    "v_xor_b32 v225, 32, v224\n"
    "v_xor_b32 v226, 64, v224\n"
    "v_xor_b32 v227, 96, v224\n"
    "v_xor_b32 v228, 128, v224\n"
    "s_add_u32 m0, s24, 3072\n"
    "s_nop 0\n"
    "buffer_load_dwordx4 v235, s[64:67], s22 offen lds\n"
    "v_xor_b32 v229, 160, v224\n"
    "v_xor_b32 v230, 192, v224\n"
    "v_xor_b32 v231, 224, v224\n"
    "v_add_u32 v240, 98304, v224\n"
    "v_add_u32 v241, 98304, v225\n"
    "v_add_u32 v242, 98304, v226\n"
    "v_add_u32 v243, 98304, v227\n"
    "v_add_u32 v244, 98304, v228\n"
    "v_add_u32 v245, 98304, v229\n"
    "v_add_u32 v246, 98304, v230\n"
    "s_add_u32 m0, s23, 0\n"
    "s_nop 0\n"
    "buffer_load_dwordx4 v236, s[60:63], s20 offen lds\n"
    "v_add_u32 v247, 98304, v231\n"
    "s_mov_b32 s16, 0x3fb8aa3b\n"
    "s_mov_b32 s17, 0x3fb8aa3b\n"
    "v_max3_f32 v100, v36, v37, v38\n"
    "v_min3_f32 v108, v36, v37, v38\n"
    "s_add_u32 s25, s27, 0\n"
    "buffer_store_dwordx4 v[152:155], v248, s[72:75], s25 offen offset:3072 sc1\n"
    "v_max3_f32 v101, v40, v41, v42\n"
    "v_min3_f32 v109, v40, v41, v42\n"
    "v_max3_f32 v102, v44, v45, v46\n"
    "v_min3_f32 v110, v44, v45, v46\n"
    "v_max3_f32 v103, v48, v49, v50\n"
    "v_min3_f32 v111, v48, v49, v50\n"
    "s_add_u32 m0, s23, 1024\n"
    "s_nop 0\n"
    "buffer_load_dwordx4 v237, s[60:63], s20 offen lds\n"
    "v_max3_f32 v104, v52, v53, v54\n"
    "v_min3_f32 v112, v52, v53, v54\n"
    "v_max3_f32 v105, v56, v57, v58\n"
    "v_min3_f32 v113, v56, v57, v58\n"
    "v_max3_f32 v106, v60, v61, v62\n"
    "v_min3_f32 v114, v60, v61, v62\n"
    "v_max3_f32 v107, v64, v65, v66\n"
    "v_min3_f32 v115, v64, v65, v66\n"
    "v_max_f32 v100, v100, v39\n"
    "v_max_f32 v108, -v108, -v39\n"
    "v_max_f32 v101, v101, v43\n"
    "s_add_u32 m0, s23, 2048\n"
    "s_nop 0\n"
    "buffer_load_dwordx4 v236, s[60:63], s21 offen lds\n"
    "v_max_f32 v109, -v109, -v43\n"
    "v_max_f32 v102, v102, v47\n"
    "v_max_f32 v110, -v110, -v47\n"
    "v_max_f32 v103, v103, v51\n"
    "v_max_f32 v111, -v111, -v51\n"
    "v_max_f32 v104, v104, v55\n"
    "v_max_f32 v112, -v112, -v55\n"
    "v_max_f32 v105, v105, v59\n"
    "v_max_f32 v113, -v113, -v59\n"
    "v_max_f32 v106, v106, v63\n"
    "s_add_u32 m0, s23, 3072\n"
    "s_nop 0\n"
    "buffer_load_dwordx4 v237, s[60:63], s21 offen lds\n"
    "v_max_f32 v114, -v114, -v63\n"
    "v_max_f32 v107, v107, v67\n"
    "v_max_f32 v115, -v115, -v67\n"
    "v_permlane32_swap_b32 v100, v108\n"
    "v_permlane32_swap_b32 v101, v109\n"
    "v_permlane32_swap_b32 v102, v110\n"
    "v_permlane32_swap_b32 v103, v111\n"
    "v_permlane32_swap_b32 v104, v112\n"
    "v_permlane32_swap_b32 v105, v113\n"
    "v_permlane32_swap_b32 v106, v114\n"
    "v_permlane32_swap_b32 v107, v115\n"
    "s_add_u32 m0, s23, 4096\n"
    "s_nop 0\n"
    "buffer_load_dwordx4 v236, s[60:63], s30 offen lds\n"
    "v_max_f32 v100, v100, v108\n"
    "s_add_u32 s25, s27, 8192\n"
    "buffer_store_dwordx4 v[160:163], v248, s[72:75], s25 offen offset:0 sc1\n"
    "v_max_f32 v101, v101, v109\n"
    "v_max_f32 v102, v102, v110\n"
    "v_max_f32 v103, v103, v111\n"
    "v_max_f32 v104, v104, v112\n"
    "v_max_f32 v105, v105, v113\n"
    "v_max_f32 v106, v106, v114\n"
    "v_max_f32 v107, v107, v115\n"
    "v_mov_b32 v108, v100\n"
    "v_mov_b32 v109, v101\n"
    "v_mov_b32 v110, v102\n"
    "s_add_u32 m0, s23, 5120\n"
    "s_nop 0\n"
    "buffer_load_dwordx4 v237, s[60:63], s30 offen lds\n"
    "v_mov_b32 v111, v103\n"
    "v_mov_b32 v112, v104\n"
    "v_mov_b32 v113, v105\n"
    "v_mov_b32 v114, v106\n"
    "v_mov_b32 v115, v107\n"
    "v_permlane16_swap_b32 v100, v108\n"
    "v_permlane16_swap_b32 v101, v109\n"
    "v_permlane16_swap_b32 v102, v110\n"
    "v_permlane16_swap_b32 v103, v111\n"
    "v_permlane16_swap_b32 v104, v112\n"
    "v_permlane16_swap_b32 v105, v113\n"
    "s_add_u32 m0, s23, 6144\n"
    "s_nop 0\n"
    "buffer_load_dwordx4 v236, s[60:63], s31 offen lds\n"
    "v_permlane16_swap_b32 v106, v114\n"
    "v_permlane16_swap_b32 v107, v115\n"
    "v_max_f32 v100, v100, v108\n"
    "v_max_f32 v101, v101, v109\n"
    "v_max_f32 v102, v102, v110\n"
    "v_max_f32 v103, v103, v111\n"
    "v_max_f32 v104, v104, v112\n"
    "v_max_f32 v105, v105, v113\n"
    "v_max_f32 v106, v106, v114\n"
    "v_max_f32 v107, v107, v115\n"
    "s_add_u32 m0, s23, 7168\n"
    "s_nop 0\n"
    "buffer_load_dwordx4 v237, s[60:63], s31 offen lds\n"
    "v_mov_b32 v108, v100\n"
    "v_mov_b32 v109, v101\n"
    "v_mov_b32 v110, v102\n"
    "v_mov_b32 v111, v103\n"
    "v_mov_b32 v112, v104\n"
    "v_mov_b32 v113, v105\n"
    "v_mov_b32 v114, v106\n"
    "v_mov_b32 v115, v107\n"
    "v_permlane32_swap_b32 v100, v108\n"
    "v_permlane32_swap_b32 v101, v109\n"
    "s_add_u32 s25, s27, 8192\n"
    "buffer_store_dwordx4 v[168:171], v248, s[72:75], s25 offen offset:1024 sc1\n"
    "v_permlane32_swap_b32 v102, v110\n"
    "s_add_u32 s20, s20, 128\n"
    "v_permlane32_swap_b32 v103, v111\n"
    "v_permlane32_swap_b32 v104, v112\n"
    "v_permlane32_swap_b32 v105, v113\n"
    "v_permlane32_swap_b32 v106, v114\n"
    "v_permlane32_swap_b32 v107, v115\n"
    "v_pk_mul_f32 v[100:101], v[100:101], s[16:17]\n"
    "v_pk_mul_f32 v[102:103], v[102:103], s[16:17]\n"
    "v_pk_mul_f32 v[104:105], v[104:105], s[16:17]\n"
    "v_pk_mul_f32 v[106:107], v[106:107], s[16:17]\n"
    "v_pk_mul_f32 v[108:109], v[108:109], s[16:17]\n"
    "v_pk_mul_f32 v[110:111], v[110:111], s[16:17]\n"
    "s_add_u32 s21, s21, 128\n"
    "v_pk_mul_f32 v[112:113], v[112:113], s[16:17]\n"
    "v_pk_mul_f32 v[114:115], v[114:115], s[16:17]\n"
    "v_pk_fma_f32 v[68:69], v[36:37], s[16:17], v[108:109] op_sel:[0,0,0] op_sel_hi:[1,1,0] neg_lo:[1,0,1] neg_hi:[1,0,1]\n"
    "v_pk_fma_f32 v[36:37], v[36:37], s[16:17], v[100:101] op_sel:[0,0,0] op_sel_hi:[1,1,0] neg_lo:[0,0,1] neg_hi:[0,0,1]\n"
    "v_pk_fma_f32 v[70:71], v[38:39], s[16:17], v[108:109] op_sel:[0,0,0] op_sel_hi:[1,1,0] neg_lo:[1,0,1] neg_hi:[1,0,1]\n"
    "v_pk_fma_f32 v[38:39], v[38:39], s[16:17], v[100:101] op_sel:[0,0,0] op_sel_hi:[1,1,0] neg_lo:[0,0,1] neg_hi:[0,0,1]\n"
    "v_pk_fma_f32 v[72:73], v[40:41], s[16:17], v[108:109] op_sel:[0,0,1] op_sel_hi:[1,1,1] neg_lo:[1,0,1] neg_hi:[1,0,1]\n"
    "v_pk_fma_f32 v[40:41], v[40:41], s[16:17], v[100:101] op_sel:[0,0,1] op_sel_hi:[1,1,1] neg_lo:[0,0,1] neg_hi:[0,0,1]\n"
    "v_pk_fma_f32 v[74:75], v[42:43], s[16:17], v[108:109] op_sel:[0,0,1] op_sel_hi:[1,1,1] neg_lo:[1,0,1] neg_hi:[1,0,1]\n"
    "v_pk_fma_f32 v[42:43], v[42:43], s[16:17], v[100:101] op_sel:[0,0,1] op_sel_hi:[1,1,1] neg_lo:[0,0,1] neg_hi:[0,0,1]\n"
    "s_add_u32 s30, s30, 128\n"
    "v_pk_fma_f32 v[76:77], v[44:45], s[16:17], v[110:111] op_sel:[0,0,0] op_sel_hi:[1,1,0] neg_lo:[1,0,1] neg_hi:[1,0,1]\n"
    "v_pk_fma_f32 v[44:45], v[44:45], s[16:17], v[102:103] op_sel:[0,0,0] op_sel_hi:[1,1,0] neg_lo:[0,0,1] neg_hi:[0,0,1]\n"
    "v_pk_fma_f32 v[78:79], v[46:47], s[16:17], v[110:111] op_sel:[0,0,0] op_sel_hi:[1,1,0] neg_lo:[1,0,1] neg_hi:[1,0,1]\n"
    "v_pk_fma_f32 v[46:47], v[46:47], s[16:17], v[102:103] op_sel:[0,0,0] op_sel_hi:[1,1,0] neg_lo:[0,0,1] neg_hi:[0,0,1]\n"
    "v_pk_fma_f32 v[80:81], v[48:49], s[16:17], v[110:111] op_sel:[0,0,1] op_sel_hi:[1,1,1] neg_lo:[1,0,1] neg_hi:[1,0,1]\n"
    "v_pk_fma_f32 v[48:49], v[48:49], s[16:17], v[102:103] op_sel:[0,0,1] op_sel_hi:[1,1,1] neg_lo:[0,0,1] neg_hi:[0,0,1]\n"
    "v_pk_fma_f32 v[82:83], v[50:51], s[16:17], v[110:111] op_sel:[0,0,1] op_sel_hi:[1,1,1] neg_lo:[1,0,1] neg_hi:[1,0,1]\n"
    "v_pk_fma_f32 v[50:51], v[50:51], s[16:17], v[102:103] op_sel:[0,0,1] op_sel_hi:[1,1,1] neg_lo:[0,0,1] neg_hi:[0,0,1]\n"
    "v_pk_fma_f32 v[84:85], v[52:53], s[16:17], v[112:113] op_sel:[0,0,0] op_sel_hi:[1,1,0] neg_lo:[1,0,1] neg_hi:[1,0,1]\n"
    "v_pk_fma_f32 v[52:53], v[52:53], s[16:17], v[104:105] op_sel:[0,0,0] op_sel_hi:[1,1,0] neg_lo:[0,0,1] neg_hi:[0,0,1]\n"
    "v_pk_fma_f32 v[86:87], v[54:55], s[16:17], v[112:113] op_sel:[0,0,0] op_sel_hi:[1,1,0] neg_lo:[1,0,1] neg_hi:[1,0,1]\n"
    "s_add_u32 s31, s31, 128\n"
    "v_pk_fma_f32 v[54:55], v[54:55], s[16:17], v[104:105] op_sel:[0,0,0] op_sel_hi:[1,1,0] neg_lo:[0,0,1] neg_hi:[0,0,1]\n"
    "v_pk_fma_f32 v[88:89], v[56:57], s[16:17], v[112:113] op_sel:[0,0,1] op_sel_hi:[1,1,1] neg_lo:[1,0,1] neg_hi:[1,0,1]\n"
    "v_pk_fma_f32 v[56:57], v[56:57], s[16:17], v[104:105] op_sel:[0,0,1] op_sel_hi:[1,1,1] neg_lo:[0,0,1] neg_hi:[0,0,1]\n"
    "v_pk_fma_f32 v[90:91], v[58:59], s[16:17], v[112:113] op_sel:[0,0,1] op_sel_hi:[1,1,1] neg_lo:[1,0,1] neg_hi:[1,0,1]\n"
    "v_pk_fma_f32 v[58:59], v[58:59], s[16:17], v[104:105] op_sel:[0,0,1] op_sel_hi:[1,1,1] neg_lo:[0,0,1] neg_hi:[0,0,1]\n"
    "v_pk_fma_f32 v[92:93], v[60:61], s[16:17], v[114:115] op_sel:[0,0,0] op_sel_hi:[1,1,0] neg_lo:[1,0,1] neg_hi:[1,0,1]\n"
    "v_pk_fma_f32 v[60:61], v[60:61], s[16:17], v[106:107] op_sel:[0,0,0] op_sel_hi:[1,1,0] neg_lo:[0,0,1] neg_hi:[0,0,1]\n"
    "v_pk_fma_f32 v[94:95], v[62:63], s[16:17], v[114:115] op_sel:[0,0,0] op_sel_hi:[1,1,0] neg_lo:[1,0,1] neg_hi:[1,0,1]\n"
    "v_pk_fma_f32 v[62:63], v[62:63], s[16:17], v[106:107] op_sel:[0,0,0] op_sel_hi:[1,1,0] neg_lo:[0,0,1] neg_hi:[0,0,1]\n"
    "v_pk_fma_f32 v[96:97], v[64:65], s[16:17], v[114:115] op_sel:[0,0,1] op_sel_hi:[1,1,1] neg_lo:[1,0,1] neg_hi:[1,0,1]\n"
    "v_pk_fma_f32 v[64:65], v[64:65], s[16:17], v[106:107] op_sel:[0,0,1] op_sel_hi:[1,1,1] neg_lo:[0,0,1] neg_hi:[0,0,1]\n"
    "s_add_u32 s22, s22, 524288\n"
    "v_pk_fma_f32 v[98:99], v[66:67], s[16:17], v[114:115] op_sel:[0,0,1] op_sel_hi:[1,1,1] neg_lo:[1,0,1] neg_hi:[1,0,1]\n"
    "s_add_u32 s25, s27, 8192\n"
    "buffer_store_dwordx4 v[176:179], v248, s[72:75], s25 offen offset:2048 sc1\n"
    "v_pk_fma_f32 v[66:67], v[66:67], s[16:17], v[106:107] op_sel:[0,0,1] op_sel_hi:[1,1,1] neg_lo:[0,0,1] neg_hi:[0,0,1]\n"
    "v_exp_f32 v36, v36\n"
    "v_exp_f32 v68, v68\n"
    "v_exp_f32 v37, v37\n"
    "v_exp_f32 v69, v69\n"
    "v_exp_f32 v38, v38\n"
    "v_exp_f32 v70, v70\n"
    "v_exp_f32 v39, v39\n"
    "v_exp_f32 v71, v71\n"
    "v_exp_f32 v40, v40\n"
    "s_add_u32 m0, s24, 98304\n"
    "s_nop 0\n"
    "buffer_load_dwordx4 v232, s[64:67], s22 offen lds\n"
    "v_exp_f32 v72, v72\n"
    "v_exp_f32 v41, v41\n"
    "v_exp_f32 v73, v73\n"
    "v_exp_f32 v42, v42\n"
    "v_exp_f32 v74, v74\n"
    "v_exp_f32 v43, v43\n"
    "v_exp_f32 v75, v75\n"
    "v_exp_f32 v44, v44\n"
    "v_exp_f32 v76, v76\n"
    "v_exp_f32 v45, v45\n"
    "v_exp_f32 v77, v77\n"
    "v_exp_f32 v46, v46\n"
    "v_exp_f32 v78, v78\n"
    "v_exp_f32 v47, v47\n"
    "v_exp_f32 v79, v79\n"
    "v_exp_f32 v48, v48\n"
    "v_exp_f32 v80, v80\n"
    "v_exp_f32 v49, v49\n"
    "v_exp_f32 v81, v81\n"
    "v_exp_f32 v50, v50\n"
    "s_add_u32 m0, s24, 99328\n"
    "s_nop 0\n"
    "buffer_load_dwordx4 v233, s[64:67], s22 offen lds\n"
    "v_exp_f32 v82, v82\n"
    "v_exp_f32 v51, v51\n"
    "v_exp_f32 v83, v83\n"
    "v_exp_f32 v52, v52\n"
    "v_exp_f32 v84, v84\n"
    "v_exp_f32 v53, v53\n"
    "v_exp_f32 v85, v85\n"
    "v_exp_f32 v54, v54\n"
    "v_exp_f32 v86, v86\n"
    "v_exp_f32 v55, v55\n"
    "v_exp_f32 v87, v87\n"
    "v_exp_f32 v56, v56\n"
    "v_exp_f32 v88, v88\n"
    "v_exp_f32 v57, v57\n"
    "v_exp_f32 v89, v89\n"
    "s_add_u32 s25, s27, 8192\n"
    "buffer_store_dwordx4 v[184:187], v248, s[72:75], s25 offen offset:3072 sc1\n"
    "v_exp_f32 v58, v58\n"
    "v_exp_f32 v90, v90\n"
    "v_exp_f32 v59, v59\n"
    "v_exp_f32 v91, v91\n"
    "v_exp_f32 v60, v60\n"
    "v_exp_f32 v92, v92\n"
    "s_add_u32 m0, s24, 100352\n"
    "s_nop 0\n"
    "buffer_load_dwordx4 v234, s[64:67], s22 offen lds\n"
    "v_exp_f32 v61, v61\n"
    "v_exp_f32 v93, v93\n"
    "v_exp_f32 v62, v62\n"
    "v_exp_f32 v94, v94\n"
    "v_exp_f32 v63, v63\n"
    "v_exp_f32 v95, v95\n"
    "v_exp_f32 v64, v64\n"
    "v_exp_f32 v96, v96\n"
    "v_exp_f32 v65, v65\n"
    "v_exp_f32 v97, v97\n"
    "v_exp_f32 v66, v66\n"
    "v_exp_f32 v98, v98\n"
    "v_exp_f32 v67, v67\n"
    "v_exp_f32 v99, v99\n"
    "v_pk_add_f32 v[116:117], v[36:37], v[38:39]\n"
    "v_pk_add_f32 v[118:119], v[68:69], v[70:71]\n"
    "v_add_f32 v100, v116, v117\n"
    "v_add_f32 v108, v118, v119\n"
    "v_pk_add_f32 v[116:117], v[40:41], v[42:43]\n"
    "v_pk_add_f32 v[118:119], v[72:73], v[74:75]\n"
    "v_add_f32 v101, v116, v117\n"
    "s_add_u32 m0, s24, 101376\n"
    "s_nop 0\n"
    "buffer_load_dwordx4 v235, s[64:67], s22 offen lds\n"
    "v_add_f32 v109, v118, v119\n"
    "v_pk_add_f32 v[116:117], v[44:45], v[46:47]\n"
    "v_pk_add_f32 v[118:119], v[76:77], v[78:79]\n"
    "v_add_f32 v102, v116, v117\n"
    "v_add_f32 v110, v118, v119\n"
    "v_pk_add_f32 v[116:117], v[48:49], v[50:51]\n"
    "v_pk_add_f32 v[118:119], v[80:81], v[82:83]\n"
    "v_add_f32 v103, v116, v117\n"
    "v_add_f32 v111, v118, v119\n"
    "v_pk_add_f32 v[116:117], v[52:53], v[54:55]\n"
    "v_pk_add_f32 v[118:119], v[84:85], v[86:87]\n"
    "v_add_f32 v104, v116, v117\n"
    "v_add_f32 v112, v118, v119\n"
    "v_pk_add_f32 v[116:117], v[56:57], v[58:59]\n"
    "v_pk_add_f32 v[118:119], v[88:89], v[90:91]\n"
    "v_add_f32 v105, v116, v117\n"
    "v_add_f32 v113, v118, v119\n"
    "s_add_u32 s25, s27, 24576\n"
    "buffer_store_dwordx4 v[132:135], v248, s[72:75], s25 offen offset:0 sc1\n"
    "v_pk_add_f32 v[116:117], v[60:61], v[62:63]\n"
    "v_pk_add_f32 v[118:119], v[92:93], v[94:95]\n"
    "v_add_f32 v106, v116, v117\n"
    "s_add_u32 m0, s23, 98304\n"
    "s_nop 0\n"
    "buffer_load_dwordx4 v236, s[60:63], s20 offen lds\n"
    "v_add_f32 v114, v118, v119\n"
    "v_pk_add_f32 v[116:117], v[64:65], v[66:67]\n"
    "v_pk_add_f32 v[118:119], v[96:97], v[98:99]\n"
    "v_add_f32 v107, v116, v117\n"
    "v_add_f32 v115, v118, v119\n"
    "v_permlane32_swap_b32 v100, v108\n"
    "v_permlane32_swap_b32 v101, v109\n"
    "v_permlane32_swap_b32 v102, v110\n"
    "v_permlane32_swap_b32 v103, v111\n"
    "v_permlane32_swap_b32 v104, v112\n"
    "v_permlane32_swap_b32 v105, v113\n"
    "v_permlane32_swap_b32 v106, v114\n"
    "v_permlane32_swap_b32 v107, v115\n"
    "v_add_f32 v100, v100, v108\n"
    "v_add_f32 v101, v101, v109\n"
    "v_add_f32 v102, v102, v110\n"
    "v_add_f32 v103, v103, v111\n"
    "v_add_f32 v104, v104, v112\n"
    "v_add_f32 v105, v105, v113\n"
    "v_add_f32 v106, v106, v114\n"
    "v_add_f32 v107, v107, v115\n"
    "s_add_u32 m0, s23, 99328\n"
    "s_nop 0\n"
    "buffer_load_dwordx4 v237, s[60:63], s20 offen lds\n"
    "v_mov_b32 v108, v100\n"
    "v_mov_b32 v109, v101\n"
    "v_mov_b32 v110, v102\n"
    "v_mov_b32 v111, v103\n"
    "v_mov_b32 v112, v104\n"
    "v_mov_b32 v113, v105\n"
    "v_mov_b32 v114, v106\n"
    "v_mov_b32 v115, v107\n"
    "v_permlane16_swap_b32 v100, v108\n"
    "v_permlane16_swap_b32 v101, v109\n"
    "v_permlane16_swap_b32 v102, v110\n"
    "v_permlane16_swap_b32 v103, v111\n"
    "v_permlane16_swap_b32 v104, v112\n"
    "v_permlane16_swap_b32 v105, v113\n"
    "v_permlane16_swap_b32 v106, v114\n"
    "v_permlane16_swap_b32 v107, v115\n"
    "v_add_f32 v100, v100, v108\n"
    "v_add_f32 v101, v101, v109\n"
    "v_add_f32 v102, v102, v110\n"
    "v_add_f32 v103, v103, v111\n"
    "s_add_u32 s25, s27, 24576\n"
    "buffer_store_dwordx4 v[140:143], v248, s[72:75], s25 offen offset:1024 sc1\n"
    "v_add_f32 v104, v104, v112\n"
    "s_add_u32 m0, s23, 100352\n"
    "s_nop 0\n"
    "buffer_load_dwordx4 v236, s[60:63], s21 offen lds\n"
    "v_add_f32 v105, v105, v113\n"
    "v_add_f32 v106, v106, v114\n"
    "v_add_f32 v107, v107, v115\n"
    "v_mov_b32 v108, v100\n"
    "v_mov_b32 v109, v101\n"
    "v_mov_b32 v110, v102\n"
    "v_mov_b32 v111, v103\n"
    "v_mov_b32 v112, v104\n"
    "v_mov_b32 v113, v105\n"
    "v_mov_b32 v114, v106\n"
    "v_mov_b32 v115, v107\n"
    "v_permlane32_swap_b32 v100, v108\n"
    "v_permlane32_swap_b32 v101, v109\n"
    "v_permlane32_swap_b32 v102, v110\n"
    "v_permlane32_swap_b32 v103, v111\n"
    "v_permlane32_swap_b32 v104, v112\n"
    "v_permlane32_swap_b32 v105, v113\n"
    "v_permlane32_swap_b32 v106, v114\n"
    "v_permlane32_swap_b32 v107, v115\n"
    "v_rcp_f32 v100, v100\n"
    "s_add_u32 m0, s23, 101376\n"
    "s_nop 0\n"
    "buffer_load_dwordx4 v237, s[60:63], s21 offen lds\n"
    "v_rcp_f32 v108, v108\n"
    "v_rcp_f32 v101, v101\n"
    "v_rcp_f32 v109, v109\n"
    "v_rcp_f32 v102, v102\n"
    "v_rcp_f32 v110, v110\n"
    "v_rcp_f32 v103, v103\n"
    "v_rcp_f32 v111, v111\n"
    "v_rcp_f32 v104, v104\n"
    "v_rcp_f32 v112, v112\n"
    "v_rcp_f32 v105, v105\n"
    "v_rcp_f32 v113, v113\n"
    "v_rcp_f32 v106, v106\n"
    "v_rcp_f32 v114, v114\n"
    "v_rcp_f32 v107, v107\n"
    "v_rcp_f32 v115, v115\n"
    "v_pk_mul_f32 v[52:53], v[52:53], v[104:105] op_sel:[0,0] op_sel_hi:[1,0]\n"
    "v_pk_mul_f32 v[84:85], v[84:85], v[112:113] op_sel:[0,0] op_sel_hi:[1,0]\n"
    "v_pk_mul_f32 v[54:55], v[54:55], v[104:105] op_sel:[0,0] op_sel_hi:[1,0]\n"
    "v_pk_mul_f32 v[86:87], v[86:87], v[112:113] op_sel:[0,0] op_sel_hi:[1,0]\n"
    "v_cvt_pk_bf16_f32 v52, v52, v53\n"
    "v_cvt_pk_bf16_f32 v53, v54, v55\n"
    "s_add_u32 m0, s23, 102400\n"
    "s_nop 0\n"
    "s_add_u32 s25, s27, 24576\n"
    "buffer_store_dwordx4 v[148:151], v248, s[72:75], s25 offen offset:2048 sc1\n"
    "buffer_load_dwordx4 v236, s[60:63], s30 offen lds\n"
    "v_cvt_pk_bf16_f32 v54, v84, v85\n"
    "v_cvt_pk_bf16_f32 v55, v86, v87\n"
    "v_pk_mul_f32 v[56:57], v[56:57], v[104:105] op_sel:[0,1] op_sel_hi:[1,1]\n"
    "v_pk_mul_f32 v[88:89], v[88:89], v[112:113] op_sel:[0,1] op_sel_hi:[1,1]\n"
    "v_pk_mul_f32 v[58:59], v[58:59], v[104:105] op_sel:[0,1] op_sel_hi:[1,1]\n"
    "v_pk_mul_f32 v[90:91], v[90:91], v[112:113] op_sel:[0,1] op_sel_hi:[1,1]\n"
    "v_cvt_pk_bf16_f32 v56, v56, v57\n"
    "v_cvt_pk_bf16_f32 v57, v58, v59\n"
    "v_cvt_pk_bf16_f32 v58, v88, v89\n"
    "v_cvt_pk_bf16_f32 v59, v90, v91\n"
    "v_pk_mul_f32 v[60:61], v[60:61], v[106:107] op_sel:[0,0] op_sel_hi:[1,0]\n"
    "v_pk_mul_f32 v[92:93], v[92:93], v[114:115] op_sel:[0,0] op_sel_hi:[1,0]\n"
    "v_pk_mul_f32 v[62:63], v[62:63], v[106:107] op_sel:[0,0] op_sel_hi:[1,0]\n"
    "v_pk_mul_f32 v[94:95], v[94:95], v[114:115] op_sel:[0,0] op_sel_hi:[1,0]\n"
    "v_cvt_pk_bf16_f32 v60, v60, v61\n"
    "v_cvt_pk_bf16_f32 v61, v62, v63\n"
    "v_cvt_pk_bf16_f32 v62, v92, v93\n"
    "v_cvt_pk_bf16_f32 v63, v94, v95\n"
    "v_pk_mul_f32 v[64:65], v[64:65], v[106:107] op_sel:[0,1] op_sel_hi:[1,1]\n"
    "v_pk_mul_f32 v[96:97], v[96:97], v[114:115] op_sel:[0,1] op_sel_hi:[1,1]\n"
    "v_pk_mul_f32 v[66:67], v[66:67], v[106:107] op_sel:[0,1] op_sel_hi:[1,1]\n"
    "s_add_u32 m0, s23, 103424\n"
    "s_nop 0\n"
    "buffer_load_dwordx4 v237, s[60:63], s30 offen lds\n"
    "v_pk_mul_f32 v[98:99], v[98:99], v[114:115] op_sel:[0,1] op_sel_hi:[1,1]\n"
    "v_cvt_pk_bf16_f32 v64, v64, v65\n"
    "v_cvt_pk_bf16_f32 v65, v66, v67\n"
    "v_cvt_pk_bf16_f32 v66, v96, v97\n"
    "v_cvt_pk_bf16_f32 v67, v98, v99\n"
    "v_pk_mul_f32 v[36:37], v[36:37], v[100:101] op_sel:[0,0] op_sel_hi:[1,0]\n"
    "v_pk_mul_f32 v[68:69], v[68:69], v[108:109] op_sel:[0,0] op_sel_hi:[1,0]\n"
    "v_pk_mul_f32 v[38:39], v[38:39], v[100:101] op_sel:[0,0] op_sel_hi:[1,0]\n"
    "v_pk_mul_f32 v[70:71], v[70:71], v[108:109] op_sel:[0,0] op_sel_hi:[1,0]\n"
    "v_cvt_pk_bf16_f32 v36, v36, v37\n"
    "v_cvt_pk_bf16_f32 v37, v38, v39\n"
    "v_cvt_pk_bf16_f32 v38, v68, v69\n"
    "v_cvt_pk_bf16_f32 v39, v70, v71\n"
    "v_pk_mul_f32 v[40:41], v[40:41], v[100:101] op_sel:[0,1] op_sel_hi:[1,1]\n"
    "v_pk_mul_f32 v[72:73], v[72:73], v[108:109] op_sel:[0,1] op_sel_hi:[1,1]\n"
    "v_pk_mul_f32 v[42:43], v[42:43], v[100:101] op_sel:[0,1] op_sel_hi:[1,1]\n"
    "v_pk_mul_f32 v[74:75], v[74:75], v[108:109] op_sel:[0,1] op_sel_hi:[1,1]\n"
    "v_cvt_pk_bf16_f32 v40, v40, v41\n"
    "v_cvt_pk_bf16_f32 v41, v42, v43\n"
    "v_cvt_pk_bf16_f32 v42, v72, v73\n"
    "v_cvt_pk_bf16_f32 v43, v74, v75\n"
    "s_add_u32 m0, s23, 104448\n"
    "s_nop 0\n"
    "buffer_load_dwordx4 v236, s[60:63], s31 offen lds\n"
    "v_pk_mul_f32 v[44:45], v[44:45], v[102:103] op_sel:[0,0] op_sel_hi:[1,0]\n"
    "v_pk_mul_f32 v[76:77], v[76:77], v[110:111] op_sel:[0,0] op_sel_hi:[1,0]\n"
    "s_add_u32 s25, s27, 24576\n"
    "buffer_store_dwordx4 v[156:159], v248, s[72:75], s25 offen offset:3072 sc1\n"
    "v_pk_mul_f32 v[46:47], v[46:47], v[102:103] op_sel:[0,0] op_sel_hi:[1,0]\n"
    "v_pk_mul_f32 v[78:79], v[78:79], v[110:111] op_sel:[0,0] op_sel_hi:[1,0]\n"
    "v_cvt_pk_bf16_f32 v44, v44, v45\n"
    "v_cvt_pk_bf16_f32 v45, v46, v47\n"
    "v_cvt_pk_bf16_f32 v46, v76, v77\n"
    "v_cvt_pk_bf16_f32 v47, v78, v79\n"
    "v_pk_mul_f32 v[48:49], v[48:49], v[102:103] op_sel:[0,1] op_sel_hi:[1,1]\n"
    "v_pk_mul_f32 v[80:81], v[80:81], v[110:111] op_sel:[0,1] op_sel_hi:[1,1]\n"
    "v_pk_mul_f32 v[50:51], v[50:51], v[102:103] op_sel:[0,1] op_sel_hi:[1,1]\n"
    "v_pk_mul_f32 v[82:83], v[82:83], v[110:111] op_sel:[0,1] op_sel_hi:[1,1]\n"
    "v_cvt_pk_bf16_f32 v48, v48, v49\n"
    "v_cvt_pk_bf16_f32 v49, v50, v51\n"
    "v_cvt_pk_bf16_f32 v50, v80, v81\n"
    "v_cvt_pk_bf16_f32 v51, v82, v83\n"
    "s_mul_i32 s15, s8, 12288\n"
    "s_add_u32 s15, s15, 49152\n"
    "v_and_b32 v120, 15, v255\n"
    "v_mul_u32_u24 v120, 72, v120\n"
    "s_add_u32 m0, s23, 105472\n"
    "s_nop 0\n"
    "buffer_load_dwordx4 v237, s[60:63], s31 offen lds\n"
    "v_lshrrev_b32 v121, 4, v255\n"
    "v_lshl_add_u32 v120, v121, 3, v120\n"
    "v_add_u32 v120, s15, v120\n"
    "v_lshrrev_b32 v121, 2, v255\n"
    "v_mul_u32_u24 v121, 72, v121\n"
    "v_and_b32 v122, 3, v255\n"
    "v_lshl_add_u32 v121, v122, 3, v121\n"
    "v_add_u32 v121, s15, v121\n"
    "ds_write_b64 v120, v[52:53] offset:0\n"
    "ds_write_b64 v120, v[54:55] offset:32\n"
    "ds_write_b64 v120, v[56:57] offset:1152\n"
    "ds_write_b64 v120, v[58:59] offset:1184\n"
    "ds_write_b64 v120, v[60:61] offset:2304\n"
    "ds_write_b64 v120, v[62:63] offset:2336\n"
    "ds_write_b64 v120, v[64:65] offset:3456\n"
    "ds_write_b64 v120, v[66:67] offset:3488\n"
    "s_lshl_b32 s14, s8, 12\n"
    "s_add_u32 s14, s14, 147456\n"
    "v_add_u32 v122, s14, v252\n"
    "v_add_u32 v120, s15, v252\n"
    "s_waitcnt lgkmcnt(0)\n"
    "s_add_u32 s20, s20, 128\n"
    "ds_read_b64_tr_b16 v[68:69], v121 offset:0\n"
    "ds_read_b64_tr_b16 v[70:71], v121 offset:1152\n"
    "ds_read_b64_tr_b16 v[72:73], v121 offset:32\n"
    "ds_read_b64_tr_b16 v[74:75], v121 offset:1184\n"
    "ds_read_b64_tr_b16 v[76:77], v121 offset:2304\n"
    "ds_read_b64_tr_b16 v[78:79], v121 offset:3456\n"
    "ds_read_b64_tr_b16 v[80:81], v121 offset:2336\n"
    "s_add_u32 s25, s26, 24576\n"
    "buffer_store_dwordx4 v[164:167], v248, s[72:75], s25 offen offset:0 sc1\n"
    "ds_read_b64_tr_b16 v[82:83], v121 offset:3488\n"
    "ds_write_b128 v122, v[36:39] offset:0\n"
    "ds_write_b128 v122, v[40:43] offset:1024\n"
    "ds_write_b128 v122, v[44:47] offset:2048\n"
    "ds_write_b128 v122, v[48:51] offset:3072\n"
    "s_waitcnt lgkmcnt(4)\n"
    "v_mfma_f32_16x16x32_bf16 v[84:87], v[68:71], v[16:19], 0\n"
    "v_mfma_f32_16x16x32_bf16 v[100:103], v[68:71], v[32:35], 0\n"
    "v_mfma_f32_16x16x32_bf16 v[88:91], v[72:75], v[16:19], 0\n"
    "v_mfma_f32_16x16x32_bf16 v[104:107], v[72:75], v[32:35], 0\n"
    "v_mfma_f32_16x16x32_bf16 v[92:95], v[76:79], v[20:23], 0\n"
    "v_mfma_f32_16x16x32_bf16 v[108:111], v[76:79], v[32:35], 0\n"
    "v_mfma_f32_16x16x32_bf16 v[96:99], v[80:83], v[20:23], 0\n"
    "v_mfma_f32_16x16x32_bf16 v[112:115], v[80:83], v[32:35], 0\n"
    "s_add_u32 s21, s21, 128\n"
    "s_mul_i32 s14, s34, 73728\n"
    "s_mul_i32 s15, s33, 4608\n"
    "s_add_u32 s15, s15, s14\n"
    "s_add_u32 s76, s58, s15\n"
    "s_addc_u32 s77, s59, 0\n"
    "s_add_u32 s76, s76, 0x3200000\n"
    "s_addc_u32 s77, s77, 0\n"
    "s_and_b32 s77, s77, 0xffff\n"
    "s_mov_b32 s78, 0x1200\n"
    "s_mov_b32 s79, 0x20000\n"
    "ds_write_b128 v120, v[84:87] offset:0\n"
    "ds_write_b128 v120, v[88:91] offset:1024\n"
    "ds_write_b128 v120, v[92:95] offset:2048\n"
    "ds_write_b128 v120, v[96:99] offset:3072\n"
    "ds_write_b128 v120, v[100:103] offset:4096\n"
    "ds_write_b128 v120, v[104:107] offset:5120\n"
    "ds_write_b128 v120, v[108:111] offset:6144\n"
    "ds_write_b128 v120, v[112:115] offset:7168\n"
    "s_lshl_b32 s15, s8, 10\n"
    "v_add_u32 v120, s15, v252\n"
    "s_add_u32 s30, s30, 128\n"
    "v_add_u32 v122, 49152, v120\n"
    "v_lshrrev_b32 v121, 4, v255\n"
    "v_lshlrev_b32 v121, 4, v121\n"
    "s_lshl_b32 s15, s8, 6\n"
    "v_add_u32 v121, s15, v121\n"
    "v_add_u32 v121, 4096, v121\n"
    "s_waitcnt vmcnt(20) lgkmcnt(0)\n"
    "s_barrier\n"
    "ds_read_b128 v[0:3], v122 offset:0\n"
    "ds_read_b128 v[16:19], v122 offset:4096\n"
    "ds_read_b128 v[4:7], v122 offset:12288\n"
    "ds_read_b128 v[20:23], v122 offset:16384\n"
    "ds_read_b128 v[8:11], v122 offset:24576\n"
    "ds_read_b128 v[24:27], v122 offset:28672\n"
    "s_add_u32 s25, s26, 24576\n"
    "buffer_store_dwordx4 v[172:175], v248, s[72:75], s25 offen offset:1024 sc1\n"
    "ds_read_b128 v[12:15], v122 offset:36864\n"
    "ds_read_b128 v[28:31], v122 offset:40960\n"
    "ds_read_b128 a[128:131], v249 offset:0\n"
    "ds_read_b128 a[132:135], v249 offset:2048\n"
    "ds_read_b64_tr_b16 a[144:145], v224 offset:0\n"
    "ds_read_b64_tr_b16 a[146:147], v224 offset:1024\n"
    "ds_read_b64_tr_b16 a[148:149], v225 offset:0\n"
    "s_add_u32 s31, s31, 128\n"
    "ds_read_b64_tr_b16 a[150:151], v225 offset:1024\n"
    "s_waitcnt lgkmcnt(6)\n"
    "ds_read_b64_tr_b16 a[152:153], v226 offset:0\n"
    "ds_read_b64_tr_b16 a[154:155], v226 offset:1024\n"
    "ds_read_b64_tr_b16 a[156:157], v227 offset:0\n"
    "ds_read_b64_tr_b16 a[158:159], v227 offset:1024\n"
    "ds_read_b64_tr_b16 a[160:161], v228 offset:0\n"
    "ds_read_b64_tr_b16 a[162:163], v228 offset:1024\n"
    "ds_read_b64_tr_b16 a[164:165], v229 offset:0\n"
    "ds_read_b64_tr_b16 a[166:167], v229 offset:1024\n"
    "ds_read_b64_tr_b16 a[168:169], v230 offset:0\n"
    "ds_read_b64_tr_b16 a[170:171], v230 offset:1024\n"
    "ds_read_b64_tr_b16 a[172:173], v231 offset:0\n"
    "ds_read_b64_tr_b16 a[174:175], v231 offset:1024\n"
    "ds_read_b128 a[136:139], v249 offset:4096\n"
    "ds_read_b128 a[140:143], v249 offset:6144\n"
    "v_add_f32 v0, v0, v4\n"
    "v_add_f32 v8, v8, v12\n"
    "v_add_f32 v1, v1, v5\n"
    "v_add_f32 v9, v9, v13\n"
    "v_add_f32 v2, v2, v6\n"
    "s_add_u32 s22, s22, 524288\n"
    "v_add_f32 v10, v10, v14\n"
    "v_add_f32 v3, v3, v7\n"
    "v_add_f32 v11, v11, v15\n"
    "v_add_f32 v0, v0, v8\n"
    "v_add_f32 v1, v1, v9\n"
    "v_add_f32 v2, v2, v10\n"
    "v_add_f32 v3, v3, v11\n"
    "v_add_f32 v16, v16, v20\n"
    "v_add_f32 v24, v24, v28\n"
    "v_add_f32 v17, v17, v21\n"
    "v_add_f32 v25, v25, v29\n"
    "v_add_f32 v18, v18, v22\n"
    "v_add_f32 v26, v26, v30\n"
    "v_add_f32 v19, v19, v23\n"
    "v_add_f32 v27, v27, v31\n"
    "v_add_f32 v16, v16, v24\n"
    "v_add_f32 v17, v17, v25\n"
    "v_add_f32 v18, v18, v26\n"
    "v_add_f32 v19, v19, v27\n"
    "buffer_store_dwordx4 v[0:3], v120, s[76:79], 0 offen sc1\n"
    "s_mov_b32 exec_lo, 0x00010001\n"
    "s_mov_b32 exec_hi, 0x00010001\n"
    "buffer_store_dwordx4 v[16:19], v121, s[76:79], 0 offen sc1\n"
    "s_mov_b64 exec, -1\n"
    "s_waitcnt lgkmcnt(0)\n"
    "s_barrier\n"
    "v_mfma_f32_16x16x32_bf16 v[0:3], a[144:147], a[128:131], 0\n"
    "ds_read_b128 a[176:179], v250 offset:0\n"
    "s_add_u32 m0, s24, 49152\n"
    "ds_read_b128 a[180:183], v250 offset:2048\n"
    "s_add_u32 s25, s26, 28672\n"
    "v_mfma_f32_16x16x32_bf16 v[32:35], a[144:147], a[132:135], 0\n"
    "buffer_load_dwordx4 v232, s[64:67], s22 offen lds\n"
    "s_add_u32 m0, s24, 50176\n"
    "v_mfma_f32_16x16x32_bf16 v[4:7], a[148:151], a[128:131], 0\n"
    "buffer_load_dwordx4 v233, s[64:67], s22 offen lds\n"
    "v_mfma_f32_16x16x32_bf16 v[36:39], a[148:151], a[132:135], 0\n"
    "ds_read_b64_tr_b16 a[192:193], v224 offset:8192\n"
    "v_mfma_f32_16x16x32_bf16 v[8:11], a[152:155], a[128:131], 0\n"
    "ds_read_b64_tr_b16 a[194:195], v224 offset:9216\n"
    "s_add_u32 m0, s24, 51200\n"
    "v_mfma_f32_16x16x32_bf16 v[40:43], a[152:155], a[132:135], 0\n"
    "buffer_load_dwordx4 v234, s[64:67], s22 offen lds\n"
    "v_mfma_f32_16x16x32_bf16 v[12:15], a[156:159], a[128:131], 0\n"
    "ds_read_b64_tr_b16 a[196:197], v225 offset:8192\n"
    "v_mfma_f32_16x16x32_bf16 v[44:47], a[156:159], a[132:135], 0\n"
    "ds_read_b64_tr_b16 a[198:199], v225 offset:9216\n"
    "s_add_u32 m0, s24, 52224\n"
    "v_mfma_f32_16x16x32_bf16 v[16:19], a[160:163], a[128:131], 0\n"
    "buffer_load_dwordx4 v235, s[64:67], s22 offen lds\n"
    "v_mfma_f32_16x16x32_bf16 v[48:51], a[160:163], a[132:135], 0\n"
    "ds_read_b64_tr_b16 a[200:201], v226 offset:8192\n"
    "v_mfma_f32_16x16x32_bf16 v[20:23], a[164:167], a[128:131], 0\n"
    "ds_read_b64_tr_b16 a[202:203], v226 offset:9216\n"
    "s_add_u32 m0, s23, 49152\n"
    "v_mfma_f32_16x16x32_bf16 v[52:55], a[164:167], a[132:135], 0\n"
    "buffer_load_dwordx4 v236, s[60:63], s20 offen lds\n"
    "v_mfma_f32_16x16x32_bf16 v[24:27], a[168:171], a[128:131], 0\n"
    "ds_read_b64_tr_b16 a[204:205], v227 offset:8192\n"
    "v_mfma_f32_16x16x32_bf16 v[56:59], a[168:171], a[132:135], 0\n"
    "ds_read_b64_tr_b16 a[206:207], v227 offset:9216\n"
    "s_add_u32 m0, s23, 50176\n"
    "v_mfma_f32_16x16x32_bf16 v[28:31], a[172:175], a[128:131], 0\n"
    "buffer_load_dwordx4 v237, s[60:63], s20 offen lds\n"
    "v_mfma_f32_16x16x32_bf16 v[60:63], a[172:175], a[132:135], 0\n"
    "ds_read_b64_tr_b16 a[208:209], v228 offset:8192\n"
    "v_mfma_f32_16x16x32_bf16 v[92:95], a[172:175], a[136:139], 0\n"
    "ds_read_b64_tr_b16 a[210:211], v228 offset:9216\n"
    "s_add_u32 m0, s23, 51200\n"
    "v_mfma_f32_16x16x32_bf16 v[124:127], a[172:175], a[140:143], 0\n"
    "buffer_load_dwordx4 v236, s[60:63], s21 offen lds\n"
    "v_mfma_f32_16x16x32_bf16 v[88:91], a[168:171], a[136:139], 0\n"
    "ds_read_b64_tr_b16 a[212:213], v229 offset:8192\n"
    "v_mfma_f32_16x16x32_bf16 v[120:123], a[168:171], a[140:143], 0\n"
    "ds_read_b64_tr_b16 a[214:215], v229 offset:9216\n"
    "s_add_u32 m0, s23, 52224\n"
    "v_mfma_f32_16x16x32_bf16 v[84:87], a[164:167], a[136:139], 0\n"
    "buffer_load_dwordx4 v237, s[60:63], s21 offen lds\n"
    "v_mfma_f32_16x16x32_bf16 v[116:119], a[164:167], a[140:143], 0\n"
    "ds_read_b64_tr_b16 a[216:217], v230 offset:8192\n"
    "v_mfma_f32_16x16x32_bf16 v[80:83], a[160:163], a[136:139], 0\n"
    "ds_read_b64_tr_b16 a[218:219], v230 offset:9216\n"
    "s_add_u32 m0, s23, 53248\n"
    "v_mfma_f32_16x16x32_bf16 v[112:115], a[160:163], a[140:143], 0\n"
    "buffer_load_dwordx4 v236, s[60:63], s30 offen lds\n"
    "v_mfma_f32_16x16x32_bf16 v[76:79], a[156:159], a[136:139], 0\n"
    "ds_read_b64_tr_b16 a[220:221], v231 offset:8192\n"
    "v_mfma_f32_16x16x32_bf16 v[108:111], a[156:159], a[140:143], 0\n"
    "ds_read_b64_tr_b16 a[222:223], v231 offset:9216\n"
    "s_add_u32 m0, s23, 54272\n"
    "v_mfma_f32_16x16x32_bf16 v[72:75], a[152:155], a[136:139], 0\n"
    "buffer_load_dwordx4 v237, s[60:63], s30 offen lds\n"
    "v_mfma_f32_16x16x32_bf16 v[104:107], a[152:155], a[140:143], 0\n"
    "ds_read_b128 a[184:187], v250 offset:4096\n"
    "v_mfma_f32_16x16x32_bf16 v[68:71], a[148:151], a[136:139], 0\n"
    "ds_read_b128 a[188:191], v250 offset:6144\n"
    "s_add_u32 m0, s23, 55296\n"
    "v_mfma_f32_16x16x32_bf16 v[100:103], a[148:151], a[140:143], 0\n"
    "buffer_load_dwordx4 v236, s[60:63], s31 offen lds\n"
    "v_mfma_f32_16x16x32_bf16 v[64:67], a[144:147], a[136:139], 0\n"
    "buffer_store_dwordx4 v[180:183], v248, s[72:75], s25 offen offset:0 sc1\n"
    "s_add_u32 s25, s26, 28672\n"
    "v_mfma_f32_16x16x32_bf16 v[96:99], a[144:147], a[140:143], 0\n"
    "s_waitcnt lgkmcnt(13)\n"
    "s_add_u32 m0, s23, 56320\n"
    "buffer_store_dwordx4 v[188:191], v248, s[72:75], s25 offen offset:1024 sc1\n"
    "v_mfma_f32_16x16x32_bf16 v[0:3], a[192:195], a[176:179], v[0:3]\n"
    "buffer_load_dwordx4 v237, s[60:63], s31 offen lds\n"
    "v_mfma_f32_16x16x32_bf16 v[32:35], a[192:195], a[180:183], v[32:35]\n"
    "s_add_u32 s20, s20, 128\n"
    "s_add_u32 s21, s21, 128\n"
    "s_add_u32 s30, s30, 128\n"
    "s_add_u32 s31, s31, 128\n"
    "s_add_u32 s22, s22, 524288\n"
    "v_mfma_f32_16x16x32_bf16 v[4:7], a[196:199], a[176:179], v[4:7]\n"
    "v_cvt_pk_bf16_f32 v192, v192, v193\n"
    "v_mfma_f32_16x16x32_bf16 v[36:39], a[196:199], a[180:183], v[36:39]\n"
    "s_waitcnt vmcnt(18) lgkmcnt(0)\n"
    "s_barrier\n"
    "s_add_u32 m0, s24, 0\n"
    "v_mfma_f32_16x16x32_bf16 v[8:11], a[200:203], a[176:179], v[8:11]\n"
    "buffer_load_dwordx4 v232, s[64:67], s22 offen lds\n"
    "v_mfma_f32_16x16x32_bf16 v[40:43], a[200:203], a[180:183], v[40:43]\n"
    "ds_read_b128 a[128:131], v253 offset:0\n"
    "v_mfma_f32_16x16x32_bf16 v[12:15], a[204:207], a[176:179], v[12:15]\n"
    "ds_read_b128 a[132:135], v253 offset:2048\n"
    "v_mfma_f32_16x16x32_bf16 v[44:47], a[204:207], a[180:183], v[44:47]\n"
    "ds_read_b64_tr_b16 a[144:145], v240 offset:0\n"
    "v_mfma_f32_16x16x32_bf16 v[16:19], a[208:211], a[176:179], v[16:19]\n"
    "ds_read_b64_tr_b16 a[146:147], v240 offset:1024\n"
    "s_add_u32 m0, s24, 1024\n"
    "v_cvt_pk_bf16_f32 v193, v194, v195\n"
    "v_mfma_f32_16x16x32_bf16 v[48:51], a[208:211], a[180:183], v[48:51]\n"
    "buffer_load_dwordx4 v233, s[64:67], s22 offen lds\n"
    "v_mfma_f32_16x16x32_bf16 v[20:23], a[212:215], a[176:179], v[20:23]\n"
    "ds_read_b64_tr_b16 a[148:149], v241 offset:0\n"
    "v_mfma_f32_16x16x32_bf16 v[52:55], a[212:215], a[180:183], v[52:55]\n"
    "ds_read_b64_tr_b16 a[150:151], v241 offset:1024\n"
    "v_mfma_f32_16x16x32_bf16 v[24:27], a[216:219], a[176:179], v[24:27]\n"
    "ds_read_b64_tr_b16 a[152:153], v242 offset:0\n"
    "v_mfma_f32_16x16x32_bf16 v[56:59], a[216:219], a[180:183], v[56:59]\n"
    "ds_read_b64_tr_b16 a[154:155], v242 offset:1024\n"
    "s_add_u32 m0, s24, 2048\n"
    "v_mfma_f32_16x16x32_bf16 v[28:31], a[220:223], a[176:179], v[28:31]\n"
    "buffer_load_dwordx4 v234, s[64:67], s22 offen lds\n"
    "v_mfma_f32_16x16x32_bf16 v[60:63], a[220:223], a[180:183], v[60:63]\n"
    "ds_read_b64_tr_b16 a[156:157], v243 offset:0\n"
    "v_mfma_f32_16x16x32_bf16 v[92:95], a[220:223], a[184:187], v[92:95]\n"
    "ds_read_b64_tr_b16 a[158:159], v243 offset:1024\n"
    "v_mfma_f32_16x16x32_bf16 v[124:127], a[220:223], a[188:191], v[124:127]\n"
    "ds_read_b64_tr_b16 a[160:161], v244 offset:0\n"
    "v_cvt_pk_bf16_f32 v194, v196, v197\n"
    "v_mfma_f32_16x16x32_bf16 v[88:91], a[216:219], a[184:187], v[88:91]\n"
    "ds_read_b64_tr_b16 a[162:163], v244 offset:1024\n"
    "s_add_u32 m0, s24, 3072\n"
    "v_mfma_f32_16x16x32_bf16 v[120:123], a[216:219], a[188:191], v[120:123]\n"
    "buffer_load_dwordx4 v235, s[64:67], s22 offen lds\n"
    "v_mfma_f32_16x16x32_bf16 v[84:87], a[212:215], a[184:187], v[84:87]\n"
    "ds_read_b64_tr_b16 a[164:165], v245 offset:0\n"
    "v_mfma_f32_16x16x32_bf16 v[116:119], a[212:215], a[188:191], v[116:119]\n"
    "ds_read_b64_tr_b16 a[166:167], v245 offset:1024\n"
    "v_mfma_f32_16x16x32_bf16 v[80:83], a[208:211], a[184:187], v[80:83]\n"
    "ds_read_b64_tr_b16 a[168:169], v246 offset:0\n"
    "v_mfma_f32_16x16x32_bf16 v[112:115], a[208:211], a[188:191], v[112:115]\n"
    "ds_read_b64_tr_b16 a[170:171], v246 offset:1024\n"
    "v_mfma_f32_16x16x32_bf16 v[76:79], a[204:207], a[184:187], v[76:79]\n"
    "ds_read_b64_tr_b16 a[172:173], v247 offset:0\n"
    "s_add_u32 m0, s23, 0\n"
    "v_cvt_pk_bf16_f32 v195, v198, v199\n"
    "s_add_u32 s25, s27, 16384\n"
    "v_mfma_f32_16x16x32_bf16 v[108:111], a[204:207], a[188:191], v[108:111]\n"
    "buffer_load_dwordx4 v236, s[60:63], s20 offen lds\n"
    "v_mfma_f32_16x16x32_bf16 v[72:75], a[200:203], a[184:187], v[72:75]\n"
    "ds_read_b64_tr_b16 a[174:175], v247 offset:1024\n"
    "v_mfma_f32_16x16x32_bf16 v[104:107], a[200:203], a[188:191], v[104:107]\n"
    "ds_read_b128 a[136:139], v253 offset:4096\n"
    "v_mfma_f32_16x16x32_bf16 v[68:71], a[196:199], a[184:187], v[68:71]\n"
    "ds_read_b128 a[140:143], v253 offset:6144\n"
    "v_mfma_f32_16x16x32_bf16 v[100:103], a[196:199], a[188:191], v[100:103]\n"
    "s_add_u32 m0, s23, 1024\n"
    "v_mfma_f32_16x16x32_bf16 v[64:67], a[192:195], a[184:187], v[64:67]\n"
    "buffer_load_dwordx4 v237, s[60:63], s20 offen lds\n"
    "v_mfma_f32_16x16x32_bf16 v[96:99], a[192:195], a[188:191], v[96:99]\n"
    "s_waitcnt lgkmcnt(13)\n"
    "buffer_store_dwordx4 v[192:195], v248, s[72:75], s25 offen offset:0 sc1\n"
    "v_mfma_f32_16x16x32_bf16 v[0:3], a[144:147], a[128:131], v[0:3]\n"
    "ds_read_b128 a[176:179], v254 offset:0\n"
    "v_accvgpr_write_b32 a0, 0\n"
    "v_mfma_f32_16x16x32_bf16 v[32:35], a[144:147], a[132:135], v[32:35]\n"
    "ds_read_b128 a[180:183], v254 offset:2048\n"
    "v_mfma_f32_16x16x32_bf16 v[4:7], a[148:151], a[128:131], v[4:7]\n"
    "ds_read_b64_tr_b16 a[192:193], v240 offset:8192\n"
    "s_add_u32 m0, s23, 2048\n"
    "v_mfma_f32_16x16x32_bf16 v[36:39], a[148:151], a[132:135], v[36:39]\n"
    "s_waitcnt lgkmcnt(12)\n"
    "buffer_load_dwordx4 v236, s[60:63], s21 offen lds\n"
    "v_mfma_f32_16x16x32_bf16 v[8:11], a[152:155], a[128:131], v[8:11]\n"
    "ds_read_b64_tr_b16 a[194:195], v240 offset:9216\n"
    "v_accvgpr_write_b32 a1, 0\n"
    "v_mfma_f32_16x16x32_bf16 v[40:43], a[152:155], a[132:135], v[40:43]\n"
    "ds_read_b64_tr_b16 a[196:197], v241 offset:8192\n"
    "v_mfma_f32_16x16x32_bf16 v[12:15], a[156:159], a[128:131], v[12:15]\n"
    "v_accvgpr_write_b32 a2, 0\n"
    "v_mfma_f32_16x16x32_bf16 v[44:47], a[156:159], a[132:135], v[44:47]\n"
    "s_waitcnt lgkmcnt(10)\n"
    "ds_read_b64_tr_b16 a[198:199], v241 offset:9216\n"
    "s_add_u32 m0, s23, 3072\n"
    "v_mfma_f32_16x16x32_bf16 v[16:19], a[160:163], a[128:131], v[16:19]\n"
    "buffer_load_dwordx4 v237, s[60:63], s21 offen lds\n"
    "v_mfma_f32_16x16x32_bf16 v[48:51], a[160:163], a[132:135], v[48:51]\n"
    "ds_read_b64_tr_b16 a[200:201], v242 offset:8192\n"
    "v_mfma_f32_16x16x32_bf16 v[20:23], a[164:167], a[128:131], v[20:23]\n"
    "ds_read_b64_tr_b16 a[202:203], v242 offset:9216\n"
    "v_mfma_f32_16x16x32_bf16 v[52:55], a[164:167], a[132:135], v[52:55]\n"
    "s_waitcnt lgkmcnt(9)\n"
    "ds_read_b64_tr_b16 a[204:205], v243 offset:8192\n"
    "v_accvgpr_write_b32 a3, 0\n"
    "v_mfma_f32_16x16x32_bf16 v[24:27], a[168:171], a[128:131], v[24:27]\n"
    "ds_read_b64_tr_b16 a[206:207], v243 offset:9216\n"
    "v_cvt_pk_bf16_f32 v200, v200, v201\n"
    "v_mfma_f32_16x16x32_bf16 v[56:59], a[168:171], a[132:135], v[56:59]\n"
    "s_add_u32 m0, s23, 4096\n"
    "v_mfma_f32_16x16x32_bf16 v[28:31], a[172:175], a[128:131], v[28:31]\n"
    "buffer_load_dwordx4 v236, s[60:63], s30 offen lds\n"
    "v_mfma_f32_16x16x32_bf16 v[60:63], a[172:175], a[132:135], v[60:63]\n"
    "ds_read_b64_tr_b16 a[208:209], v244 offset:8192\n"
    "v_accvgpr_write_b32 a4, 0\n"
    "v_mfma_f32_16x16x32_bf16 v[92:95], a[172:175], a[136:139], v[92:95]\n"
    "s_waitcnt lgkmcnt(8)\n"
    "ds_read_b64_tr_b16 a[210:211], v244 offset:9216\n"
    "v_mfma_f32_16x16x32_bf16 v[124:127], a[172:175], a[140:143], v[124:127]\n"
    "ds_read_b64_tr_b16 a[212:213], v245 offset:8192\n"
    "v_mfma_f32_16x16x32_bf16 v[88:91], a[168:171], a[136:139], v[88:91]\n"
    "ds_read_b64_tr_b16 a[214:215], v245 offset:9216\n"
    "s_add_u32 m0, s23, 5120\n"
    "v_cvt_pk_bf16_f32 v201, v202, v203\n"
    "v_mfma_f32_16x16x32_bf16 v[120:123], a[168:171], a[140:143], v[120:123]\n"
    "buffer_load_dwordx4 v237, s[60:63], s30 offen lds\n"
    "v_mfma_f32_16x16x32_bf16 v[84:87], a[164:167], a[136:139], v[84:87]\n"
    "ds_read_b64_tr_b16 a[216:217], v246 offset:8192\n"
    "v_accvgpr_write_b32 a5, 0\n"
    "v_mfma_f32_16x16x32_bf16 v[116:119], a[164:167], a[140:143], v[116:119]\n"
    "v_mfma_f32_16x16x32_bf16 v[80:83], a[160:163], a[136:139], v[80:83]\n"
    "ds_read_b64_tr_b16 a[218:219], v246 offset:9216\n"
    "v_mfma_f32_16x16x32_bf16 v[112:115], a[160:163], a[140:143], v[112:115]\n"
    "ds_read_b64_tr_b16 a[220:221], v247 offset:8192\n"
    "s_add_u32 m0, s23, 6144\n"
    "v_accvgpr_write_b32 a6, 0\n"
    "v_mfma_f32_16x16x32_bf16 v[76:79], a[156:159], a[136:139], v[76:79]\n"
    "buffer_load_dwordx4 v236, s[60:63], s31 offen lds\n"
    "v_mfma_f32_16x16x32_bf16 v[108:111], a[156:159], a[140:143], v[108:111]\n"
    "ds_read_b64_tr_b16 a[222:223], v247 offset:9216\n"
    "v_mfma_f32_16x16x32_bf16 v[72:75], a[152:155], a[136:139], v[72:75]\n"
    "ds_read_b128 a[184:187], v254 offset:4096\n"
    "v_cvt_pk_bf16_f32 v202, v204, v205\n"
    "v_mfma_f32_16x16x32_bf16 v[104:107], a[152:155], a[140:143], v[104:107]\n"
    "ds_read_b128 a[188:191], v254 offset:6144\n"
    "v_mfma_f32_16x16x32_bf16 v[68:71], a[148:151], a[136:139], v[68:71]\n"
    "s_add_u32 m0, s23, 7168\n"
    "v_accvgpr_write_b32 a7, 0\n"
    "v_mfma_f32_16x16x32_bf16 v[100:103], a[148:151], a[140:143], v[100:103]\n"
    "buffer_load_dwordx4 v237, s[60:63], s31 offen lds\n"
    "v_mfma_f32_16x16x32_bf16 v[64:67], a[144:147], a[136:139], v[64:67]\n"
    "s_add_u32 s20, s20, 128\n"
    "s_add_u32 s21, s21, 128\n"
    "s_add_u32 s30, s30, 128\n"
    "s_add_u32 s31, s31, 128\n"
    "s_add_u32 s22, s22, 524288\n"
    "v_mfma_f32_16x16x32_bf16 v[96:99], a[144:147], a[140:143], v[96:99]\n"
    "s_waitcnt lgkmcnt(13)\n"
    "v_cvt_pk_bf16_f32 v203, v206, v207\n"
    "s_add_u32 s25, s27, 16384\n"
    "v_accvgpr_write_b32 a8, 0\n"
    "v_mfma_f32_16x16x32_bf16 v[0:3], a[192:195], a[176:179], v[0:3]\n"
    "v_mfma_f32_16x16x32_bf16 v[32:35], a[192:195], a[180:183], v[32:35]\n"
    "v_accvgpr_write_b32 a9, 0\n"
    "v_mfma_f32_16x16x32_bf16 v[4:7], a[196:199], a[176:179], v[4:7]\n"
    "buffer_store_dwordx4 v[200:203], v248, s[72:75], s25 offen offset:1024 sc1\n"
    "v_mfma_f32_16x16x32_bf16 v[36:39], a[196:199], a[180:183], v[36:39]\n"
    "s_waitcnt vmcnt(14) lgkmcnt(0)\n"
    "s_barrier\n"
    "s_cmp_lg_u32 s8, 0\n"
    "s_cbranch_scc1 L_pub4_%=\n"
    "s_lshl_b32 s14, s34, 7\n"
    "s_lshl_b32 s15, s33, 3\n"
    "s_add_u32 s14, s14, s15\n"
    "s_add_u32 s14, s14, 8192\n"
    "s_add_u32 s14, s14, 0x3400000\n"
    "v_accvgpr_write_b32 a252, s28\n"
    "v_accvgpr_write_b32 a253, s29\n"
    "v_mov_b32 v251, s14\n"
    "s_mov_b64 exec, 1\n"
    "global_store_dwordx2 v251, a[252:253], s[58:59] sc1\n"
    "s_mov_b64 exec, -1\n"
    "L_pub4_%=:\n"
    "s_add_u32 m0, s24, 98304\n"
    "v_mfma_f32_16x16x32_bf16 v[8:11], a[200:203], a[176:179], v[8:11]\n"
    "buffer_load_dwordx4 v232, s[64:67], s22 offen lds\n"
    "v_mfma_f32_16x16x32_bf16 v[40:43], a[200:203], a[180:183], v[40:43]\n"
    "ds_read_b128 a[128:131], v249 offset:49152\n"
    "v_mfma_f32_16x16x32_bf16 v[12:15], a[204:207], a[176:179], v[12:15]\n"
    "ds_read_b128 a[132:135], v249 offset:51200\n"
    "v_accvgpr_write_b32 a10, 0\n"
    "v_mfma_f32_16x16x32_bf16 v[44:47], a[204:207], a[180:183], v[44:47]\n"
    "ds_read_b64_tr_b16 a[144:145], v224 offset:49152\n"
    "v_mfma_f32_16x16x32_bf16 v[16:19], a[208:211], a[176:179], v[16:19]\n"
    "ds_read_b64_tr_b16 a[146:147], v224 offset:50176\n"
    "s_add_u32 m0, s24, 99328\n"
    "v_mfma_f32_16x16x32_bf16 v[48:51], a[208:211], a[180:183], v[48:51]\n"
    "buffer_load_dwordx4 v233, s[64:67], s22 offen lds\n"
    "v_mfma_f32_16x16x32_bf16 v[20:23], a[212:215], a[176:179], v[20:23]\n"
    "ds_read_b64_tr_b16 a[148:149], v225 offset:49152\n"
    "v_mfma_f32_16x16x32_bf16 v[52:55], a[212:215], a[180:183], v[52:55]\n"
    "ds_read_b64_tr_b16 a[150:151], v225 offset:50176\n"
    "v_accvgpr_write_b32 a11, 0\n"
    "v_mfma_f32_16x16x32_bf16 v[24:27], a[216:219], a[176:179], v[24:27]\n"
    "ds_read_b64_tr_b16 a[152:153], v226 offset:49152\n"
    "v_mfma_f32_16x16x32_bf16 v[56:59], a[216:219], a[180:183], v[56:59]\n"
    "ds_read_b64_tr_b16 a[154:155], v226 offset:50176\n"
    "s_add_u32 m0, s24, 100352\n"
    "v_mfma_f32_16x16x32_bf16 v[28:31], a[220:223], a[176:179], v[28:31]\n"
    "buffer_load_dwordx4 v234, s[64:67], s22 offen lds\n"
    "v_mfma_f32_16x16x32_bf16 v[60:63], a[220:223], a[180:183], v[60:63]\n"
    "ds_read_b64_tr_b16 a[156:157], v227 offset:49152\n"
    "v_accvgpr_write_b32 a12, 0\n"
    "v_mfma_f32_16x16x32_bf16 v[92:95], a[220:223], a[184:187], v[92:95]\n"
    "ds_read_b64_tr_b16 a[158:159], v227 offset:50176\n"
    "v_mfma_f32_16x16x32_bf16 v[124:127], a[220:223], a[188:191], v[124:127]\n"
    "ds_read_b64_tr_b16 a[160:161], v228 offset:49152\n"
    "v_mfma_f32_16x16x32_bf16 v[88:91], a[216:219], a[184:187], v[88:91]\n"
    "ds_read_b64_tr_b16 a[162:163], v228 offset:50176\n"
    "s_add_u32 m0, s24, 101376\n"
    "v_cvt_pk_bf16_f32 v208, v208, v209\n"
    "v_mfma_f32_16x16x32_bf16 v[120:123], a[216:219], a[188:191], v[120:123]\n"
    "buffer_load_dwordx4 v235, s[64:67], s22 offen lds\n"
    "v_mfma_f32_16x16x32_bf16 v[84:87], a[212:215], a[184:187], v[84:87]\n"
    "ds_read_b64_tr_b16 a[164:165], v229 offset:49152\n"
    "v_accvgpr_write_b32 a13, 0\n"
    "v_mfma_f32_16x16x32_bf16 v[116:119], a[212:215], a[188:191], v[116:119]\n"
    "ds_read_b64_tr_b16 a[166:167], v229 offset:50176\n"
    "v_mfma_f32_16x16x32_bf16 v[80:83], a[208:211], a[184:187], v[80:83]\n"
    "ds_read_b64_tr_b16 a[168:169], v230 offset:49152\n"
    "v_mfma_f32_16x16x32_bf16 v[112:115], a[208:211], a[188:191], v[112:115]\n"
    "ds_read_b64_tr_b16 a[170:171], v230 offset:50176\n"
    "v_mfma_f32_16x16x32_bf16 v[76:79], a[204:207], a[184:187], v[76:79]\n"
    "ds_read_b64_tr_b16 a[172:173], v231 offset:49152\n"
    "s_add_u32 m0, s23, 98304\n"
    "v_accvgpr_write_b32 a14, 0\n"
    "v_mfma_f32_16x16x32_bf16 v[108:111], a[204:207], a[188:191], v[108:111]\n"
    "buffer_load_dwordx4 v236, s[60:63], s20 offen lds\n"
    "v_mfma_f32_16x16x32_bf16 v[72:75], a[200:203], a[184:187], v[72:75]\n"
    "ds_read_b64_tr_b16 a[174:175], v231 offset:50176\n"
    "v_mfma_f32_16x16x32_bf16 v[104:107], a[200:203], a[188:191], v[104:107]\n"
    "ds_read_b128 a[136:139], v249 offset:53248\n"
    "v_cvt_pk_bf16_f32 v209, v210, v211\n"
    "v_mfma_f32_16x16x32_bf16 v[68:71], a[196:199], a[184:187], v[68:71]\n"
    "ds_read_b128 a[140:143], v249 offset:55296\n"
    "v_mfma_f32_16x16x32_bf16 v[100:103], a[196:199], a[188:191], v[100:103]\n"
    "s_add_u32 m0, s23, 99328\n"
    "v_accvgpr_write_b32 a15, 0\n"
    "v_mfma_f32_16x16x32_bf16 v[64:67], a[192:195], a[184:187], v[64:67]\n"
    "buffer_load_dwordx4 v237, s[60:63], s20 offen lds\n"
    "v_mfma_f32_16x16x32_bf16 v[96:99], a[192:195], a[188:191], v[96:99]\n"
    "s_waitcnt lgkmcnt(13)\n"
    "v_mfma_f32_16x16x32_bf16 v[0:3], a[144:147], a[128:131], v[0:3]\n"
    "ds_read_b128 a[176:179], v250 offset:49152\n"
    "v_cvt_pk_bf16_f32 v210, v212, v213\n"
    "v_mfma_f32_16x16x32_bf16 v[32:35], a[144:147], a[132:135], v[32:35]\n"
    "ds_read_b128 a[180:183], v250 offset:51200\n"
    "v_accvgpr_write_b32 a16, 0\n"
    "v_mfma_f32_16x16x32_bf16 v[4:7], a[148:151], a[128:131], v[4:7]\n"
    "ds_read_b64_tr_b16 a[192:193], v224 offset:57344\n"
    "s_add_u32 m0, s23, 100352\n"
    "v_mfma_f32_16x16x32_bf16 v[36:39], a[148:151], a[132:135], v[36:39]\n"
    "s_waitcnt lgkmcnt(12)\n"
    "buffer_load_dwordx4 v236, s[60:63], s21 offen lds\n"
    "v_mfma_f32_16x16x32_bf16 v[8:11], a[152:155], a[128:131], v[8:11]\n"
    "ds_read_b64_tr_b16 a[194:195], v224 offset:58368\n"
    "v_mfma_f32_16x16x32_bf16 v[40:43], a[152:155], a[132:135], v[40:43]\n"
    "ds_read_b64_tr_b16 a[196:197], v225 offset:57344\n"
    "v_accvgpr_write_b32 a17, 0\n"
    "v_mfma_f32_16x16x32_bf16 v[12:15], a[156:159], a[128:131], v[12:15]\n"
    "v_cvt_pk_bf16_f32 v211, v214, v215\n"
    "s_add_u32 s25, s27, 16384\n"
    "v_mfma_f32_16x16x32_bf16 v[44:47], a[156:159], a[132:135], v[44:47]\n"
    "s_waitcnt lgkmcnt(10)\n"
    "ds_read_b64_tr_b16 a[198:199], v225 offset:58368\n"
    "s_add_u32 m0, s23, 101376\n"
    "v_mfma_f32_16x16x32_bf16 v[16:19], a[160:163], a[128:131], v[16:19]\n"
    "buffer_load_dwordx4 v237, s[60:63], s21 offen lds\n"
    "v_mfma_f32_16x16x32_bf16 v[48:51], a[160:163], a[132:135], v[48:51]\n"
    "ds_read_b64_tr_b16 a[200:201], v226 offset:57344\n"
    "v_accvgpr_write_b32 a18, 0\n"
    "v_mfma_f32_16x16x32_bf16 v[20:23], a[164:167], a[128:131], v[20:23]\n"
    "ds_read_b64_tr_b16 a[202:203], v226 offset:58368\n"
    "v_mfma_f32_16x16x32_bf16 v[52:55], a[164:167], a[132:135], v[52:55]\n"
    "s_waitcnt lgkmcnt(9)\n"
    "ds_read_b64_tr_b16 a[204:205], v227 offset:57344\n"
    "v_mfma_f32_16x16x32_bf16 v[24:27], a[168:171], a[128:131], v[24:27]\n"
    "ds_read_b64_tr_b16 a[206:207], v227 offset:58368\n"
    "v_mfma_f32_16x16x32_bf16 v[56:59], a[168:171], a[132:135], v[56:59]\n"
    "s_add_u32 m0, s23, 102400\n"
    "buffer_store_dwordx4 v[208:211], v248, s[72:75], s25 offen offset:2048 sc1\n"
    "v_mfma_f32_16x16x32_bf16 v[28:31], a[172:175], a[128:131], v[28:31]\n"
    "buffer_load_dwordx4 v236, s[60:63], s30 offen lds\n"
    "v_mfma_f32_16x16x32_bf16 v[60:63], a[172:175], a[132:135], v[60:63]\n"
    "ds_read_b64_tr_b16 a[208:209], v228 offset:57344\n"
    "v_accvgpr_write_b32 a19, 0\n"
    "v_mfma_f32_16x16x32_bf16 v[92:95], a[172:175], a[136:139], v[92:95]\n"
    "s_waitcnt lgkmcnt(8)\n"
    "ds_read_b64_tr_b16 a[210:211], v228 offset:58368\n"
    "v_mfma_f32_16x16x32_bf16 v[124:127], a[172:175], a[140:143], v[124:127]\n"
    "ds_read_b64_tr_b16 a[212:213], v229 offset:57344\n"
    "v_mfma_f32_16x16x32_bf16 v[88:91], a[168:171], a[136:139], v[88:91]\n"
    "ds_read_b64_tr_b16 a[214:215], v229 offset:58368\n"
    "s_add_u32 m0, s23, 103424\n"
    "v_accvgpr_write_b32 a20, 0\n"
    "v_mfma_f32_16x16x32_bf16 v[120:123], a[168:171], a[140:143], v[120:123]\n"
    "buffer_load_dwordx4 v237, s[60:63], s30 offen lds\n"
    "v_mfma_f32_16x16x32_bf16 v[84:87], a[164:167], a[136:139], v[84:87]\n"
    "ds_read_b64_tr_b16 a[216:217], v230 offset:57344\n"
    "v_mfma_f32_16x16x32_bf16 v[116:119], a[164:167], a[140:143], v[116:119]\n"
    "v_accvgpr_write_b32 a21, 0\n"
    "v_mfma_f32_16x16x32_bf16 v[80:83], a[160:163], a[136:139], v[80:83]\n"
    "ds_read_b64_tr_b16 a[218:219], v230 offset:58368\n"
    "v_mfma_f32_16x16x32_bf16 v[112:115], a[160:163], a[140:143], v[112:115]\n"
    "ds_read_b64_tr_b16 a[220:221], v231 offset:57344\n"
    "s_add_u32 m0, s23, 104448\n"
    "v_mfma_f32_16x16x32_bf16 v[76:79], a[156:159], a[136:139], v[76:79]\n"
    "buffer_load_dwordx4 v236, s[60:63], s31 offen lds\n"
    "v_mfma_f32_16x16x32_bf16 v[108:111], a[156:159], a[140:143], v[108:111]\n"
    "ds_read_b64_tr_b16 a[222:223], v231 offset:58368\n"
    "v_mfma_f32_16x16x32_bf16 v[72:75], a[152:155], a[136:139], v[72:75]\n"
    "ds_read_b128 a[184:187], v250 offset:53248\n"
    "v_accvgpr_write_b32 a22, 0\n"
    "v_mfma_f32_16x16x32_bf16 v[104:107], a[152:155], a[140:143], v[104:107]\n"
    "ds_read_b128 a[188:191], v250 offset:55296\n"
    "v_cvt_pk_bf16_f32 v216, v216, v217\n"
    "v_mfma_f32_16x16x32_bf16 v[68:71], a[148:151], a[136:139], v[68:71]\n"
    "s_add_u32 m0, s23, 105472\n"
    "v_mfma_f32_16x16x32_bf16 v[100:103], a[148:151], a[140:143], v[100:103]\n"
    "buffer_load_dwordx4 v237, s[60:63], s31 offen lds\n"
    "v_mfma_f32_16x16x32_bf16 v[64:67], a[144:147], a[136:139], v[64:67]\n"
    "s_add_u32 s20, s20, 128\n"
    "s_add_u32 s21, s21, 128\n"
    "s_add_u32 s30, s30, 128\n"
    "s_add_u32 s31, s31, 128\n"
    "s_add_u32 s22, s22, 524288\n"
    "v_accvgpr_write_b32 a23, 0\n"
    "v_mfma_f32_16x16x32_bf16 v[96:99], a[144:147], a[140:143], v[96:99]\n"
    "s_waitcnt lgkmcnt(13)\n"
    "v_mfma_f32_16x16x32_bf16 v[0:3], a[192:195], a[176:179], v[0:3]\n"
    "v_cvt_pk_bf16_f32 v217, v218, v219\n"
    "v_accvgpr_write_b32 a24, 0\n"
    "v_mfma_f32_16x16x32_bf16 v[32:35], a[192:195], a[180:183], v[32:35]\n"
    "v_mfma_f32_16x16x32_bf16 v[4:7], a[196:199], a[176:179], v[4:7]\n"
    "v_accvgpr_write_b32 a25, 0\n"
    "v_mfma_f32_16x16x32_bf16 v[36:39], a[196:199], a[180:183], v[36:39]\n"
    "s_waitcnt vmcnt(14) lgkmcnt(0)\n"
    "s_barrier\n"
    "s_add_u32 m0, s24, 49152\n"
    "v_cvt_pk_bf16_f32 v218, v220, v221\n"
    "v_mfma_f32_16x16x32_bf16 v[8:11], a[200:203], a[176:179], v[8:11]\n"
    "buffer_load_dwordx4 v232, s[64:67], s22 offen lds\n"
    "v_mfma_f32_16x16x32_bf16 v[40:43], a[200:203], a[180:183], v[40:43]\n"
    "ds_read_b128 a[128:131], v249 offset:0\n"
    "v_accvgpr_write_b32 a26, 0\n"
    "v_mfma_f32_16x16x32_bf16 v[12:15], a[204:207], a[176:179], v[12:15]\n"
    "ds_read_b128 a[132:135], v249 offset:2048\n"
    "v_mfma_f32_16x16x32_bf16 v[44:47], a[204:207], a[180:183], v[44:47]\n"
    "ds_read_b64_tr_b16 a[144:145], v224 offset:0\n"
    "v_mfma_f32_16x16x32_bf16 v[16:19], a[208:211], a[176:179], v[16:19]\n"
    "ds_read_b64_tr_b16 a[146:147], v224 offset:1024\n"
    "s_add_u32 m0, s24, 50176\n"
    "v_mfma_f32_16x16x32_bf16 v[48:51], a[208:211], a[180:183], v[48:51]\n"
    "buffer_load_dwordx4 v233, s[64:67], s22 offen lds\n"
    "v_mfma_f32_16x16x32_bf16 v[20:23], a[212:215], a[176:179], v[20:23]\n"
    "ds_read_b64_tr_b16 a[148:149], v225 offset:0\n"
    "v_cvt_pk_bf16_f32 v219, v222, v223\n"
    "s_add_u32 s25, s27, 16384\n"
    "v_mfma_f32_16x16x32_bf16 v[52:55], a[212:215], a[180:183], v[52:55]\n"
    "ds_read_b64_tr_b16 a[150:151], v225 offset:1024\n"
    "v_accvgpr_write_b32 a27, 0\n"
    "v_mfma_f32_16x16x32_bf16 v[24:27], a[216:219], a[176:179], v[24:27]\n"
    "ds_read_b64_tr_b16 a[152:153], v226 offset:0\n"
    "v_mfma_f32_16x16x32_bf16 v[56:59], a[216:219], a[180:183], v[56:59]\n"
    "ds_read_b64_tr_b16 a[154:155], v226 offset:1024\n"
    "s_add_u32 m0, s24, 51200\n"
    "v_mfma_f32_16x16x32_bf16 v[28:31], a[220:223], a[176:179], v[28:31]\n"
    "buffer_load_dwordx4 v234, s[64:67], s22 offen lds\n"
    "v_mfma_f32_16x16x32_bf16 v[60:63], a[220:223], a[180:183], v[60:63]\n"
    "ds_read_b64_tr_b16 a[156:157], v227 offset:0\n"
    "v_accvgpr_write_b32 a28, 0\n"
    "v_mfma_f32_16x16x32_bf16 v[92:95], a[220:223], a[184:187], v[92:95]\n"
    "ds_read_b64_tr_b16 a[158:159], v227 offset:1024\n"
    "v_mfma_f32_16x16x32_bf16 v[124:127], a[220:223], a[188:191], v[124:127]\n"
    "ds_read_b64_tr_b16 a[160:161], v228 offset:0\n"
    "v_mfma_f32_16x16x32_bf16 v[88:91], a[216:219], a[184:187], v[88:91]\n"
    "ds_read_b64_tr_b16 a[162:163], v228 offset:1024\n"
    "s_add_u32 m0, s24, 52224\n"
    "v_accvgpr_write_b32 a29, 0\n"
    "v_mfma_f32_16x16x32_bf16 v[120:123], a[216:219], a[188:191], v[120:123]\n"
    "buffer_load_dwordx4 v235, s[64:67], s22 offen lds\n"
    "v_mfma_f32_16x16x32_bf16 v[84:87], a[212:215], a[184:187], v[84:87]\n"
    "ds_read_b64_tr_b16 a[164:165], v229 offset:0\n"
    "v_mfma_f32_16x16x32_bf16 v[116:119], a[212:215], a[188:191], v[116:119]\n"
    "ds_read_b64_tr_b16 a[166:167], v229 offset:1024\n"
    "v_mfma_f32_16x16x32_bf16 v[80:83], a[208:211], a[184:187], v[80:83]\n"
    "ds_read_b64_tr_b16 a[168:169], v230 offset:0\n"
    "v_mfma_f32_16x16x32_bf16 v[112:115], a[208:211], a[188:191], v[112:115]\n"
    "ds_read_b64_tr_b16 a[170:171], v230 offset:1024\n"
    "v_accvgpr_write_b32 a30, 0\n"
    "v_mfma_f32_16x16x32_bf16 v[76:79], a[204:207], a[184:187], v[76:79]\n"
    "ds_read_b64_tr_b16 a[172:173], v231 offset:0\n"
    "s_add_u32 m0, s23, 49152\n"
    "v_mfma_f32_16x16x32_bf16 v[108:111], a[204:207], a[188:191], v[108:111]\n"
    "buffer_load_dwordx4 v236, s[60:63], s20 offen lds\n"
    "v_mfma_f32_16x16x32_bf16 v[72:75], a[200:203], a[184:187], v[72:75]\n"
    "ds_read_b64_tr_b16 a[174:175], v231 offset:1024\n"
    "v_mfma_f32_16x16x32_bf16 v[104:107], a[200:203], a[188:191], v[104:107]\n"
    "ds_read_b128 a[136:139], v249 offset:4096\n"
    "v_accvgpr_write_b32 a31, 0\n"
    "v_mfma_f32_16x16x32_bf16 v[68:71], a[196:199], a[184:187], v[68:71]\n"
    "ds_read_b128 a[140:143], v249 offset:6144\n"
    "v_mfma_f32_16x16x32_bf16 v[100:103], a[196:199], a[188:191], v[100:103]\n"
    "s_add_u32 m0, s23, 50176\n"
    "buffer_store_dwordx4 v[216:219], v248, s[72:75], s25 offen offset:3072 sc1\n"
    "v_mfma_f32_16x16x32_bf16 v[64:67], a[192:195], a[184:187], v[64:67]\n"
    "buffer_load_dwordx4 v237, s[60:63], s20 offen lds\n"
    "v_mfma_f32_16x16x32_bf16 v[96:99], a[192:195], a[188:191], v[96:99]\n"
    "s_waitcnt lgkmcnt(13)\n"
    "v_mfma_f32_16x16x32_bf16 v[0:3], a[144:147], a[128:131], v[0:3]\n"
    "ds_read_b128 a[176:179], v250 offset:0\n"
    "v_accvgpr_write_b32 a32, 0\n"
    "v_mfma_f32_16x16x32_bf16 v[32:35], a[144:147], a[132:135], v[32:35]\n"
    "ds_read_b128 a[180:183], v250 offset:2048\n"
    "v_mfma_f32_16x16x32_bf16 v[4:7], a[148:151], a[128:131], v[4:7]\n"
    "ds_read_b64_tr_b16 a[192:193], v224 offset:8192\n"
    "s_add_u32 m0, s23, 51200\n"
    "v_mfma_f32_16x16x32_bf16 v[36:39], a[148:151], a[132:135], v[36:39]\n"
    "s_waitcnt lgkmcnt(12)\n"
    "buffer_load_dwordx4 v236, s[60:63], s21 offen lds\n"
    "v_mfma_f32_16x16x32_bf16 v[8:11], a[152:155], a[128:131], v[8:11]\n"
    "ds_read_b64_tr_b16 a[194:195], v224 offset:9216\n"
    "v_accvgpr_write_b32 a33, 0\n"
    "v_mfma_f32_16x16x32_bf16 v[40:43], a[152:155], a[132:135], v[40:43]\n"
    "ds_read_b64_tr_b16 a[196:197], v225 offset:8192\n"
    "v_mfma_f32_16x16x32_bf16 v[12:15], a[156:159], a[128:131], v[12:15]\n"
    "v_accvgpr_write_b32 a34, 0\n"
    "v_mfma_f32_16x16x32_bf16 v[44:47], a[156:159], a[132:135], v[44:47]\n"
    "s_waitcnt lgkmcnt(10)\n"
    "ds_read_b64_tr_b16 a[198:199], v225 offset:9216\n"
    "s_add_u32 m0, s23, 52224\n"
    "v_mfma_f32_16x16x32_bf16 v[16:19], a[160:163], a[128:131], v[16:19]\n"
    "buffer_load_dwordx4 v237, s[60:63], s21 offen lds\n"
    "v_mfma_f32_16x16x32_bf16 v[48:51], a[160:163], a[132:135], v[48:51]\n"
    "ds_read_b64_tr_b16 a[200:201], v226 offset:8192\n"
    "v_mfma_f32_16x16x32_bf16 v[20:23], a[164:167], a[128:131], v[20:23]\n"
    "ds_read_b64_tr_b16 a[202:203], v226 offset:9216\n"
    "v_mfma_f32_16x16x32_bf16 v[52:55], a[164:167], a[132:135], v[52:55]\n"
    "s_waitcnt lgkmcnt(9)\n"
    "ds_read_b64_tr_b16 a[204:205], v227 offset:8192\n"
    "v_accvgpr_write_b32 a35, 0\n"
    "v_mfma_f32_16x16x32_bf16 v[24:27], a[168:171], a[128:131], v[24:27]\n"
    "ds_read_b64_tr_b16 a[206:207], v227 offset:9216\n"
    "v_mfma_f32_16x16x32_bf16 v[56:59], a[168:171], a[132:135], v[56:59]\n"
    "s_add_u32 m0, s23, 53248\n"
    "v_accvgpr_write_b32 a36, 0\n"
    "v_mfma_f32_16x16x32_bf16 v[28:31], a[172:175], a[128:131], v[28:31]\n"
    "buffer_load_dwordx4 v236, s[60:63], s30 offen lds\n"
    "v_mfma_f32_16x16x32_bf16 v[60:63], a[172:175], a[132:135], v[60:63]\n"
    "ds_read_b64_tr_b16 a[208:209], v228 offset:8192\n"
    "v_mfma_f32_16x16x32_bf16 v[92:95], a[172:175], a[136:139], v[92:95]\n"
    "s_waitcnt lgkmcnt(8)\n"
    "ds_read_b64_tr_b16 a[210:211], v228 offset:9216\n"
    "v_mfma_f32_16x16x32_bf16 v[124:127], a[172:175], a[140:143], v[124:127]\n"
    "ds_read_b64_tr_b16 a[212:213], v229 offset:8192\n"
    "v_accvgpr_write_b32 a37, 0\n"
    "v_mfma_f32_16x16x32_bf16 v[88:91], a[168:171], a[136:139], v[88:91]\n"
    "ds_read_b64_tr_b16 a[214:215], v229 offset:9216\n"
    "s_add_u32 m0, s23, 54272\n"
    "v_mfma_f32_16x16x32_bf16 v[120:123], a[168:171], a[140:143], v[120:123]\n"
    "buffer_load_dwordx4 v237, s[60:63], s30 offen lds\n"
    "v_mfma_f32_16x16x32_bf16 v[84:87], a[164:167], a[136:139], v[84:87]\n"
    "ds_read_b64_tr_b16 a[216:217], v230 offset:8192\n"
    "v_mfma_f32_16x16x32_bf16 v[116:119], a[164:167], a[140:143], v[116:119]\n"
    "v_accvgpr_write_b32 a38, 0\n"
    "v_mfma_f32_16x16x32_bf16 v[80:83], a[160:163], a[136:139], v[80:83]\n"
    "ds_read_b64_tr_b16 a[218:219], v230 offset:9216\n"
    "v_mfma_f32_16x16x32_bf16 v[112:115], a[160:163], a[140:143], v[112:115]\n"
    "ds_read_b64_tr_b16 a[220:221], v231 offset:8192\n"
    "s_add_u32 m0, s23, 55296\n"
    "v_mfma_f32_16x16x32_bf16 v[76:79], a[156:159], a[136:139], v[76:79]\n"
    "buffer_load_dwordx4 v236, s[60:63], s31 offen lds\n"
    "v_mfma_f32_16x16x32_bf16 v[108:111], a[156:159], a[140:143], v[108:111]\n"
    "ds_read_b64_tr_b16 a[222:223], v231 offset:9216\n"
    "v_accvgpr_write_b32 a39, 0\n"
    "v_mfma_f32_16x16x32_bf16 v[72:75], a[152:155], a[136:139], v[72:75]\n"
    "ds_read_b128 a[184:187], v250 offset:4096\n"
    "v_mfma_f32_16x16x32_bf16 v[104:107], a[152:155], a[140:143], v[104:107]\n"
    "ds_read_b128 a[188:191], v250 offset:6144\n"
    "v_mfma_f32_16x16x32_bf16 v[68:71], a[148:151], a[136:139], v[68:71]\n"
    "s_add_u32 m0, s23, 56320\n"
    "v_accvgpr_write_b32 a40, 0\n"
    "v_mfma_f32_16x16x32_bf16 v[100:103], a[148:151], a[140:143], v[100:103]\n"
    "buffer_load_dwordx4 v237, s[60:63], s31 offen lds\n"
    "v_mfma_f32_16x16x32_bf16 v[64:67], a[144:147], a[136:139], v[64:67]\n"
    "s_add_u32 s20, s20, 128\n"
    "s_add_u32 s21, s21, 128\n"
    "s_add_u32 s30, s30, 128\n"
    "s_add_u32 s31, s31, 128\n"
    "s_add_u32 s22, s22, 524288\n"
    "v_mfma_f32_16x16x32_bf16 v[96:99], a[144:147], a[140:143], v[96:99]\n"
    "s_waitcnt lgkmcnt(13)\n"
    "v_accvgpr_write_b32 a41, 0\n"
    "v_mfma_f32_16x16x32_bf16 v[0:3], a[192:195], a[176:179], v[0:3]\n"
    "v_accvgpr_write_b32 a42, 0\n"
    "v_mfma_f32_16x16x32_bf16 v[32:35], a[192:195], a[180:183], v[32:35]\n"
    "v_mfma_f32_16x16x32_bf16 v[4:7], a[196:199], a[176:179], v[4:7]\n"
    "v_accvgpr_write_b32 a43, 0\n"
    "v_mfma_f32_16x16x32_bf16 v[36:39], a[196:199], a[180:183], v[36:39]\n"
    "s_waitcnt vmcnt(13) lgkmcnt(0)\n"
    "s_barrier\n"
    "s_add_u32 m0, s24, 0\n"
    "v_mfma_f32_16x16x32_bf16 v[8:11], a[200:203], a[176:179], v[8:11]\n"
    "buffer_load_dwordx4 v232, s[64:67], s22 offen lds\n"
    "v_mfma_f32_16x16x32_bf16 v[40:43], a[200:203], a[180:183], v[40:43]\n"
    "ds_read_b128 a[128:131], v253 offset:0\n"
    "v_accvgpr_write_b32 a44, 0\n"
    "v_mfma_f32_16x16x32_bf16 v[12:15], a[204:207], a[176:179], v[12:15]\n"
    "ds_read_b128 a[132:135], v253 offset:2048\n"
    "v_mfma_f32_16x16x32_bf16 v[44:47], a[204:207], a[180:183], v[44:47]\n"
    "ds_read_b64_tr_b16 a[144:145], v240 offset:0\n"
    "v_mfma_f32_16x16x32_bf16 v[16:19], a[208:211], a[176:179], v[16:19]\n"
    "ds_read_b64_tr_b16 a[146:147], v240 offset:1024\n"
    "s_add_u32 m0, s24, 1024\n"
    "v_accvgpr_write_b32 a45, 0\n"
    "v_mfma_f32_16x16x32_bf16 v[48:51], a[208:211], a[180:183], v[48:51]\n"
    "buffer_load_dwordx4 v233, s[64:67], s22 offen lds\n"
    "v_mfma_f32_16x16x32_bf16 v[20:23], a[212:215], a[176:179], v[20:23]\n"
    "ds_read_b64_tr_b16 a[148:149], v241 offset:0\n"
    "v_mfma_f32_16x16x32_bf16 v[52:55], a[212:215], a[180:183], v[52:55]\n"
    "ds_read_b64_tr_b16 a[150:151], v241 offset:1024\n"
    "v_mfma_f32_16x16x32_bf16 v[24:27], a[216:219], a[176:179], v[24:27]\n"
    "ds_read_b64_tr_b16 a[152:153], v242 offset:0\n"
    "v_mfma_f32_16x16x32_bf16 v[56:59], a[216:219], a[180:183], v[56:59]\n"
    "ds_read_b64_tr_b16 a[154:155], v242 offset:1024\n"
    "s_add_u32 m0, s24, 2048\n"
    "v_accvgpr_write_b32 a46, 0\n"
    "v_mfma_f32_16x16x32_bf16 v[28:31], a[220:223], a[176:179], v[28:31]\n"
    "buffer_load_dwordx4 v234, s[64:67], s22 offen lds\n"
    "v_mfma_f32_16x16x32_bf16 v[60:63], a[220:223], a[180:183], v[60:63]\n"
    "ds_read_b64_tr_b16 a[156:157], v243 offset:0\n"
    "v_mfma_f32_16x16x32_bf16 v[92:95], a[220:223], a[184:187], v[92:95]\n"
    "ds_read_b64_tr_b16 a[158:159], v243 offset:1024\n"
    "v_mfma_f32_16x16x32_bf16 v[124:127], a[220:223], a[188:191], v[124:127]\n"
    "ds_read_b64_tr_b16 a[160:161], v244 offset:0\n"
    "v_accvgpr_write_b32 a47, 0\n"
    "v_mfma_f32_16x16x32_bf16 v[88:91], a[216:219], a[184:187], v[88:91]\n"
    "ds_read_b64_tr_b16 a[162:163], v244 offset:1024\n"
    "s_add_u32 m0, s24, 3072\n"
    "v_mfma_f32_16x16x32_bf16 v[120:123], a[216:219], a[188:191], v[120:123]\n"
    "buffer_load_dwordx4 v235, s[64:67], s22 offen lds\n"
    "v_mfma_f32_16x16x32_bf16 v[84:87], a[212:215], a[184:187], v[84:87]\n"
    "ds_read_b64_tr_b16 a[164:165], v245 offset:0\n"
    "v_mfma_f32_16x16x32_bf16 v[116:119], a[212:215], a[188:191], v[116:119]\n"
    "ds_read_b64_tr_b16 a[166:167], v245 offset:1024\n"
    "v_mfma_f32_16x16x32_bf16 v[80:83], a[208:211], a[184:187], v[80:83]\n"
    "ds_read_b64_tr_b16 a[168:169], v246 offset:0\n"
    "v_accvgpr_write_b32 a48, 0\n"
    "v_mfma_f32_16x16x32_bf16 v[112:115], a[208:211], a[188:191], v[112:115]\n"
    "ds_read_b64_tr_b16 a[170:171], v246 offset:1024\n"
    "v_mfma_f32_16x16x32_bf16 v[76:79], a[204:207], a[184:187], v[76:79]\n"
    "ds_read_b64_tr_b16 a[172:173], v247 offset:0\n"
    "s_add_u32 m0, s23, 0\n"
    "v_mfma_f32_16x16x32_bf16 v[108:111], a[204:207], a[188:191], v[108:111]\n"
    "buffer_load_dwordx4 v236, s[60:63], s20 offen lds\n"
    "v_mfma_f32_16x16x32_bf16 v[72:75], a[200:203], a[184:187], v[72:75]\n"
    "ds_read_b64_tr_b16 a[174:175], v247 offset:1024\n"
    "v_accvgpr_write_b32 a49, 0\n"
    "v_mfma_f32_16x16x32_bf16 v[104:107], a[200:203], a[188:191], v[104:107]\n"
    "ds_read_b128 a[136:139], v253 offset:4096\n"
    "v_mfma_f32_16x16x32_bf16 v[68:71], a[196:199], a[184:187], v[68:71]\n"
    "ds_read_b128 a[140:143], v253 offset:6144\n"
    "v_mfma_f32_16x16x32_bf16 v[100:103], a[196:199], a[188:191], v[100:103]\n"
    "s_add_u32 m0, s23, 1024\n"
    "v_accvgpr_write_b32 a50, 0\n"
    "v_mfma_f32_16x16x32_bf16 v[64:67], a[192:195], a[184:187], v[64:67]\n"
    "buffer_load_dwordx4 v237, s[60:63], s20 offen lds\n"
    "v_mfma_f32_16x16x32_bf16 v[96:99], a[192:195], a[188:191], v[96:99]\n"
    "s_waitcnt lgkmcnt(13)\n"
    "v_mfma_f32_16x16x32_bf16 v[0:3], a[144:147], a[128:131], v[0:3]\n"
    "ds_read_b128 a[176:179], v254 offset:0\n"
    "v_accvgpr_write_b32 a51, 0\n"
    "v_mfma_f32_16x16x32_bf16 v[32:35], a[144:147], a[132:135], v[32:35]\n"
    "ds_read_b128 a[180:183], v254 offset:2048\n"
    "v_mfma_f32_16x16x32_bf16 v[4:7], a[148:151], a[128:131], v[4:7]\n"
    "ds_read_b64_tr_b16 a[192:193], v240 offset:8192\n"
    "s_add_u32 m0, s23, 2048\n"
    "v_mfma_f32_16x16x32_bf16 v[36:39], a[148:151], a[132:135], v[36:39]\n"
    "s_waitcnt lgkmcnt(12)\n"
    "buffer_load_dwordx4 v236, s[60:63], s21 offen lds\n"
    "v_mfma_f32_16x16x32_bf16 v[8:11], a[152:155], a[128:131], v[8:11]\n"
    "ds_read_b64_tr_b16 a[194:195], v240 offset:9216\n"
    "v_accvgpr_write_b32 a52, 0\n"
    "v_mfma_f32_16x16x32_bf16 v[40:43], a[152:155], a[132:135], v[40:43]\n"
    "ds_read_b64_tr_b16 a[196:197], v241 offset:8192\n"
    "v_mfma_f32_16x16x32_bf16 v[12:15], a[156:159], a[128:131], v[12:15]\n"
    "v_accvgpr_write_b32 a53, 0\n"
    "v_mfma_f32_16x16x32_bf16 v[44:47], a[156:159], a[132:135], v[44:47]\n"
    "s_waitcnt lgkmcnt(10)\n"
    "ds_read_b64_tr_b16 a[198:199], v241 offset:9216\n"
    "s_add_u32 m0, s23, 3072\n"
    "v_mfma_f32_16x16x32_bf16 v[16:19], a[160:163], a[128:131], v[16:19]\n"
    "buffer_load_dwordx4 v237, s[60:63], s21 offen lds\n"
    "v_mfma_f32_16x16x32_bf16 v[48:51], a[160:163], a[132:135], v[48:51]\n"
    "ds_read_b64_tr_b16 a[200:201], v242 offset:8192\n"
    "v_mfma_f32_16x16x32_bf16 v[20:23], a[164:167], a[128:131], v[20:23]\n"
    "ds_read_b64_tr_b16 a[202:203], v242 offset:9216\n"
    "v_mfma_f32_16x16x32_bf16 v[52:55], a[164:167], a[132:135], v[52:55]\n"
    "s_waitcnt lgkmcnt(9)\n"
    "ds_read_b64_tr_b16 a[204:205], v243 offset:8192\n"
    "v_accvgpr_write_b32 a54, 0\n"
    "v_mfma_f32_16x16x32_bf16 v[24:27], a[168:171], a[128:131], v[24:27]\n"
    "ds_read_b64_tr_b16 a[206:207], v243 offset:9216\n"
    "v_mfma_f32_16x16x32_bf16 v[56:59], a[168:171], a[132:135], v[56:59]\n"
    "s_add_u32 m0, s23, 4096\n"
    "v_accvgpr_write_b32 a55, 0\n"
    "v_mfma_f32_16x16x32_bf16 v[28:31], a[172:175], a[128:131], v[28:31]\n"
    "buffer_load_dwordx4 v236, s[60:63], s30 offen lds\n"
    "v_mfma_f32_16x16x32_bf16 v[60:63], a[172:175], a[132:135], v[60:63]\n"
    "ds_read_b64_tr_b16 a[208:209], v244 offset:8192\n"
    "v_mfma_f32_16x16x32_bf16 v[92:95], a[172:175], a[136:139], v[92:95]\n"
    "s_waitcnt lgkmcnt(8)\n"
    "ds_read_b64_tr_b16 a[210:211], v244 offset:9216\n"
    "v_mfma_f32_16x16x32_bf16 v[124:127], a[172:175], a[140:143], v[124:127]\n"
    "ds_read_b64_tr_b16 a[212:213], v245 offset:8192\n"
    "v_mfma_f32_16x16x32_bf16 v[88:91], a[168:171], a[136:139], v[88:91]\n"
    "ds_read_b64_tr_b16 a[214:215], v245 offset:9216\n"
    "s_add_u32 m0, s23, 5120\n"
    "v_accvgpr_write_b32 a56, 0\n"
    "v_mfma_f32_16x16x32_bf16 v[120:123], a[168:171], a[140:143], v[120:123]\n"
    "buffer_load_dwordx4 v237, s[60:63], s30 offen lds\n"
    "v_mfma_f32_16x16x32_bf16 v[84:87], a[164:167], a[136:139], v[84:87]\n"
    "ds_read_b64_tr_b16 a[216:217], v246 offset:8192\n"
    "v_mfma_f32_16x16x32_bf16 v[116:119], a[164:167], a[140:143], v[116:119]\n"
    "v_accvgpr_write_b32 a57, 0\n"
    "v_mfma_f32_16x16x32_bf16 v[80:83], a[160:163], a[136:139], v[80:83]\n"
    "ds_read_b64_tr_b16 a[218:219], v246 offset:9216\n"
    "v_mfma_f32_16x16x32_bf16 v[112:115], a[160:163], a[140:143], v[112:115]\n"
    "ds_read_b64_tr_b16 a[220:221], v247 offset:8192\n"
    "s_add_u32 m0, s23, 6144\n"
    "v_mfma_f32_16x16x32_bf16 v[76:79], a[156:159], a[136:139], v[76:79]\n"
    "buffer_load_dwordx4 v236, s[60:63], s31 offen lds\n"
    "v_mfma_f32_16x16x32_bf16 v[108:111], a[156:159], a[140:143], v[108:111]\n"
    "ds_read_b64_tr_b16 a[222:223], v247 offset:9216\n"
    "v_accvgpr_write_b32 a58, 0\n"
    "v_mfma_f32_16x16x32_bf16 v[72:75], a[152:155], a[136:139], v[72:75]\n"
    "ds_read_b128 a[184:187], v254 offset:4096\n"
    "v_mfma_f32_16x16x32_bf16 v[104:107], a[152:155], a[140:143], v[104:107]\n"
    "ds_read_b128 a[188:191], v254 offset:6144\n"
    "v_mfma_f32_16x16x32_bf16 v[68:71], a[148:151], a[136:139], v[68:71]\n"
    "s_add_u32 m0, s23, 7168\n"
    "v_accvgpr_write_b32 a59, 0\n"
    "v_mfma_f32_16x16x32_bf16 v[100:103], a[148:151], a[140:143], v[100:103]\n"
    "buffer_load_dwordx4 v237, s[60:63], s31 offen lds\n"
    "v_mfma_f32_16x16x32_bf16 v[64:67], a[144:147], a[136:139], v[64:67]\n"
    "s_add_u32 s20, s20, 128\n"
    "s_add_u32 s21, s21, 128\n"
    "s_add_u32 s30, s30, 128\n"
    "s_add_u32 s31, s31, 128\n"
    "s_add_u32 s22, s22, 524288\n"
    "v_mfma_f32_16x16x32_bf16 v[96:99], a[144:147], a[140:143], v[96:99]\n"
    "s_waitcnt lgkmcnt(13)\n"
    "v_accvgpr_write_b32 a60, 0\n"
    "v_mfma_f32_16x16x32_bf16 v[0:3], a[192:195], a[176:179], v[0:3]\n"
    "v_accvgpr_write_b32 a61, 0\n"
    "v_mfma_f32_16x16x32_bf16 v[32:35], a[192:195], a[180:183], v[32:35]\n"
    "v_mfma_f32_16x16x32_bf16 v[4:7], a[196:199], a[176:179], v[4:7]\n"
    "v_accvgpr_write_b32 a62, 0\n"
    "v_mfma_f32_16x16x32_bf16 v[36:39], a[196:199], a[180:183], v[36:39]\n"
    "s_waitcnt vmcnt(12) lgkmcnt(0)\n"
    "s_barrier\n"
    "s_cmp_lg_u32 s8, 0\n"
    "s_cbranch_scc1 L_pub5_%=\n"
    "s_lshl_b32 s14, s12, 6\n"
    "s_lshl_b32 s15, s13, 3\n"
    "s_add_u32 s14, s14, s15\n"
    "s_add_u32 s14, s14, 4096\n"
    "s_add_u32 s14, s14, 0x3400000\n"
    "v_accvgpr_write_b32 a252, s28\n"
    "v_accvgpr_write_b32 a253, s29\n"
    "v_mov_b32 v251, s14\n"
    "s_mov_b64 exec, 1\n"
    "global_store_dwordx2 v251, a[252:253], s[58:59] sc1\n"
    "s_mov_b64 exec, -1\n"
    "L_pub5_%=:\n"
    "s_add_u32 m0, s24, 98304\n"
    "v_mfma_f32_16x16x32_bf16 v[8:11], a[200:203], a[176:179], v[8:11]\n"
    "buffer_load_dwordx4 v232, s[64:67], s22 offen lds\n"
    "v_mfma_f32_16x16x32_bf16 v[40:43], a[200:203], a[180:183], v[40:43]\n"
    "ds_read_b128 a[128:131], v249 offset:49152\n"
    "v_mfma_f32_16x16x32_bf16 v[12:15], a[204:207], a[176:179], v[12:15]\n"
    "ds_read_b128 a[132:135], v249 offset:51200\n"
    "v_mfma_f32_16x16x32_bf16 v[44:47], a[204:207], a[180:183], v[44:47]\n"
    "ds_read_b64_tr_b16 a[144:145], v224 offset:49152\n"
    "v_accvgpr_write_b32 a63, 0\n"
    "v_mfma_f32_16x16x32_bf16 v[16:19], a[208:211], a[176:179], v[16:19]\n"
    "ds_read_b64_tr_b16 a[146:147], v224 offset:50176\n"
    "s_add_u32 m0, s24, 99328\n"
    "v_mfma_f32_16x16x32_bf16 v[48:51], a[208:211], a[180:183], v[48:51]\n"
    "buffer_load_dwordx4 v233, s[64:67], s22 offen lds\n"
    "v_mfma_f32_16x16x32_bf16 v[20:23], a[212:215], a[176:179], v[20:23]\n"
    "ds_read_b64_tr_b16 a[148:149], v225 offset:49152\n"
    "v_mfma_f32_16x16x32_bf16 v[52:55], a[212:215], a[180:183], v[52:55]\n"
    "ds_read_b64_tr_b16 a[150:151], v225 offset:50176\n"
    "v_mfma_f32_16x16x32_bf16 v[24:27], a[216:219], a[176:179], v[24:27]\n"
    "ds_read_b64_tr_b16 a[152:153], v226 offset:49152\n"
    "v_accvgpr_write_b32 a64, 0\n"
    "v_mfma_f32_16x16x32_bf16 v[56:59], a[216:219], a[180:183], v[56:59]\n"
    "ds_read_b64_tr_b16 a[154:155], v226 offset:50176\n"
    "s_add_u32 m0, s24, 100352\n"
    "v_mfma_f32_16x16x32_bf16 v[28:31], a[220:223], a[176:179], v[28:31]\n"
    "buffer_load_dwordx4 v234, s[64:67], s22 offen lds\n"
    "v_mfma_f32_16x16x32_bf16 v[60:63], a[220:223], a[180:183], v[60:63]\n"
    "ds_read_b64_tr_b16 a[156:157], v227 offset:49152\n"
    "v_mfma_f32_16x16x32_bf16 v[92:95], a[220:223], a[184:187], v[92:95]\n"
    "ds_read_b64_tr_b16 a[158:159], v227 offset:50176\n"
    "v_accvgpr_write_b32 a65, 0\n"
    "v_mfma_f32_16x16x32_bf16 v[124:127], a[220:223], a[188:191], v[124:127]\n"
    "ds_read_b64_tr_b16 a[160:161], v228 offset:49152\n"
    "v_mfma_f32_16x16x32_bf16 v[88:91], a[216:219], a[184:187], v[88:91]\n"
    "ds_read_b64_tr_b16 a[162:163], v228 offset:50176\n"
    "s_add_u32 m0, s24, 101376\n"
    "v_mfma_f32_16x16x32_bf16 v[120:123], a[216:219], a[188:191], v[120:123]\n"
    "buffer_load_dwordx4 v235, s[64:67], s22 offen lds\n"
    "v_mfma_f32_16x16x32_bf16 v[84:87], a[212:215], a[184:187], v[84:87]\n"
    "ds_read_b64_tr_b16 a[164:165], v229 offset:49152\n"
    "v_accvgpr_write_b32 a66, 0\n"
    "v_mfma_f32_16x16x32_bf16 v[116:119], a[212:215], a[188:191], v[116:119]\n"
    "ds_read_b64_tr_b16 a[166:167], v229 offset:50176\n"
    "v_mfma_f32_16x16x32_bf16 v[80:83], a[208:211], a[184:187], v[80:83]\n"
    "ds_read_b64_tr_b16 a[168:169], v230 offset:49152\n"
    "v_mfma_f32_16x16x32_bf16 v[112:115], a[208:211], a[188:191], v[112:115]\n"
    "ds_read_b64_tr_b16 a[170:171], v230 offset:50176\n"
    "v_mfma_f32_16x16x32_bf16 v[76:79], a[204:207], a[184:187], v[76:79]\n"
    "ds_read_b64_tr_b16 a[172:173], v231 offset:49152\n"
    "s_add_u32 m0, s23, 98304\n"
    "v_accvgpr_write_b32 a67, 0\n"
    "v_mfma_f32_16x16x32_bf16 v[108:111], a[204:207], a[188:191], v[108:111]\n"
    "buffer_load_dwordx4 v236, s[60:63], s20 offen lds\n"
    "v_mfma_f32_16x16x32_bf16 v[72:75], a[200:203], a[184:187], v[72:75]\n"
    "ds_read_b64_tr_b16 a[174:175], v231 offset:50176\n"
    "v_mfma_f32_16x16x32_bf16 v[104:107], a[200:203], a[188:191], v[104:107]\n"
    "ds_read_b128 a[136:139], v249 offset:53248\n"
    "v_mfma_f32_16x16x32_bf16 v[68:71], a[196:199], a[184:187], v[68:71]\n"
    "ds_read_b128 a[140:143], v249 offset:55296\n"
    "v_accvgpr_write_b32 a68, 0\n"
    "v_mfma_f32_16x16x32_bf16 v[100:103], a[196:199], a[188:191], v[100:103]\n"
    "s_add_u32 m0, s23, 99328\n"
    "v_mfma_f32_16x16x32_bf16 v[64:67], a[192:195], a[184:187], v[64:67]\n"
    "buffer_load_dwordx4 v237, s[60:63], s20 offen lds\n"
    "v_mfma_f32_16x16x32_bf16 v[96:99], a[192:195], a[188:191], v[96:99]\n"
    "s_waitcnt lgkmcnt(13)\n"
    "v_accvgpr_write_b32 a69, 0\n"
    "v_mfma_f32_16x16x32_bf16 v[0:3], a[144:147], a[128:131], v[0:3]\n"
    "ds_read_b128 a[176:179], v250 offset:49152\n"
    "v_mfma_f32_16x16x32_bf16 v[32:35], a[144:147], a[132:135], v[32:35]\n"
    "ds_read_b128 a[180:183], v250 offset:51200\n"
    "v_mfma_f32_16x16x32_bf16 v[4:7], a[148:151], a[128:131], v[4:7]\n"
    "ds_read_b64_tr_b16 a[192:193], v224 offset:57344\n"
    "s_add_u32 m0, s23, 100352\n"
    "v_accvgpr_write_b32 a70, 0\n"
    "v_mfma_f32_16x16x32_bf16 v[36:39], a[148:151], a[132:135], v[36:39]\n"
    "s_waitcnt lgkmcnt(12)\n"
    "buffer_load_dwordx4 v236, s[60:63], s21 offen lds\n"
    "v_mfma_f32_16x16x32_bf16 v[8:11], a[152:155], a[128:131], v[8:11]\n"
    "ds_read_b64_tr_b16 a[194:195], v224 offset:58368\n"
    "v_mfma_f32_16x16x32_bf16 v[40:43], a[152:155], a[132:135], v[40:43]\n"
    "ds_read_b64_tr_b16 a[196:197], v225 offset:57344\n"
    "v_mfma_f32_16x16x32_bf16 v[12:15], a[156:159], a[128:131], v[12:15]\n"
    "v_accvgpr_write_b32 a71, 0\n"
    "v_mfma_f32_16x16x32_bf16 v[44:47], a[156:159], a[132:135], v[44:47]\n"
    "s_waitcnt lgkmcnt(10)\n"
    "ds_read_b64_tr_b16 a[198:199], v225 offset:58368\n"
    "s_add_u32 m0, s23, 101376\n"
    "v_mfma_f32_16x16x32_bf16 v[16:19], a[160:163], a[128:131], v[16:19]\n"
    "buffer_load_dwordx4 v237, s[60:63], s21 offen lds\n"
    "v_mfma_f32_16x16x32_bf16 v[48:51], a[160:163], a[132:135], v[48:51]\n"
    "ds_read_b64_tr_b16 a[200:201], v226 offset:57344\n"
    "v_mfma_f32_16x16x32_bf16 v[20:23], a[164:167], a[128:131], v[20:23]\n"
    "ds_read_b64_tr_b16 a[202:203], v226 offset:58368\n"
    "v_accvgpr_write_b32 a72, 0\n"
    "v_mfma_f32_16x16x32_bf16 v[52:55], a[164:167], a[132:135], v[52:55]\n"
    "s_waitcnt lgkmcnt(9)\n"
    "ds_read_b64_tr_b16 a[204:205], v227 offset:57344\n"
    "v_mfma_f32_16x16x32_bf16 v[24:27], a[168:171], a[128:131], v[24:27]\n"
    "ds_read_b64_tr_b16 a[206:207], v227 offset:58368\n"
    "v_mfma_f32_16x16x32_bf16 v[56:59], a[168:171], a[132:135], v[56:59]\n"
    "s_add_u32 m0, s23, 102400\n"
    "v_accvgpr_write_b32 a73, 0\n"
    "v_mfma_f32_16x16x32_bf16 v[28:31], a[172:175], a[128:131], v[28:31]\n"
    "buffer_load_dwordx4 v236, s[60:63], s30 offen lds\n"
    "v_mfma_f32_16x16x32_bf16 v[60:63], a[172:175], a[132:135], v[60:63]\n"
    "ds_read_b64_tr_b16 a[208:209], v228 offset:57344\n"
    "v_mfma_f32_16x16x32_bf16 v[92:95], a[172:175], a[136:139], v[92:95]\n"
    "s_waitcnt lgkmcnt(8)\n"
    "ds_read_b64_tr_b16 a[210:211], v228 offset:58368\n"
    "v_accvgpr_write_b32 a74, 0\n"
    "v_mfma_f32_16x16x32_bf16 v[124:127], a[172:175], a[140:143], v[124:127]\n"
    "ds_read_b64_tr_b16 a[212:213], v229 offset:57344\n"
    "v_mfma_f32_16x16x32_bf16 v[88:91], a[168:171], a[136:139], v[88:91]\n"
    "ds_read_b64_tr_b16 a[214:215], v229 offset:58368\n"
    "s_add_u32 m0, s23, 103424\n"
    "v_mfma_f32_16x16x32_bf16 v[120:123], a[168:171], a[140:143], v[120:123]\n"
    "buffer_load_dwordx4 v237, s[60:63], s30 offen lds\n"
    "v_mfma_f32_16x16x32_bf16 v[84:87], a[164:167], a[136:139], v[84:87]\n"
    "ds_read_b64_tr_b16 a[216:217], v230 offset:57344\n"
    "v_mfma_f32_16x16x32_bf16 v[116:119], a[164:167], a[140:143], v[116:119]\n"
    "v_accvgpr_write_b32 a75, 0\n"
    "v_mfma_f32_16x16x32_bf16 v[80:83], a[160:163], a[136:139], v[80:83]\n"
    "ds_read_b64_tr_b16 a[218:219], v230 offset:58368\n"
    "v_mfma_f32_16x16x32_bf16 v[112:115], a[160:163], a[140:143], v[112:115]\n"
    "ds_read_b64_tr_b16 a[220:221], v231 offset:57344\n"
    "s_add_u32 m0, s23, 104448\n"
    "v_accvgpr_write_b32 a76, 0\n"
    "v_mfma_f32_16x16x32_bf16 v[76:79], a[156:159], a[136:139], v[76:79]\n"
    "buffer_load_dwordx4 v236, s[60:63], s31 offen lds\n"
    "v_mfma_f32_16x16x32_bf16 v[108:111], a[156:159], a[140:143], v[108:111]\n"
    "ds_read_b64_tr_b16 a[222:223], v231 offset:58368\n"
    "v_mfma_f32_16x16x32_bf16 v[72:75], a[152:155], a[136:139], v[72:75]\n"
    "ds_read_b128 a[184:187], v250 offset:53248\n"
    "v_mfma_f32_16x16x32_bf16 v[104:107], a[152:155], a[140:143], v[104:107]\n"
    "ds_read_b128 a[188:191], v250 offset:55296\n"
    "v_accvgpr_write_b32 a77, 0\n"
    "v_mfma_f32_16x16x32_bf16 v[68:71], a[148:151], a[136:139], v[68:71]\n"
    "s_add_u32 m0, s23, 105472\n"
    "v_mfma_f32_16x16x32_bf16 v[100:103], a[148:151], a[140:143], v[100:103]\n"
    "buffer_load_dwordx4 v237, s[60:63], s31 offen lds\n"
    "v_mfma_f32_16x16x32_bf16 v[64:67], a[144:147], a[136:139], v[64:67]\n"
    "s_add_u32 s20, s20, 128\n"
    "s_add_u32 s21, s21, 128\n"
    "s_add_u32 s30, s30, 128\n"
    "s_add_u32 s31, s31, 128\n"
    "s_add_u32 s22, s22, 524288\n"
    "v_accvgpr_write_b32 a78, 0\n"
    "v_mfma_f32_16x16x32_bf16 v[96:99], a[144:147], a[140:143], v[96:99]\n"
    "s_waitcnt lgkmcnt(13)\n"
    "v_mfma_f32_16x16x32_bf16 v[0:3], a[192:195], a[176:179], v[0:3]\n"
    "v_accvgpr_write_b32 a79, 0\n"
    "v_mfma_f32_16x16x32_bf16 v[32:35], a[192:195], a[180:183], v[32:35]\n"
    "v_mfma_f32_16x16x32_bf16 v[4:7], a[196:199], a[176:179], v[4:7]\n"
    "v_accvgpr_write_b32 a80, 0\n"
    "v_mfma_f32_16x16x32_bf16 v[36:39], a[196:199], a[180:183], v[36:39]\n"
    "s_waitcnt vmcnt(12) lgkmcnt(0)\n"
    "s_barrier\n"
    "v_accvgpr_write_b32 a81, 0\n"
    "v_mfma_f32_16x16x32_bf16 v[8:11], a[200:203], a[176:179], v[8:11]\n"
    "ds_read_b128 a[128:131], v249 offset:0\n"
    "v_mfma_f32_16x16x32_bf16 v[40:43], a[200:203], a[180:183], v[40:43]\n"
    "ds_read_b128 a[132:135], v249 offset:2048\n"
    "s_getpc_b64 s[14:15]\n"
    "L_pc9_%=:\n"
    "v_lshlrev_b32 v251, 7, v255\n"
    "v_mfma_f32_16x16x32_bf16 v[12:15], a[204:207], a[176:179], v[12:15]\n"
    "ds_read_b64_tr_b16 a[144:145], v224 offset:0\n"
    "v_lshl_add_u32 v251, s8, 13, v251\n"
    "v_mfma_f32_16x16x32_bf16 v[44:47], a[204:207], a[180:183], v[44:47]\n"
    "ds_read_b64_tr_b16 a[146:147], v224 offset:1024\n"
    "v_min_u32 v251, L_end_%=-L_att_start_%=-4, v251\n"
    "s_add_u32 s14, s14, L_att_start_%=-L_pc9_%=\n"
    "s_addc_u32 s15, s15, 0\n"
    "v_mfma_f32_16x16x32_bf16 v[16:19], a[208:211], a[176:179], v[16:19]\n"
    "global_load_dword a233, v251, s[14:15]\n"
    "s_sub_u32 s14, 16, s13\n"
    "s_mul_i32 s14, s14, 32768\n"
    "s_mul_i32 s15, s12, 1082368\n"
    "s_add_u32 s60, s58, s15\n"
    "s_addc_u32 s61, s59, 0\n"
    "s_add_u32 s60, s60, 0x6000000\n"
    "s_addc_u32 s61, s61, 0\n"
    "s_and_b32 s61, s61, 0xffff\n"
    "s_mov_b32 s62, s14\n"
    "s_mov_b32 s63, 0x20000\n"
    "s_add_u32 s14, s14, 16384\n"
    "s_add_u32 s64, s58, s15\n"
    "s_addc_u32 s65, s59, 0\n"
    "s_add_u32 s64, s64, 0x607c000\n"
    "s_addc_u32 s65, s65, 0\n"
    "s_and_b32 s65, s65, 0xffff\n"
    "s_mov_b32 s66, s14\n"
    "s_mov_b32 s67, 0x20000\n"
    "s_add_u32 s68, s58, 0x8800000\n"
    "s_addc_u32 s69, s59, 0\n"
    "s_and_b32 s69, s69, 0xffff\n"
    "s_mov_b32 s70, 0x1000000\n"
    "s_mov_b32 s71, 0x20000\n"
    "v_mfma_f32_16x16x32_bf16 v[48:51], a[208:211], a[180:183], v[48:51]\n"
    "ds_read_b64_tr_b16 a[148:149], v225 offset:0\n"
    "v_lshlrev_b32 v198, 4, v255\n"
    "s_lshl_b32 s14, s8, 12\n"
    "v_mfma_f32_16x16x32_bf16 v[20:23], a[212:215], a[176:179], v[20:23]\n"
    "ds_read_b64_tr_b16 a[150:151], v225 offset:1024\n"
    "v_add_u32 v200, s14, v198\n"
    "s_lshl_b32 s14, s13, 3\n"
    "s_add_u32 s17, s14, s8\n"
    "s_sub_u32 s31, 127, s17\n"
    "s_sub_u32 s15, 7, s8\n"
    "s_add_u32 s18, s14, s15\n"
    "s_sub_u32 s30, 127, s18\n"
    "v_mfma_f32_16x16x32_bf16 v[52:55], a[212:215], a[180:183], v[52:55]\n"
    "ds_read_b64_tr_b16 a[152:153], v226 offset:0\n"
    "v_and_b32 v201, 15, v255\n"
    "v_mfma_f32_16x16x32_bf16 v[24:27], a[216:219], a[176:179], v[24:27]\n"
    "v_lshrrev_b32 v203, 4, v255\n"
    "v_lshlrev_b32 v203, 2, v203\n"
    "v_mfma_f32_16x16x32_bf16 v[56:59], a[216:219], a[180:183], v[56:59]\n"
    "ds_read_b64_tr_b16 a[154:155], v226 offset:1024\n"
    "v_sub_u32 v201, v201, v203\n"
    "v_mfma_f32_16x16x32_bf16 v[28:31], a[220:223], a[176:179], v[28:31]\n"
    "ds_read_b64_tr_b16 a[156:157], v227 offset:0\n"
    "v_mov_b32 v202, 0xff800000\n"
    "v_mfma_f32_16x16x32_bf16 v[60:63], a[220:223], a[180:183], v[60:63]\n"
    "ds_read_b64_tr_b16 a[158:159], v227 offset:1024\n"
    "v_cmp_le_i32 s[72:73], 0, v201\n"
    "v_mfma_f32_16x16x32_bf16 v[92:95], a[220:223], a[184:187], v[92:95]\n"
    "v_cmp_le_i32 s[74:75], 1, v201\n"
    "v_cmp_le_i32 s[76:77], 2, v201\n"
    "v_mfma_f32_16x16x32_bf16 v[124:127], a[220:223], a[188:191], v[124:127]\n"
    "ds_read_b64_tr_b16 a[160:161], v228 offset:0\n"
    "v_cmp_le_i32 s[78:79], 3, v201\n"
    "v_mfma_f32_16x16x32_bf16 v[88:91], a[216:219], a[184:187], v[88:91]\n"
    "ds_read_b64_tr_b16 a[162:163], v228 offset:1024\n"
    "v_mov_b32 v192, 0\n"
    "v_mfma_f32_16x16x32_bf16 v[120:123], a[216:219], a[188:191], v[120:123]\n"
    "ds_read_b64_tr_b16 a[164:165], v229 offset:0\n"
    "v_mov_b32 v193, 0\n"
    "v_mfma_f32_16x16x32_bf16 v[84:87], a[212:215], a[184:187], v[84:87]\n"
    "ds_read_b64_tr_b16 a[166:167], v229 offset:1024\n"
    "v_mov_b32 v194, 0\n"
    "v_mfma_f32_16x16x32_bf16 v[116:119], a[212:215], a[188:191], v[116:119]\n"
    "v_mov_b32 v195, 0\n"
    "s_lshl_b32 s14, s34, 7\n"
    "s_add_u32 s14, s14, 8128\n"
    "s_add_u32 s14, s14, 0x3400000\n"
    "v_min_u32 v204, 23, v255\n"
    "v_mfma_f32_16x16x32_bf16 v[80:83], a[208:211], a[184:187], v[80:83]\n"
    "ds_read_b64_tr_b16 a[168:169], v230 offset:0\n"
    "v_lshlrev_b32 v204, 3, v204\n"
    "v_mfma_f32_16x16x32_bf16 v[112:115], a[208:211], a[188:191], v[112:115]\n"
    "ds_read_b64_tr_b16 a[170:171], v230 offset:1024\n"
    "v_add_u32 v204, s14, v204\n"
    "s_lshl_b32 s14, s12, 6\n"
    "s_add_u32 s14, s14, 4096\n"
    "s_add_u32 s14, s14, 0x3400000\n"
    "v_mfma_f32_16x16x32_bf16 v[76:79], a[204:207], a[184:187], v[76:79]\n"
    "ds_read_b64_tr_b16 a[172:173], v231 offset:0\n"
    "v_min_u32 v205, 7, v255\n"
    "v_mfma_f32_16x16x32_bf16 v[108:111], a[204:207], a[188:191], v[108:111]\n"
    "v_lshlrev_b32 v205, 3, v205\n"
    "v_add_u32 v205, s14, v205\n"
    "v_mfma_f32_16x16x32_bf16 v[72:75], a[200:203], a[184:187], v[72:75]\n"
    "ds_read_b64_tr_b16 a[174:175], v231 offset:1024\n"
    "v_cmp_gt_u32 vcc, 8, v255\n"
    "s_nop 1\n"
    "v_mfma_f32_16x16x32_bf16 v[104:107], a[200:203], a[188:191], v[104:107]\n"
    "ds_read_b128 a[136:139], v249 offset:4096\n"
    "v_cndmask_b32 v204, v204, v205, vcc\n"
    "v_mfma_f32_16x16x32_bf16 v[68:71], a[196:199], a[184:187], v[68:71]\n"
    "ds_read_b128 a[140:143], v249 offset:6144\n"
    "v_mfma_f32_16x16x32_bf16 v[100:103], a[196:199], a[188:191], v[100:103]\n"
    "v_accvgpr_write_b32 a82, 0\n"
    "v_mfma_f32_16x16x32_bf16 v[64:67], a[192:195], a[184:187], v[64:67]\n"
    "v_mfma_f32_16x16x32_bf16 v[96:99], a[192:195], a[188:191], v[96:99]\n"
    "s_waitcnt lgkmcnt(13)\n"
    "v_accvgpr_write_b32 a83, 0\n"
    "v_mfma_f32_16x16x32_bf16 v[0:3], a[144:147], a[128:131], v[0:3]\n"
    "ds_read_b128 a[176:179], v250 offset:0\n"
    "v_mfma_f32_16x16x32_bf16 v[32:35], a[144:147], a[132:135], v[32:35]\n"
    "ds_read_b128 a[180:183], v250 offset:2048\n"
    "v_accvgpr_write_b32 a84, 0\n"
    "v_mfma_f32_16x16x32_bf16 v[4:7], a[148:151], a[128:131], v[4:7]\n"
    "v_mfma_f32_16x16x32_bf16 v[36:39], a[148:151], a[132:135], v[36:39]\n"
    "s_waitcnt lgkmcnt(11)\n"
    "ds_read_b64_tr_b16 a[192:193], v224 offset:8192\n"
    "v_accvgpr_write_b32 a85, 0\n"
    "v_mfma_f32_16x16x32_bf16 v[8:11], a[152:155], a[128:131], v[8:11]\n"
    "ds_read_b64_tr_b16 a[194:195], v224 offset:9216\n"
    "v_mfma_f32_16x16x32_bf16 v[40:43], a[152:155], a[132:135], v[40:43]\n"
    "v_mfma_f32_16x16x32_bf16 v[12:15], a[156:159], a[128:131], v[12:15]\n"
    "ds_read_b64_tr_b16 a[196:197], v225 offset:8192\n"
    "v_accvgpr_write_b32 a86, 0\n"
    "v_mfma_f32_16x16x32_bf16 v[44:47], a[156:159], a[132:135], v[44:47]\n"
    "s_waitcnt lgkmcnt(10)\n"
    "ds_read_b64_tr_b16 a[198:199], v225 offset:9216\n"
    "v_mfma_f32_16x16x32_bf16 v[16:19], a[160:163], a[128:131], v[16:19]\n"
    "v_accvgpr_write_b32 a87, 0\n"
    "v_mfma_f32_16x16x32_bf16 v[48:51], a[160:163], a[132:135], v[48:51]\n"
    "ds_read_b64_tr_b16 a[200:201], v226 offset:8192\n"
    "v_mfma_f32_16x16x32_bf16 v[20:23], a[164:167], a[128:131], v[20:23]\n"
    "ds_read_b64_tr_b16 a[202:203], v226 offset:9216\n"
    "v_mfma_f32_16x16x32_bf16 v[52:55], a[164:167], a[132:135], v[52:55]\n"
    "s_waitcnt lgkmcnt(9)\n"
    "v_accvgpr_write_b32 a88, 0\n"
    "v_mfma_f32_16x16x32_bf16 v[24:27], a[168:171], a[128:131], v[24:27]\n"
    "ds_read_b64_tr_b16 a[204:205], v227 offset:8192\n"
    "v_mfma_f32_16x16x32_bf16 v[56:59], a[168:171], a[132:135], v[56:59]\n"
    "ds_read_b64_tr_b16 a[206:207], v227 offset:9216\n"
    "v_mfma_f32_16x16x32_bf16 v[28:31], a[172:175], a[128:131], v[28:31]\n"
    "v_accvgpr_write_b32 a89, 0\n"
    "v_mfma_f32_16x16x32_bf16 v[60:63], a[172:175], a[132:135], v[60:63]\n"
    "ds_read_b64_tr_b16 a[208:209], v228 offset:8192\n"
    "v_mfma_f32_16x16x32_bf16 v[92:95], a[172:175], a[136:139], v[92:95]\n"
    "s_waitcnt lgkmcnt(8)\n"
    "ds_read_b64_tr_b16 a[210:211], v228 offset:9216\n"
    "v_accvgpr_write_b32 a90, 0\n"
    "v_mfma_f32_16x16x32_bf16 v[124:127], a[172:175], a[140:143], v[124:127]\n"
    "v_mfma_f32_16x16x32_bf16 v[88:91], a[168:171], a[136:139], v[88:91]\n"
    "ds_read_b64_tr_b16 a[212:213], v229 offset:8192\n"
    "v_mfma_f32_16x16x32_bf16 v[120:123], a[168:171], a[140:143], v[120:123]\n"
    "ds_read_b64_tr_b16 a[214:215], v229 offset:9216\n"
    "v_accvgpr_write_b32 a91, 0\n"
    "v_mfma_f32_16x16x32_bf16 v[84:87], a[164:167], a[136:139], v[84:87]\n"
    "v_mfma_f32_16x16x32_bf16 v[116:119], a[164:167], a[140:143], v[116:119]\n"
    "ds_read_b64_tr_b16 a[216:217], v230 offset:8192\n"
    "v_accvgpr_write_b32 a92, 0\n"
    "v_mfma_f32_16x16x32_bf16 v[80:83], a[160:163], a[136:139], v[80:83]\n"
    "ds_read_b64_tr_b16 a[218:219], v230 offset:9216\n"
    "v_mfma_f32_16x16x32_bf16 v[112:115], a[160:163], a[140:143], v[112:115]\n"
    "v_accvgpr_write_b32 a93, 0\n"
    "v_mfma_f32_16x16x32_bf16 v[76:79], a[156:159], a[136:139], v[76:79]\n"
    "ds_read_b64_tr_b16 a[220:221], v231 offset:8192\n"
    "v_mfma_f32_16x16x32_bf16 v[108:111], a[156:159], a[140:143], v[108:111]\n"
    "ds_read_b64_tr_b16 a[222:223], v231 offset:9216\n"
    "v_mfma_f32_16x16x32_bf16 v[72:75], a[152:155], a[136:139], v[72:75]\n"
    "v_accvgpr_write_b32 a94, 0\n"
    "v_mfma_f32_16x16x32_bf16 v[104:107], a[152:155], a[140:143], v[104:107]\n"
    "ds_read_b128 a[184:187], v250 offset:4096\n"
    "v_mfma_f32_16x16x32_bf16 v[68:71], a[148:151], a[136:139], v[68:71]\n"
    "ds_read_b128 a[188:191], v250 offset:6144\n"
    "v_mfma_f32_16x16x32_bf16 v[100:103], a[148:151], a[140:143], v[100:103]\n"
    "v_accvgpr_write_b32 a95, 0\n"
    "v_mfma_f32_16x16x32_bf16 v[64:67], a[144:147], a[136:139], v[64:67]\n"
    "v_mfma_f32_16x16x32_bf16 v[96:99], a[144:147], a[140:143], v[96:99]\n"
    "s_waitcnt lgkmcnt(13)\n"
    "v_accvgpr_write_b32 a96, 0\n"
    "v_mfma_f32_16x16x32_bf16 v[0:3], a[192:195], a[176:179], v[0:3]\n"
    "v_accvgpr_write_b32 a97, 0\n"
    "v_mfma_f32_16x16x32_bf16 v[32:35], a[192:195], a[180:183], v[32:35]\n"
    "v_mfma_f32_16x16x32_bf16 v[4:7], a[196:199], a[176:179], v[4:7]\n"
    "v_accvgpr_write_b32 a98, 0\n"
    "v_mfma_f32_16x16x32_bf16 v[36:39], a[196:199], a[180:183], v[36:39]\n"
    "s_waitcnt vmcnt(1) lgkmcnt(0)\n"
    "s_barrier\n"
    "v_mfma_f32_16x16x32_bf16 v[8:11], a[200:203], a[176:179], v[8:11]\n"
    "ds_read_b128 a[128:131], v253 offset:0\n"
    "v_accvgpr_write_b32 a99, 0\n"
    "v_mfma_f32_16x16x32_bf16 v[40:43], a[200:203], a[180:183], v[40:43]\n"
    "global_load_dwordx2 v[206:207], v204, s[58:59] sc1\n"
    "s_mov_b32 s16, 0\n"
    "s_mov_b32 s21, 0\n"
    "v_mfma_f32_16x16x32_bf16 v[12:15], a[204:207], a[176:179], v[12:15]\n"
    "ds_read_b128 a[132:135], v253 offset:2048\n"
    "v_mov_b32 v199, v198\n"
    "v_mfma_f32_16x16x32_bf16 v[44:47], a[204:207], a[180:183], v[44:47]\n"
    "ds_read_b64_tr_b16 a[144:145], v240 offset:0\n"
    "v_mfma_f32_16x16x32_bf16 v[16:19], a[208:211], a[176:179], v[16:19]\n"
    "ds_read_b64_tr_b16 a[146:147], v240 offset:1024\n"
    "v_mov_b32 v196, 0\n"
    "v_mfma_f32_16x16x32_bf16 v[48:51], a[208:211], a[180:183], v[48:51]\n"
    "v_mov_b32 v197, 0\n"
    "v_mfma_f32_16x16x32_bf16 v[20:23], a[212:215], a[176:179], v[20:23]\n"
    "ds_read_b64_tr_b16 a[148:149], v241 offset:0\n"
    "v_accvgpr_write_b32 a100, 0\n"
    "v_mfma_f32_16x16x32_bf16 v[52:55], a[212:215], a[180:183], v[52:55]\n"
    "ds_read_b64_tr_b16 a[150:151], v241 offset:1024\n"
    "v_mov_b32 v164, v193\n"
    "v_mfma_f32_16x16x32_bf16 v[24:27], a[216:219], a[176:179], v[24:27]\n"
    "ds_read_b64_tr_b16 a[152:153], v242 offset:0\n"
    "v_mfma_f32_16x16x32_bf16 v[56:59], a[216:219], a[180:183], v[56:59]\n"
    "ds_read_b64_tr_b16 a[154:155], v242 offset:1024\n"
    "v_mov_b32 v165, v193\n"
    "v_mfma_f32_16x16x32_bf16 v[28:31], a[220:223], a[176:179], v[28:31]\n"
    "v_accvgpr_write_b32 a101, 0\n"
    "v_mfma_f32_16x16x32_bf16 v[60:63], a[220:223], a[180:183], v[60:63]\n"
    "ds_read_b64_tr_b16 a[156:157], v243 offset:0\n"
    "v_mov_b32 v166, v193\n"
    "v_mfma_f32_16x16x32_bf16 v[92:95], a[220:223], a[184:187], v[92:95]\n"
    "ds_read_b64_tr_b16 a[158:159], v243 offset:1024\n"
    "v_mfma_f32_16x16x32_bf16 v[124:127], a[220:223], a[188:191], v[124:127]\n"
    "ds_read_b64_tr_b16 a[160:161], v244 offset:0\n"
    "v_mov_b32 v167, v193\n"
    "v_mfma_f32_16x16x32_bf16 v[88:91], a[216:219], a[184:187], v[88:91]\n"
    "ds_read_b64_tr_b16 a[162:163], v244 offset:1024\n"
    "v_mfma_f32_16x16x32_bf16 v[120:123], a[216:219], a[188:191], v[120:123]\n"
    "v_mov_b32 v168, v194\n"
    "v_mfma_f32_16x16x32_bf16 v[84:87], a[212:215], a[184:187], v[84:87]\n"
    "ds_read_b64_tr_b16 a[164:165], v245 offset:0\n"
    "v_mov_b32 v169, v194\n"
    "v_mfma_f32_16x16x32_bf16 v[116:119], a[212:215], a[188:191], v[116:119]\n"
    "ds_read_b64_tr_b16 a[166:167], v245 offset:1024\n"
    "v_accvgpr_write_b32 a102, 0\n"
    "v_mfma_f32_16x16x32_bf16 v[80:83], a[208:211], a[184:187], v[80:83]\n"
    "ds_read_b64_tr_b16 a[168:169], v246 offset:0\n"
    "v_mov_b32 v170, v194\n"
    "v_mfma_f32_16x16x32_bf16 v[112:115], a[208:211], a[188:191], v[112:115]\n"
    "ds_read_b64_tr_b16 a[170:171], v246 offset:1024\n"
    "v_mfma_f32_16x16x32_bf16 v[76:79], a[204:207], a[184:187], v[76:79]\n"
    "v_mov_b32 v171, v194\n"
    "v_mfma_f32_16x16x32_bf16 v[108:111], a[204:207], a[188:191], v[108:111]\n"
    "ds_read_b64_tr_b16 a[172:173], v247 offset:0\n"
    "v_accvgpr_write_b32 a103, 0\n"
    "v_mfma_f32_16x16x32_bf16 v[72:75], a[200:203], a[184:187], v[72:75]\n"
    "ds_read_b64_tr_b16 a[174:175], v247 offset:1024\n"
    "v_mov_b32 v172, v195\n"
    "v_mfma_f32_16x16x32_bf16 v[104:107], a[200:203], a[188:191], v[104:107]\n"
    "ds_read_b128 a[136:139], v253 offset:4096\n"
    "v_mfma_f32_16x16x32_bf16 v[68:71], a[196:199], a[184:187], v[68:71]\n"
    "ds_read_b128 a[140:143], v253 offset:6144\n"
    "v_mov_b32 v173, v195\n"
    "v_mfma_f32_16x16x32_bf16 v[100:103], a[196:199], a[188:191], v[100:103]\n"
    "v_mov_b32 v174, v195\n"
    "v_mfma_f32_16x16x32_bf16 v[64:67], a[192:195], a[184:187], v[64:67]\n"
    "v_mov_b32 v175, v195\n"
    "v_mfma_f32_16x16x32_bf16 v[96:99], a[192:195], a[188:191], v[96:99]\n"
    "s_waitcnt lgkmcnt(13)\n"
    "v_accvgpr_write_b32 a104, 0\n"
    "v_mfma_f32_16x16x32_bf16 v[0:3], a[144:147], a[128:131], v[0:3]\n"
    "ds_read_b128 a[176:179], v254 offset:0\n"
    "v_mov_b32 v180, v193\n"
    "v_mfma_f32_16x16x32_bf16 v[32:35], a[144:147], a[132:135], v[32:35]\n"
    "ds_read_b128 a[180:183], v254 offset:2048\n"
    "v_mfma_f32_16x16x32_bf16 v[4:7], a[148:151], a[128:131], v[4:7]\n"
    "v_mov_b32 v181, v193\n"
    "v_accvgpr_write_b32 a105, 0\n"
    "v_mfma_f32_16x16x32_bf16 v[36:39], a[148:151], a[132:135], v[36:39]\n"
    "s_waitcnt lgkmcnt(11)\n"
    "ds_read_b64_tr_b16 a[192:193], v240 offset:8192\n"
    "v_mov_b32 v182, v193\n"
    "v_mfma_f32_16x16x32_bf16 v[8:11], a[152:155], a[128:131], v[8:11]\n"
    "ds_read_b64_tr_b16 a[194:195], v240 offset:9216\n"
    "v_mfma_f32_16x16x32_bf16 v[40:43], a[152:155], a[132:135], v[40:43]\n"
    "v_mov_b32 v183, v193\n"
    "v_mfma_f32_16x16x32_bf16 v[12:15], a[156:159], a[128:131], v[12:15]\n"
    "ds_read_b64_tr_b16 a[196:197], v241 offset:8192\n"
    "v_mov_b32 v184, v194\n"
    "v_mfma_f32_16x16x32_bf16 v[44:47], a[156:159], a[132:135], v[44:47]\n"
    "s_waitcnt lgkmcnt(10)\n"
    "ds_read_b64_tr_b16 a[198:199], v241 offset:9216\n"
    "v_accvgpr_write_b32 a106, 0\n"
    "v_mfma_f32_16x16x32_bf16 v[16:19], a[160:163], a[128:131], v[16:19]\n"
    "v_mov_b32 v185, v194\n"
    "v_mfma_f32_16x16x32_bf16 v[48:51], a[160:163], a[132:135], v[48:51]\n"
    "ds_read_b64_tr_b16 a[200:201], v242 offset:8192\n"
    "v_mfma_f32_16x16x32_bf16 v[20:23], a[164:167], a[128:131], v[20:23]\n"
    "ds_read_b64_tr_b16 a[202:203], v242 offset:9216\n"
    "v_mov_b32 v186, v194\n"
    "v_mfma_f32_16x16x32_bf16 v[52:55], a[164:167], a[132:135], v[52:55]\n"
    "s_waitcnt lgkmcnt(9)\n"
    "v_mov_b32 v187, v194\n"
    "v_mfma_f32_16x16x32_bf16 v[24:27], a[168:171], a[128:131], v[24:27]\n"
    "ds_read_b64_tr_b16 a[204:205], v243 offset:8192\n"
    "v_accvgpr_write_b32 a107, 0\n"
    "v_mfma_f32_16x16x32_bf16 v[56:59], a[168:171], a[132:135], v[56:59]\n"
    "ds_read_b64_tr_b16 a[206:207], v243 offset:9216\n"
    "v_mov_b32 v188, v195\n"
    "v_mfma_f32_16x16x32_bf16 v[28:31], a[172:175], a[128:131], v[28:31]\n"
    "v_mov_b32 v189, v195\n"
    "v_mfma_f32_16x16x32_bf16 v[60:63], a[172:175], a[132:135], v[60:63]\n"
    "ds_read_b64_tr_b16 a[208:209], v244 offset:8192\n"
    "v_mfma_f32_16x16x32_bf16 v[92:95], a[172:175], a[136:139], v[92:95]\n"
    "s_waitcnt lgkmcnt(8)\n"
    "ds_read_b64_tr_b16 a[210:211], v244 offset:9216\n"
    "v_accvgpr_write_b32 a108, 0\n"
    "v_mfma_f32_16x16x32_bf16 v[124:127], a[172:175], a[140:143], v[124:127]\n"
    "v_mov_b32 v190, v195\n"
    "v_mfma_f32_16x16x32_bf16 v[88:91], a[168:171], a[136:139], v[88:91]\n"
    "ds_read_b64_tr_b16 a[212:213], v245 offset:8192\n"
    "v_mov_b32 v191, v195\n"
    "s_lshl_b32 s14, s16, 1\n"
    "s_cmp_gt_u32 s17, s14\n"
    "s_cselect_b64 vcc, -1, 0\n"
    "s_cmp_eq_u32 s17, s14\n"
    "s_cselect_b64 s[44:45], s[72:73], vcc\n"
    "s_cselect_b64 s[46:47], s[74:75], vcc\n"
    "s_cselect_b64 s[48:49], s[76:77], vcc\n"
    "s_cselect_b64 s[50:51], s[78:79], vcc\n"
    "v_mfma_f32_16x16x32_bf16 v[120:123], a[168:171], a[140:143], v[120:123]\n"
    "ds_read_b64_tr_b16 a[214:215], v245 offset:9216\n"
    "v_mfma_f32_16x16x32_bf16 v[84:87], a[164:167], a[136:139], v[84:87]\n"
    "v_cndmask_b32 v160, v202, v192, s[44:45]\n"
    "v_accvgpr_write_b32 a109, 0\n"
    "v_mfma_f32_16x16x32_bf16 v[116:119], a[164:167], a[140:143], v[116:119]\n"
    "ds_read_b64_tr_b16 a[216:217], v246 offset:8192\n"
    "v_cndmask_b32 v161, v202, v192, s[46:47]\n"
    "v_mfma_f32_16x16x32_bf16 v[80:83], a[160:163], a[136:139], v[80:83]\n"
    "ds_read_b64_tr_b16 a[218:219], v246 offset:9216\n"
    "v_mfma_f32_16x16x32_bf16 v[112:115], a[160:163], a[140:143], v[112:115]\n"
    "v_cndmask_b32 v162, v202, v192, s[48:49]\n"
    "v_mfma_f32_16x16x32_bf16 v[76:79], a[156:159], a[136:139], v[76:79]\n"
    "ds_read_b64_tr_b16 a[220:221], v247 offset:8192\n"
    "v_mfma_f32_16x16x32_bf16 v[108:111], a[156:159], a[140:143], v[108:111]\n"
    "ds_read_b64_tr_b16 a[222:223], v247 offset:9216\n"
    "v_cndmask_b32 v163, v202, v192, s[50:51]\n"
    "s_add_u32 s14, s14, 1\n"
    "s_cmp_gt_u32 s17, s14\n"
    "s_cselect_b64 vcc, -1, 0\n"
    "s_cmp_eq_u32 s17, s14\n"
    "s_cselect_b64 s[44:45], s[72:73], vcc\n"
    "s_cselect_b64 s[46:47], s[74:75], vcc\n"
    "s_cselect_b64 s[48:49], s[76:77], vcc\n"
    "s_cselect_b64 s[50:51], s[78:79], vcc\n"
    "v_mfma_f32_16x16x32_bf16 v[72:75], a[152:155], a[136:139], v[72:75]\n"
    "v_cndmask_b32 v176, v202, v192, s[44:45]\n"
    "v_accvgpr_write_b32 a110, 0\n"
    "v_mfma_f32_16x16x32_bf16 v[104:107], a[152:155], a[140:143], v[104:107]\n"
    "ds_read_b128 a[184:187], v254 offset:4096\n"
    "v_mfma_f32_16x16x32_bf16 v[68:71], a[148:151], a[136:139], v[68:71]\n"
    "ds_read_b128 a[188:191], v254 offset:6144\n"
    "v_cndmask_b32 v177, v202, v192, s[46:47]\n"
    "v_mfma_f32_16x16x32_bf16 v[100:103], a[148:151], a[140:143], v[100:103]\n"
    "v_cndmask_b32 v178, v202, v192, s[48:49]\n"
    "v_mfma_f32_16x16x32_bf16 v[64:67], a[144:147], a[136:139], v[64:67]\n"
    "v_cndmask_b32 v179, v202, v192, s[50:51]\n"
    "v_accvgpr_write_b32 a111, 0\n"
    "v_mfma_f32_16x16x32_bf16 v[96:99], a[144:147], a[140:143], v[96:99]\n"
    "s_waitcnt lgkmcnt(13)\n"
    "v_mfma_f32_16x16x32_bf16 v[0:3], a[192:195], a[176:179], v[0:3]\n"
    "v_accvgpr_write_b32 a112, 0\n"
    "v_mfma_f32_16x16x32_bf16 v[32:35], a[192:195], a[180:183], v[32:35]\n"
    "v_mfma_f32_16x16x32_bf16 v[4:7], a[196:199], a[176:179], v[4:7]\n"
    "v_accvgpr_write_b32 a113, 0\n"
    "v_mfma_f32_16x16x32_bf16 v[36:39], a[196:199], a[180:183], v[36:39]\n"
    "s_waitcnt vmcnt(0)\n"
    "v_cmp_ne_u64 s[98:99], s[28:29], v[206:207]\n"
    "s_nop 0\n"
    "s_cmp_lg_u64 s[98:99], 0\n"
    "s_cbranch_scc1 L_spin6_%=\n"
    "L_ok7_%=:\n"
    "s_waitcnt lgkmcnt(9)\n"
    "v_mfma_f32_16x16x32_bf16 v[8:11], a[200:203], a[176:179], v[8:11]\n"
    "s_lshl_b32 s19, s8, 12\n"
    "s_mov_b32 s20, 0\n"
    "s_mov_b32 m0, s19\n"
    "v_accvgpr_write_b32 a114, 0\n"
    "v_mfma_f32_16x16x32_bf16 v[40:43], a[200:203], a[180:183], v[40:43]\n"
    "buffer_load_dwordx4 v200, s[60:63], s20 offen offset:0 sc1 lds\n"
    "v_mfma_f32_16x16x32_bf16 v[12:15], a[204:207], a[176:179], v[12:15]\n"
    "v_mfma_f32_16x16x32_bf16 v[44:47], a[204:207], a[180:183], v[44:47]\n"
    "s_waitcnt lgkmcnt(5)\n"
    "v_accvgpr_write_b32 a115, 0\n"
    "v_mfma_f32_16x16x32_bf16 v[16:19], a[208:211], a[176:179], v[16:19]\n"
    "v_accvgpr_write_b32 a116, 0\n"
    "v_mfma_f32_16x16x32_bf16 v[48:51], a[208:211], a[180:183], v[48:51]\n"
    "v_mfma_f32_16x16x32_bf16 v[20:23], a[212:215], a[176:179], v[20:23]\n"
    "v_accvgpr_write_b32 a117, 0\n"
    "v_mfma_f32_16x16x32_bf16 v[52:55], a[212:215], a[180:183], v[52:55]\n"
    "s_waitcnt lgkmcnt(1)\n"
    "buffer_load_dwordx4 v200, s[60:63], s20 offen offset:1024 sc1 lds\n"
    "v_mfma_f32_16x16x32_bf16 v[24:27], a[216:219], a[176:179], v[24:27]\n"
    "v_mfma_f32_16x16x32_bf16 v[56:59], a[216:219], a[180:183], v[56:59]\n"
    "v_accvgpr_write_b32 a118, 0\n"
    "v_mfma_f32_16x16x32_bf16 v[28:31], a[220:223], a[176:179], v[28:31]\n"
    "v_accvgpr_write_b32 a119, 0\n"
    "v_mfma_f32_16x16x32_bf16 v[60:63], a[220:223], a[180:183], v[60:63]\n"
    "v_mfma_f32_16x16x32_bf16 v[92:95], a[220:223], a[184:187], v[92:95]\n"
    "s_waitcnt lgkmcnt(0)\n"
    "v_accvgpr_write_b32 a120, 0\n"
    "v_mfma_f32_16x16x32_bf16 v[124:127], a[220:223], a[188:191], v[124:127]\n"
    "v_mfma_f32_16x16x32_bf16 v[88:91], a[216:219], a[184:187], v[88:91]\n"
    "buffer_load_dwordx4 v200, s[60:63], s20 offen offset:2048 sc1 lds\n"
    "v_mfma_f32_16x16x32_bf16 v[120:123], a[216:219], a[188:191], v[120:123]\n"
    "v_accvgpr_write_b32 a121, 0\n"
    "v_mfma_f32_16x16x32_bf16 v[84:87], a[212:215], a[184:187], v[84:87]\n"
    "v_accvgpr_write_b32 a122, 0\n"
    "v_mfma_f32_16x16x32_bf16 v[116:119], a[212:215], a[188:191], v[116:119]\n"
    "v_mfma_f32_16x16x32_bf16 v[80:83], a[208:211], a[184:187], v[80:83]\n"
    "v_accvgpr_write_b32 a123, 0\n"
    "v_mfma_f32_16x16x32_bf16 v[112:115], a[208:211], a[188:191], v[112:115]\n"
    "v_mfma_f32_16x16x32_bf16 v[76:79], a[204:207], a[184:187], v[76:79]\n"
    "v_accvgpr_write_b32 a124, 0\n"
    "v_mfma_f32_16x16x32_bf16 v[108:111], a[204:207], a[188:191], v[108:111]\n"
    "buffer_load_dwordx4 v200, s[60:63], s20 offen offset:3072 sc1 lds\n"
    "s_add_u32 s20, s20, 16384\n"
    "s_add_u32 s19, s19, 32768\n"
    "s_and_b32 s19, s19, 0x1ffff\n"
    "v_mfma_f32_16x16x32_bf16 v[72:75], a[200:203], a[184:187], v[72:75]\n"
    "v_accvgpr_write_b32 a125, 0\n"
    "v_mfma_f32_16x16x32_bf16 v[104:107], a[200:203], a[188:191], v[104:107]\n"
    "v_mfma_f32_16x16x32_bf16 v[68:71], a[196:199], a[184:187], v[68:71]\n"
    "v_accvgpr_write_b32 a126, 0\n"
    "v_mfma_f32_16x16x32_bf16 v[100:103], a[196:199], a[188:191], v[100:103]\n"
    "v_mfma_f32_16x16x32_bf16 v[64:67], a[192:195], a[184:187], v[64:67]\n"
    "v_accvgpr_write_b32 a127, 0\n"
    "v_mfma_f32_16x16x32_bf16 v[96:99], a[192:195], a[188:191], v[96:99]\n"
    "s_mov_b32 s26, 0x3e0293ee\n"
    "s_mov_b32 m0, s19\n"
    "s_nop 0\n"
    "buffer_load_dwordx4 v200, s[60:63], s20 offen offset:0 sc1 lds\n"
    "s_mov_b32 s27, 0x3e0293ee\n"
    "v_pk_mul_f32 v[0:1], v[0:1], s[26:27]\n"
    "v_pk_mul_f32 v[2:3], v[2:3], s[26:27]\n"
    "v_pk_mul_f32 v[4:5], v[4:5], s[26:27]\n"
    "v_pk_mul_f32 v[6:7], v[6:7], s[26:27]\n"
    "v_cvt_pk_bf16_f32 v0, v0, v1\n"
    "v_cvt_pk_bf16_f32 v1, v2, v3\n"
    "buffer_load_dwordx4 v200, s[60:63], s20 offen offset:1024 sc1 lds\n"
    "v_cvt_pk_bf16_f32 v2, v4, v5\n"
    "v_cvt_pk_bf16_f32 v3, v6, v7\n"
    "v_pk_mul_f32 v[8:9], v[8:9], s[26:27]\n"
    "v_pk_mul_f32 v[10:11], v[10:11], s[26:27]\n"
    "v_pk_mul_f32 v[12:13], v[12:13], s[26:27]\n"
    "v_pk_mul_f32 v[14:15], v[14:15], s[26:27]\n"
    "v_cvt_pk_bf16_f32 v4, v8, v9\n"
    "buffer_load_dwordx4 v200, s[60:63], s20 offen offset:2048 sc1 lds\n"
    "v_cvt_pk_bf16_f32 v5, v10, v11\n"
    "v_cvt_pk_bf16_f32 v6, v12, v13\n"
    "v_cvt_pk_bf16_f32 v7, v14, v15\n"
    "v_pk_mul_f32 v[16:17], v[16:17], s[26:27]\n"
    "v_pk_mul_f32 v[18:19], v[18:19], s[26:27]\n"
    "v_pk_mul_f32 v[20:21], v[20:21], s[26:27]\n"
    "v_pk_mul_f32 v[22:23], v[22:23], s[26:27]\n"
    "buffer_load_dwordx4 v200, s[60:63], s20 offen offset:3072 sc1 lds\n"
    "v_cvt_pk_bf16_f32 v8, v16, v17\n"
    "v_cvt_pk_bf16_f32 v9, v18, v19\n"
    "v_cvt_pk_bf16_f32 v10, v20, v21\n"
    "v_cvt_pk_bf16_f32 v11, v22, v23\n"
    "v_pk_mul_f32 v[24:25], v[24:25], s[26:27]\n"
    "v_pk_mul_f32 v[26:27], v[26:27], s[26:27]\n"
    "v_pk_mul_f32 v[28:29], v[28:29], s[26:27]\n"
    "s_add_u32 m0, s19, 16384\n"
    "s_nop 0\n"
    "buffer_load_dwordx4 v200, s[64:67], s20 offen offset:0 sc1 lds\n"
    "v_pk_mul_f32 v[30:31], v[30:31], s[26:27]\n"
    "v_cvt_pk_bf16_f32 v12, v24, v25\n"
    "v_cvt_pk_bf16_f32 v13, v26, v27\n"
    "v_cvt_pk_bf16_f32 v14, v28, v29\n"
    "v_cvt_pk_bf16_f32 v15, v30, v31\n"
    "v_pk_mul_f32 v[32:33], v[32:33], s[26:27]\n"
    "v_pk_mul_f32 v[34:35], v[34:35], s[26:27]\n"
    "buffer_load_dwordx4 v200, s[64:67], s20 offen offset:1024 sc1 lds\n"
    "v_pk_mul_f32 v[36:37], v[36:37], s[26:27]\n"
    "v_pk_mul_f32 v[38:39], v[38:39], s[26:27]\n"
    "v_cvt_pk_bf16_f32 v16, v32, v33\n"
    "v_cvt_pk_bf16_f32 v17, v34, v35\n"
    "v_cvt_pk_bf16_f32 v18, v36, v37\n"
    "v_cvt_pk_bf16_f32 v19, v38, v39\n"
    "v_pk_mul_f32 v[40:41], v[40:41], s[26:27]\n"
    "buffer_load_dwordx4 v200, s[64:67], s20 offen offset:2048 sc1 lds\n"
    "v_pk_mul_f32 v[42:43], v[42:43], s[26:27]\n"
    "v_pk_mul_f32 v[44:45], v[44:45], s[26:27]\n"
    "v_pk_mul_f32 v[46:47], v[46:47], s[26:27]\n"
    "v_cvt_pk_bf16_f32 v20, v40, v41\n"
    "v_cvt_pk_bf16_f32 v21, v42, v43\n"
    "v_cvt_pk_bf16_f32 v22, v44, v45\n"
    "v_cvt_pk_bf16_f32 v23, v46, v47\n"
    "buffer_load_dwordx4 v200, s[64:67], s20 offen offset:3072 sc1 lds\n"
    "v_pk_mul_f32 v[48:49], v[48:49], s[26:27]\n"
    "v_pk_mul_f32 v[50:51], v[50:51], s[26:27]\n"
    "v_pk_mul_f32 v[52:53], v[52:53], s[26:27]\n"
    "v_pk_mul_f32 v[54:55], v[54:55], s[26:27]\n"
    "v_cvt_pk_bf16_f32 v24, v48, v49\n"
    "v_cvt_pk_bf16_f32 v25, v50, v51\n"
    "v_cvt_pk_bf16_f32 v26, v52, v53\n"
    "s_add_u32 s20, s20, 16384\n"
    "v_cvt_pk_bf16_f32 v27, v54, v55\n"
    "v_pk_mul_f32 v[56:57], v[56:57], s[26:27]\n"
    "v_pk_mul_f32 v[58:59], v[58:59], s[26:27]\n"
    "v_pk_mul_f32 v[60:61], v[60:61], s[26:27]\n"
    "v_pk_mul_f32 v[62:63], v[62:63], s[26:27]\n"
    "v_cvt_pk_bf16_f32 v28, v56, v57\n"
    "v_cvt_pk_bf16_f32 v29, v58, v59\n"
    "s_add_u32 s19, s19, 32768\n"
    "v_cvt_pk_bf16_f32 v30, v60, v61\n"
    "v_cvt_pk_bf16_f32 v31, v62, v63\n"
    "v_pk_mul_f32 v[64:65], v[64:65], s[26:27]\n"
    "v_pk_mul_f32 v[66:67], v[66:67], s[26:27]\n"
    "v_pk_mul_f32 v[68:69], v[68:69], s[26:27]\n"
    "v_pk_mul_f32 v[70:71], v[70:71], s[26:27]\n"
    "v_cvt_pk_bf16_f32 v32, v64, v65\n"
    "s_and_b32 s19, s19, 0x1ffff\n"
    "v_cvt_pk_bf16_f32 v33, v66, v67\n"
    "v_cvt_pk_bf16_f32 v34, v68, v69\n"
    "v_cvt_pk_bf16_f32 v35, v70, v71\n"
    "v_pk_mul_f32 v[72:73], v[72:73], s[26:27]\n"
    "v_pk_mul_f32 v[74:75], v[74:75], s[26:27]\n"
    "v_pk_mul_f32 v[76:77], v[76:77], s[26:27]\n"
    "v_pk_mul_f32 v[78:79], v[78:79], s[26:27]\n"
    "s_waitcnt vmcnt(8)\n"
    "s_barrier\n"
    "ds_read_b128 a[192:195], v199 offset:0\n"
    "ds_read_b128 a[196:199], v199 offset:1024\n"
    "ds_read_b128 a[200:203], v199 offset:2048\n"
    "ds_read_b128 a[204:207], v199 offset:3072\n"
    "ds_read_b128 a[208:211], v199 offset:4096\n"
    "ds_read_b128 a[212:215], v199 offset:5120\n"
    "ds_read_b128 a[216:219], v199 offset:6144\n"
    "ds_read_b128 a[220:223], v199 offset:7168\n"
    "v_cvt_pk_bf16_f32 v36, v72, v73\n"
    "v_cvt_pk_bf16_f32 v37, v74, v75\n"
    "v_cvt_pk_bf16_f32 v38, v76, v77\n"
    "v_cvt_pk_bf16_f32 v39, v78, v79\n"
    "v_pk_mul_f32 v[80:81], v[80:81], s[26:27]\n"
    "v_pk_mul_f32 v[82:83], v[82:83], s[26:27]\n"
    "v_pk_mul_f32 v[84:85], v[84:85], s[26:27]\n"
    "v_pk_mul_f32 v[86:87], v[86:87], s[26:27]\n"
    "v_cvt_pk_bf16_f32 v40, v80, v81\n"
    "v_cvt_pk_bf16_f32 v41, v82, v83\n"
    "v_cvt_pk_bf16_f32 v42, v84, v85\n"
    "v_cvt_pk_bf16_f32 v43, v86, v87\n"
    "v_pk_mul_f32 v[88:89], v[88:89], s[26:27]\n"
    "v_pk_mul_f32 v[90:91], v[90:91], s[26:27]\n"
    "v_pk_mul_f32 v[92:93], v[92:93], s[26:27]\n"
    "v_pk_mul_f32 v[94:95], v[94:95], s[26:27]\n"
    "v_cvt_pk_bf16_f32 v44, v88, v89\n"
    "v_cvt_pk_bf16_f32 v45, v90, v91\n"
    "v_cvt_pk_bf16_f32 v46, v92, v93\n"
    "v_cvt_pk_bf16_f32 v47, v94, v95\n"
    "v_pk_mul_f32 v[96:97], v[96:97], s[26:27]\n"
    "v_pk_mul_f32 v[98:99], v[98:99], s[26:27]\n"
    "v_pk_mul_f32 v[100:101], v[100:101], s[26:27]\n"
    "v_pk_mul_f32 v[102:103], v[102:103], s[26:27]\n"
    "v_cvt_pk_bf16_f32 v48, v96, v97\n"
    "v_cvt_pk_bf16_f32 v49, v98, v99\n"
    "v_cvt_pk_bf16_f32 v50, v100, v101\n"
    "v_cvt_pk_bf16_f32 v51, v102, v103\n"
    "v_pk_mul_f32 v[104:105], v[104:105], s[26:27]\n"
    "v_pk_mul_f32 v[106:107], v[106:107], s[26:27]\n"
    "v_pk_mul_f32 v[108:109], v[108:109], s[26:27]\n"
    "v_pk_mul_f32 v[110:111], v[110:111], s[26:27]\n"
    "v_cvt_pk_bf16_f32 v52, v104, v105\n"
    "v_cvt_pk_bf16_f32 v53, v106, v107\n"
    "v_cvt_pk_bf16_f32 v54, v108, v109\n"
    "v_cvt_pk_bf16_f32 v55, v110, v111\n"
    "v_pk_mul_f32 v[112:113], v[112:113], s[26:27]\n"
    "v_pk_mul_f32 v[114:115], v[114:115], s[26:27]\n"
    "v_pk_mul_f32 v[116:117], v[116:117], s[26:27]\n"
    "v_pk_mul_f32 v[118:119], v[118:119], s[26:27]\n"
    "v_cvt_pk_bf16_f32 v56, v112, v113\n"
    "v_cvt_pk_bf16_f32 v57, v114, v115\n"
    "v_cvt_pk_bf16_f32 v58, v116, v117\n"
    "v_cvt_pk_bf16_f32 v59, v118, v119\n"
    "v_pk_mul_f32 v[120:121], v[120:121], s[26:27]\n"
    "v_pk_mul_f32 v[122:123], v[122:123], s[26:27]\n"
    "v_pk_mul_f32 v[124:125], v[124:125], s[26:27]\n"
    "v_pk_mul_f32 v[126:127], v[126:127], s[26:27]\n"
    "v_cvt_pk_bf16_f32 v60, v120, v121\n"
    "v_cvt_pk_bf16_f32 v61, v122, v123\n"
    "v_cvt_pk_bf16_f32 v62, v124, v125\n"
    "v_cvt_pk_bf16_f32 v63, v126, v127\n"
    "v_mov_b32 v248, 0x3f803f80\n"
    "v_mov_b32 v249, 0x3f803f80\n"
    "v_mov_b32 v250, 0x3f803f80\n"
    "v_mov_b32 v251, 0x3f803f80\n"
    "s_mov_b32 s24, 0\n"
    "s_mov_b32 s23, 0x5d800000\n"
    "s_mov_b32 s95, 0\n"
    "L_att_start_%=:\n"
    "s_mov_b64 s[26:27], 0\n"
    "v_mov_b32 v240, 0\n"
    "v_mov_b32 v241, 0\n"
    "v_mov_b32 v242, 0\n"
    "v_mov_b32 v243, 0\n"
    "v_mov_b32 v244, 0\n"
    "v_mov_b32 v245, 0\n"
    "v_mov_b32 v246, 0\n"
    "v_mov_b32 v247, 0\n"
    "s_waitcnt lgkmcnt(0)\n"
    "v_mfma_f32_16x16x32_bf16 v[64:67], a[192:195], v[0:3], v[160:163]\n"
    "s_mov_b32 m0, s19\n"
    "s_nop 0\n"
    "buffer_load_dwordx4 v200, s[60:63], s20 offen offset:0 sc1 lds\n"
    "v_mfma_f32_16x16x32_bf16 v[64:67], a[196:199], v[4:7], v[64:67]\n"
    "s_add_u32 s16, s16, 1\n"
    "ds_read_b128 a[128:131], v199 offset:8192\n"
    "v_mfma_f32_16x16x32_bf16 v[64:67], a[200:203], v[8:11], v[64:67]\n"
    "s_add_u32 s21, s21, 32768\n"
    "v_mfma_f32_16x16x32_bf16 v[64:67], a[204:207], v[12:15], v[64:67]\n"
    "s_and_b32 s21, s21, 0x1ffff\n"
    "buffer_load_dwordx4 v200, s[60:63], s20 offen offset:1024 sc1 lds\n"
    "v_mfma_f32_16x16x32_bf16 v[68:71], a[192:195], v[16:19], v[164:167]\n"
    "ds_read_b128 a[132:135], v199 offset:9216\n"
    "v_mfma_f32_16x16x32_bf16 v[68:71], a[196:199], v[20:23], v[68:71]\n"
    "v_mfma_f32_16x16x32_bf16 v[68:71], a[200:203], v[24:27], v[68:71]\n"
    "v_mfma_f32_16x16x32_bf16 v[68:71], a[204:207], v[28:31], v[68:71]\n"
    "buffer_load_dwordx4 v200, s[60:63], s20 offen offset:2048 sc1 lds\n"
    "v_mfma_f32_16x16x32_bf16 v[72:75], a[192:195], v[32:35], v[168:171]\n"
    "ds_read_b128 a[136:139], v199 offset:10240\n"
    "v_mfma_f32_16x16x32_bf16 v[72:75], a[196:199], v[36:39], v[72:75]\n"
    "v_mfma_f32_16x16x32_bf16 v[72:75], a[200:203], v[40:43], v[72:75]\n"
    "v_mfma_f32_16x16x32_bf16 v[72:75], a[204:207], v[44:47], v[72:75]\n"
    "buffer_load_dwordx4 v200, s[60:63], s20 offen offset:3072 sc1 lds\n"
    "v_mfma_f32_16x16x32_bf16 v[76:79], a[192:195], v[48:51], v[172:175]\n"
    "ds_read_b128 a[140:143], v199 offset:11264\n"
    "v_mfma_f32_16x16x32_bf16 v[76:79], a[196:199], v[52:55], v[76:79]\n"
    "v_mfma_f32_16x16x32_bf16 v[76:79], a[200:203], v[56:59], v[76:79]\n"
    "v_mfma_f32_16x16x32_bf16 v[76:79], a[204:207], v[60:63], v[76:79]\n"
    "s_add_u32 m0, s19, 16384\n"
    "s_nop 0\n"
    "buffer_load_dwordx4 v200, s[64:67], s20 offen offset:0 sc1 lds\n"
    "v_mfma_f32_16x16x32_bf16 v[80:83], a[208:211], v[0:3], v[176:179]\n"
    "ds_read_b128 a[144:147], v199 offset:12288\n"
    "v_mfma_f32_16x16x32_bf16 v[80:83], a[212:215], v[4:7], v[80:83]\n"
    "v_mfma_f32_16x16x32_bf16 v[80:83], a[216:219], v[8:11], v[80:83]\n"
    "buffer_load_dwordx4 v200, s[64:67], s20 offen offset:1024 sc1 lds\n"
    "v_mfma_f32_16x16x32_bf16 v[80:83], a[220:223], v[12:15], v[80:83]\n"
    "v_mfma_f32_16x16x32_bf16 v[84:87], a[208:211], v[16:19], v[180:183]\n"
    "ds_read_b128 a[148:151], v199 offset:13312\n"
    "v_mfma_f32_16x16x32_bf16 v[84:87], a[212:215], v[20:23], v[84:87]\n"
    "v_mfma_f32_16x16x32_bf16 v[84:87], a[216:219], v[24:27], v[84:87]\n"
    "buffer_load_dwordx4 v200, s[64:67], s20 offen offset:2048 sc1 lds\n"
    "v_mfma_f32_16x16x32_bf16 v[84:87], a[220:223], v[28:31], v[84:87]\n"
    "v_mfma_f32_16x16x32_bf16 v[88:91], a[208:211], v[32:35], v[184:187]\n"
    "ds_read_b128 a[152:155], v199 offset:14336\n"
    "v_mfma_f32_16x16x32_bf16 v[88:91], a[212:215], v[36:39], v[88:91]\n"
    "v_mfma_f32_16x16x32_bf16 v[88:91], a[216:219], v[40:43], v[88:91]\n"
    "buffer_load_dwordx4 v200, s[64:67], s20 offen offset:3072 sc1 lds\n"
    "s_add_u32 s20, s20, 16384\n"
    "s_add_u32 s19, s19, 32768\n"
    "s_and_b32 s19, s19, 0x1ffff\n"
    "v_mfma_f32_16x16x32_bf16 v[88:91], a[220:223], v[44:47], v[88:91]\n"
    "v_mfma_f32_16x16x32_bf16 v[92:95], a[208:211], v[48:51], v[188:191]\n"
    "s_waitcnt vmcnt(8)\n"
    "s_barrier\n"
    "v_mfma_f32_16x16x32_bf16 v[92:95], a[212:215], v[52:55], v[92:95]\n"
    "ds_read_b128 a[156:159], v199 offset:15360\n"
    "v_mfma_f32_16x16x32_bf16 v[92:95], a[216:219], v[56:59], v[92:95]\n"
    "v_mfma_f32_16x16x32_bf16 v[92:95], a[220:223], v[60:63], v[92:95]\n"
    "v_add_u32 v199, s21, v198\n"
    "s_cmp_lg_u32 s24, 0\n"
    "s_cbranch_scc1 L_havemax3_%=\n"
    "v_max3_f32 v192, v64, v65, v66\n"
    "v_max3_f32 v193, v68, v69, v70\n"
    "v_max3_f32 v194, v72, v73, v74\n"
    "v_max3_f32 v195, v76, v77, v78\n"
    "v_max3_f32 v192, v192, v67, v80\n"
    "v_max3_f32 v193, v193, v71, v84\n"
    "v_max3_f32 v194, v194, v75, v88\n"
    "v_max3_f32 v195, v195, v79, v92\n"
    "v_max3_f32 v192, v192, v81, v82\n"
    "v_max3_f32 v193, v193, v85, v86\n"
    "v_max3_f32 v194, v194, v89, v90\n"
    "v_max3_f32 v195, v195, v93, v94\n"
    "v_max_f32 v192, v192, v83\n"
    "v_max_f32 v193, v193, v87\n"
    "v_max_f32 v194, v194, v91\n"
    "v_max_f32 v195, v195, v95\n"
    "v_mov_b32 v208, v192\n"
    "v_mov_b32 v209, v193\n"
    "v_mov_b32 v210, v194\n"
    "v_mov_b32 v211, v195\n"
    "v_permlane32_swap_b32 v192, v208\n"
    "v_permlane32_swap_b32 v193, v209\n"
    "v_permlane32_swap_b32 v194, v210\n"
    "v_permlane32_swap_b32 v195, v211\n"
    "v_max_f32 v192, v192, v208\n"
    "v_max_f32 v193, v193, v209\n"
    "v_max_f32 v194, v194, v210\n"
    "v_max_f32 v195, v195, v211\n"
    "v_mov_b32 v208, v192\n"
    "v_mov_b32 v209, v193\n"
    "v_mov_b32 v210, v194\n"
    "v_mov_b32 v211, v195\n"
    "v_permlane16_swap_b32 v192, v208\n"
    "v_permlane16_swap_b32 v193, v209\n"
    "v_permlane16_swap_b32 v194, v210\n"
    "v_permlane16_swap_b32 v195, v211\n"
    "v_max_f32 v192, v192, v208\n"
    "v_max_f32 v193, v193, v209\n"
    "v_max_f32 v194, v194, v210\n"
    "v_max_f32 v195, v195, v211\n"
    "v_sub_f32 v192, 0xc2200000, v192\n"
    "v_sub_f32 v193, 0xc2200000, v193\n"
    "v_sub_f32 v194, 0xc2200000, v194\n"
    "v_sub_f32 v195, 0xc2200000, v195\n"
    "v_pk_add_f32 v[64:65], v[64:65], v[192:193] op_sel:[0,0] op_sel_hi:[1,0]\n"
    "v_pk_add_f32 v[66:67], v[66:67], v[192:193] op_sel:[0,0] op_sel_hi:[1,0]\n"
    "v_pk_add_f32 v[68:69], v[68:69], v[192:193] op_sel:[0,1] op_sel_hi:[1,1]\n"
    "v_pk_add_f32 v[70:71], v[70:71], v[192:193] op_sel:[0,1] op_sel_hi:[1,1]\n"
    "v_pk_add_f32 v[72:73], v[72:73], v[194:195] op_sel:[0,0] op_sel_hi:[1,0]\n"
    "v_pk_add_f32 v[74:75], v[74:75], v[194:195] op_sel:[0,0] op_sel_hi:[1,0]\n"
    "v_pk_add_f32 v[76:77], v[76:77], v[194:195] op_sel:[0,1] op_sel_hi:[1,1]\n"
    "v_pk_add_f32 v[78:79], v[78:79], v[194:195] op_sel:[0,1] op_sel_hi:[1,1]\n"
    "v_pk_add_f32 v[80:81], v[80:81], v[192:193] op_sel:[0,0] op_sel_hi:[1,0]\n"
    "v_pk_add_f32 v[82:83], v[82:83], v[192:193] op_sel:[0,0] op_sel_hi:[1,0]\n"
    "v_pk_add_f32 v[84:85], v[84:85], v[192:193] op_sel:[0,1] op_sel_hi:[1,1]\n"
    "v_pk_add_f32 v[86:87], v[86:87], v[192:193] op_sel:[0,1] op_sel_hi:[1,1]\n"
    "v_pk_add_f32 v[88:89], v[88:89], v[194:195] op_sel:[0,0] op_sel_hi:[1,0]\n"
    "v_pk_add_f32 v[90:91], v[90:91], v[194:195] op_sel:[0,0] op_sel_hi:[1,0]\n"
    "v_pk_add_f32 v[92:93], v[92:93], v[194:195] op_sel:[0,1] op_sel_hi:[1,1]\n"
    "v_pk_add_f32 v[94:95], v[94:95], v[194:195] op_sel:[0,1] op_sel_hi:[1,1]\n"
    "L_havemax3_%=:\n"
    "v_mov_b32 v164, v193\n"
    "v_mov_b32 v165, v193\n"
    "v_mov_b32 v166, v193\n"
    "v_mov_b32 v167, v193\n"
    "v_mov_b32 v168, v194\n"
    "v_mov_b32 v169, v194\n"
    "v_mov_b32 v170, v194\n"
    "v_mov_b32 v171, v194\n"
    "v_mov_b32 v172, v195\n"
    "v_mov_b32 v173, v195\n"
    "v_mov_b32 v174, v195\n"
    "v_mov_b32 v175, v195\n"
    "v_mov_b32 v180, v193\n"
    "v_mov_b32 v181, v193\n"
    "v_mov_b32 v182, v193\n"
    "v_mov_b32 v183, v193\n"
    "v_mov_b32 v184, v194\n"
    "v_mov_b32 v185, v194\n"
    "v_mov_b32 v186, v194\n"
    "v_mov_b32 v187, v194\n"
    "v_mov_b32 v188, v195\n"
    "v_mov_b32 v189, v195\n"
    "v_mov_b32 v190, v195\n"
    "v_mov_b32 v191, v195\n"
    "s_lshl_b32 s14, s16, 1\n"
    "s_cmp_gt_u32 s17, s14\n"
    "s_cselect_b64 vcc, -1, 0\n"
    "s_cmp_eq_u32 s17, s14\n"
    "s_cselect_b64 s[44:45], s[72:73], vcc\n"
    "s_cselect_b64 s[46:47], s[74:75], vcc\n"
    "s_cselect_b64 s[48:49], s[76:77], vcc\n"
    "s_cselect_b64 s[50:51], s[78:79], vcc\n"
    "v_cndmask_b32 v160, v202, v192, s[44:45]\n"
    "v_cndmask_b32 v161, v202, v192, s[46:47]\n"
    "v_cndmask_b32 v162, v202, v192, s[48:49]\n"
    "v_cndmask_b32 v163, v202, v192, s[50:51]\n"
    "s_add_u32 s14, s14, 1\n"
    "s_cmp_gt_u32 s17, s14\n"
    "s_cselect_b64 vcc, -1, 0\n"
    "s_cmp_eq_u32 s17, s14\n"
    "s_cselect_b64 s[44:45], s[72:73], vcc\n"
    "s_cselect_b64 s[46:47], s[74:75], vcc\n"
    "s_cselect_b64 s[48:49], s[76:77], vcc\n"
    "s_cselect_b64 s[50:51], s[78:79], vcc\n"
    "v_cndmask_b32 v176, v202, v192, s[44:45]\n"
    "v_cndmask_b32 v177, v202, v192, s[46:47]\n"
    "v_cndmask_b32 v178, v202, v192, s[48:49]\n"
    "v_cndmask_b32 v179, v202, v192, s[50:51]\n"
    "s_lshl_b32 s22, s13, 2\n"
    "s_lshl_b32 s14, s9, 2\n"
    "s_add_u32 s14, s14, s8\n"
    "s_mul_i32 s14, s14, 4096\n"
    "s_add_u32 s14, s14, 0\n"
    "v_min_u32 v203, 31, v255\n"
    "v_lshlrev_b32 v203, 7, v203\n"
    "v_add_u32 v203, s14, v203\n"
    "global_load_dword v206, v203, s[54:55]\n"
    "s_lshl_b32 s14, s9, 2\n"
    "s_add_u32 s14, s14, s8\n"
    "s_mul_i32 s14, s14, 512\n"
    "s_add_u32 s14, s14, 0\n"
    "v_min_u32 v203, 3, v255\n"
    "v_lshlrev_b32 v203, 7, v203\n"
    "v_add_u32 v203, s14, v203\n"
    "global_load_dword v207, v203, s[42:43]\n"
    "s_waitcnt lgkmcnt(0)\n"
    "v_mfma_f32_16x16x32_bf16 v[96:99], a[128:131], v[0:3], v[160:163]\n"
    "s_mov_b32 m0, s19\n"
    "v_exp_f32 v64, v64\n"
    "v_exp_f32 v65, v65\n"
    "v_mfma_f32_16x16x32_bf16 v[96:99], a[132:135], v[4:7], v[96:99]\n"
    "s_add_u32 s16, s16, 1\n"
    "ds_read_b128 a[192:195], v199 offset:0\n"
    "buffer_load_dwordx4 v200, s[60:63], s20 offen offset:0 sc1 lds\n"
    "v_add_f32 v196, v196, v64\n"
    "v_mfma_f32_16x16x32_bf16 v[96:99], a[136:139], v[8:11], v[96:99]\n"
    "ds_read_b128 a[196:199], v199 offset:1024\n"
    "v_exp_f32 v66, v66\n"
    "v_add_f32 v196, v196, v65\n"
    "v_mfma_f32_16x16x32_bf16 v[96:99], a[140:143], v[12:15], v[96:99]\n"
    "v_exp_f32 v67, v67\n"
    "v_cvt_pk_bf16_f32 v128, v64, v65\n"
    "v_mfma_f32_16x16x32_bf16 v[100:103], a[128:131], v[16:19], v[164:167]\n"
    "s_lshl_b32 s14, s16, 1\n"
    "s_cmp_gt_u32 s18, s14\n"
    "ds_read_b128 a[200:203], v199 offset:2048\n"
    "v_exp_f32 v68, v68\n"
    "v_add_f32 v196, v196, v66\n"
    "v_mfma_f32_16x16x32_bf16 v[100:103], a[132:135], v[20:23], v[100:103]\n"
    "s_cselect_b64 vcc, -1, 0\n"
    "s_cmp_eq_u32 s18, s14\n"
    "v_exp_f32 v69, v69\n"
    "v_add_f32 v196, v196, v67\n"
    "v_cvt_pk_bf16_f32 v129, v66, v67\n"
    "v_mfma_f32_16x16x32_bf16 v[100:103], a[136:139], v[24:27], v[100:103]\n"
    "s_cselect_b64 s[44:45], s[72:73], vcc\n"
    "s_cselect_b64 s[46:47], s[74:75], vcc\n"
    "v_exp_f32 v70, v70\n"
    "v_exp_f32 v71, v71\n"
    "v_mfma_f32_16x16x32_bf16 v[100:103], a[140:143], v[28:31], v[100:103]\n"
    "s_cselect_b64 s[48:49], s[76:77], vcc\n"
    "s_cselect_b64 s[50:51], s[78:79], vcc\n"
    "ds_read_b128 a[204:207], v199 offset:3072\n"
    "v_exp_f32 v72, v72\n"
    "v_add_f32 v197, v197, v68\n"
    "v_mfma_f32_16x16x32_bf16 v[104:107], a[128:131], v[32:35], v[168:171]\n"
    "ds_read_b128 a[224:227], v199 offset:16384\n"
    "v_exp_f32 v73, v73\n"
    "v_add_f32 v197, v197, v69\n"
    "v_mfma_f32_16x16x32_bf16 v[104:107], a[132:135], v[36:39], v[104:107]\n"
    "buffer_load_dwordx4 v200, s[60:63], s20 offen offset:1024 sc1 lds\n"
    "v_cvt_pk_bf16_f32 v132, v68, v69\n"
    "v_add_f32 v197, v197, v70\n"
    "v_mfma_f32_16x16x32_bf16 v[104:107], a[136:139], v[40:43], v[104:107]\n"
    "ds_read_b128 a[228:231], v199 offset:17408\n"
    "v_exp_f32 v74, v74\n"
    "v_add_f32 v197, v197, v71\n"
    "v_mfma_f32_16x16x32_bf16 v[104:107], a[140:143], v[44:47], v[104:107]\n"
    "v_exp_f32 v75, v75\n"
    "v_cvt_pk_bf16_f32 v133, v70, v71\n"
    "v_cvt_pk_bf16_f32 v136, v72, v73\n"
    "v_mfma_f32_16x16x32_bf16 v[108:111], a[128:131], v[48:51], v[172:175]\n"
    "v_exp_f32 v76, v76\n"
    "v_exp_f32 v77, v77\n"
    "v_mfma_f32_16x16x32_bf16 v[108:111], a[132:135], v[52:55], v[108:111]\n"
    "ds_read_b128 a[232:235], v199 offset:18432\n"
    "v_exp_f32 v78, v78\n"
    "v_cvt_pk_bf16_f32 v137, v74, v75\n"
    "v_mfma_f32_16x16x32_bf16 v[108:111], a[136:139], v[56:59], v[108:111]\n"
    "ds_read_b128 a[236:239], v199 offset:19456\n"
    "v_exp_f32 v79, v79\n"
    "v_cvt_pk_bf16_f32 v140, v76, v77\n"
    "v_mfma_f32_16x16x32_bf16 v[108:111], a[140:143], v[60:63], v[108:111]\n"
    "v_exp_f32 v80, v80\n"
    "v_cvt_pk_bf16_f32 v141, v78, v79\n"
    "v_cndmask_b32 v164, v202, v193, s[44:45]\n"
    "v_mfma_f32_16x16x32_bf16 v[112:115], a[144:147], v[0:3], v[176:179]\n"
    "ds_read_b128 a[240:243], v199 offset:20480\n"
    "v_exp_f32 v81, v81\n"
    "v_cndmask_b32 v165, v202, v193, s[46:47]\n"
    "v_cndmask_b32 v166, v202, v193, s[48:49]\n"
    "v_cndmask_b32 v167, v202, v193, s[50:51]\n"
    "v_mfma_f32_16x16x32_bf16 v[112:115], a[148:151], v[4:7], v[112:115]\n"
    "s_add_u32 s14, s14, 1\n"
    "s_cmp_gt_u32 s18, s14\n"
    "buffer_load_dwordx4 v200, s[60:63], s20 offen offset:2048 sc1 lds\n"
    "v_add_f32 v196, v196, v80\n"
    "v_add_f32 v196, v196, v81\n"
    "v_mfma_f32_16x16x32_bf16 v[112:115], a[152:155], v[8:11], v[112:115]\n"
    "s_cselect_b64 vcc, -1, 0\n"
    "s_cmp_eq_u32 s18, s14\n"
    "v_exp_f32 v82, v82\n"
    "v_exp_f32 v83, v83\n"
    "v_mfma_f32_16x16x32_bf16 v[112:115], a[156:159], v[12:15], v[112:115]\n"
    "s_cselect_b64 s[44:45], s[72:73], vcc\n"
    "s_cselect_b64 s[46:47], s[74:75], vcc\n"
    "ds_read_b128 a[244:247], v199 offset:21504\n"
    "v_exp_f32 v84, v84\n"
    "v_cvt_pk_bf16_f32 v130, v80, v81\n"
    "v_mfma_f32_16x16x32_bf16 v[116:119], a[144:147], v[16:19], v[180:183]\n"
    "s_cselect_b64 s[48:49], s[76:77], vcc\n"
    "s_cselect_b64 s[50:51], s[78:79], vcc\n"
    "ds_read_b128 a[248:251], v199 offset:22528\n"
    "v_exp_f32 v85, v85\n"
    "v_add_f32 v196, v196, v82\n"
    "v_mfma_f32_16x16x32_bf16 v[116:119], a[148:151], v[20:23], v[116:119]\n"
    "v_exp_f32 v86, v86\n"
    "v_add_f32 v196, v196, v83\n"
    "v_cvt_pk_bf16_f32 v131, v82, v83\n"
    "v_mfma_f32_16x16x32_bf16 v[116:119], a[152:155], v[24:27], v[116:119]\n"
    "ds_read_b128 a[252:255], v199 offset:23552\n"
    "v_exp_f32 v87, v87\n"
    "v_add_f32 v197, v197, v84\n"
    "v_mfma_f32_16x16x32_bf16 v[116:119], a[156:159], v[28:31], v[116:119]\n"
    "v_exp_f32 v88, v88\n"
    "v_add_f32 v197, v197, v85\n"
    "v_cvt_pk_bf16_f32 v134, v84, v85\n"
    "v_mfma_f32_16x16x32_bf16 v[120:123], a[144:147], v[32:35], v[184:187]\n"
    "v_exp_f32 v89, v89\n"
    "buffer_load_dwordx4 v200, s[60:63], s20 offen offset:3072 sc1 lds\n"
    "v_mfma_f32_16x16x32_bf16 v[120:123], a[148:151], v[36:39], v[120:123]\n"
    "ds_read_b128 a[208:211], v199 offset:4096\n"
    "v_exp_f32 v90, v90\n"
    "v_add_f32 v197, v197, v86\n"
    "v_mfma_f32_16x16x32_bf16 v[120:123], a[152:155], v[40:43], v[120:123]\n"
    "ds_read_b128 a[212:215], v199 offset:5120\n"
    "v_exp_f32 v91, v91\n"
    "v_add_f32 v197, v197, v87\n"
    "v_mfma_f32_16x16x32_bf16 v[120:123], a[156:159], v[44:47], v[120:123]\n"
    "v_exp_f32 v92, v92\n"
    "v_cvt_pk_bf16_f32 v135, v86, v87\n"
    "v_cvt_pk_bf16_f32 v138, v88, v89\n"
    "v_mfma_f32_16x16x32_bf16 v[124:127], a[144:147], v[48:51], v[188:191]\n"
    "ds_read_b128 a[216:219], v199 offset:6144\n"
    "v_exp_f32 v93, v93\n"
    "v_cvt_pk_bf16_f32 v139, v90, v91\n"
    "v_mfma_f32_16x16x32_bf16 v[124:127], a[148:151], v[52:55], v[124:127]\n"
    "v_exp_f32 v94, v94\n"
    "v_cvt_pk_bf16_f32 v142, v92, v93\n"
    "v_cndmask_b32 v180, v202, v193, s[44:45]\n"
    "v_mfma_f32_16x16x32_bf16 v[124:127], a[152:155], v[56:59], v[124:127]\n"
    "ds_read_b128 a[220:223], v199 offset:7168\n"
    "v_exp_f32 v95, v95\n"
    "v_cndmask_b32 v181, v202, v193, s[46:47]\n"
    "v_mfma_f32_16x16x32_bf16 v[124:127], a[156:159], v[60:63], v[124:127]\n"
    "v_cvt_pk_bf16_f32 v143, v94, v95\n"
    "v_cndmask_b32 v182, v202, v193, s[48:49]\n"
    "v_cndmask_b32 v183, v202, v193, s[50:51]\n"
    "s_cmp_eq_u32 s13, 0\n"
    "s_cbranch_scc1 L_att_U1_%=\n"
    "L_att_B4_%=:\n"
    "s_waitcnt lgkmcnt(8)\n"
    "v_mfma_f32_16x16x32_bf16 v[64:67], a[192:195], v[0:3], v[160:163]\n"
    "s_add_u32 m0, s19, 16384\n"
    "v_exp_f32 v96, v96\n"
    "v_mfma_f32_16x16x32_bf16 v[64:67], a[196:199], v[4:7], v[64:67]\n"
    "s_add_u32 s16, s16, 1\n"
    "v_exp_f32 v97, v97\n"
    "v_mfma_f32_16x16x32_bf16 v[64:67], a[200:203], v[8:11], v[64:67]\n"
    "s_add_u32 s21, s21, 32768\n"
    "ds_read_b128 a[128:131], v199 offset:8192\n"
    "v_add_f32 v196, v196, v96\n"
    "v_mfma_f32_16x16x32_bf16 v[64:67], a[204:207], v[12:15], v[64:67]\n"
    "s_and_b32 s21, s21, 0x1ffff\n"
    "buffer_load_dwordx4 v200, s[64:67], s20 offen offset:0 sc1 lds\n"
    "v_mfma_f32_16x16x32_bf16 v[68:71], a[192:195], v[16:19], v[164:167]\n"
    "ds_read_b128 a[132:135], v199 offset:9216\n"
    "v_add_f32 v196, v196, v97\n"
    "v_mfma_f32_16x16x32_bf16 v[68:71], a[196:199], v[20:23], v[68:71]\n"
    "v_exp_f32 v98, v98\n"
    "v_mfma_f32_16x16x32_bf16 v[68:71], a[200:203], v[24:27], v[68:71]\n"
    "v_cvt_pk_bf16_f32 v144, v96, v97\n"
    "v_mfma_f32_16x16x32_bf16 v[68:71], a[204:207], v[28:31], v[68:71]\n"
    "v_exp_f32 v99, v99\n"
    "v_mfma_f32_16x16x32_bf16 a[0:3], a[224:227], v[128:131], a[0:3]\n"
    "v_exp_f32 v100, v100\n"
    "v_mfma_f32_16x16x32_bf16 a[32:35], a[224:227], v[132:135], a[32:35]\n"
    "ds_read_b128 a[136:139], v199 offset:10240\n"
    "v_add_f32 v196, v196, v98\n"
    "v_mfma_f32_16x16x32_bf16 a[4:7], a[228:231], v[128:131], a[4:7]\n"
    "v_exp_f32 v101, v101\n"
    "v_mfma_f32_16x16x32_bf16 a[36:39], a[228:231], v[132:135], a[36:39]\n"
    "v_add_f32 v196, v196, v99\n"
    "v_cvt_pk_bf16_f32 v145, v98, v99\n"
    "v_mfma_f32_16x16x32_bf16 a[8:11], a[232:235], v[128:131], a[8:11]\n"
    "v_exp_f32 v102, v102\n"
    "v_mfma_f32_16x16x32_bf16 a[40:43], a[232:235], v[132:135], a[40:43]\n"
    "ds_read_b128 a[140:143], v199 offset:11264\n"
    "v_add_f32 v197, v197, v100\n"
    "v_mfma_f32_16x16x32_bf16 a[12:15], a[236:239], v[128:131], a[12:15]\n"
    "v_exp_f32 v103, v103\n"
    "v_mfma_f32_16x16x32_bf16 a[44:47], a[236:239], v[132:135], a[44:47]\n"
    "v_add_f32 v197, v197, v101\n"
    "v_cvt_pk_bf16_f32 v148, v100, v101\n"
    "v_mfma_f32_16x16x32_bf16 v[72:75], a[192:195], v[32:35], v[168:171]\n"
    "v_exp_f32 v104, v104\n"
    "v_mfma_f32_16x16x32_bf16 v[72:75], a[196:199], v[36:39], v[72:75]\n"
    "v_exp_f32 v105, v105\n"
    "v_mfma_f32_16x16x32_bf16 v[72:75], a[200:203], v[40:43], v[72:75]\n"
    "ds_read_b128 a[160:163], v199 offset:24576\n"
    "v_add_f32 v197, v197, v102\n"
    "v_mfma_f32_16x16x32_bf16 v[72:75], a[204:207], v[44:47], v[72:75]\n"
    "buffer_load_dwordx4 v200, s[64:67], s20 offen offset:1024 sc1 lds\n"
    "v_mfma_f32_16x16x32_bf16 v[76:79], a[192:195], v[48:51], v[172:175]\n"
    "ds_read_b128 a[164:167], v199 offset:25600\n"
    "v_add_f32 v197, v197, v103\n"
    "v_mfma_f32_16x16x32_bf16 v[76:79], a[196:199], v[52:55], v[76:79]\n"
    "v_exp_f32 v106, v106\n"
    "v_mfma_f32_16x16x32_bf16 v[76:79], a[200:203], v[56:59], v[76:79]\n"
    "v_cvt_pk_bf16_f32 v149, v102, v103\n"
    "v_cvt_pk_bf16_f32 v152, v104, v105\n"
    "v_mfma_f32_16x16x32_bf16 v[76:79], a[204:207], v[60:63], v[76:79]\n"
    "s_waitcnt lgkmcnt(6)\n"
    "v_exp_f32 v107, v107\n"
    "v_mfma_f32_16x16x32_bf16 a[16:19], a[240:243], v[128:131], a[16:19]\n"
    "v_exp_f32 v108, v108\n"
    "v_mfma_f32_16x16x32_bf16 a[48:51], a[240:243], v[132:135], a[48:51]\n"
    "ds_read_b128 a[168:171], v199 offset:26624\n"
    "v_cvt_pk_bf16_f32 v153, v106, v107\n"
    "v_mfma_f32_16x16x32_bf16 a[20:23], a[244:247], v[128:131], a[20:23]\n"
    "v_exp_f32 v109, v109\n"
    "v_mfma_f32_16x16x32_bf16 a[52:55], a[244:247], v[132:135], a[52:55]\n"
    "v_cvt_pk_bf16_f32 v156, v108, v109\n"
    "v_mfma_f32_16x16x32_bf16 a[24:27], a[248:251], v[128:131], a[24:27]\n"
    "v_exp_f32 v110, v110\n"
    "v_mfma_f32_16x16x32_bf16 a[56:59], a[248:251], v[132:135], a[56:59]\n"
    "ds_read_b128 a[172:175], v199 offset:27648\n"
    "v_mfma_f32_16x16x32_bf16 a[28:31], a[252:255], v[128:131], a[28:31]\n"
    "v_exp_f32 v111, v111\n"
    "v_mfma_f32_16x16x32_bf16 a[60:63], a[252:255], v[132:135], a[60:63]\n"
    "v_cvt_pk_bf16_f32 v157, v110, v111\n"
    "v_mfma_f32_16x16x32_bf16 a[92:95], a[252:255], v[136:139], a[92:95]\n"
    "v_exp_f32 v112, v112\n"
    "v_mfma_f32_16x16x32_bf16 v[80:83], a[208:211], v[0:3], v[176:179]\n"
    "v_exp_f32 v113, v113\n"
    "v_mfma_f32_16x16x32_bf16 v[80:83], a[212:215], v[4:7], v[80:83]\n"
    "ds_read_b128 a[176:179], v199 offset:28672\n"
    "v_add_f32 v196, v196, v112\n"
    "v_mfma_f32_16x16x32_bf16 v[80:83], a[216:219], v[8:11], v[80:83]\n"
    "buffer_load_dwordx4 v200, s[64:67], s20 offen offset:2048 sc1 lds\n"
    "v_mfma_f32_16x16x32_bf16 v[80:83], a[220:223], v[12:15], v[80:83]\n"
    "v_add_f32 v196, v196, v113\n"
    "v_cvt_pk_bf16_f32 v146, v112, v113\n"
    "v_mfma_f32_16x16x32_bf16 v[84:87], a[208:211], v[16:19], v[180:183]\n"
    "v_exp_f32 v114, v114\n"
    "v_mfma_f32_16x16x32_bf16 v[84:87], a[212:215], v[20:23], v[84:87]\n"
    "ds_read_b128 a[180:183], v199 offset:29696\n"
    "v_mfma_f32_16x16x32_bf16 v[84:87], a[216:219], v[24:27], v[84:87]\n"
    "v_exp_f32 v115, v115\n"
    "v_mfma_f32_16x16x32_bf16 v[84:87], a[220:223], v[28:31], v[84:87]\n"
    "v_exp_f32 v116, v116\n"
    "v_mfma_f32_16x16x32_bf16 a[124:127], a[252:255], v[140:143], a[124:127]\n"
    "ds_read_b128 a[184:187], v199 offset:30720\n"
    "v_add_f32 v196, v196, v114\n"
    "v_mfma_f32_16x16x32_bf16 a[88:91], a[248:251], v[136:139], a[88:91]\n"
    "v_exp_f32 v117, v117\n"
    "v_mfma_f32_16x16x32_bf16 a[120:123], a[248:251], v[140:143], a[120:123]\n"
    "v_add_f32 v196, v196, v115\n"
    "v_cvt_pk_bf16_f32 v147, v114, v115\n"
    "v_mfma_f32_16x16x32_bf16 a[84:87], a[244:247], v[136:139], a[84:87]\n"
    "v_exp_f32 v118, v118\n"
    "v_mfma_f32_16x16x32_bf16 a[116:119], a[244:247], v[140:143], a[116:119]\n"
    "ds_read_b128 a[188:191], v199 offset:31744\n"
    "v_add_f32 v197, v197, v116\n"
    "v_mfma_f32_16x16x32_bf16 a[80:83], a[240:243], v[136:139], a[80:83]\n"
    "v_exp_f32 v119, v119\n"
    "v_mfma_f32_16x16x32_bf16 a[112:115], a[240:243], v[140:143], a[112:115]\n"
    "v_add_f32 v197, v197, v117\n"
    "v_cvt_pk_bf16_f32 v150, v116, v117\n"
    "v_mfma_f32_16x16x32_bf16 a[76:79], a[236:239], v[136:139], a[76:79]\n"
    "v_exp_f32 v120, v120\n"
    "v_mfma_f32_16x16x32_bf16 v[88:91], a[208:211], v[32:35], v[184:187]\n"
    "v_exp_f32 v121, v121\n"
    "v_mfma_f32_16x16x32_bf16 v[88:91], a[212:215], v[36:39], v[88:91]\n"
    "ds_read_b128 a[144:147], v199 offset:12288\n"
    "v_add_f32 v197, v197, v118\n"
    "v_mfma_f32_16x16x32_bf16 v[88:91], a[216:219], v[40:43], v[88:91]\n"
    "buffer_load_dwordx4 v200, s[64:67], s20 offen offset:3072 sc1 lds\n"
    "v_mfma_f32_16x16x32_bf16 v[88:91], a[220:223], v[44:47], v[88:91]\n"
    "s_add_u32 s20, s20, 16384\n"
    "v_add_f32 v197, v197, v119\n"
    "v_cvt_pk_bf16_f32 v151, v118, v119\n"
    "v_mfma_f32_16x16x32_bf16 v[92:95], a[208:211], v[48:51], v[188:191]\n"
    "s_add_u32 s19, s19, 32768\n"
    "v_exp_f32 v122, v122\n"
    "v_mfma_f32_16x16x32_bf16 v[92:95], a[212:215], v[52:55], v[92:95]\n"
    "s_and_b32 s19, s19, 0x1ffff\n"
    "ds_read_b128 a[148:151], v199 offset:13312\n"
    "v_cvt_pk_bf16_f32 v154, v120, v121\n"
    "v_mfma_f32_16x16x32_bf16 v[92:95], a[216:219], v[56:59], v[92:95]\n"
    "v_exp_f32 v123, v123\n"
    "v_mfma_f32_16x16x32_bf16 v[92:95], a[220:223], v[60:63], v[92:95]\n"
    "v_exp_f32 v124, v124\n"
    "v_mfma_f32_16x16x32_bf16 a[108:111], a[236:239], v[140:143], a[108:111]\n"
    "ds_read_b128 a[152:155], v199 offset:14336\n"
    "v_cvt_pk_bf16_f32 v155, v122, v123\n"
    "v_mfma_f32_16x16x32_bf16 a[72:75], a[232:235], v[136:139], a[72:75]\n"
    "v_exp_f32 v125, v125\n"
    "v_mfma_f32_16x16x32_bf16 a[104:107], a[232:235], v[140:143], a[104:107]\n"
    "v_cvt_pk_bf16_f32 v158, v124, v125\n"
    "v_mfma_f32_16x16x32_bf16 a[68:71], a[228:231], v[136:139], a[68:71]\n"
    "v_exp_f32 v126, v126\n"
    "v_mfma_f32_16x16x32_bf16 a[100:103], a[228:231], v[140:143], a[100:103]\n"
    "ds_read_b128 a[156:159], v199 offset:15360\n"
    "v_mfma_f32_16x16x32_bf16 a[64:67], a[224:227], v[136:139], a[64:67]\n"
    "s_waitcnt vmcnt(8)\n"
    "s_barrier\n"
    "v_mfma_f32_16x16x32_bf16 a[96:99], a[224:227], v[140:143], a[96:99]\n"
    "v_exp_f32 v127, v127\n"
    "v_mfma_f32_16x16x32_bf16 v[240:243], v[248:251], v[136:139], v[240:243]\n"
    "v_cvt_pk_bf16_f32 v159, v126, v127\n"
    "v_mfma_f32_16x16x32_bf16 v[244:247], v[248:251], v[140:143], v[244:247]\n"
    "s_waitcnt lgkmcnt(8)\n"
    "v_add_u32 v199, s21, v198\n"
    "v_mfma_f32_16x16x32_bf16 v[96:99], a[128:131], v[0:3], v[160:163]\n"
    "s_mov_b32 m0, s19\n"
    "v_exp_f32 v64, v64\n"
    "v_mfma_f32_16x16x32_bf16 v[96:99], a[132:135], v[4:7], v[96:99]\n"
    "s_add_u32 s16, s16, 1\n"
    "v_exp_f32 v65, v65\n"
    "v_mfma_f32_16x16x32_bf16 v[96:99], a[136:139], v[8:11], v[96:99]\n"
    "ds_read_b128 a[192:195], v199 offset:0\n"
    "v_add_f32 v196, v196, v64\n"
    "v_mfma_f32_16x16x32_bf16 v[96:99], a[140:143], v[12:15], v[96:99]\n"
    "buffer_load_dwordx4 v200, s[60:63], s20 offen offset:0 sc1 lds\n"
    "v_mfma_f32_16x16x32_bf16 v[100:103], a[128:131], v[16:19], v[164:167]\n"
    "ds_read_b128 a[196:199], v199 offset:1024\n"
    "v_add_f32 v196, v196, v65\n"
    "v_mfma_f32_16x16x32_bf16 v[100:103], a[132:135], v[20:23], v[100:103]\n"
    "v_exp_f32 v66, v66\n"
    "v_mfma_f32_16x16x32_bf16 v[100:103], a[136:139], v[24:27], v[100:103]\n"
    "v_cvt_pk_bf16_f32 v128, v64, v65\n"
    "v_mfma_f32_16x16x32_bf16 v[100:103], a[140:143], v[28:31], v[100:103]\n"
    "v_exp_f32 v67, v67\n"
    "v_mfma_f32_16x16x32_bf16 a[0:3], a[160:163], v[144:147], a[0:3]\n"
    "v_exp_f32 v68, v68\n"
    "v_mfma_f32_16x16x32_bf16 a[32:35], a[160:163], v[148:151], a[32:35]\n"
    "ds_read_b128 a[200:203], v199 offset:2048\n"
    "v_add_f32 v196, v196, v66\n"
    "v_mfma_f32_16x16x32_bf16 a[4:7], a[164:167], v[144:147], a[4:7]\n"
    "v_exp_f32 v69, v69\n"
    "v_mfma_f32_16x16x32_bf16 a[36:39], a[164:167], v[148:151], a[36:39]\n"
    "v_add_f32 v196, v196, v67\n"
    "v_cvt_pk_bf16_f32 v129, v66, v67\n"
    "v_mfma_f32_16x16x32_bf16 a[8:11], a[168:171], v[144:147], a[8:11]\n"
    "v_exp_f32 v70, v70\n"
    "v_mfma_f32_16x16x32_bf16 a[40:43], a[168:171], v[148:151], a[40:43]\n"
    "ds_read_b128 a[204:207], v199 offset:3072\n"
    "v_add_f32 v197, v197, v68\n"
    "v_mfma_f32_16x16x32_bf16 a[12:15], a[172:175], v[144:147], a[12:15]\n"
    "v_exp_f32 v71, v71\n"
    "v_mfma_f32_16x16x32_bf16 a[44:47], a[172:175], v[148:151], a[44:47]\n"
    "v_add_f32 v197, v197, v69\n"
    "v_cvt_pk_bf16_f32 v132, v68, v69\n"
    "v_mfma_f32_16x16x32_bf16 v[104:107], a[128:131], v[32:35], v[168:171]\n"
    "v_exp_f32 v72, v72\n"
    "v_mfma_f32_16x16x32_bf16 v[104:107], a[132:135], v[36:39], v[104:107]\n"
    "v_exp_f32 v73, v73\n"
    "v_mfma_f32_16x16x32_bf16 v[104:107], a[136:139], v[40:43], v[104:107]\n"
    "ds_read_b128 a[224:227], v199 offset:16384\n"
    "v_add_f32 v197, v197, v70\n"
    "v_mfma_f32_16x16x32_bf16 v[104:107], a[140:143], v[44:47], v[104:107]\n"
    "buffer_load_dwordx4 v200, s[60:63], s20 offen offset:1024 sc1 lds\n"
    "v_mfma_f32_16x16x32_bf16 v[108:111], a[128:131], v[48:51], v[172:175]\n"
    "ds_read_b128 a[228:231], v199 offset:17408\n"
    "v_add_f32 v197, v197, v71\n"
    "v_mfma_f32_16x16x32_bf16 v[108:111], a[132:135], v[52:55], v[108:111]\n"
    "v_exp_f32 v74, v74\n"
    "v_mfma_f32_16x16x32_bf16 v[108:111], a[136:139], v[56:59], v[108:111]\n"
    "v_cvt_pk_bf16_f32 v133, v70, v71\n"
    "v_cvt_pk_bf16_f32 v136, v72, v73\n"
    "v_mfma_f32_16x16x32_bf16 v[108:111], a[140:143], v[60:63], v[108:111]\n"
    "s_waitcnt lgkmcnt(6)\n"
    "v_exp_f32 v75, v75\n"
    "v_mfma_f32_16x16x32_bf16 a[16:19], a[176:179], v[144:147], a[16:19]\n"
    "v_exp_f32 v76, v76\n"
    "v_mfma_f32_16x16x32_bf16 a[48:51], a[176:179], v[148:151], a[48:51]\n"
    "ds_read_b128 a[232:235], v199 offset:18432\n"
    "v_cvt_pk_bf16_f32 v137, v74, v75\n"
    "v_mfma_f32_16x16x32_bf16 a[20:23], a[180:183], v[144:147], a[20:23]\n"
    "v_exp_f32 v77, v77\n"
    "v_mfma_f32_16x16x32_bf16 a[52:55], a[180:183], v[148:151], a[52:55]\n"
    "v_cvt_pk_bf16_f32 v140, v76, v77\n"
    "v_mfma_f32_16x16x32_bf16 a[24:27], a[184:187], v[144:147], a[24:27]\n"
    "v_exp_f32 v78, v78\n"
    "v_mfma_f32_16x16x32_bf16 a[56:59], a[184:187], v[148:151], a[56:59]\n"
    "ds_read_b128 a[236:239], v199 offset:19456\n"
    "v_mfma_f32_16x16x32_bf16 a[28:31], a[188:191], v[144:147], a[28:31]\n"
    "v_exp_f32 v79, v79\n"
    "v_mfma_f32_16x16x32_bf16 a[60:63], a[188:191], v[148:151], a[60:63]\n"
    "v_cvt_pk_bf16_f32 v141, v78, v79\n"
    "v_mfma_f32_16x16x32_bf16 a[92:95], a[188:191], v[152:155], a[92:95]\n"
    "v_exp_f32 v80, v80\n"
    "v_mfma_f32_16x16x32_bf16 v[112:115], a[144:147], v[0:3], v[176:179]\n"
    "v_exp_f32 v81, v81\n"
    "v_mfma_f32_16x16x32_bf16 v[112:115], a[148:151], v[4:7], v[112:115]\n"
    "ds_read_b128 a[240:243], v199 offset:20480\n"
    "v_add_f32 v196, v196, v80\n"
    "v_mfma_f32_16x16x32_bf16 v[112:115], a[152:155], v[8:11], v[112:115]\n"
    "buffer_load_dwordx4 v200, s[60:63], s20 offen offset:2048 sc1 lds\n"
    "v_mfma_f32_16x16x32_bf16 v[112:115], a[156:159], v[12:15], v[112:115]\n"
    "v_add_f32 v196, v196, v81\n"
    "v_cvt_pk_bf16_f32 v130, v80, v81\n"
    "v_mfma_f32_16x16x32_bf16 v[116:119], a[144:147], v[16:19], v[180:183]\n"
    "v_exp_f32 v82, v82\n"
    "v_mfma_f32_16x16x32_bf16 v[116:119], a[148:151], v[20:23], v[116:119]\n"
    "ds_read_b128 a[244:247], v199 offset:21504\n"
    "v_mfma_f32_16x16x32_bf16 v[116:119], a[152:155], v[24:27], v[116:119]\n"
    "v_exp_f32 v83, v83\n"
    "v_mfma_f32_16x16x32_bf16 v[116:119], a[156:159], v[28:31], v[116:119]\n"
    "v_exp_f32 v84, v84\n"
    "v_mfma_f32_16x16x32_bf16 a[124:127], a[188:191], v[156:159], a[124:127]\n"
    "ds_read_b128 a[248:251], v199 offset:22528\n"
    "v_add_f32 v196, v196, v82\n"
    "v_mfma_f32_16x16x32_bf16 a[88:91], a[184:187], v[152:155], a[88:91]\n"
    "v_exp_f32 v85, v85\n"
    "v_mfma_f32_16x16x32_bf16 a[120:123], a[184:187], v[156:159], a[120:123]\n"
    "v_add_f32 v196, v196, v83\n"
    "v_cvt_pk_bf16_f32 v131, v82, v83\n"
    "v_mfma_f32_16x16x32_bf16 a[84:87], a[180:183], v[152:155], a[84:87]\n"
    "v_exp_f32 v86, v86\n"
    "v_mfma_f32_16x16x32_bf16 a[116:119], a[180:183], v[156:159], a[116:119]\n"
    "ds_read_b128 a[252:255], v199 offset:23552\n"
    "v_add_f32 v197, v197, v84\n"
    "v_mfma_f32_16x16x32_bf16 a[80:83], a[176:179], v[152:155], a[80:83]\n"
    "v_exp_f32 v87, v87\n"
    "v_mfma_f32_16x16x32_bf16 a[112:115], a[176:179], v[156:159], a[112:115]\n"
    "v_add_f32 v197, v197, v85\n"
    "v_cvt_pk_bf16_f32 v134, v84, v85\n"
    "v_mfma_f32_16x16x32_bf16 a[76:79], a[172:175], v[152:155], a[76:79]\n"
    "v_exp_f32 v88, v88\n"
    "v_mfma_f32_16x16x32_bf16 v[120:123], a[144:147], v[32:35], v[184:187]\n"
    "v_exp_f32 v89, v89\n"
    "v_mfma_f32_16x16x32_bf16 v[120:123], a[148:151], v[36:39], v[120:123]\n"
    "ds_read_b128 a[208:211], v199 offset:4096\n"
    "v_add_f32 v197, v197, v86\n"
    "v_mfma_f32_16x16x32_bf16 v[120:123], a[152:155], v[40:43], v[120:123]\n"
    "buffer_load_dwordx4 v200, s[60:63], s20 offen offset:3072 sc1 lds\n"
    "v_mfma_f32_16x16x32_bf16 v[120:123], a[156:159], v[44:47], v[120:123]\n"
    "v_add_f32 v197, v197, v87\n"
    "v_cvt_pk_bf16_f32 v135, v86, v87\n"
    "v_mfma_f32_16x16x32_bf16 v[124:127], a[144:147], v[48:51], v[188:191]\n"
    "v_exp_f32 v90, v90\n"
    "v_mfma_f32_16x16x32_bf16 v[124:127], a[148:151], v[52:55], v[124:127]\n"
    "ds_read_b128 a[212:215], v199 offset:5120\n"
    "v_cvt_pk_bf16_f32 v138, v88, v89\n"
    "v_mfma_f32_16x16x32_bf16 v[124:127], a[152:155], v[56:59], v[124:127]\n"
    "v_exp_f32 v91, v91\n"
    "v_mfma_f32_16x16x32_bf16 v[124:127], a[156:159], v[60:63], v[124:127]\n"
    "v_exp_f32 v92, v92\n"
    "v_mfma_f32_16x16x32_bf16 a[108:111], a[172:175], v[156:159], a[108:111]\n"
    "ds_read_b128 a[216:219], v199 offset:6144\n"
    "v_cvt_pk_bf16_f32 v139, v90, v91\n"
    "v_mfma_f32_16x16x32_bf16 a[72:75], a[168:171], v[152:155], a[72:75]\n"
    "v_exp_f32 v93, v93\n"
    "v_mfma_f32_16x16x32_bf16 a[104:107], a[168:171], v[156:159], a[104:107]\n"
    "v_cvt_pk_bf16_f32 v142, v92, v93\n"
    "v_mfma_f32_16x16x32_bf16 a[68:71], a[164:167], v[152:155], a[68:71]\n"
    "v_exp_f32 v94, v94\n"
    "v_mfma_f32_16x16x32_bf16 a[100:103], a[164:167], v[156:159], a[100:103]\n"
    "ds_read_b128 a[220:223], v199 offset:7168\n"
    "v_mfma_f32_16x16x32_bf16 a[64:67], a[160:163], v[152:155], a[64:67]\n"
    "v_exp_f32 v95, v95\n"
    "v_mfma_f32_16x16x32_bf16 a[96:99], a[160:163], v[156:159], a[96:99]\n"
    "s_cmp_lg_u32 s16, s22\n"
    "v_cvt_pk_bf16_f32 v143, v94, v95\n"
    "v_mfma_f32_16x16x32_bf16 v[240:243], v[248:251], v[152:155], v[240:243]\n"
    "v_mfma_f32_16x16x32_bf16 v[244:247], v[248:251], v[156:159], v[244:247]\n"
    "s_cbranch_scc1 L_att_B4_%=\n"
    "s_lshl_b32 s14, s16, 1\n"
    "s_cmp_gt_u32 s17, s14\n"
    "s_cselect_b64 vcc, -1, 0\n"
    "s_cmp_eq_u32 s17, s14\n"
    "s_cselect_b64 s[44:45], s[72:73], vcc\n"
    "s_cselect_b64 s[46:47], s[74:75], vcc\n"
    "s_cselect_b64 s[48:49], s[76:77], vcc\n"
    "s_cselect_b64 s[50:51], s[78:79], vcc\n"
    "v_cndmask_b32 v160, v202, v192, s[44:45]\n"
    "v_cndmask_b32 v161, v202, v192, s[46:47]\n"
    "v_cndmask_b32 v162, v202, v192, s[48:49]\n"
    "v_cndmask_b32 v163, v202, v192, s[50:51]\n"
    "s_add_u32 s14, s14, 1\n"
    "s_cmp_gt_u32 s17, s14\n"
    "s_cselect_b64 vcc, -1, 0\n"
    "s_cmp_eq_u32 s17, s14\n"
    "s_cselect_b64 s[44:45], s[72:73], vcc\n"
    "s_cselect_b64 s[46:47], s[74:75], vcc\n"
    "s_cselect_b64 s[48:49], s[76:77], vcc\n"
    "s_cselect_b64 s[50:51], s[78:79], vcc\n"
    "v_cndmask_b32 v176, v202, v192, s[44:45]\n"
    "v_cndmask_b32 v177, v202, v192, s[46:47]\n"
    "v_cndmask_b32 v178, v202, v192, s[48:49]\n"
    "v_cndmask_b32 v179, v202, v192, s[50:51]\n"
    "s_waitcnt lgkmcnt(8)\n"
    "v_mfma_f32_16x16x32_bf16 v[64:67], a[192:195], v[0:3], v[160:163]\n"
    "s_add_u32 m0, s19, 16384\n"
    "v_exp_f32 v96, v96\n"
    "v_mfma_f32_16x16x32_bf16 v[64:67], a[196:199], v[4:7], v[64:67]\n"
    "s_add_u32 s16, s16, 1\n"
    "v_exp_f32 v97, v97\n"
    "v_mfma_f32_16x16x32_bf16 v[64:67], a[200:203], v[8:11], v[64:67]\n"
    "s_add_u32 s21, s21, 32768\n"
    "ds_read_b128 a[128:131], v199 offset:8192\n"
    "v_add_f32 v196, v196, v96\n"
    "v_mfma_f32_16x16x32_bf16 v[64:67], a[204:207], v[12:15], v[64:67]\n"
    "s_and_b32 s21, s21, 0x1ffff\n"
    "buffer_load_dwordx4 v200, s[64:67], s20 offen offset:0 sc1 lds\n"
    "v_mfma_f32_16x16x32_bf16 v[68:71], a[192:195], v[16:19], v[164:167]\n"
    "s_lshl_b32 s14, s16, 1\n"
    "ds_read_b128 a[132:135], v199 offset:9216\n"
    "v_add_f32 v196, v196, v97\n"
    "v_mfma_f32_16x16x32_bf16 v[68:71], a[196:199], v[20:23], v[68:71]\n"
    "s_cmp_gt_u32 s17, s14\n"
    "v_exp_f32 v98, v98\n"
    "v_mfma_f32_16x16x32_bf16 v[68:71], a[200:203], v[24:27], v[68:71]\n"
    "s_cselect_b64 vcc, -1, 0\n"
    "v_cvt_pk_bf16_f32 v144, v96, v97\n"
    "v_mfma_f32_16x16x32_bf16 v[68:71], a[204:207], v[28:31], v[68:71]\n"
    "s_cmp_eq_u32 s17, s14\n"
    "v_exp_f32 v99, v99\n"
    "v_mfma_f32_16x16x32_bf16 a[0:3], a[224:227], v[128:131], a[0:3]\n"
    "s_cselect_b64 s[44:45], s[72:73], vcc\n"
    "v_exp_f32 v100, v100\n"
    "v_mfma_f32_16x16x32_bf16 a[32:35], a[224:227], v[132:135], a[32:35]\n"
    "s_cselect_b64 s[46:47], s[74:75], vcc\n"
    "ds_read_b128 a[136:139], v199 offset:10240\n"
    "v_add_f32 v196, v196, v98\n"
    "v_mfma_f32_16x16x32_bf16 a[4:7], a[228:231], v[128:131], a[4:7]\n"
    "s_cselect_b64 s[48:49], s[76:77], vcc\n"
    "v_exp_f32 v101, v101\n"
    "v_mfma_f32_16x16x32_bf16 a[36:39], a[228:231], v[132:135], a[36:39]\n"
    "s_cselect_b64 s[50:51], s[78:79], vcc\n"
    "v_add_f32 v196, v196, v99\n"
    "v_cvt_pk_bf16_f32 v145, v98, v99\n"
    "v_mfma_f32_16x16x32_bf16 a[8:11], a[232:235], v[128:131], a[8:11]\n"
    "v_exp_f32 v102, v102\n"
    "v_mfma_f32_16x16x32_bf16 a[40:43], a[232:235], v[132:135], a[40:43]\n"
    "ds_read_b128 a[140:143], v199 offset:11264\n"
    "v_add_f32 v197, v197, v100\n"
    "v_mfma_f32_16x16x32_bf16 a[12:15], a[236:239], v[128:131], a[12:15]\n"
    "v_exp_f32 v103, v103\n"
    "v_mfma_f32_16x16x32_bf16 a[44:47], a[236:239], v[132:135], a[44:47]\n"
    "v_add_f32 v197, v197, v101\n"
    "v_cvt_pk_bf16_f32 v148, v100, v101\n"
    "v_mfma_f32_16x16x32_bf16 v[72:75], a[192:195], v[32:35], v[168:171]\n"
    "v_exp_f32 v104, v104\n"
    "v_mfma_f32_16x16x32_bf16 v[72:75], a[196:199], v[36:39], v[72:75]\n"
    "v_exp_f32 v105, v105\n"
    "v_mfma_f32_16x16x32_bf16 v[72:75], a[200:203], v[40:43], v[72:75]\n"
    "ds_read_b128 a[160:163], v199 offset:24576\n"
    "v_add_f32 v197, v197, v102\n"
    "v_mfma_f32_16x16x32_bf16 v[72:75], a[204:207], v[44:47], v[72:75]\n"
    "buffer_load_dwordx4 v200, s[64:67], s20 offen offset:1024 sc1 lds\n"
    "v_mfma_f32_16x16x32_bf16 v[76:79], a[192:195], v[48:51], v[172:175]\n"
    "ds_read_b128 a[164:167], v199 offset:25600\n"
    "v_add_f32 v197, v197, v103\n"
    "v_mfma_f32_16x16x32_bf16 v[76:79], a[196:199], v[52:55], v[76:79]\n"
    "v_exp_f32 v106, v106\n"
    "v_mfma_f32_16x16x32_bf16 v[76:79], a[200:203], v[56:59], v[76:79]\n"
    "v_cvt_pk_bf16_f32 v149, v102, v103\n"
    "v_cvt_pk_bf16_f32 v152, v104, v105\n"
    "v_mfma_f32_16x16x32_bf16 v[76:79], a[204:207], v[60:63], v[76:79]\n"
    "s_waitcnt lgkmcnt(6)\n"
    "v_exp_f32 v107, v107\n"
    "v_mfma_f32_16x16x32_bf16 a[16:19], a[240:243], v[128:131], a[16:19]\n"
    "v_exp_f32 v108, v108\n"
    "v_mfma_f32_16x16x32_bf16 a[48:51], a[240:243], v[132:135], a[48:51]\n"
    "ds_read_b128 a[168:171], v199 offset:26624\n"
    "v_cvt_pk_bf16_f32 v153, v106, v107\n"
    "v_mfma_f32_16x16x32_bf16 a[20:23], a[244:247], v[128:131], a[20:23]\n"
    "v_exp_f32 v109, v109\n"
    "v_mfma_f32_16x16x32_bf16 a[52:55], a[244:247], v[132:135], a[52:55]\n"
    "v_cvt_pk_bf16_f32 v156, v108, v109\n"
    "v_cndmask_b32 v160, v202, v192, s[44:45]\n"
    "v_mfma_f32_16x16x32_bf16 a[24:27], a[248:251], v[128:131], a[24:27]\n"
    "v_exp_f32 v110, v110\n"
    "v_mfma_f32_16x16x32_bf16 a[56:59], a[248:251], v[132:135], a[56:59]\n"
    "ds_read_b128 a[172:175], v199 offset:27648\n"
    "v_cndmask_b32 v161, v202, v192, s[46:47]\n"
    "v_mfma_f32_16x16x32_bf16 a[28:31], a[252:255], v[128:131], a[28:31]\n"
    "v_exp_f32 v111, v111\n"
    "v_mfma_f32_16x16x32_bf16 a[60:63], a[252:255], v[132:135], a[60:63]\n"
    "v_cvt_pk_bf16_f32 v157, v110, v111\n"
    "v_cndmask_b32 v162, v202, v192, s[48:49]\n"
    "v_mfma_f32_16x16x32_bf16 a[92:95], a[252:255], v[136:139], a[92:95]\n"
    "v_exp_f32 v112, v112\n"
    "v_mfma_f32_16x16x32_bf16 v[80:83], a[208:211], v[0:3], v[176:179]\n"
    "v_exp_f32 v113, v113\n"
    "v_cndmask_b32 v163, v202, v192, s[50:51]\n"
    "v_mfma_f32_16x16x32_bf16 v[80:83], a[212:215], v[4:7], v[80:83]\n"
    "s_add_u32 s14, s14, 1\n"
    "ds_read_b128 a[176:179], v199 offset:28672\n"
    "v_add_f32 v196, v196, v112\n"
    "v_mfma_f32_16x16x32_bf16 v[80:83], a[216:219], v[8:11], v[80:83]\n"
    "s_cmp_gt_u32 s17, s14\n"
    "buffer_load_dwordx4 v200, s[64:67], s20 offen offset:2048 sc1 lds\n"
    "v_mfma_f32_16x16x32_bf16 v[80:83], a[220:223], v[12:15], v[80:83]\n"
    "s_cselect_b64 vcc, -1, 0\n"
    "v_add_f32 v196, v196, v113\n"
    "v_cvt_pk_bf16_f32 v146, v112, v113\n"
    "v_mfma_f32_16x16x32_bf16 v[84:87], a[208:211], v[16:19], v[180:183]\n"
    "s_cmp_eq_u32 s17, s14\n"
    "v_exp_f32 v114, v114\n"
    "v_mfma_f32_16x16x32_bf16 v[84:87], a[212:215], v[20:23], v[84:87]\n"
    "s_cselect_b64 s[44:45], s[72:73], vcc\n"
    "ds_read_b128 a[180:183], v199 offset:29696\n"
    "v_mfma_f32_16x16x32_bf16 v[84:87], a[216:219], v[24:27], v[84:87]\n"
    "s_cselect_b64 s[46:47], s[74:75], vcc\n"
    "v_exp_f32 v115, v115\n"
    "v_mfma_f32_16x16x32_bf16 v[84:87], a[220:223], v[28:31], v[84:87]\n"
    "s_cselect_b64 s[48:49], s[76:77], vcc\n"
    "v_exp_f32 v116, v116\n"
    "v_mfma_f32_16x16x32_bf16 a[124:127], a[252:255], v[140:143], a[124:127]\n"
    "s_cselect_b64 s[50:51], s[78:79], vcc\n"
    "ds_read_b128 a[184:187], v199 offset:30720\n"
    "v_add_f32 v196, v196, v114\n"
    "v_mfma_f32_16x16x32_bf16 a[88:91], a[248:251], v[136:139], a[88:91]\n"
    "v_exp_f32 v117, v117\n"
    "v_mfma_f32_16x16x32_bf16 a[120:123], a[248:251], v[140:143], a[120:123]\n"
    "v_add_f32 v196, v196, v115\n"
    "v_cvt_pk_bf16_f32 v147, v114, v115\n"
    "v_mfma_f32_16x16x32_bf16 a[84:87], a[244:247], v[136:139], a[84:87]\n"
    "v_exp_f32 v118, v118\n"
    "v_mfma_f32_16x16x32_bf16 a[116:119], a[244:247], v[140:143], a[116:119]\n"
    "ds_read_b128 a[188:191], v199 offset:31744\n"
    "v_add_f32 v197, v197, v116\n"
    "v_mfma_f32_16x16x32_bf16 a[80:83], a[240:243], v[136:139], a[80:83]\n"
    "v_exp_f32 v119, v119\n"
    "v_mfma_f32_16x16x32_bf16 a[112:115], a[240:243], v[140:143], a[112:115]\n"
    "v_add_f32 v197, v197, v117\n"
    "v_cvt_pk_bf16_f32 v150, v116, v117\n"
    "v_mfma_f32_16x16x32_bf16 a[76:79], a[236:239], v[136:139], a[76:79]\n"
    "v_exp_f32 v120, v120\n"
    "v_mfma_f32_16x16x32_bf16 v[88:91], a[208:211], v[32:35], v[184:187]\n"
    "v_exp_f32 v121, v121\n"
    "v_mfma_f32_16x16x32_bf16 v[88:91], a[212:215], v[36:39], v[88:91]\n"
    "ds_read_b128 a[144:147], v199 offset:12288\n"
    "v_add_f32 v197, v197, v118\n"
    "v_mfma_f32_16x16x32_bf16 v[88:91], a[216:219], v[40:43], v[88:91]\n"
    "buffer_load_dwordx4 v200, s[64:67], s20 offen offset:3072 sc1 lds\n"
    "v_mfma_f32_16x16x32_bf16 v[88:91], a[220:223], v[44:47], v[88:91]\n"
    "s_add_u32 s20, s20, 16384\n"
    "v_add_f32 v197, v197, v119\n"
    "v_cvt_pk_bf16_f32 v151, v118, v119\n"
    "v_mfma_f32_16x16x32_bf16 v[92:95], a[208:211], v[48:51], v[188:191]\n"
    "s_add_u32 s19, s19, 32768\n"
    "v_exp_f32 v122, v122\n"
    "v_mfma_f32_16x16x32_bf16 v[92:95], a[212:215], v[52:55], v[92:95]\n"
    "s_and_b32 s19, s19, 0x1ffff\n"
    "ds_read_b128 a[148:151], v199 offset:13312\n"
    "v_cvt_pk_bf16_f32 v154, v120, v121\n"
    "v_mfma_f32_16x16x32_bf16 v[92:95], a[216:219], v[56:59], v[92:95]\n"
    "v_exp_f32 v123, v123\n"
    "v_mfma_f32_16x16x32_bf16 v[92:95], a[220:223], v[60:63], v[92:95]\n"
    "v_exp_f32 v124, v124\n"
    "v_mfma_f32_16x16x32_bf16 a[108:111], a[236:239], v[140:143], a[108:111]\n"
    "ds_read_b128 a[152:155], v199 offset:14336\n"
    "v_cvt_pk_bf16_f32 v155, v122, v123\n"
    "v_mfma_f32_16x16x32_bf16 a[72:75], a[232:235], v[136:139], a[72:75]\n"
    "v_exp_f32 v125, v125\n"
    "v_mfma_f32_16x16x32_bf16 a[104:107], a[232:235], v[140:143], a[104:107]\n"
    "v_cvt_pk_bf16_f32 v158, v124, v125\n"
    "v_cndmask_b32 v176, v202, v192, s[44:45]\n"
    "v_mfma_f32_16x16x32_bf16 a[68:71], a[228:231], v[136:139], a[68:71]\n"
    "v_exp_f32 v126, v126\n"
    "v_mfma_f32_16x16x32_bf16 a[100:103], a[228:231], v[140:143], a[100:103]\n"
    "ds_read_b128 a[156:159], v199 offset:15360\n"
    "v_cndmask_b32 v177, v202, v192, s[46:47]\n"
    "v_mfma_f32_16x16x32_bf16 a[64:67], a[224:227], v[136:139], a[64:67]\n"
    "s_waitcnt vmcnt(8)\n"
    "s_barrier\n"
    "v_mfma_f32_16x16x32_bf16 a[96:99], a[224:227], v[140:143], a[96:99]\n"
    "v_exp_f32 v127, v127\n"
    "v_mfma_f32_16x16x32_bf16 v[240:243], v[248:251], v[136:139], v[240:243]\n"
    "v_cvt_pk_bf16_f32 v159, v126, v127\n"
    "v_cndmask_b32 v178, v202, v192, s[48:49]\n"
    "v_mfma_f32_16x16x32_bf16 v[244:247], v[248:251], v[140:143], v[244:247]\n"
    "s_waitcnt lgkmcnt(8)\n"
    "v_add_u32 v199, s21, v198\n"
    "v_cndmask_b32 v179, v202, v192, s[50:51]\n"
    "v_mfma_f32_16x16x32_bf16 v[96:99], a[128:131], v[0:3], v[160:163]\n"
    "s_mov_b32 m0, s19\n"
    "v_exp_f32 v64, v64\n"
    "v_mfma_f32_16x16x32_bf16 v[96:99], a[132:135], v[4:7], v[96:99]\n"
    "s_add_u32 s16, s16, 1\n"
    "v_exp_f32 v65, v65\n"
    "v_mfma_f32_16x16x32_bf16 v[96:99], a[136:139], v[8:11], v[96:99]\n"
    "ds_read_b128 a[192:195], v199 offset:0\n"
    "v_add_f32 v196, v196, v64\n"
    "v_mfma_f32_16x16x32_bf16 v[96:99], a[140:143], v[12:15], v[96:99]\n"
    "buffer_load_dwordx4 v200, s[60:63], s20 offen offset:0 sc1 lds\n"
    "v_mfma_f32_16x16x32_bf16 v[100:103], a[128:131], v[16:19], v[164:167]\n"
    "s_lshl_b32 s14, s16, 1\n"
    "ds_read_b128 a[196:199], v199 offset:1024\n"
    "v_add_f32 v196, v196, v65\n"
    "v_mfma_f32_16x16x32_bf16 v[100:103], a[132:135], v[20:23], v[100:103]\n"
    "s_cmp_gt_u32 s18, s14\n"
    "v_exp_f32 v66, v66\n"
    "v_mfma_f32_16x16x32_bf16 v[100:103], a[136:139], v[24:27], v[100:103]\n"
    "s_cselect_b64 vcc, -1, 0\n"
    "v_cvt_pk_bf16_f32 v128, v64, v65\n"
    "v_mfma_f32_16x16x32_bf16 v[100:103], a[140:143], v[28:31], v[100:103]\n"
    "s_cmp_eq_u32 s18, s14\n"
    "v_exp_f32 v67, v67\n"
    "v_mfma_f32_16x16x32_bf16 a[0:3], a[160:163], v[144:147], a[0:3]\n"
    "s_cselect_b64 s[44:45], s[72:73], vcc\n"
    "ds_read_b128 a[200:203], v199 offset:2048\n"
    "v_add_f32 v196, v196, v66\n"
    "v_mfma_f32_16x16x32_bf16 a[32:35], a[160:163], v[148:151], a[32:35]\n"
    "s_cselect_b64 s[46:47], s[74:75], vcc\n"
    "v_exp_f32 v68, v68\n"
    "v_mfma_f32_16x16x32_bf16 a[4:7], a[164:167], v[144:147], a[4:7]\n"
    "s_cselect_b64 s[48:49], s[76:77], vcc\n"
    "v_exp_f32 v69, v69\n"
    "v_mfma_f32_16x16x32_bf16 a[36:39], a[164:167], v[148:151], a[36:39]\n"
    "s_cselect_b64 s[50:51], s[78:79], vcc\n"
    "v_add_f32 v196, v196, v67\n"
    "v_cvt_pk_bf16_f32 v129, v66, v67\n"
    "v_mfma_f32_16x16x32_bf16 a[8:11], a[168:171], v[144:147], a[8:11]\n"
    "v_exp_f32 v70, v70\n"
    "v_mfma_f32_16x16x32_bf16 a[40:43], a[168:171], v[148:151], a[40:43]\n"
    "ds_read_b128 a[204:207], v199 offset:3072\n"
    "v_add_f32 v197, v197, v68\n"
    "v_mfma_f32_16x16x32_bf16 a[12:15], a[172:175], v[144:147], a[12:15]\n"
    "v_exp_f32 v71, v71\n"
    "v_mfma_f32_16x16x32_bf16 a[44:47], a[172:175], v[148:151], a[44:47]\n"
    "v_add_f32 v197, v197, v69\n"
    "v_cvt_pk_bf16_f32 v132, v68, v69\n"
    "v_mfma_f32_16x16x32_bf16 v[104:107], a[128:131], v[32:35], v[168:171]\n"
    "v_exp_f32 v72, v72\n"
    "v_mfma_f32_16x16x32_bf16 v[104:107], a[132:135], v[36:39], v[104:107]\n"
    "ds_read_b128 a[224:227], v199 offset:16384\n"
    "v_add_f32 v197, v197, v70\n"
    "v_mfma_f32_16x16x32_bf16 v[104:107], a[136:139], v[40:43], v[104:107]\n"
    "v_exp_f32 v73, v73\n"
    "v_mfma_f32_16x16x32_bf16 v[104:107], a[140:143], v[44:47], v[104:107]\n"
    "buffer_load_dwordx4 v200, s[60:63], s20 offen offset:1024 sc1 lds\n"
    "v_mfma_f32_16x16x32_bf16 v[108:111], a[128:131], v[48:51], v[172:175]\n"
    "ds_read_b128 a[228:231], v199 offset:17408\n"
    "v_add_f32 v197, v197, v71\n"
    "v_mfma_f32_16x16x32_bf16 v[108:111], a[132:135], v[52:55], v[108:111]\n"
    "v_exp_f32 v74, v74\n"
    "v_mfma_f32_16x16x32_bf16 v[108:111], a[136:139], v[56:59], v[108:111]\n"
    "v_cvt_pk_bf16_f32 v133, v70, v71\n"
    "v_cvt_pk_bf16_f32 v136, v72, v73\n"
    "v_mfma_f32_16x16x32_bf16 v[108:111], a[140:143], v[60:63], v[108:111]\n"
    "s_waitcnt lgkmcnt(6)\n"
    "v_exp_f32 v75, v75\n"
    "v_mfma_f32_16x16x32_bf16 a[16:19], a[176:179], v[144:147], a[16:19]\n"
    "ds_read_b128 a[232:235], v199 offset:18432\n"
    "v_cvt_pk_bf16_f32 v137, v74, v75\n"
    "v_mfma_f32_16x16x32_bf16 a[48:51], a[176:179], v[148:151], a[48:51]\n"
    "v_exp_f32 v76, v76\n"
    "v_mfma_f32_16x16x32_bf16 a[20:23], a[180:183], v[144:147], a[20:23]\n"
    "v_cndmask_b32 v164, v202, v193, s[44:45]\n"
    "v_cndmask_b32 v165, v202, v193, s[46:47]\n"
    "v_mfma_f32_16x16x32_bf16 a[52:55], a[180:183], v[148:151], a[52:55]\n"
    "v_exp_f32 v77, v77\n"
    "v_mfma_f32_16x16x32_bf16 a[24:27], a[184:187], v[144:147], a[24:27]\n"
    "v_exp_f32 v78, v78\n"
    "v_mfma_f32_16x16x32_bf16 a[56:59], a[184:187], v[148:151], a[56:59]\n"
    "ds_read_b128 a[236:239], v199 offset:19456\n"
    "v_cvt_pk_bf16_f32 v140, v76, v77\n"
    "v_mfma_f32_16x16x32_bf16 a[28:31], a[188:191], v[144:147], a[28:31]\n"
    "v_exp_f32 v79, v79\n"
    "v_mfma_f32_16x16x32_bf16 a[60:63], a[188:191], v[148:151], a[60:63]\n"
    "v_cvt_pk_bf16_f32 v141, v78, v79\n"
    "v_cndmask_b32 v166, v202, v193, s[48:49]\n"
    "v_mfma_f32_16x16x32_bf16 a[92:95], a[188:191], v[152:155], a[92:95]\n"
    "v_exp_f32 v80, v80\n"
    "v_mfma_f32_16x16x32_bf16 v[112:115], a[144:147], v[0:3], v[176:179]\n"
    "ds_read_b128 a[240:243], v199 offset:20480\n"
    "v_cndmask_b32 v167, v202, v193, s[50:51]\n"
    "v_mfma_f32_16x16x32_bf16 v[112:115], a[148:151], v[4:7], v[112:115]\n"
    "s_add_u32 s14, s14, 1\n"
    "v_exp_f32 v81, v81\n"
    "v_mfma_f32_16x16x32_bf16 v[112:115], a[152:155], v[8:11], v[112:115]\n"
    "s_cmp_gt_u32 s18, s14\n"
    "v_add_f32 v196, v196, v80\n"
    "v_add_f32 v196, v196, v81\n"
    "v_mfma_f32_16x16x32_bf16 v[112:115], a[156:159], v[12:15], v[112:115]\n"
    "s_cselect_b64 vcc, -1, 0\n"
    "buffer_load_dwordx4 v200, s[60:63], s20 offen offset:2048 sc1 lds\n"
    "v_mfma_f32_16x16x32_bf16 v[116:119], a[144:147], v[16:19], v[180:183]\n"
    "s_cmp_eq_u32 s18, s14\n"
    "v_exp_f32 v82, v82\n"
    "v_mfma_f32_16x16x32_bf16 v[116:119], a[148:151], v[20:23], v[116:119]\n"
    "s_cselect_b64 s[44:45], s[72:73], vcc\n"
    "ds_read_b128 a[244:247], v199 offset:21504\n"
    "v_cvt_pk_bf16_f32 v130, v80, v81\n"
    "v_mfma_f32_16x16x32_bf16 v[116:119], a[152:155], v[24:27], v[116:119]\n"
    "s_cselect_b64 s[46:47], s[74:75], vcc\n"
    "v_exp_f32 v83, v83\n"
    "v_mfma_f32_16x16x32_bf16 v[116:119], a[156:159], v[28:31], v[116:119]\n"
    "s_cselect_b64 s[48:49], s[76:77], vcc\n"
    "v_add_f32 v196, v196, v82\n"
    "v_add_f32 v196, v196, v83\n"
    "v_mfma_f32_16x16x32_bf16 a[124:127], a[188:191], v[156:159], a[124:127]\n"
    "s_cselect_b64 s[50:51], s[78:79], vcc\n"
    "v_exp_f32 v84, v84\n"
    "v_mfma_f32_16x16x32_bf16 a[88:91], a[184:187], v[152:155], a[88:91]\n"
    "ds_read_b128 a[248:251], v199 offset:22528\n"
    "v_cvt_pk_bf16_f32 v131, v82, v83\n"
    "v_mfma_f32_16x16x32_bf16 a[120:123], a[184:187], v[156:159], a[120:123]\n"
    "v_exp_f32 v85, v85\n"
    "v_mfma_f32_16x16x32_bf16 a[84:87], a[180:183], v[152:155], a[84:87]\n"
    "v_add_f32 v197, v197, v84\n"
    "v_add_f32 v197, v197, v85\n"
    "v_mfma_f32_16x16x32_bf16 a[116:119], a[180:183], v[156:159], a[116:119]\n"
    "v_exp_f32 v86, v86\n"
    "v_mfma_f32_16x16x32_bf16 a[80:83], a[176:179], v[152:155], a[80:83]\n"
    "v_exp_f32 v87, v87\n"
    "v_mfma_f32_16x16x32_bf16 a[112:115], a[176:179], v[156:159], a[112:115]\n"
    "ds_read_b128 a[252:255], v199 offset:23552\n"
    "v_cvt_pk_bf16_f32 v134, v84, v85\n"
    "v_mfma_f32_16x16x32_bf16 a[76:79], a[172:175], v[152:155], a[76:79]\n"
    "v_exp_f32 v88, v88\n"
    "v_mfma_f32_16x16x32_bf16 v[120:123], a[144:147], v[32:35], v[184:187]\n"
    "ds_read_b128 a[208:211], v199 offset:4096\n"
    "v_add_f32 v197, v197, v86\n"
    "v_mfma_f32_16x16x32_bf16 v[120:123], a[148:151], v[36:39], v[120:123]\n"
    "v_exp_f32 v89, v89\n"
    "v_mfma_f32_16x16x32_bf16 v[120:123], a[152:155], v[40:43], v[120:123]\n"
    "v_add_f32 v197, v197, v87\n"
    "v_cvt_pk_bf16_f32 v135, v86, v87\n"
    "v_mfma_f32_16x16x32_bf16 v[120:123], a[156:159], v[44:47], v[120:123]\n"
    "buffer_load_dwordx4 v200, s[60:63], s20 offen offset:3072 sc1 lds\n"
    "v_mfma_f32_16x16x32_bf16 v[124:127], a[144:147], v[48:51], v[188:191]\n"
    "ds_read_b128 a[212:215], v199 offset:5120\n"
    "v_cvt_pk_bf16_f32 v138, v88, v89\n"
    "v_mfma_f32_16x16x32_bf16 v[124:127], a[148:151], v[52:55], v[124:127]\n"
    "v_exp_f32 v90, v90\n"
    "v_mfma_f32_16x16x32_bf16 v[124:127], a[152:155], v[56:59], v[124:127]\n"
    "v_exp_f32 v91, v91\n"
    "v_mfma_f32_16x16x32_bf16 v[124:127], a[156:159], v[60:63], v[124:127]\n"
    "v_cvt_pk_bf16_f32 v139, v90, v91\n"
    "v_cndmask_b32 v180, v202, v193, s[44:45]\n"
    "v_mfma_f32_16x16x32_bf16 a[108:111], a[172:175], v[156:159], a[108:111]\n"
    "v_exp_f32 v92, v92\n"
    "v_mfma_f32_16x16x32_bf16 a[72:75], a[168:171], v[152:155], a[72:75]\n"
    "ds_read_b128 a[216:219], v199 offset:6144\n"
    "v_cndmask_b32 v181, v202, v193, s[46:47]\n"
    "v_mfma_f32_16x16x32_bf16 a[104:107], a[168:171], v[156:159], a[104:107]\n"
    "v_exp_f32 v93, v93\n"
    "v_mfma_f32_16x16x32_bf16 a[68:71], a[164:167], v[152:155], a[68:71]\n"
    "v_cvt_pk_bf16_f32 v142, v92, v93\n"
    "v_cndmask_b32 v182, v202, v193, s[48:49]\n"
    "v_mfma_f32_16x16x32_bf16 a[100:103], a[164:167], v[156:159], a[100:103]\n"
    "v_exp_f32 v94, v94\n"
    "v_mfma_f32_16x16x32_bf16 a[64:67], a[160:163], v[152:155], a[64:67]\n"
    "ds_read_b128 a[220:223], v199 offset:7168\n"
    "v_cndmask_b32 v183, v202, v193, s[50:51]\n"
    "v_mfma_f32_16x16x32_bf16 a[96:99], a[160:163], v[156:159], a[96:99]\n"
    "v_exp_f32 v95, v95\n"
    "v_mfma_f32_16x16x32_bf16 v[240:243], v[248:251], v[152:155], v[240:243]\n"
    "v_cvt_pk_bf16_f32 v143, v94, v95\n"
    "v_mfma_f32_16x16x32_bf16 v[244:247], v[248:251], v[156:159], v[244:247]\n"
    "L_att_U1_%=:\n"
    "s_waitcnt lgkmcnt(8)\n"
    "v_mfma_f32_16x16x32_bf16 v[68:71], a[192:195], v[16:19], v[164:167]\n"
    "s_add_u32 m0, s19, 16384\n"
    "ds_read_b128 a[128:131], v199 offset:8192\n"
    "v_exp_f32 v96, v96\n"
    "v_mfma_f32_16x16x32_bf16 v[68:71], a[196:199], v[20:23], v[68:71]\n"
    "s_add_u32 s16, s16, 1\n"
    "v_exp_f32 v97, v97\n"
    "v_mfma_f32_16x16x32_bf16 v[68:71], a[200:203], v[24:27], v[68:71]\n"
    "s_add_u32 s21, s21, 32768\n"
    "v_add_f32 v196, v196, v96\n"
    "v_add_f32 v196, v196, v97\n"
    "v_cvt_pk_bf16_f32 v144, v96, v97\n"
    "v_mfma_f32_16x16x32_bf16 v[68:71], a[204:207], v[28:31], v[68:71]\n"
    "s_and_b32 s21, s21, 0x1ffff\n"
    "ds_read_b128 a[132:135], v199 offset:9216\n"
    "buffer_load_dwordx4 v200, s[64:67], s20 offen offset:0 sc1 lds\n"
    "v_mfma_f32_16x16x32_bf16 v[72:75], a[192:195], v[32:35], v[168:171]\n"
    "s_lshl_b32 s14, s16, 1\n"
    "v_exp_f32 v98, v98\n"
    "v_mfma_f32_16x16x32_bf16 v[72:75], a[196:199], v[36:39], v[72:75]\n"
    "s_cmp_gt_u32 s18, s14\n"
    "v_mfma_f32_16x16x32_bf16 v[72:75], a[200:203], v[40:43], v[72:75]\n"
    "s_cselect_b64 vcc, -1, 0\n"
    "v_exp_f32 v99, v99\n"
    "v_mfma_f32_16x16x32_bf16 v[72:75], a[204:207], v[44:47], v[72:75]\n"
    "s_cmp_eq_u32 s18, s14\n"
    "ds_read_b128 a[136:139], v199 offset:10240\n"
    "v_exp_f32 v100, v100\n"
    "v_mfma_f32_16x16x32_bf16 a[0:3], a[224:227], v[128:131], a[0:3]\n"
    "s_cselect_b64 s[44:45], s[72:73], vcc\n"
    "v_add_f32 v196, v196, v98\n"
    "v_add_f32 v196, v196, v99\n"
    "v_cvt_pk_bf16_f32 v145, v98, v99\n"
    "v_mfma_f32_16x16x32_bf16 a[32:35], a[224:227], v[132:135], a[32:35]\n"
    "s_cselect_b64 s[46:47], s[74:75], vcc\n"
    "v_exp_f32 v101, v101\n"
    "v_mfma_f32_16x16x32_bf16 a[4:7], a[228:231], v[128:131], a[4:7]\n"
    "s_cselect_b64 s[48:49], s[76:77], vcc\n"
    "ds_read_b128 a[140:143], v199 offset:11264\n"
    "v_add_f32 v197, v197, v100\n"
    "v_add_f32 v197, v197, v101\n"
    "v_mfma_f32_16x16x32_bf16 a[36:39], a[228:231], v[132:135], a[36:39]\n"
    "s_cselect_b64 s[50:51], s[78:79], vcc\n"
    "v_exp_f32 v102, v102\n"
    "v_cvt_pk_bf16_f32 v148, v100, v101\n"
    "v_mfma_f32_16x16x32_bf16 a[8:11], a[232:235], v[128:131], a[8:11]\n"
    "v_exp_f32 v103, v103\n"
    "v_cndmask_b32 v164, v202, v193, s[44:45]\n"
    "v_mfma_f32_16x16x32_bf16 a[40:43], a[232:235], v[132:135], a[40:43]\n"
    "v_add_f32 v197, v197, v102\n"
    "v_add_f32 v197, v197, v103\n"
    "v_cvt_pk_bf16_f32 v149, v102, v103\n"
    "v_mfma_f32_16x16x32_bf16 a[12:15], a[236:239], v[128:131], a[12:15]\n"
    "ds_read_b128 a[160:163], v199 offset:24576\n"
    "v_exp_f32 v104, v104\n"
    "v_mfma_f32_16x16x32_bf16 a[44:47], a[236:239], v[132:135], a[44:47]\n"
    "s_waitcnt lgkmcnt(5)\n"
    "v_exp_f32 v105, v105\n"
    "v_cndmask_b32 v165, v202, v193, s[46:47]\n"
    "v_mfma_f32_16x16x32_bf16 a[16:19], a[240:243], v[128:131], a[16:19]\n"
    "v_cvt_pk_bf16_f32 v152, v104, v105\n"
    "v_cndmask_b32 v166, v202, v193, s[48:49]\n"
    "v_cndmask_b32 v167, v202, v193, s[50:51]\n"
    "v_mfma_f32_16x16x32_bf16 a[48:51], a[240:243], v[132:135], a[48:51]\n"
    "s_add_u32 s14, s14, 1\n"
    "buffer_load_dwordx4 v200, s[64:67], s20 offen offset:1024 sc1 lds\n"
    "v_mfma_f32_16x16x32_bf16 a[20:23], a[244:247], v[128:131], a[20:23]\n"
    "s_cmp_gt_u32 s18, s14\n"
    "ds_read_b128 a[164:167], v199 offset:25600\n"
    "v_mfma_f32_16x16x32_bf16 v[76:79], a[192:195], v[48:51], v[172:175]\n"
    "s_cselect_b64 vcc, -1, 0\n"
    "v_exp_f32 v106, v106\n"
    "v_mfma_f32_16x16x32_bf16 v[76:79], a[196:199], v[52:55], v[76:79]\n"
    "s_cmp_eq_u32 s18, s14\n"
    "v_exp_f32 v107, v107\n"
    "v_mfma_f32_16x16x32_bf16 v[76:79], a[200:203], v[56:59], v[76:79]\n"
    "s_cselect_b64 s[44:45], s[72:73], vcc\n"
    "ds_read_b128 a[168:171], v199 offset:26624\n"
    "v_cvt_pk_bf16_f32 v153, v106, v107\n"
    "v_mfma_f32_16x16x32_bf16 v[76:79], a[204:207], v[60:63], v[76:79]\n"
    "s_cselect_b64 s[46:47], s[74:75], vcc\n"
    "v_exp_f32 v108, v108\n"
    "v_mfma_f32_16x16x32_bf16 v[84:87], a[208:211], v[16:19], v[180:183]\n"
    "s_cselect_b64 s[48:49], s[76:77], vcc\n"
    "v_exp_f32 v109, v109\n"
    "v_mfma_f32_16x16x32_bf16 v[84:87], a[212:215], v[20:23], v[84:87]\n"
    "s_cselect_b64 s[50:51], s[78:79], vcc\n"
    "v_cvt_pk_bf16_f32 v156, v108, v109\n"
    "v_mfma_f32_16x16x32_bf16 v[84:87], a[216:219], v[24:27], v[84:87]\n"
    "ds_read_b128 a[172:175], v199 offset:27648\n"
    "v_exp_f32 v110, v110\n"
    "v_mfma_f32_16x16x32_bf16 v[84:87], a[220:223], v[28:31], v[84:87]\n"
    "v_exp_f32 v111, v111\n"
    "v_cndmask_b32 v180, v202, v193, s[44:45]\n"
    "v_mfma_f32_16x16x32_bf16 a[52:55], a[244:247], v[132:135], a[52:55]\n"
    "v_cvt_pk_bf16_f32 v157, v110, v111\n"
    "v_cndmask_b32 v181, v202, v193, s[46:47]\n"
    "v_cndmask_b32 v182, v202, v193, s[48:49]\n"
    "v_mfma_f32_16x16x32_bf16 a[24:27], a[248:251], v[128:131], a[24:27]\n"
    "v_exp_f32 v112, v112\n"
    "v_cndmask_b32 v183, v202, v193, s[50:51]\n"
    "v_mfma_f32_16x16x32_bf16 a[56:59], a[248:251], v[132:135], a[56:59]\n"
    "ds_read_b128 a[176:179], v199 offset:28672\n"
    "v_mfma_f32_16x16x32_bf16 a[28:31], a[252:255], v[128:131], a[28:31]\n"
    "v_exp_f32 v113, v113\n"
    "v_mfma_f32_16x16x32_bf16 a[60:63], a[252:255], v[132:135], a[60:63]\n"
    "buffer_load_dwordx4 v200, s[64:67], s20 offen offset:2048 sc1 lds\n"
    "v_add_f32 v196, v196, v112\n"
    "v_mfma_f32_16x16x32_bf16 a[92:95], a[252:255], v[136:139], a[92:95]\n"
    "ds_read_b128 a[180:183], v199 offset:29696\n"
    "v_add_f32 v196, v196, v113\n"
    "v_cvt_pk_bf16_f32 v146, v112, v113\n"
    "v_mfma_f32_16x16x32_bf16 a[124:127], a[252:255], v[140:143], a[124:127]\n"
    "v_exp_f32 v114, v114\n"
    "v_mfma_f32_16x16x32_bf16 a[88:91], a[248:251], v[136:139], a[88:91]\n"
    "v_exp_f32 v115, v115\n"
    "v_mfma_f32_16x16x32_bf16 a[120:123], a[248:251], v[140:143], a[120:123]\n"
    "v_add_f32 v196, v196, v114\n"
    "v_add_f32 v196, v196, v115\n"
    "v_cvt_pk_bf16_f32 v147, v114, v115\n"
    "v_mfma_f32_16x16x32_bf16 a[84:87], a[244:247], v[136:139], a[84:87]\n"
    "ds_read_b128 a[184:187], v199 offset:30720\n"
    "v_exp_f32 v116, v116\n"
    "v_mfma_f32_16x16x32_bf16 a[116:119], a[244:247], v[140:143], a[116:119]\n"
    "v_mfma_f32_16x16x32_bf16 v[88:91], a[208:211], v[32:35], v[184:187]\n"
    "v_exp_f32 v117, v117\n"
    "v_mfma_f32_16x16x32_bf16 v[88:91], a[212:215], v[36:39], v[88:91]\n"
    "ds_read_b128 a[188:191], v199 offset:31744\n"
    "v_exp_f32 v118, v118\n"
    "v_mfma_f32_16x16x32_bf16 v[88:91], a[216:219], v[40:43], v[88:91]\n"
    "v_add_f32 v197, v197, v116\n"
    "v_add_f32 v197, v197, v117\n"
    "v_cvt_pk_bf16_f32 v150, v116, v117\n"
    "v_mfma_f32_16x16x32_bf16 v[88:91], a[220:223], v[44:47], v[88:91]\n"
    "v_exp_f32 v119, v119\n"
    "v_mfma_f32_16x16x32_bf16 v[92:95], a[208:211], v[48:51], v[188:191]\n"
    "v_exp_f32 v120, v120\n"
    "v_add_f32 v197, v197, v118\n"
    "v_mfma_f32_16x16x32_bf16 v[92:95], a[212:215], v[52:55], v[92:95]\n"
    "ds_read_b128 a[144:147], v199 offset:12288\n"
    "v_add_f32 v197, v197, v119\n"
    "v_cvt_pk_bf16_f32 v151, v118, v119\n"
    "v_mfma_f32_16x16x32_bf16 v[92:95], a[216:219], v[56:59], v[92:95]\n"
    "v_exp_f32 v121, v121\n"
    "v_mfma_f32_16x16x32_bf16 v[92:95], a[220:223], v[60:63], v[92:95]\n"
    "buffer_load_dwordx4 v200, s[64:67], s20 offen offset:3072 sc1 lds\n"
    "v_cvt_pk_bf16_f32 v154, v120, v121\n"
    "v_mfma_f32_16x16x32_bf16 a[80:83], a[240:243], v[136:139], a[80:83]\n"
    "s_add_u32 s20, s20, 16384\n"
    "v_mfma_f32_16x16x32_bf16 a[112:115], a[240:243], v[140:143], a[112:115]\n"
    "s_add_u32 s19, s19, 32768\n"
    "ds_read_b128 a[148:151], v199 offset:13312\n"
    "v_exp_f32 v122, v122\n"
    "v_mfma_f32_16x16x32_bf16 a[76:79], a[236:239], v[136:139], a[76:79]\n"
    "s_and_b32 s19, s19, 0x1ffff\n"
    "v_mfma_f32_16x16x32_bf16 a[108:111], a[236:239], v[140:143], a[108:111]\n"
    "v_exp_f32 v123, v123\n"
    "v_mfma_f32_16x16x32_bf16 a[72:75], a[232:235], v[136:139], a[72:75]\n"
    "ds_read_b128 a[152:155], v199 offset:14336\n"
    "v_exp_f32 v124, v124\n"
    "v_mfma_f32_16x16x32_bf16 a[104:107], a[232:235], v[140:143], a[104:107]\n"
    "v_cvt_pk_bf16_f32 v155, v122, v123\n"
    "v_mfma_f32_16x16x32_bf16 a[68:71], a[228:231], v[136:139], a[68:71]\n"
    "v_exp_f32 v125, v125\n"
    "v_mfma_f32_16x16x32_bf16 a[100:103], a[228:231], v[140:143], a[100:103]\n"
    "v_exp_f32 v126, v126\n"
    "v_cvt_pk_bf16_f32 v158, v124, v125\n"
    "v_mfma_f32_16x16x32_bf16 a[64:67], a[224:227], v[136:139], a[64:67]\n"
    "s_waitcnt vmcnt(8)\n"
    "s_barrier\n"
    "ds_read_b128 a[156:159], v199 offset:15360\n"
    "v_mfma_f32_16x16x32_bf16 a[96:99], a[224:227], v[140:143], a[96:99]\n"
    "v_exp_f32 v127, v127\n"
    "v_mfma_f32_16x16x32_bf16 v[240:243], v[248:251], v[136:139], v[240:243]\n"
    "v_cvt_pk_bf16_f32 v159, v126, v127\n"
    "v_mfma_f32_16x16x32_bf16 v[244:247], v[248:251], v[140:143], v[244:247]\n"
    "s_waitcnt lgkmcnt(8)\n"
    "v_add_u32 v199, s21, v198\n"
    "v_mfma_f32_16x16x32_bf16 v[100:103], a[128:131], v[16:19], v[164:167]\n"
    "s_mov_b32 m0, s19\n"
    "v_exp_f32 v68, v68\n"
    "v_mfma_f32_16x16x32_bf16 v[100:103], a[132:135], v[20:23], v[100:103]\n"
    "s_add_u32 s16, s16, 1\n"
    "ds_read_b128 a[192:195], v199 offset:0\n"
    "v_mfma_f32_16x16x32_bf16 v[100:103], a[136:139], v[24:27], v[100:103]\n"
    "v_exp_f32 v69, v69\n"
    "v_mfma_f32_16x16x32_bf16 v[100:103], a[140:143], v[28:31], v[100:103]\n"
    "ds_read_b128 a[196:199], v199 offset:1024\n"
    "v_add_f32 v197, v197, v68\n"
    "v_mfma_f32_16x16x32_bf16 v[104:107], a[128:131], v[32:35], v[168:171]\n"
    "s_lshl_b32 s14, s16, 1\n"
    "buffer_load_dwordx4 v200, s[60:63], s20 offen offset:0 sc1 lds\n"
    "v_mfma_f32_16x16x32_bf16 v[104:107], a[132:135], v[36:39], v[104:107]\n"
    "s_cmp_gt_u32 s30, s14\n"
    "v_add_f32 v197, v197, v69\n"
    "v_cvt_pk_bf16_f32 v132, v68, v69\n"
    "v_mfma_f32_16x16x32_bf16 v[104:107], a[136:139], v[40:43], v[104:107]\n"
    "s_cselect_b64 vcc, -1, 0\n"
    "v_exp_f32 v70, v70\n"
    "v_mfma_f32_16x16x32_bf16 v[104:107], a[140:143], v[44:47], v[104:107]\n"
    "s_cmp_eq_u32 s30, s14\n"
    "ds_read_b128 a[200:203], v199 offset:2048\n"
    "v_mfma_f32_16x16x32_bf16 a[0:3], a[160:163], v[144:147], a[0:3]\n"
    "s_cselect_b64 s[44:45], s[72:73], vcc\n"
    "v_exp_f32 v71, v71\n"
    "v_mfma_f32_16x16x32_bf16 a[32:35], a[160:163], v[148:151], a[32:35]\n"
    "s_cselect_b64 s[46:47], s[74:75], vcc\n"
    "v_add_f32 v197, v197, v70\n"
    "v_add_f32 v197, v197, v71\n"
    "v_mfma_f32_16x16x32_bf16 a[4:7], a[164:167], v[144:147], a[4:7]\n"
    "s_cselect_b64 s[48:49], s[76:77], vcc\n"
    "v_exp_f32 v72, v72\n"
    "v_mfma_f32_16x16x32_bf16 a[36:39], a[164:167], v[148:151], a[36:39]\n"
    "s_cselect_b64 s[50:51], s[78:79], vcc\n"
    "ds_read_b128 a[204:207], v199 offset:3072\n"
    "v_cvt_pk_bf16_f32 v133, v70, v71\n"
    "v_mfma_f32_16x16x32_bf16 a[8:11], a[168:171], v[144:147], a[8:11]\n"
    "v_exp_f32 v73, v73\n"
    "v_mfma_f32_16x16x32_bf16 a[40:43], a[168:171], v[148:151], a[40:43]\n"
    "v_cvt_pk_bf16_f32 v136, v72, v73\n"
    "v_cndmask_b32 v168, v202, v194, s[44:45]\n"
    "v_mfma_f32_16x16x32_bf16 a[12:15], a[172:175], v[144:147], a[12:15]\n"
    "v_exp_f32 v74, v74\n"
    "v_mfma_f32_16x16x32_bf16 a[44:47], a[172:175], v[148:151], a[44:47]\n"
    "s_waitcnt lgkmcnt(4)\n"
    "ds_read_b128 a[224:227], v199 offset:16384\n"
    "v_cndmask_b32 v169, v202, v194, s[46:47]\n"
    "v_mfma_f32_16x16x32_bf16 a[16:19], a[176:179], v[144:147], a[16:19]\n"
    "v_exp_f32 v75, v75\n"
    "v_mfma_f32_16x16x32_bf16 a[48:51], a[176:179], v[148:151], a[48:51]\n"
    "v_cvt_pk_bf16_f32 v137, v74, v75\n"
    "v_cndmask_b32 v170, v202, v194, s[48:49]\n"
    "v_mfma_f32_16x16x32_bf16 a[20:23], a[180:183], v[144:147], a[20:23]\n"
    "buffer_load_dwordx4 v200, s[60:63], s20 offen offset:1024 sc1 lds\n"
    "v_mfma_f32_16x16x32_bf16 v[108:111], a[128:131], v[48:51], v[172:175]\n"
    "ds_read_b128 a[228:231], v199 offset:17408\n"
    "v_cndmask_b32 v171, v202, v194, s[50:51]\n"
    "v_mfma_f32_16x16x32_bf16 v[108:111], a[132:135], v[52:55], v[108:111]\n"
    "s_add_u32 s14, s14, 1\n"
    "v_exp_f32 v76, v76\n"
    "v_mfma_f32_16x16x32_bf16 v[108:111], a[136:139], v[56:59], v[108:111]\n"
    "s_cmp_gt_u32 s30, s14\n"
    "ds_read_b128 a[232:235], v199 offset:18432\n"
    "v_mfma_f32_16x16x32_bf16 v[108:111], a[140:143], v[60:63], v[108:111]\n"
    "s_cselect_b64 vcc, -1, 0\n"
    "v_exp_f32 v77, v77\n"
    "v_mfma_f32_16x16x32_bf16 v[116:119], a[144:147], v[16:19], v[180:183]\n"
    "s_cmp_eq_u32 s30, s14\n"
    "v_cvt_pk_bf16_f32 v140, v76, v77\n"
    "v_mfma_f32_16x16x32_bf16 v[116:119], a[148:151], v[20:23], v[116:119]\n"
    "s_cselect_b64 s[44:45], s[72:73], vcc\n"
    "v_exp_f32 v78, v78\n"
    "v_mfma_f32_16x16x32_bf16 v[116:119], a[152:155], v[24:27], v[116:119]\n"
    "s_cselect_b64 s[46:47], s[74:75], vcc\n"
    "ds_read_b128 a[236:239], v199 offset:19456\n"
    "v_mfma_f32_16x16x32_bf16 v[116:119], a[156:159], v[28:31], v[116:119]\n"
    "s_cselect_b64 s[48:49], s[76:77], vcc\n"
    "v_exp_f32 v79, v79\n"
    "v_mfma_f32_16x16x32_bf16 a[52:55], a[180:183], v[148:151], a[52:55]\n"
    "s_cselect_b64 s[50:51], s[78:79], vcc\n"
    "v_cvt_pk_bf16_f32 v141, v78, v79\n"
    "v_mfma_f32_16x16x32_bf16 a[24:27], a[184:187], v[144:147], a[24:27]\n"
    "v_exp_f32 v84, v84\n"
    "v_mfma_f32_16x16x32_bf16 a[56:59], a[184:187], v[148:151], a[56:59]\n"
    "ds_read_b128 a[240:243], v199 offset:20480\n"
    "v_mfma_f32_16x16x32_bf16 a[28:31], a[188:191], v[144:147], a[28:31]\n"
    "v_exp_f32 v85, v85\n"
    "v_mfma_f32_16x16x32_bf16 a[60:63], a[188:191], v[148:151], a[60:63]\n"
    "v_add_f32 v197, v197, v84\n"
    "v_add_f32 v197, v197, v85\n"
    "v_mfma_f32_16x16x32_bf16 a[92:95], a[188:191], v[152:155], a[92:95]\n"
    "buffer_load_dwordx4 v200, s[60:63], s20 offen offset:2048 sc1 lds\n"
    "v_mfma_f32_16x16x32_bf16 a[124:127], a[188:191], v[156:159], a[124:127]\n"
    "ds_read_b128 a[244:247], v199 offset:21504\n"
    "v_cvt_pk_bf16_f32 v134, v84, v85\n"
    "v_mfma_f32_16x16x32_bf16 a[88:91], a[184:187], v[152:155], a[88:91]\n"
    "v_exp_f32 v86, v86\n"
    "v_mfma_f32_16x16x32_bf16 a[120:123], a[184:187], v[156:159], a[120:123]\n"
    "v_mfma_f32_16x16x32_bf16 a[84:87], a[180:183], v[152:155], a[84:87]\n"
    "v_exp_f32 v87, v87\n"
    "v_mfma_f32_16x16x32_bf16 a[116:119], a[180:183], v[156:159], a[116:119]\n"
    "ds_read_b128 a[248:251], v199 offset:22528\n"
    "v_add_f32 v197, v197, v86\n"
    "v_mfma_f32_16x16x32_bf16 v[120:123], a[144:147], v[32:35], v[184:187]\n"
    "v_exp_f32 v88, v88\n"
    "v_mfma_f32_16x16x32_bf16 v[120:123], a[148:151], v[36:39], v[120:123]\n"
    "ds_read_b128 a[252:255], v199 offset:23552\n"
    "v_add_f32 v197, v197, v87\n"
    "v_mfma_f32_16x16x32_bf16 v[120:123], a[152:155], v[40:43], v[120:123]\n"
    "v_exp_f32 v89, v89\n"
    "v_mfma_f32_16x16x32_bf16 v[120:123], a[156:159], v[44:47], v[120:123]\n"
    "v_cvt_pk_bf16_f32 v135, v86, v87\n"
    "v_cvt_pk_bf16_f32 v138, v88, v89\n"
    "v_mfma_f32_16x16x32_bf16 v[124:127], a[144:147], v[48:51], v[188:191]\n"
    "v_exp_f32 v90, v90\n"
    "v_mfma_f32_16x16x32_bf16 v[124:127], a[148:151], v[52:55], v[124:127]\n"
    "ds_read_b128 a[208:211], v199 offset:4096\n"
    "v_cndmask_b32 v184, v202, v194, s[44:45]\n"
    "v_mfma_f32_16x16x32_bf16 v[124:127], a[152:155], v[56:59], v[124:127]\n"
    "v_exp_f32 v91, v91\n"
    "v_mfma_f32_16x16x32_bf16 v[124:127], a[156:159], v[60:63], v[124:127]\n"
    "v_cvt_pk_bf16_f32 v139, v90, v91\n"
    "v_cndmask_b32 v185, v202, v194, s[46:47]\n"
    "v_mfma_f32_16x16x32_bf16 a[80:83], a[176:179], v[152:155], a[80:83]\n"
    "buffer_load_dwordx4 v200, s[60:63], s20 offen offset:3072 sc1 lds\n"
    "v_mfma_f32_16x16x32_bf16 a[112:115], a[176:179], v[156:159], a[112:115]\n"
    "ds_read_b128 a[212:215], v199 offset:5120\n"
    "v_cndmask_b32 v186, v202, v194, s[48:49]\n"
    "v_mfma_f32_16x16x32_bf16 a[76:79], a[172:175], v[152:155], a[76:79]\n"
    "v_exp_f32 v92, v92\n"
    "v_mfma_f32_16x16x32_bf16 a[108:111], a[172:175], v[156:159], a[108:111]\n"
    "v_cndmask_b32 v187, v202, v194, s[50:51]\n"
    "v_mfma_f32_16x16x32_bf16 a[72:75], a[168:171], v[152:155], a[72:75]\n"
    "v_exp_f32 v93, v93\n"
    "v_mfma_f32_16x16x32_bf16 a[104:107], a[168:171], v[156:159], a[104:107]\n"
    "ds_read_b128 a[216:219], v199 offset:6144\n"
    "v_cvt_pk_bf16_f32 v142, v92, v93\n"
    "v_mfma_f32_16x16x32_bf16 a[68:71], a[164:167], v[152:155], a[68:71]\n"
    "v_exp_f32 v94, v94\n"
    "v_mfma_f32_16x16x32_bf16 a[100:103], a[164:167], v[156:159], a[100:103]\n"
    "v_mfma_f32_16x16x32_bf16 a[64:67], a[160:163], v[152:155], a[64:67]\n"
    "v_exp_f32 v95, v95\n"
    "v_mfma_f32_16x16x32_bf16 a[96:99], a[160:163], v[156:159], a[96:99]\n"
    "ds_read_b128 a[220:223], v199 offset:7168\n"
    "v_cvt_pk_bf16_f32 v143, v94, v95\n"
    "v_mfma_f32_16x16x32_bf16 v[240:243], v[248:251], v[152:155], v[240:243]\n"
    "v_mfma_f32_16x16x32_bf16 v[244:247], v[248:251], v[156:159], v[244:247]\n"
    "s_waitcnt lgkmcnt(8)\n"
    "v_mfma_f32_16x16x32_bf16 v[72:75], a[192:195], v[32:35], v[168:171]\n"
    "s_add_u32 m0, s19, 16384\n"
    "ds_read_b128 a[128:131], v199 offset:8192\n"
    "v_exp_f32 v100, v100\n"
    "v_mfma_f32_16x16x32_bf16 v[72:75], a[196:199], v[36:39], v[72:75]\n"
    "s_add_u32 s16, s16, 1\n"
    "v_exp_f32 v101, v101\n"
    "v_mfma_f32_16x16x32_bf16 v[72:75], a[200:203], v[40:43], v[72:75]\n"
    "s_add_u32 s21, s21, 32768\n"
    "ds_read_b128 a[132:135], v199 offset:9216\n"
    "buffer_load_dwordx4 v200, s[64:67], s20 offen offset:0 sc1 lds\n"
    "v_mfma_f32_16x16x32_bf16 v[72:75], a[204:207], v[44:47], v[72:75]\n"
    "s_and_b32 s21, s21, 0x1ffff\n"
    "v_add_f32 v197, v197, v100\n"
    "v_add_f32 v197, v197, v101\n"
    "v_cvt_pk_bf16_f32 v148, v100, v101\n"
    "v_mfma_f32_16x16x32_bf16 v[76:79], a[192:195], v[48:51], v[172:175]\n"
    "s_lshl_b32 s14, s16, 1\n"
    "v_exp_f32 v102, v102\n"
    "v_mfma_f32_16x16x32_bf16 v[76:79], a[196:199], v[52:55], v[76:79]\n"
    "s_cmp_gt_u32 s30, s14\n"
    "ds_read_b128 a[136:139], v199 offset:10240\n"
    "v_exp_f32 v103, v103\n"
    "v_mfma_f32_16x16x32_bf16 v[76:79], a[200:203], v[56:59], v[76:79]\n"
    "s_cselect_b64 vcc, -1, 0\n"
    "v_add_f32 v197, v197, v102\n"
    "v_add_f32 v197, v197, v103\n"
    "v_cvt_pk_bf16_f32 v149, v102, v103\n"
    "v_mfma_f32_16x16x32_bf16 v[76:79], a[204:207], v[60:63], v[76:79]\n"
    "s_cmp_eq_u32 s30, s14\n"
    "ds_read_b128 a[140:143], v199 offset:11264\n"
    "v_exp_f32 v104, v104\n"
    "v_mfma_f32_16x16x32_bf16 a[32:35], a[224:227], v[132:135], a[32:35]\n"
    "s_cselect_b64 s[44:45], s[72:73], vcc\n"
    "v_exp_f32 v105, v105\n"
    "v_mfma_f32_16x16x32_bf16 a[64:67], a[224:227], v[136:139], a[64:67]\n"
    "s_cselect_b64 s[46:47], s[74:75], vcc\n"
    "v_cvt_pk_bf16_f32 v152, v104, v105\n"
    "v_mfma_f32_16x16x32_bf16 a[36:39], a[228:231], v[132:135], a[36:39]\n"
    "s_cselect_b64 s[48:49], s[76:77], vcc\n"
    "ds_read_b128 a[160:163], v199 offset:24576\n"
    "v_exp_f32 v106, v106\n"
    "v_mfma_f32_16x16x32_bf16 a[68:71], a[228:231], v[136:139], a[68:71]\n"
    "s_cselect_b64 s[50:51], s[78:79], vcc\n"
    "v_exp_f32 v107, v107\n"
    "v_mfma_f32_16x16x32_bf16 a[40:43], a[232:235], v[132:135], a[40:43]\n"
    "v_cvt_pk_bf16_f32 v153, v106, v107\n"
    "v_cndmask_b32 v168, v202, v194, s[44:45]\n"
    "v_cndmask_b32 v169, v202, v194, s[46:47]\n"
    "v_mfma_f32_16x16x32_bf16 a[72:75], a[232:235], v[136:139], a[72:75]\n"
    "ds_read_b128 a[164:167], v199 offset:25600\n"
    "buffer_load_dwordx4 v200, s[64:67], s20 offen offset:1024 sc1 lds\n"
    "v_mfma_f32_16x16x32_bf16 a[44:47], a[236:239], v[132:135], a[44:47]\n"
    "v_exp_f32 v108, v108\n"
    "v_cndmask_b32 v170, v202, v194, s[48:49]\n"
    "v_mfma_f32_16x16x32_bf16 a[76:79], a[236:239], v[136:139], a[76:79]\n"
    "s_waitcnt lgkmcnt(6)\n"
    "ds_read_b128 a[168:171], v199 offset:26624\n"
    "v_cndmask_b32 v171, v202, v194, s[50:51]\n"
    "v_mfma_f32_16x16x32_bf16 a[48:51], a[240:243], v[132:135], a[48:51]\n"
    "s_add_u32 s14, s14, 1\n"
    "v_exp_f32 v109, v109\n"
    "v_mfma_f32_16x16x32_bf16 a[80:83], a[240:243], v[136:139], a[80:83]\n"
    "s_cmp_gt_u32 s30, s14\n"
    "v_exp_f32 v110, v110\n"
    "v_cvt_pk_bf16_f32 v156, v108, v109\n"
    "v_mfma_f32_16x16x32_bf16 a[52:55], a[244:247], v[132:135], a[52:55]\n"
    "s_cselect_b64 vcc, -1, 0\n"
    "ds_read_b128 a[172:175], v199 offset:27648\n"
    "v_mfma_f32_16x16x32_bf16 a[84:87], a[244:247], v[136:139], a[84:87]\n"
    "s_cmp_eq_u32 s30, s14\n"
    "v_exp_f32 v111, v111\n"
    "v_mfma_f32_16x16x32_bf16 a[56:59], a[248:251], v[132:135], a[56:59]\n"
    "s_cselect_b64 s[44:45], s[72:73], vcc\n"
    "v_exp_f32 v116, v116\n"
    "v_cvt_pk_bf16_f32 v157, v110, v111\n"
    "v_mfma_f32_16x16x32_bf16 v[88:91], a[208:211], v[32:35], v[184:187]\n"
    "s_cselect_b64 s[46:47], s[74:75], vcc\n"
    "ds_read_b128 a[144:147], v199 offset:12288\n"
    "v_exp_f32 v117, v117\n"
    "v_mfma_f32_16x16x32_bf16 v[88:91], a[212:215], v[36:39], v[88:91]\n"
    "s_cselect_b64 s[48:49], s[76:77], vcc\n"
    "v_add_f32 v197, v197, v116\n"
    "v_add_f32 v197, v197, v117\n"
    "v_cvt_pk_bf16_f32 v150, v116, v117\n"
    "v_mfma_f32_16x16x32_bf16 v[88:91], a[216:219], v[40:43], v[88:91]\n"
    "s_cselect_b64 s[50:51], s[78:79], vcc\n"
    "ds_read_b128 a[148:151], v199 offset:13312\n"
    "buffer_load_dwordx4 v200, s[64:67], s20 offen offset:2048 sc1 lds\n"
    "v_mfma_f32_16x16x32_bf16 v[88:91], a[220:223], v[44:47], v[88:91]\n"
    "v_exp_f32 v118, v118\n"
    "v_cndmask_b32 v184, v202, v194, s[44:45]\n"
    "v_mfma_f32_16x16x32_bf16 v[92:95], a[208:211], v[48:51], v[188:191]\n"
    "v_cndmask_b32 v185, v202, v194, s[46:47]\n"
    "v_cndmask_b32 v186, v202, v194, s[48:49]\n"
    "v_cndmask_b32 v187, v202, v194, s[50:51]\n"
    "v_mfma_f32_16x16x32_bf16 v[92:95], a[212:215], v[52:55], v[92:95]\n"
    "ds_read_b128 a[152:155], v199 offset:14336\n"
    "v_exp_f32 v119, v119\n"
    "v_mfma_f32_16x16x32_bf16 v[92:95], a[216:219], v[56:59], v[92:95]\n"
    "v_exp_f32 v120, v120\n"
    "v_add_f32 v197, v197, v118\n"
    "v_mfma_f32_16x16x32_bf16 v[92:95], a[220:223], v[60:63], v[92:95]\n"
    "ds_read_b128 a[156:159], v199 offset:15360\n"
    "v_add_f32 v197, v197, v119\n"
    "v_cvt_pk_bf16_f32 v151, v118, v119\n"
    "v_mfma_f32_16x16x32_bf16 a[88:91], a[248:251], v[136:139], a[88:91]\n"
    "v_exp_f32 v121, v121\n"
    "v_mfma_f32_16x16x32_bf16 a[60:63], a[252:255], v[132:135], a[60:63]\n"
    "v_exp_f32 v122, v122\n"
    "v_cvt_pk_bf16_f32 v154, v120, v121\n"
    "v_mfma_f32_16x16x32_bf16 a[92:95], a[252:255], v[136:139], a[92:95]\n"
    "ds_read_b128 a[176:179], v199 offset:28672\n"
    "v_mfma_f32_16x16x32_bf16 a[124:127], a[252:255], v[140:143], a[124:127]\n"
    "v_exp_f32 v123, v123\n"
    "v_mfma_f32_16x16x32_bf16 a[120:123], a[248:251], v[140:143], a[120:123]\n"
    "buffer_load_dwordx4 v200, s[64:67], s20 offen offset:3072 sc1 lds\n"
    "v_cvt_pk_bf16_f32 v155, v122, v123\n"
    "v_mfma_f32_16x16x32_bf16 a[116:119], a[244:247], v[140:143], a[116:119]\n"
    "s_add_u32 s20, s20, 16384\n"
    "ds_read_b128 a[180:183], v199 offset:29696\n"
    "v_mfma_f32_16x16x32_bf16 a[112:115], a[240:243], v[140:143], a[112:115]\n"
    "s_add_u32 s19, s19, 32768\n"
    "v_exp_f32 v124, v124\n"
    "v_mfma_f32_16x16x32_bf16 a[108:111], a[236:239], v[140:143], a[108:111]\n"
    "s_and_b32 s19, s19, 0x1ffff\n"
    "ds_read_b128 a[184:187], v199 offset:30720\n"
    "v_exp_f32 v125, v125\n"
    "v_mfma_f32_16x16x32_bf16 a[104:107], a[232:235], v[140:143], a[104:107]\n"
    "v_cvt_pk_bf16_f32 v158, v124, v125\n"
    "v_mfma_f32_16x16x32_bf16 a[100:103], a[228:231], v[140:143], a[100:103]\n"
    "s_waitcnt vmcnt(8)\n"
    "s_barrier\n"
    "v_exp_f32 v126, v126\n"
    "v_mfma_f32_16x16x32_bf16 a[96:99], a[224:227], v[140:143], a[96:99]\n"
    "ds_read_b128 a[188:191], v199 offset:31744\n"
    "v_exp_f32 v127, v127\n"
    "v_mfma_f32_16x16x32_bf16 v[240:243], v[248:251], v[136:139], v[240:243]\n"
    "v_cvt_pk_bf16_f32 v159, v126, v127\n"
    "v_mfma_f32_16x16x32_bf16 v[244:247], v[248:251], v[140:143], v[244:247]\n"
    "s_waitcnt lgkmcnt(8)\n"
    "v_add_u32 v199, s21, v198\n"
    "v_mfma_f32_16x16x32_bf16 v[104:107], a[128:131], v[32:35], v[168:171]\n"
    "s_mov_b32 m0, s19\n"
    "v_exp_f32 v72, v72\n"
    "v_mfma_f32_16x16x32_bf16 v[104:107], a[132:135], v[36:39], v[104:107]\n"
    "s_add_u32 s16, s16, 1\n"
    "ds_read_b128 a[192:195], v199 offset:0\n"
    "v_mfma_f32_16x16x32_bf16 v[104:107], a[136:139], v[40:43], v[104:107]\n"
    "buffer_load_dwordx4 v200, s[60:63], s20 offen offset:0 sc1 lds\n"
    "v_mfma_f32_16x16x32_bf16 v[104:107], a[140:143], v[44:47], v[104:107]\n"
    "ds_read_b128 a[196:199], v199 offset:1024\n"
    "v_mfma_f32_16x16x32_bf16 v[108:111], a[128:131], v[48:51], v[172:175]\n"
    "s_lshl_b32 s14, s16, 1\n"
    "v_exp_f32 v73, v73\n"
    "v_mfma_f32_16x16x32_bf16 v[108:111], a[132:135], v[52:55], v[108:111]\n"
    "s_cmp_gt_u32 s31, s14\n"
    "ds_read_b128 a[200:203], v199 offset:2048\n"
    "v_cvt_pk_bf16_f32 v136, v72, v73\n"
    "v_mfma_f32_16x16x32_bf16 v[108:111], a[136:139], v[56:59], v[108:111]\n"
    "s_cselect_b64 vcc, -1, 0\n"
    "v_exp_f32 v74, v74\n"
    "v_mfma_f32_16x16x32_bf16 v[108:111], a[140:143], v[60:63], v[108:111]\n"
    "s_cmp_eq_u32 s31, s14\n"
    "ds_read_b128 a[204:207], v199 offset:3072\n"
    "v_mfma_f32_16x16x32_bf16 a[32:35], a[160:163], v[148:151], a[32:35]\n"
    "s_cselect_b64 s[44:45], s[72:73], vcc\n"
    "v_exp_f32 v75, v75\n"
    "v_mfma_f32_16x16x32_bf16 a[64:67], a[160:163], v[152:155], a[64:67]\n"
    "s_cselect_b64 s[46:47], s[74:75], vcc\n"
    "v_cvt_pk_bf16_f32 v137, v74, v75\n"
    "v_mfma_f32_16x16x32_bf16 a[36:39], a[164:167], v[148:151], a[36:39]\n"
    "s_cselect_b64 s[48:49], s[76:77], vcc\n"
    "v_exp_f32 v76, v76\n"
    "v_mfma_f32_16x16x32_bf16 a[68:71], a[164:167], v[152:155], a[68:71]\n"
    "s_cselect_b64 s[50:51], s[78:79], vcc\n"
    "ds_read_b128 a[224:227], v199 offset:16384\n"
    "v_mfma_f32_16x16x32_bf16 a[40:43], a[168:171], v[148:151], a[40:43]\n"
    "buffer_load_dwordx4 v200, s[60:63], s20 offen offset:1024 sc1 lds\n"
    "v_mfma_f32_16x16x32_bf16 a[72:75], a[168:171], v[152:155], a[72:75]\n"
    "ds_read_b128 a[228:231], v199 offset:17408\n"
    "v_cndmask_b32 v172, v202, v195, s[44:45]\n"
    "v_mfma_f32_16x16x32_bf16 a[44:47], a[172:175], v[148:151], a[44:47]\n"
    "v_exp_f32 v77, v77\n"
    "v_mfma_f32_16x16x32_bf16 a[76:79], a[172:175], v[152:155], a[76:79]\n"
    "s_waitcnt lgkmcnt(2)\n"
    "ds_read_b128 a[232:235], v199 offset:18432\n"
    "v_cvt_pk_bf16_f32 v140, v76, v77\n"
    "v_mfma_f32_16x16x32_bf16 a[48:51], a[176:179], v[148:151], a[48:51]\n"
    "v_exp_f32 v78, v78\n"
    "v_mfma_f32_16x16x32_bf16 a[80:83], a[176:179], v[152:155], a[80:83]\n"
    "v_cndmask_b32 v173, v202, v195, s[46:47]\n"
    "v_cndmask_b32 v174, v202, v195, s[48:49]\n"
    "v_mfma_f32_16x16x32_bf16 a[52:55], a[180:183], v[148:151], a[52:55]\n"
    "v_exp_f32 v79, v79\n"
    "v_mfma_f32_16x16x32_bf16 a[84:87], a[180:183], v[152:155], a[84:87]\n"
    "ds_read_b128 a[236:239], v199 offset:19456\n"
    "v_cvt_pk_bf16_f32 v141, v78, v79\n"
    "v_mfma_f32_16x16x32_bf16 a[56:59], a[184:187], v[148:151], a[56:59]\n"
    "v_exp_f32 v88, v88\n"
    "v_mfma_f32_16x16x32_bf16 v[120:123], a[144:147], v[32:35], v[184:187]\n"
    "ds_read_b128 a[208:211], v199 offset:4096\n"
    "v_cndmask_b32 v175, v202, v195, s[50:51]\n"
    "v_mfma_f32_16x16x32_bf16 v[120:123], a[148:151], v[36:39], v[120:123]\n"
    "s_add_u32 s14, s14, 1\n"
    "buffer_load_dwordx4 v200, s[60:63], s20 offen offset:2048 sc1 lds\n"
    "v_mfma_f32_16x16x32_bf16 v[120:123], a[152:155], v[40:43], v[120:123]\n"
    "s_cmp_gt_u32 s31, s14\n"
    "ds_read_b128 a[212:215], v199 offset:5120\n"
    "v_mfma_f32_16x16x32_bf16 v[120:123], a[156:159], v[44:47], v[120:123]\n"
    "s_cselect_b64 vcc, -1, 0\n"
    "v_exp_f32 v89, v89\n"
    "v_mfma_f32_16x16x32_bf16 v[124:127], a[144:147], v[48:51], v[188:191]\n"
    "s_cmp_eq_u32 s31, s14\n"
    "v_cvt_pk_bf16_f32 v138, v88, v89\n"
    "v_mfma_f32_16x16x32_bf16 v[124:127], a[148:151], v[52:55], v[124:127]\n"
    "s_cselect_b64 s[44:45], s[72:73], vcc\n"
    "v_exp_f32 v90, v90\n"
    "v_mfma_f32_16x16x32_bf16 v[124:127], a[152:155], v[56:59], v[124:127]\n"
    "s_cselect_b64 s[46:47], s[74:75], vcc\n"
    "ds_read_b128 a[216:219], v199 offset:6144\n"
    "v_mfma_f32_16x16x32_bf16 v[124:127], a[156:159], v[60:63], v[124:127]\n"
    "s_cselect_b64 s[48:49], s[76:77], vcc\n"
    "v_exp_f32 v91, v91\n"
    "v_mfma_f32_16x16x32_bf16 a[88:91], a[184:187], v[152:155], a[88:91]\n"
    "s_cselect_b64 s[50:51], s[78:79], vcc\n"
    "ds_read_b128 a[220:223], v199 offset:7168\n"
    "v_cvt_pk_bf16_f32 v139, v90, v91\n"
    "v_mfma_f32_16x16x32_bf16 a[60:63], a[188:191], v[148:151], a[60:63]\n"
    "v_exp_f32 v92, v92\n"
    "v_mfma_f32_16x16x32_bf16 a[92:95], a[188:191], v[152:155], a[92:95]\n"
    "ds_read_b128 a[240:243], v199 offset:20480\n"
    "v_cndmask_b32 v188, v202, v195, s[44:45]\n"
    "v_mfma_f32_16x16x32_bf16 a[124:127], a[188:191], v[156:159], a[124:127]\n"
    "buffer_load_dwordx4 v200, s[60:63], s20 offen offset:3072 sc1 lds\n"
    "v_mfma_f32_16x16x32_bf16 a[120:123], a[184:187], v[156:159], a[120:123]\n"
    "v_cndmask_b32 v189, v202, v195, s[46:47]\n"
    "v_cndmask_b32 v190, v202, v195, s[48:49]\n"
    "v_mfma_f32_16x16x32_bf16 a[116:119], a[180:183], v[156:159], a[116:119]\n"
    "v_exp_f32 v93, v93\n"
    "v_mfma_f32_16x16x32_bf16 a[112:115], a[176:179], v[156:159], a[112:115]\n"
    "ds_read_b128 a[244:247], v199 offset:21504\n"
    "v_cvt_pk_bf16_f32 v142, v92, v93\n"
    "v_mfma_f32_16x16x32_bf16 a[108:111], a[172:175], v[156:159], a[108:111]\n"
    "v_exp_f32 v94, v94\n"
    "v_mfma_f32_16x16x32_bf16 a[104:107], a[168:171], v[156:159], a[104:107]\n"
    "ds_read_b128 a[248:251], v199 offset:22528\n"
    "v_cndmask_b32 v191, v202, v195, s[50:51]\n"
    "v_mfma_f32_16x16x32_bf16 a[100:103], a[164:167], v[156:159], a[100:103]\n"
    "v_exp_f32 v95, v95\n"
    "v_mfma_f32_16x16x32_bf16 a[96:99], a[160:163], v[156:159], a[96:99]\n"
    "ds_read_b128 a[252:255], v199 offset:23552\n"
    "v_cvt_pk_bf16_f32 v143, v94, v95\n"
    "v_mfma_f32_16x16x32_bf16 v[240:243], v[248:251], v[152:155], v[240:243]\n"
    "v_mfma_f32_16x16x32_bf16 v[244:247], v[248:251], v[156:159], v[244:247]\n"
    "v_mov_b32 v64, v196\n"
    "v_mov_b32 v65, v197\n"
    "s_nop 0\n"
    "v_permlane32_swap_b32 v196, v64\n"
    "v_permlane32_swap_b32 v197, v65\n"
    "v_add_f32 v196, v196, v64\n"
    "v_add_f32 v197, v197, v65\n"
    "v_mov_b32 v64, v196\n"
    "v_mov_b32 v65, v197\n"
    "s_nop 0\n"
    "v_permlane16_swap_b32 v196, v64\n"
    "v_permlane16_swap_b32 v197, v65\n"
    "v_add_f32 v196, v196, v64\n"
    "v_add_f32 v197, v197, v65\n"
    "v_cmp_nge_f32 vcc, s23, v196\n"
    "v_rcp_f32 v196, v196\n"
    "s_or_b64 s[26:27], s[26:27], vcc\n"
    "v_cmp_nge_f32 vcc, s23, v197\n"
    "v_rcp_f32 v197, v197\n"
    "s_or_b64 s[26:27], s[26:27], vcc\n"
    "s_lshl_b32 s15, s12, 12\n"
    "s_mul_i32 s25, s17, 131072\n"
    "s_add_u32 s25, s25, s15\n"
    "s_mul_i32 s35, s18, 131072\n"
    "s_add_u32 s35, s35, s15\n"
    "s_cmp_eq_u32 s95, 0\n"
    "s_cselect_b32 s25, s25, 0x7f000000\n"
    "s_cselect_b32 s35, s35, 0x7f000000\n"
    "v_accvgpr_read_b32 v64, a0\n"
    "v_accvgpr_read_b32 v65, a1\n"
    "v_accvgpr_read_b32 v66, a2\n"
    "v_accvgpr_read_b32 v67, a3\n"
    "v_accvgpr_read_b32 v68, a4\n"
    "v_accvgpr_read_b32 v69, a5\n"
    "v_accvgpr_read_b32 v70, a6\n"
    "v_accvgpr_read_b32 v71, a7\n"
    "v_pk_mul_f32 v[64:65], v[64:65], v[196:197] op_sel:[0,0] op_sel_hi:[1,0]\n"
    "v_pk_mul_f32 v[66:67], v[66:67], v[196:197] op_sel:[0,0] op_sel_hi:[1,0]\n"
    "v_pk_mul_f32 v[68:69], v[68:69], v[196:197] op_sel:[0,0] op_sel_hi:[1,0]\n"
    "v_pk_mul_f32 v[70:71], v[70:71], v[196:197] op_sel:[0,0] op_sel_hi:[1,0]\n"
    "v_cvt_pk_bf16_f32 v208, v64, v65\n"
    "v_cvt_pk_bf16_f32 v209, v66, v67\n"
    "v_cvt_pk_bf16_f32 v210, v68, v69\n"
    "v_cvt_pk_bf16_f32 v211, v70, v71\n"
    "v_accvgpr_read_b32 v72, a8\n"
    "v_accvgpr_read_b32 v73, a9\n"
    "v_accvgpr_read_b32 v74, a10\n"
    "v_accvgpr_read_b32 v75, a11\n"
    "v_accvgpr_read_b32 v76, a12\n"
    "v_accvgpr_read_b32 v77, a13\n"
    "v_accvgpr_read_b32 v78, a14\n"
    "v_accvgpr_read_b32 v79, a15\n"
    "v_pk_mul_f32 v[72:73], v[72:73], v[196:197] op_sel:[0,0] op_sel_hi:[1,0]\n"
    "v_pk_mul_f32 v[74:75], v[74:75], v[196:197] op_sel:[0,0] op_sel_hi:[1,0]\n"
    "v_pk_mul_f32 v[76:77], v[76:77], v[196:197] op_sel:[0,0] op_sel_hi:[1,0]\n"
    "v_pk_mul_f32 v[78:79], v[78:79], v[196:197] op_sel:[0,0] op_sel_hi:[1,0]\n"
    "v_cvt_pk_bf16_f32 v212, v72, v73\n"
    "v_cvt_pk_bf16_f32 v213, v74, v75\n"
    "v_cvt_pk_bf16_f32 v214, v76, v77\n"
    "v_cvt_pk_bf16_f32 v215, v78, v79\n"
    "v_accvgpr_read_b32 v64, a16\n"
    "v_accvgpr_read_b32 v65, a17\n"
    "v_accvgpr_read_b32 v66, a18\n"
    "v_accvgpr_read_b32 v67, a19\n"
    "v_accvgpr_read_b32 v68, a20\n"
    "v_accvgpr_read_b32 v69, a21\n"
    "v_accvgpr_read_b32 v70, a22\n"
    "v_accvgpr_read_b32 v71, a23\n"
    "v_pk_mul_f32 v[64:65], v[64:65], v[196:197] op_sel:[0,0] op_sel_hi:[1,0]\n"
    "v_pk_mul_f32 v[66:67], v[66:67], v[196:197] op_sel:[0,0] op_sel_hi:[1,0]\n"
    "v_pk_mul_f32 v[68:69], v[68:69], v[196:197] op_sel:[0,0] op_sel_hi:[1,0]\n"
    "v_pk_mul_f32 v[70:71], v[70:71], v[196:197] op_sel:[0,0] op_sel_hi:[1,0]\n"
    "v_cvt_pk_bf16_f32 v216, v64, v65\n"
    "v_cvt_pk_bf16_f32 v217, v66, v67\n"
    "v_cvt_pk_bf16_f32 v218, v68, v69\n"
    "v_cvt_pk_bf16_f32 v219, v70, v71\n"
    "v_accvgpr_read_b32 v72, a24\n"
    "v_accvgpr_read_b32 v73, a25\n"
    "v_accvgpr_read_b32 v74, a26\n"
    "v_accvgpr_read_b32 v75, a27\n"
    "v_accvgpr_read_b32 v76, a28\n"
    "v_accvgpr_read_b32 v77, a29\n"
    "v_accvgpr_read_b32 v78, a30\n"
    "v_accvgpr_read_b32 v79, a31\n"
    "v_pk_mul_f32 v[72:73], v[72:73], v[196:197] op_sel:[0,0] op_sel_hi:[1,0]\n"
    "v_pk_mul_f32 v[74:75], v[74:75], v[196:197] op_sel:[0,0] op_sel_hi:[1,0]\n"
    "v_pk_mul_f32 v[76:77], v[76:77], v[196:197] op_sel:[0,0] op_sel_hi:[1,0]\n"
    "v_pk_mul_f32 v[78:79], v[78:79], v[196:197] op_sel:[0,0] op_sel_hi:[1,0]\n"
    "v_cvt_pk_bf16_f32 v220, v72, v73\n"
    "v_cvt_pk_bf16_f32 v221, v74, v75\n"
    "v_cvt_pk_bf16_f32 v222, v76, v77\n"
    "v_cvt_pk_bf16_f32 v223, v78, v79\n"
    "v_accvgpr_read_b32 v64, a32\n"
    "v_accvgpr_read_b32 v65, a33\n"
    "v_accvgpr_read_b32 v66, a34\n"
    "v_accvgpr_read_b32 v67, a35\n"
    "v_accvgpr_read_b32 v68, a36\n"
    "v_accvgpr_read_b32 v69, a37\n"
    "v_accvgpr_read_b32 v70, a38\n"
    "v_accvgpr_read_b32 v71, a39\n"
    "v_pk_mul_f32 v[64:65], v[64:65], v[196:197] op_sel:[0,1] op_sel_hi:[1,1]\n"
    "v_pk_mul_f32 v[66:67], v[66:67], v[196:197] op_sel:[0,1] op_sel_hi:[1,1]\n"
    "v_pk_mul_f32 v[68:69], v[68:69], v[196:197] op_sel:[0,1] op_sel_hi:[1,1]\n"
    "v_pk_mul_f32 v[70:71], v[70:71], v[196:197] op_sel:[0,1] op_sel_hi:[1,1]\n"
    "v_cvt_pk_bf16_f32 v224, v64, v65\n"
    "v_cvt_pk_bf16_f32 v225, v66, v67\n"
    "v_cvt_pk_bf16_f32 v226, v68, v69\n"
    "v_cvt_pk_bf16_f32 v227, v70, v71\n"
    "v_accvgpr_read_b32 v72, a40\n"
    "v_accvgpr_read_b32 v73, a41\n"
    "v_accvgpr_read_b32 v74, a42\n"
    "v_accvgpr_read_b32 v75, a43\n"
    "v_accvgpr_read_b32 v76, a44\n"
    "v_accvgpr_read_b32 v77, a45\n"
    "v_accvgpr_read_b32 v78, a46\n"
    "v_accvgpr_read_b32 v79, a47\n"
    "v_pk_mul_f32 v[72:73], v[72:73], v[196:197] op_sel:[0,1] op_sel_hi:[1,1]\n"
    "v_pk_mul_f32 v[74:75], v[74:75], v[196:197] op_sel:[0,1] op_sel_hi:[1,1]\n"
    "v_pk_mul_f32 v[76:77], v[76:77], v[196:197] op_sel:[0,1] op_sel_hi:[1,1]\n"
    "v_pk_mul_f32 v[78:79], v[78:79], v[196:197] op_sel:[0,1] op_sel_hi:[1,1]\n"
    "v_cvt_pk_bf16_f32 v228, v72, v73\n"
    "v_cvt_pk_bf16_f32 v229, v74, v75\n"
    "v_cvt_pk_bf16_f32 v230, v76, v77\n"
    "v_cvt_pk_bf16_f32 v231, v78, v79\n"
    "v_accvgpr_read_b32 v64, a48\n"
    "v_accvgpr_read_b32 v65, a49\n"
    "v_accvgpr_read_b32 v66, a50\n"
    "v_accvgpr_read_b32 v67, a51\n"
    "v_accvgpr_read_b32 v68, a52\n"
    "v_accvgpr_read_b32 v69, a53\n"
    "v_accvgpr_read_b32 v70, a54\n"
    "v_accvgpr_read_b32 v71, a55\n"
    "v_pk_mul_f32 v[64:65], v[64:65], v[196:197] op_sel:[0,1] op_sel_hi:[1,1]\n"
    "v_pk_mul_f32 v[66:67], v[66:67], v[196:197] op_sel:[0,1] op_sel_hi:[1,1]\n"
    "v_pk_mul_f32 v[68:69], v[68:69], v[196:197] op_sel:[0,1] op_sel_hi:[1,1]\n"
    "v_pk_mul_f32 v[70:71], v[70:71], v[196:197] op_sel:[0,1] op_sel_hi:[1,1]\n"
    "v_cvt_pk_bf16_f32 v232, v64, v65\n"
    "v_cvt_pk_bf16_f32 v233, v66, v67\n"
    "v_cvt_pk_bf16_f32 v234, v68, v69\n"
    "v_cvt_pk_bf16_f32 v235, v70, v71\n"
    "v_accvgpr_read_b32 v72, a56\n"
    "v_accvgpr_read_b32 v73, a57\n"
    "v_accvgpr_read_b32 v74, a58\n"
    "v_accvgpr_read_b32 v75, a59\n"
    "v_accvgpr_read_b32 v76, a60\n"
    "v_accvgpr_read_b32 v77, a61\n"
    "v_accvgpr_read_b32 v78, a62\n"
    "v_accvgpr_read_b32 v79, a63\n"
    "v_pk_mul_f32 v[72:73], v[72:73], v[196:197] op_sel:[0,1] op_sel_hi:[1,1]\n"
    "v_pk_mul_f32 v[74:75], v[74:75], v[196:197] op_sel:[0,1] op_sel_hi:[1,1]\n"
    "v_pk_mul_f32 v[76:77], v[76:77], v[196:197] op_sel:[0,1] op_sel_hi:[1,1]\n"
    "v_pk_mul_f32 v[78:79], v[78:79], v[196:197] op_sel:[0,1] op_sel_hi:[1,1]\n"
    "v_cvt_pk_bf16_f32 v236, v72, v73\n"
    "v_cvt_pk_bf16_f32 v237, v74, v75\n"
    "v_cvt_pk_bf16_f32 v238, v76, v77\n"
    "v_cvt_pk_bf16_f32 v239, v78, v79\n"
    "s_lshl_b32 s14, s13, 2\n"
    "s_sub_u32 s22, 60, s14\n"
    "s_and_b32 s40, s12, 3\n"
    "s_lshl_b32 s14, s10, 19\n"
    "s_lshl_b32 s15, s40, 8\n"
    "s_add_u32 s14, s14, s15\n"
    "s_add_u32 s36, s54, s14\n"
    "s_addc_u32 s37, s55, 0\n"
    "s_and_b32 s37, s37, 0xffff\n"
    "s_sub_u32 s38, 524288, s15\n"
    "s_mov_b32 s39, 0x20000\n"
    "v_lshrrev_b32 v252, 4, v255\n"
    "v_mul_u32_u24 v204, 1024, v252\n"
    "v_or_b32 v252, 0, v252\n"
    "v_lshlrev_b32 v252, 1, v252\n"
    "v_and_b32 v253, 15, v255\n"
    "v_xor_b32 v252, v252, v253\n"
    "v_lshl_add_u32 v204, v252, 4, v204\n"
    "v_xor_b32 v205, 128, v204\n"
    "v_add_u32 v205, 4096, v205\n"
    "v_add_u32 v206, 8192, v204\n"
    "v_add_u32 v207, 8192, v205\n"
    "s_lshl_b32 s52, s8, 14\n"
    "s_lshl_b32 s14, s40, 17\n"
    "s_add_u32 s52, s52, s14\n"
    "s_and_b32 s14, s13, 1\n"
    "s_lshl_b32 s14, s14, 16\n"
    "s_add_u32 s53, s14, 32768\n"
    "s_lshl_b32 s14, s8, 12\n"
    "s_add_u32 s41, s53, s14\n"
    "s_mul_i32 s14, s34, 73728\n"
    "s_add_u32 s14, s14, 0x3200000\n"
    "s_add_u32 s96, s58, s14\n"
    "s_addc_u32 s97, s59, 0\n"
    "s_lshl_b32 s14, s8, 10\n"
    "v_add_u32 v252, s14, v198\n"
    "v_and_b32 v253, 15, v255\n"
    "v_mul_u32_u24 v253, 4608, v253\n"
    "v_and_b32 v254, 48, v255\n"
    "v_add_u32 v253, v253, v254\n"
    "s_lshl_b32 s14, s8, 6\n"
    "s_add_u32 s14, s14, 4096\n"
    "v_add_u32 v253, s14, v253\n"
    "s_lshl_b32 s14, s8, 12\n"
    "s_add_u32 s14, s14, 147456\n"
    "v_add_u32 v254, s14, v198\n"
    "global_load_dwordx4 a[0:3], v253, s[96:97] sc1\n"
    "s_cmp_eq_u32 s13, 7\n"
    "s_cbranch_scc1 L_att_U2s_%=\n"
    "v_mfma_f32_16x16x32_bf16 v[72:75], a[192:195], v[32:35], v[168:171]\n"
    "s_add_u32 m0, s19, 16384\n"
    "ds_read_b128 a[128:131], v199 offset:8192\n"
    "v_exp_f32 v104, v104\n"
    "v_mfma_f32_16x16x32_bf16 v[72:75], a[196:199], v[36:39], v[72:75]\n"
    "s_add_u32 s16, s16, 1\n"
    "global_load_dwordx4 v[64:67], v252, s[96:97] sc1\n"
    "s_add_u32 s96, s96, 4608\n"
    "s_addc_u32 s97, s97, 0\n"
    "global_load_dwordx4 v[68:71], v252, s[96:97] sc1\n"
    "s_add_u32 s96, s96, 4608\n"
    "s_addc_u32 s97, s97, 0\n"
    "v_mfma_f32_16x16x32_bf16 v[72:75], a[200:203], v[40:43], v[72:75]\n"
    "s_add_u32 s21, s21, 32768\n"
    "ds_read_b128 a[132:135], v199 offset:9216\n"
    "v_exp_f32 v105, v105\n"
    "v_mfma_f32_16x16x32_bf16 v[72:75], a[204:207], v[44:47], v[72:75]\n"
    "s_and_b32 s21, s21, 0x1ffff\n"
    "global_load_dwordx4 v[80:83], v252, s[96:97] sc1\n"
    "s_add_u32 s96, s96, 4608\n"
    "s_addc_u32 s97, s97, 0\n"
    "v_cvt_pk_bf16_f32 v152, v104, v105\n"
    "v_mfma_f32_16x16x32_bf16 v[76:79], a[192:195], v[48:51], v[172:175]\n"
    "ds_read_b128 a[136:139], v199 offset:10240\n"
    "v_exp_f32 v106, v106\n"
    "v_mfma_f32_16x16x32_bf16 v[76:79], a[196:199], v[52:55], v[76:79]\n"
    "global_load_dwordx4 v[84:87], v252, s[96:97] sc1\n"
    "s_add_u32 s96, s96, 4608\n"
    "s_addc_u32 s97, s97, 0\n"
    "v_mfma_f32_16x16x32_bf16 v[76:79], a[200:203], v[56:59], v[76:79]\n"
    "ds_read_b128 a[140:143], v199 offset:11264\n"
    "v_exp_f32 v107, v107\n"
    "v_mfma_f32_16x16x32_bf16 v[76:79], a[204:207], v[60:63], v[76:79]\n"
    "s_waitcnt lgkmcnt(8)\n"
    "global_load_dwordx4 v[96:99], v252, s[96:97] sc1\n"
    "s_add_u32 s96, s96, 4608\n"
    "s_addc_u32 s97, s97, 0\n"
    "v_cvt_pk_bf16_f32 v153, v106, v107\n"
    "v_mfma_f32_16x16x32_bf16 a[64:67], a[224:227], v[136:139], a[64:67]\n"
    "ds_read_b128 a[160:163], v199 offset:24576\n"
    "global_load_dwordx4 v[100:103], v252, s[96:97] sc1\n"
    "s_add_u32 s96, s96, 4608\n"
    "s_addc_u32 s97, s97, 0\n"
    "v_mfma_f32_16x16x32_bf16 a[96:99], a[224:227], v[140:143], a[96:99]\n"
    "v_exp_f32 v108, v108\n"
    "v_mfma_f32_16x16x32_bf16 a[68:71], a[228:231], v[136:139], a[68:71]\n"
    "ds_read_b128 a[164:167], v199 offset:25600\n"
    "global_load_dwordx4 v[112:115], v252, s[96:97] sc1\n"
    "s_add_u32 s96, s96, 4608\n"
    "s_addc_u32 s97, s97, 0\n"
    "v_mfma_f32_16x16x32_bf16 a[100:103], a[228:231], v[140:143], a[100:103]\n"
    "v_exp_f32 v109, v109\n"
    "global_load_dwordx4 v[116:119], v252, s[96:97] sc1\n"
    "s_add_u32 s96, s96, 4608\n"
    "s_addc_u32 s97, s97, 0\n"
    "v_mfma_f32_16x16x32_bf16 a[72:75], a[232:235], v[136:139], a[72:75]\n"
    "ds_read_b128 a[168:171], v199 offset:26624\n"
    "v_exp_f32 v110, v110\n"
    "v_mfma_f32_16x16x32_bf16 a[104:107], a[232:235], v[140:143], a[104:107]\n"
    "global_load_dwordx4 v[128:131], v252, s[96:97] sc1\n"
    "s_add_u32 s96, s96, 4608\n"
    "s_addc_u32 s97, s97, 0\n"
    "v_cvt_pk_bf16_f32 v156, v108, v109\n"
    "v_mfma_f32_16x16x32_bf16 a[76:79], a[236:239], v[136:139], a[76:79]\n"
    "ds_read_b128 a[172:175], v199 offset:27648\n"
    "v_exp_f32 v111, v111\n"
    "v_mfma_f32_16x16x32_bf16 a[108:111], a[236:239], v[140:143], a[108:111]\n"
    "s_waitcnt lgkmcnt(4)\n"
    "global_load_dwordx4 v[132:135], v252, s[96:97] sc1\n"
    "s_add_u32 s96, s96, 4608\n"
    "s_addc_u32 s97, s97, 0\n"
    "v_cvt_pk_bf16_f32 v157, v110, v111\n"
    "v_mfma_f32_16x16x32_bf16 a[80:83], a[240:243], v[136:139], a[80:83]\n"
    "global_load_dwordx4 v[144:147], v252, s[96:97] sc1\n"
    "s_add_u32 s96, s96, 4608\n"
    "s_addc_u32 s97, s97, 0\n"
    "v_mfma_f32_16x16x32_bf16 v[88:91], a[208:211], v[32:35], v[184:187]\n"
    "ds_read_b128 a[144:147], v199 offset:12288\n"
    "v_exp_f32 v120, v120\n"
    "v_mfma_f32_16x16x32_bf16 v[88:91], a[212:215], v[36:39], v[88:91]\n"
    "global_load_dwordx4 v[148:151], v252, s[96:97] sc1\n"
    "s_add_u32 s96, s96, 4608\n"
    "s_addc_u32 s97, s97, 0\n"
    "v_mfma_f32_16x16x32_bf16 v[88:91], a[216:219], v[40:43], v[88:91]\n"
    "ds_read_b128 a[148:151], v199 offset:13312\n"
    "v_exp_f32 v121, v121\n"
    "v_mfma_f32_16x16x32_bf16 v[88:91], a[220:223], v[44:47], v[88:91]\n"
    "global_load_dwordx4 v[160:163], v252, s[96:97] sc1\n"
    "s_add_u32 s96, s96, 4608\n"
    "s_addc_u32 s97, s97, 0\n"
    "v_cvt_pk_bf16_f32 v154, v120, v121\n"
    "v_mfma_f32_16x16x32_bf16 v[92:95], a[208:211], v[48:51], v[188:191]\n"
    "ds_read_b128 a[152:155], v199 offset:14336\n"
    "v_exp_f32 v122, v122\n"
    "v_mfma_f32_16x16x32_bf16 v[92:95], a[212:215], v[52:55], v[92:95]\n"
    "global_load_dwordx4 v[164:167], v252, s[96:97] sc1\n"
    "s_add_u32 s96, s96, 4608\n"
    "s_addc_u32 s97, s97, 0\n"
    "v_exp_f32 v123, v123\n"
    "v_mfma_f32_16x16x32_bf16 v[92:95], a[216:219], v[56:59], v[92:95]\n"
    "ds_read_b128 a[156:159], v199 offset:15360\n"
    "global_load_dwordx4 v[176:179], v252, s[96:97] sc1\n"
    "s_add_u32 s96, s96, 4608\n"
    "s_addc_u32 s97, s97, 0\n"
    "v_mfma_f32_16x16x32_bf16 v[92:95], a[220:223], v[60:63], v[92:95]\n"
    "global_load_dwordx4 v[180:183], v252, s[96:97] sc1\n"
    "s_add_u32 s96, s96, 4608\n"
    "s_addc_u32 s97, s97, 0\n"
    "v_cvt_pk_bf16_f32 v155, v122, v123\n"
    "v_mfma_f32_16x16x32_bf16 a[112:115], a[240:243], v[140:143], a[112:115]\n"
    "ds_read_b128 a[176:179], v199 offset:28672\n"
    "v_exp_f32 v124, v124\n"
    "v_mfma_f32_16x16x32_bf16 a[84:87], a[244:247], v[136:139], a[84:87]\n"
    "buffer_load_dwordx4 v200, s[64:67], s20 offen offset:0 sc1 lds\n"
    "v_mfma_f32_16x16x32_bf16 a[116:119], a[244:247], v[140:143], a[116:119]\n"
    "ds_read_b128 a[180:183], v199 offset:29696\n"
    "v_exp_f32 v125, v125\n"
    "v_mfma_f32_16x16x32_bf16 a[88:91], a[248:251], v[136:139], a[88:91]\n"
    "buffer_load_dwordx4 v200, s[64:67], s20 offen offset:1024 sc1 lds\n"
    "v_cvt_pk_bf16_f32 v158, v124, v125\n"
    "v_mfma_f32_16x16x32_bf16 a[120:123], a[248:251], v[140:143], a[120:123]\n"
    "ds_read_b128 a[184:187], v199 offset:30720\n"
    "v_exp_f32 v126, v126\n"
    "v_mfma_f32_16x16x32_bf16 a[92:95], a[252:255], v[136:139], a[92:95]\n"
    "s_waitcnt vmcnt(22)\n"
    "s_barrier\n"
    "buffer_load_dwordx4 v200, s[64:67], s20 offen offset:2048 sc1 lds\n"
    "v_mfma_f32_16x16x32_bf16 a[124:127], a[252:255], v[140:143], a[124:127]\n"
    "ds_read_b128 a[188:191], v199 offset:31744\n"
    "v_exp_f32 v127, v127\n"
    "v_mfma_f32_16x16x32_bf16 v[240:243], v[248:251], v[136:139], v[240:243]\n"
    "buffer_load_dwordx4 v200, s[64:67], s20 offen offset:3072 sc1 lds\n"
    "v_cvt_pk_bf16_f32 v159, v126, v127\n"
    "v_mfma_f32_16x16x32_bf16 v[244:247], v[248:251], v[140:143], v[244:247]\n"
    "s_add_u32 s20, s20, 16384\n"
    "s_add_u32 s19, s19, 32768\n"
    "s_and_b32 s19, s19, 0x1ffff\n"
    "v_add_u32 v199, s21, v198\n"
    "v_mfma_f32_16x16x32_bf16 v[104:107], a[128:131], v[32:35], v[168:171]\n"
    "s_mov_b32 m0, s19\n"
    "ds_read_b128 a[192:195], v199 offset:0\n"
    "v_exp_f32 v72, v72\n"
    "v_mfma_f32_16x16x32_bf16 v[104:107], a[132:135], v[36:39], v[104:107]\n"
    "s_add_u32 s16, s16, 1\n"
    "buffer_store_dwordx4 v[208:211], v198, s[68:71], s25 offen offset:0 sc1\n"
    "v_mfma_f32_16x16x32_bf16 v[104:107], a[136:139], v[40:43], v[104:107]\n"
    "ds_read_b128 a[196:199], v199 offset:1024\n"
    "v_exp_f32 v73, v73\n"
    "v_mfma_f32_16x16x32_bf16 v[104:107], a[140:143], v[44:47], v[104:107]\n"
    "buffer_store_dwordx4 v[212:215], v198, s[68:71], s25 offen offset:1024 sc1\n"
    "v_cvt_pk_bf16_f32 v136, v72, v73\n"
    "v_mfma_f32_16x16x32_bf16 v[108:111], a[128:131], v[48:51], v[172:175]\n"
    "ds_read_b128 a[200:203], v199 offset:2048\n"
    "v_exp_f32 v74, v74\n"
    "v_mfma_f32_16x16x32_bf16 v[108:111], a[132:135], v[52:55], v[108:111]\n"
    "buffer_store_dwordx4 v[216:219], v198, s[68:71], s25 offen offset:2048 sc1\n"
    "v_mfma_f32_16x16x32_bf16 v[108:111], a[136:139], v[56:59], v[108:111]\n"
    "ds_read_b128 a[204:207], v199 offset:3072\n"
    "v_mfma_f32_16x16x32_bf16 v[108:111], a[140:143], v[60:63], v[108:111]\n"
    "s_waitcnt lgkmcnt(8)\n"
    "v_exp_f32 v75, v75\n"
    "v_mfma_f32_16x16x32_bf16 a[64:67], a[160:163], v[152:155], a[64:67]\n"
    "ds_read_b128 a[224:227], v199 offset:16384\n"
    "v_exp_f32 v76, v76\n"
    "v_mfma_f32_16x16x32_bf16 a[96:99], a[160:163], v[156:159], a[96:99]\n"
    "buffer_store_dwordx4 v[220:223], v198, s[68:71], s25 offen offset:3072 sc1\n"
    "v_cvt_pk_bf16_f32 v137, v74, v75\n"
    "v_mfma_f32_16x16x32_bf16 a[68:71], a[164:167], v[152:155], a[68:71]\n"
    "ds_read_b128 a[228:231], v199 offset:17408\n"
    "v_exp_f32 v77, v77\n"
    "v_mfma_f32_16x16x32_bf16 a[100:103], a[164:167], v[156:159], a[100:103]\n"
    "buffer_store_dwordx4 v[224:227], v198, s[68:71], s35 offen offset:0 sc1\n"
    "v_cvt_pk_bf16_f32 v140, v76, v77\n"
    "v_mfma_f32_16x16x32_bf16 a[72:75], a[168:171], v[152:155], a[72:75]\n"
    "ds_read_b128 a[232:235], v199 offset:18432\n"
    "v_exp_f32 v78, v78\n"
    "v_mfma_f32_16x16x32_bf16 a[104:107], a[168:171], v[156:159], a[104:107]\n"
    "v_mfma_f32_16x16x32_bf16 a[76:79], a[172:175], v[152:155], a[76:79]\n"
    "ds_read_b128 a[236:239], v199 offset:19456\n"
    "buffer_store_dwordx4 v[228:231], v198, s[68:71], s35 offen offset:1024 sc1\n"
    "v_mfma_f32_16x16x32_bf16 a[108:111], a[172:175], v[156:159], a[108:111]\n"
    "s_waitcnt lgkmcnt(4)\n"
    "v_exp_f32 v79, v79\n"
    "v_mfma_f32_16x16x32_bf16 a[80:83], a[176:179], v[152:155], a[80:83]\n"
    "v_exp_f32 v88, v88\n"
    "v_cvt_pk_bf16_f32 v141, v78, v79\n"
    "v_mfma_f32_16x16x32_bf16 v[120:123], a[144:147], v[32:35], v[184:187]\n"
    "ds_read_b128 a[208:211], v199 offset:4096\n"
    "buffer_store_dwordx4 v[232:235], v198, s[68:71], s35 offen offset:2048 sc1\n"
    "v_mfma_f32_16x16x32_bf16 v[120:123], a[148:151], v[36:39], v[120:123]\n"
    "v_exp_f32 v89, v89\n"
    "v_mfma_f32_16x16x32_bf16 v[120:123], a[152:155], v[40:43], v[120:123]\n"
    "ds_read_b128 a[212:215], v199 offset:5120\n"
    "v_cvt_pk_bf16_f32 v138, v88, v89\n"
    "v_mfma_f32_16x16x32_bf16 v[120:123], a[156:159], v[44:47], v[120:123]\n"
    "buffer_store_dwordx4 v[236:239], v198, s[68:71], s35 offen offset:3072 sc1\n"
    "v_mfma_f32_16x16x32_bf16 v[124:127], a[144:147], v[48:51], v[188:191]\n"
    "ds_read_b128 a[216:219], v199 offset:6144\n"
    "v_exp_f32 v90, v90\n"
    "v_mfma_f32_16x16x32_bf16 v[124:127], a[148:151], v[52:55], v[124:127]\n"
    "buffer_load_dwordx4 v200, s[60:63], s20 offen offset:0 sc1 lds\n"
    "v_mfma_f32_16x16x32_bf16 v[124:127], a[152:155], v[56:59], v[124:127]\n"
    "ds_read_b128 a[220:223], v199 offset:7168\n"
    "v_exp_f32 v91, v91\n"
    "v_mfma_f32_16x16x32_bf16 v[124:127], a[156:159], v[60:63], v[124:127]\n"
    "v_exp_f32 v92, v92\n"
    "v_cvt_pk_bf16_f32 v139, v90, v91\n"
    "v_mfma_f32_16x16x32_bf16 a[112:115], a[176:179], v[156:159], a[112:115]\n"
    "ds_read_b128 a[240:243], v199 offset:20480\n"
    "buffer_load_dwordx4 v200, s[60:63], s20 offen offset:1024 sc1 lds\n"
    "v_mfma_f32_16x16x32_bf16 a[84:87], a[180:183], v[152:155], a[84:87]\n"
    "v_mfma_f32_16x16x32_bf16 a[116:119], a[180:183], v[156:159], a[116:119]\n"
    "ds_read_b128 a[244:247], v199 offset:21504\n"
    "v_exp_f32 v93, v93\n"
    "v_mfma_f32_16x16x32_bf16 a[88:91], a[184:187], v[152:155], a[88:91]\n"
    "buffer_load_dwordx4 v200, s[60:63], s20 offen offset:2048 sc1 lds\n"
    "v_cvt_pk_bf16_f32 v142, v92, v93\n"
    "v_mfma_f32_16x16x32_bf16 a[120:123], a[184:187], v[156:159], a[120:123]\n"
    "ds_read_b128 a[248:251], v199 offset:22528\n"
    "v_exp_f32 v94, v94\n"
    "v_mfma_f32_16x16x32_bf16 a[92:95], a[188:191], v[152:155], a[92:95]\n"
    "buffer_load_dwordx4 v200, s[60:63], s20 offen offset:3072 sc1 lds\n"
    "v_mfma_f32_16x16x32_bf16 a[124:127], a[188:191], v[156:159], a[124:127]\n"
    "ds_read_b128 a[252:255], v199 offset:23552\n"
    "v_exp_f32 v95, v95\n"
    "v_mfma_f32_16x16x32_bf16 v[240:243], v[248:251], v[152:155], v[240:243]\n"
    "v_cvt_pk_bf16_f32 v143, v94, v95\n"
    "v_mfma_f32_16x16x32_bf16 v[244:247], v[248:251], v[156:159], v[244:247]\n"
    "L_att_B2_%=:\n"
    "v_mfma_f32_16x16x32_bf16 v[72:75], a[192:195], v[32:35], v[168:171]\n"
    "s_add_u32 m0, s19, 16384\n"
    "v_exp_f32 v104, v104\n"
    "v_mfma_f32_16x16x32_bf16 v[72:75], a[196:199], v[36:39], v[72:75]\n"
    "s_add_u32 s16, s16, 1\n"
    "buffer_load_dwordx4 v200, s[64:67], s20 offen offset:0 sc1 lds\n"
    "v_mfma_f32_16x16x32_bf16 v[72:75], a[200:203], v[40:43], v[72:75]\n"
    "s_add_u32 s21, s21, 32768\n"
    "ds_read_b128 a[132:135], v199 offset:9216\n"
    "ds_read_b128 a[128:131], v199 offset:8192\n"
    "v_mfma_f32_16x16x32_bf16 v[72:75], a[204:207], v[44:47], v[72:75]\n"
    "s_and_b32 s21, s21, 0x1ffff\n"
    "v_exp_f32 v105, v105\n"
    "v_mfma_f32_16x16x32_bf16 v[76:79], a[192:195], v[48:51], v[172:175]\n"
    "v_exp_f32 v106, v106\n"
    "v_mfma_f32_16x16x32_bf16 v[76:79], a[196:199], v[52:55], v[76:79]\n"
    "ds_read_b128 a[136:139], v199 offset:10240\n"
    "v_cvt_pk_bf16_f32 v152, v104, v105\n"
    "v_mfma_f32_16x16x32_bf16 v[76:79], a[200:203], v[56:59], v[76:79]\n"
    "v_exp_f32 v107, v107\n"
    "v_mfma_f32_16x16x32_bf16 v[76:79], a[204:207], v[60:63], v[76:79]\n"
    "s_waitcnt lgkmcnt(7)\n"
    "ds_read_b128 a[140:143], v199 offset:11264\n"
    "v_cvt_pk_bf16_f32 v153, v106, v107\n"
    "v_mfma_f32_16x16x32_bf16 a[64:67], a[224:227], v[136:139], a[64:67]\n"
    "v_exp_f32 v108, v108\n"
    "v_mfma_f32_16x16x32_bf16 a[96:99], a[224:227], v[140:143], a[96:99]\n"
    "buffer_load_dwordx4 v200, s[64:67], s20 offen offset:1024 sc1 lds\n"
    "v_mfma_f32_16x16x32_bf16 a[68:71], a[228:231], v[136:139], a[68:71]\n"
    "ds_read_b128 a[164:167], v199 offset:25600\n"
    "ds_read_b128 a[160:163], v199 offset:24576\n"
    "v_mfma_f32_16x16x32_bf16 a[100:103], a[228:231], v[140:143], a[100:103]\n"
    "v_exp_f32 v109, v109\n"
    "v_mfma_f32_16x16x32_bf16 a[72:75], a[232:235], v[136:139], a[72:75]\n"
    "v_exp_f32 v110, v110\n"
    "v_mfma_f32_16x16x32_bf16 a[104:107], a[232:235], v[140:143], a[104:107]\n"
    "ds_read_b128 a[168:171], v199 offset:26624\n"
    "v_cvt_pk_bf16_f32 v156, v108, v109\n"
    "v_mfma_f32_16x16x32_bf16 a[76:79], a[236:239], v[136:139], a[76:79]\n"
    "v_exp_f32 v111, v111\n"
    "v_mfma_f32_16x16x32_bf16 a[108:111], a[236:239], v[140:143], a[108:111]\n"
    "s_waitcnt lgkmcnt(3)\n"
    "ds_read_b128 a[172:175], v199 offset:27648\n"
    "v_cvt_pk_bf16_f32 v157, v110, v111\n"
    "v_mfma_f32_16x16x32_bf16 a[80:83], a[240:243], v[136:139], a[80:83]\n"
    "v_exp_f32 v120, v120\n"
    "v_mfma_f32_16x16x32_bf16 v[88:91], a[208:211], v[32:35], v[184:187]\n"
    "buffer_load_dwordx4 v200, s[64:67], s20 offen offset:2048 sc1 lds\n"
    "v_mfma_f32_16x16x32_bf16 v[88:91], a[212:215], v[36:39], v[88:91]\n"
    "ds_read_b128 a[144:147], v199 offset:12288\n"
    "v_mfma_f32_16x16x32_bf16 v[88:91], a[216:219], v[40:43], v[88:91]\n"
    "v_exp_f32 v121, v121\n"
    "v_mfma_f32_16x16x32_bf16 v[88:91], a[220:223], v[44:47], v[88:91]\n"
    "v_exp_f32 v122, v122\n"
    "v_mfma_f32_16x16x32_bf16 v[92:95], a[208:211], v[48:51], v[188:191]\n"
    "ds_read_b128 a[152:155], v199 offset:14336\n"
    "ds_read_b128 a[148:151], v199 offset:13312\n"
    "v_mfma_f32_16x16x32_bf16 v[92:95], a[212:215], v[52:55], v[92:95]\n"
    "v_exp_f32 v123, v123\n"
    "v_mfma_f32_16x16x32_bf16 v[92:95], a[216:219], v[56:59], v[92:95]\n"
    "ds_read_b128 a[156:159], v199 offset:15360\n"
    "v_cvt_pk_bf16_f32 v154, v120, v121\n"
    "v_mfma_f32_16x16x32_bf16 v[92:95], a[220:223], v[60:63], v[92:95]\n"
    "v_exp_f32 v124, v124\n"
    "v_mfma_f32_16x16x32_bf16 a[112:115], a[240:243], v[140:143], a[112:115]\n"
    "buffer_load_dwordx4 v200, s[64:67], s20 offen offset:3072 sc1 lds\n"
    "v_mfma_f32_16x16x32_bf16 a[84:87], a[244:247], v[136:139], a[84:87]\n"
    "s_add_u32 s20, s20, 16384\n"
    "ds_read_b128 a[176:179], v199 offset:28672\n"
    "v_cvt_pk_bf16_f32 v155, v122, v123\n"
    "v_mfma_f32_16x16x32_bf16 a[116:119], a[244:247], v[140:143], a[116:119]\n"
    "s_add_u32 s19, s19, 32768\n"
    "v_exp_f32 v125, v125\n"
    "v_mfma_f32_16x16x32_bf16 a[88:91], a[248:251], v[136:139], a[88:91]\n"
    "s_and_b32 s19, s19, 0x1ffff\n"
    "v_exp_f32 v126, v126\n"
    "v_mfma_f32_16x16x32_bf16 a[120:123], a[248:251], v[140:143], a[120:123]\n"
    "ds_read_b128 a[184:187], v199 offset:30720\n"
    "ds_read_b128 a[180:183], v199 offset:29696\n"
    "v_mfma_f32_16x16x32_bf16 a[92:95], a[252:255], v[136:139], a[92:95]\n"
    "s_waitcnt vmcnt(8)\n"
    "s_barrier\n"
    "v_mfma_f32_16x16x32_bf16 a[124:127], a[252:255], v[140:143], a[124:127]\n"
    "v_exp_f32 v127, v127\n"
    "v_mfma_f32_16x16x32_bf16 v[240:243], v[248:251], v[136:139], v[240:243]\n"
    "ds_read_b128 a[188:191], v199 offset:31744\n"
    "v_cvt_pk_bf16_f32 v158, v124, v125\n"
    "v_mfma_f32_16x16x32_bf16 v[244:247], v[248:251], v[140:143], v[244:247]\n"
    "v_cvt_pk_bf16_f32 v159, v126, v127\n"
    "v_add_u32 v199, s21, v198\n"
    "v_mfma_f32_16x16x32_bf16 v[104:107], a[128:131], v[32:35], v[168:171]\n"
    "s_mov_b32 m0, s19\n"
    "v_exp_f32 v72, v72\n"
    "v_mfma_f32_16x16x32_bf16 v[104:107], a[132:135], v[36:39], v[104:107]\n"
    "s_add_u32 s16, s16, 1\n"
    "buffer_load_dwordx4 v200, s[60:63], s20 offen offset:0 sc1 lds\n"
    "v_mfma_f32_16x16x32_bf16 v[104:107], a[136:139], v[40:43], v[104:107]\n"
    "ds_read_b128 a[196:199], v199 offset:1024\n"
    "ds_read_b128 a[192:195], v199 offset:0\n"
    "v_mfma_f32_16x16x32_bf16 v[104:107], a[140:143], v[44:47], v[104:107]\n"
    "v_exp_f32 v73, v73\n"
    "v_mfma_f32_16x16x32_bf16 v[108:111], a[128:131], v[48:51], v[172:175]\n"
    "v_exp_f32 v74, v74\n"
    "v_mfma_f32_16x16x32_bf16 v[108:111], a[132:135], v[52:55], v[108:111]\n"
    "ds_read_b128 a[200:203], v199 offset:2048\n"
    "v_cvt_pk_bf16_f32 v136, v72, v73\n"
    "v_mfma_f32_16x16x32_bf16 v[108:111], a[136:139], v[56:59], v[108:111]\n"
    "v_exp_f32 v75, v75\n"
    "v_mfma_f32_16x16x32_bf16 v[108:111], a[140:143], v[60:63], v[108:111]\n"
    "s_waitcnt lgkmcnt(6)\n"
    "ds_read_b128 a[204:207], v199 offset:3072\n"
    "v_cvt_pk_bf16_f32 v137, v74, v75\n"
    "v_mfma_f32_16x16x32_bf16 a[64:67], a[160:163], v[152:155], a[64:67]\n"
    "v_exp_f32 v76, v76\n"
    "v_mfma_f32_16x16x32_bf16 a[96:99], a[160:163], v[156:159], a[96:99]\n"
    "buffer_load_dwordx4 v200, s[60:63], s20 offen offset:1024 sc1 lds\n"
    "v_mfma_f32_16x16x32_bf16 a[68:71], a[164:167], v[152:155], a[68:71]\n"
    "ds_read_b128 a[228:231], v199 offset:17408\n"
    "ds_read_b128 a[224:227], v199 offset:16384\n"
    "v_mfma_f32_16x16x32_bf16 a[100:103], a[164:167], v[156:159], a[100:103]\n"
    "v_exp_f32 v77, v77\n"
    "v_mfma_f32_16x16x32_bf16 a[72:75], a[168:171], v[152:155], a[72:75]\n"
    "v_exp_f32 v78, v78\n"
    "v_mfma_f32_16x16x32_bf16 a[104:107], a[168:171], v[156:159], a[104:107]\n"
    "ds_read_b128 a[232:235], v199 offset:18432\n"
    "v_cvt_pk_bf16_f32 v140, v76, v77\n"
    "v_mfma_f32_16x16x32_bf16 a[76:79], a[172:175], v[152:155], a[76:79]\n"
    "v_exp_f32 v79, v79\n"
    "v_mfma_f32_16x16x32_bf16 a[108:111], a[172:175], v[156:159], a[108:111]\n"
    "ds_read_b128 a[236:239], v199 offset:19456\n"
    "v_cvt_pk_bf16_f32 v141, v78, v79\n"
    "v_mfma_f32_16x16x32_bf16 a[80:83], a[176:179], v[152:155], a[80:83]\n"
    "v_exp_f32 v88, v88\n"
    "v_mfma_f32_16x16x32_bf16 v[120:123], a[144:147], v[32:35], v[184:187]\n"
    "buffer_load_dwordx4 v200, s[60:63], s20 offen offset:2048 sc1 lds\n"
    "v_mfma_f32_16x16x32_bf16 v[120:123], a[148:151], v[36:39], v[120:123]\n"
    "ds_read_b128 a[208:211], v199 offset:4096\n"
    "v_mfma_f32_16x16x32_bf16 v[120:123], a[152:155], v[40:43], v[120:123]\n"
    "v_exp_f32 v89, v89\n"
    "v_mfma_f32_16x16x32_bf16 v[120:123], a[156:159], v[44:47], v[120:123]\n"
    "v_exp_f32 v90, v90\n"
    "v_mfma_f32_16x16x32_bf16 v[124:127], a[144:147], v[48:51], v[188:191]\n"
    "ds_read_b128 a[216:219], v199 offset:6144\n"
    "ds_read_b128 a[212:215], v199 offset:5120\n"
    "v_mfma_f32_16x16x32_bf16 v[124:127], a[148:151], v[52:55], v[124:127]\n"
    "v_exp_f32 v91, v91\n"
    "v_mfma_f32_16x16x32_bf16 v[124:127], a[152:155], v[56:59], v[124:127]\n"
    "ds_read_b128 a[220:223], v199 offset:7168\n"
    "v_cvt_pk_bf16_f32 v138, v88, v89\n"
    "v_mfma_f32_16x16x32_bf16 v[124:127], a[156:159], v[60:63], v[124:127]\n"
    "v_exp_f32 v92, v92\n"
    "v_mfma_f32_16x16x32_bf16 a[112:115], a[176:179], v[156:159], a[112:115]\n"
    "s_waitcnt lgkmcnt(6)\n"
    "buffer_load_dwordx4 v200, s[60:63], s20 offen offset:3072 sc1 lds\n"
    "v_mfma_f32_16x16x32_bf16 a[84:87], a[180:183], v[152:155], a[84:87]\n"
    "ds_read_b128 a[240:243], v199 offset:20480\n"
    "v_cvt_pk_bf16_f32 v139, v90, v91\n"
    "v_mfma_f32_16x16x32_bf16 a[116:119], a[180:183], v[156:159], a[116:119]\n"
    "v_exp_f32 v93, v93\n"
    "v_mfma_f32_16x16x32_bf16 a[88:91], a[184:187], v[152:155], a[88:91]\n"
    "v_exp_f32 v94, v94\n"
    "v_mfma_f32_16x16x32_bf16 a[120:123], a[184:187], v[156:159], a[120:123]\n"
    "ds_read_b128 a[248:251], v199 offset:22528\n"
    "ds_read_b128 a[244:247], v199 offset:21504\n"
    "v_mfma_f32_16x16x32_bf16 a[92:95], a[188:191], v[152:155], a[92:95]\n"
    "v_exp_f32 v95, v95\n"
    "v_mfma_f32_16x16x32_bf16 a[124:127], a[188:191], v[156:159], a[124:127]\n"
    "s_cmp_lg_u32 s16, s22\n"
    "ds_read_b128 a[252:255], v199 offset:23552\n"
    "v_cvt_pk_bf16_f32 v142, v92, v93\n"
    "v_mfma_f32_16x16x32_bf16 v[240:243], v[248:251], v[152:155], v[240:243]\n"
    "v_cvt_pk_bf16_f32 v143, v94, v95\n"
    "v_mfma_f32_16x16x32_bf16 v[244:247], v[248:251], v[156:159], v[244:247]\n"
    "s_cbranch_scc1 L_att_B2_%=\n"
    "s_getpc_b64 s[14:15]\n"
    "L_pcB_%=:\n"
    "v_lshlrev_b32 v252, 7, v255\n"
    "v_lshl_add_u32 v252, s8, 13, v252\n"
    "v_min_u32 v252, L_end_%=-L_pcB_%=-4, v252\n"
    "global_load_dword v252, v252, s[14:15]\n"
    "s_lshl_b32 s14, s16, 1\n"
    "s_cmp_gt_u32 s30, s14\n"
    "s_cselect_b64 vcc, -1, 0\n"
    "s_cmp_eq_u32 s30, s14\n"
    "s_cselect_b64 s[44:45], s[72:73], vcc\n"
    "s_cselect_b64 s[46:47], s[74:75], vcc\n"
    "s_cselect_b64 s[48:49], s[76:77], vcc\n"
    "s_cselect_b64 s[50:51], s[78:79], vcc\n"
    "v_cndmask_b32 v168, v202, v194, s[44:45]\n"
    "v_cndmask_b32 v169, v202, v194, s[46:47]\n"
    "v_cndmask_b32 v170, v202, v194, s[48:49]\n"
    "v_cndmask_b32 v171, v202, v194, s[50:51]\n"
    "s_add_u32 s14, s14, 1\n"
    "s_cmp_gt_u32 s30, s14\n"
    "s_cselect_b64 vcc, -1, 0\n"
    "s_cmp_eq_u32 s30, s14\n"
    "s_cselect_b64 s[44:45], s[72:73], vcc\n"
    "s_cselect_b64 s[46:47], s[74:75], vcc\n"
    "s_cselect_b64 s[48:49], s[76:77], vcc\n"
    "s_cselect_b64 s[50:51], s[78:79], vcc\n"
    "v_cndmask_b32 v184, v202, v194, s[44:45]\n"
    "v_cndmask_b32 v185, v202, v194, s[46:47]\n"
    "v_cndmask_b32 v186, v202, v194, s[48:49]\n"
    "v_cndmask_b32 v187, v202, v194, s[50:51]\n"
    "v_mfma_f32_16x16x32_bf16 v[72:75], a[192:195], v[32:35], v[168:171]\n"
    "s_add_u32 m0, s19, 16384\n"
    "v_exp_f32 v104, v104\n"
    "v_mfma_f32_16x16x32_bf16 v[72:75], a[196:199], v[36:39], v[72:75]\n"
    "s_add_u32 s16, s16, 1\n"
    "buffer_load_dwordx4 v200, s[64:67], s20 offen offset:0 sc1 lds\n"
    "v_mfma_f32_16x16x32_bf16 v[72:75], a[200:203], v[40:43], v[72:75]\n"
    "s_add_u32 s21, s21, 32768\n"
    "ds_read_b128 a[132:135], v199 offset:9216\n"
    "ds_read_b128 a[128:131], v199 offset:8192\n"
    "v_mfma_f32_16x16x32_bf16 v[72:75], a[204:207], v[44:47], v[72:75]\n"
    "s_and_b32 s21, s21, 0x1ffff\n"
    "v_exp_f32 v105, v105\n"
    "v_mfma_f32_16x16x32_bf16 v[76:79], a[192:195], v[48:51], v[172:175]\n"
    "s_lshl_b32 s14, s16, 1\n"
    "s_cmp_gt_u32 s30, s14\n"
    "v_exp_f32 v106, v106\n"
    "v_mfma_f32_16x16x32_bf16 v[76:79], a[196:199], v[52:55], v[76:79]\n"
    "s_cselect_b64 vcc, -1, 0\n"
    "s_cmp_eq_u32 s30, s14\n"
    "ds_read_b128 a[136:139], v199 offset:10240\n"
    "v_cvt_pk_bf16_f32 v152, v104, v105\n"
    "v_mfma_f32_16x16x32_bf16 v[76:79], a[200:203], v[56:59], v[76:79]\n"
    "s_cselect_b64 s[44:45], s[72:73], vcc\n"
    "s_cselect_b64 s[46:47], s[74:75], vcc\n"
    "v_exp_f32 v107, v107\n"
    "v_mfma_f32_16x16x32_bf16 v[76:79], a[204:207], v[60:63], v[76:79]\n"
    "s_cselect_b64 s[48:49], s[76:77], vcc\n"
    "s_cselect_b64 s[50:51], s[78:79], vcc\n"
    "ds_read_b128 a[140:143], v199 offset:11264\n"
    "v_cvt_pk_bf16_f32 v153, v106, v107\n"
    "v_mfma_f32_16x16x32_bf16 a[64:67], a[224:227], v[136:139], a[64:67]\n"
    "v_exp_f32 v108, v108\n"
    "v_mfma_f32_16x16x32_bf16 a[96:99], a[224:227], v[140:143], a[96:99]\n"
    "buffer_load_dwordx4 v200, s[64:67], s20 offen offset:1024 sc1 lds\n"
    "v_mfma_f32_16x16x32_bf16 a[68:71], a[228:231], v[136:139], a[68:71]\n"
    "ds_read_b128 a[164:167], v199 offset:25600\n"
    "ds_read_b128 a[160:163], v199 offset:24576\n"
    "v_mfma_f32_16x16x32_bf16 a[100:103], a[228:231], v[140:143], a[100:103]\n"
    "s_waitcnt lgkmcnt(8)\n"
    "v_exp_f32 v109, v109\n"
    "v_mfma_f32_16x16x32_bf16 a[72:75], a[232:235], v[136:139], a[72:75]\n"
    "v_exp_f32 v110, v110\n"
    "v_mfma_f32_16x16x32_bf16 a[104:107], a[232:235], v[140:143], a[104:107]\n"
    "ds_read_b128 a[168:171], v199 offset:26624\n"
    "v_cvt_pk_bf16_f32 v156, v108, v109\n"
    "v_mfma_f32_16x16x32_bf16 a[76:79], a[236:239], v[136:139], a[76:79]\n"
    "v_exp_f32 v111, v111\n"
    "v_mfma_f32_16x16x32_bf16 a[108:111], a[236:239], v[140:143], a[108:111]\n"
    "ds_read_b128 a[172:175], v199 offset:27648\n"
    "v_cvt_pk_bf16_f32 v157, v110, v111\n"
    "v_mfma_f32_16x16x32_bf16 a[80:83], a[240:243], v[136:139], a[80:83]\n"
    "v_exp_f32 v120, v120\n"
    "v_mfma_f32_16x16x32_bf16 v[88:91], a[208:211], v[32:35], v[184:187]\n"
    "buffer_load_dwordx4 v200, s[64:67], s20 offen offset:2048 sc1 lds\n"
    "v_cndmask_b32 v168, v202, v194, s[44:45]\n"
    "v_cndmask_b32 v169, v202, v194, s[46:47]\n"
    "v_cndmask_b32 v170, v202, v194, s[48:49]\n"
    "v_cndmask_b32 v171, v202, v194, s[50:51]\n"
    "v_mfma_f32_16x16x32_bf16 v[88:91], a[212:215], v[36:39], v[88:91]\n"
    "s_add_u32 s14, s14, 1\n"
    "s_cmp_gt_u32 s30, s14\n"
    "ds_read_b128 a[144:147], v199 offset:12288\n"
    "v_mfma_f32_16x16x32_bf16 v[88:91], a[216:219], v[40:43], v[88:91]\n"
    "s_cselect_b64 vcc, -1, 0\n"
    "s_cmp_eq_u32 s30, s14\n"
    "v_exp_f32 v121, v121\n"
    "v_mfma_f32_16x16x32_bf16 v[88:91], a[220:223], v[44:47], v[88:91]\n"
    "s_cselect_b64 s[44:45], s[72:73], vcc\n"
    "s_cselect_b64 s[46:47], s[74:75], vcc\n"
    "v_exp_f32 v122, v122\n"
    "v_mfma_f32_16x16x32_bf16 v[92:95], a[208:211], v[48:51], v[188:191]\n"
    "s_cselect_b64 s[48:49], s[76:77], vcc\n"
    "s_cselect_b64 s[50:51], s[78:79], vcc\n"
    "ds_read_b128 a[152:155], v199 offset:14336\n"
    "ds_read_b128 a[148:151], v199 offset:13312\n"
    "v_mfma_f32_16x16x32_bf16 v[92:95], a[212:215], v[52:55], v[92:95]\n"
    "v_exp_f32 v123, v123\n"
    "v_mfma_f32_16x16x32_bf16 v[92:95], a[216:219], v[56:59], v[92:95]\n"
    "ds_read_b128 a[156:159], v199 offset:15360\n"
    "v_cvt_pk_bf16_f32 v154, v120, v121\n"
    "v_mfma_f32_16x16x32_bf16 v[92:95], a[220:223], v[60:63], v[92:95]\n"
    "v_exp_f32 v124, v124\n"
    "v_mfma_f32_16x16x32_bf16 a[112:115], a[240:243], v[140:143], a[112:115]\n"
    "s_waitcnt lgkmcnt(6)\n"
    "buffer_load_dwordx4 v200, s[64:67], s20 offen offset:3072 sc1 lds\n"
    "v_mfma_f32_16x16x32_bf16 a[84:87], a[244:247], v[136:139], a[84:87]\n"
    "s_add_u32 s20, s20, 16384\n"
    "ds_read_b128 a[176:179], v199 offset:28672\n"
    "v_cvt_pk_bf16_f32 v155, v122, v123\n"
    "v_mfma_f32_16x16x32_bf16 a[116:119], a[244:247], v[140:143], a[116:119]\n"
    "s_add_u32 s19, s19, 32768\n"
    "v_exp_f32 v125, v125\n"
    "v_mfma_f32_16x16x32_bf16 a[88:91], a[248:251], v[136:139], a[88:91]\n"
    "s_and_b32 s19, s19, 0x1ffff\n"
    "v_exp_f32 v126, v126\n"
    "v_mfma_f32_16x16x32_bf16 a[120:123], a[248:251], v[140:143], a[120:123]\n"
    "ds_read_b128 a[184:187], v199 offset:30720\n"
    "ds_read_b128 a[180:183], v199 offset:29696\n"
    "v_mfma_f32_16x16x32_bf16 a[92:95], a[252:255], v[136:139], a[92:95]\n"
    "s_waitcnt vmcnt(8)\n"
    "s_barrier\n"
    "s_cmp_lg_u64 s[26:27], 0\n"
    "s_cbranch_scc1 L_pa2_%=\n"
    "s_mov_b32 s95, 1\n"
    "s_lshl_b32 s15, s13, 10\n"
    "s_lshl_b32 s98, s12, 5\n"
    "s_add_u32 s15, s15, s98\n"
    "s_lshl_b32 s98, s8, 3\n"
    "s_add_u32 s15, s15, s98\n"
    "s_add_u32 s15, s15, 0x3406000\n"
    "v_accvgpr_write_b32 a8, s28\n"
    "v_accvgpr_write_b32 a9, s29\n"
    "v_mov_b32 v253, s15\n"
    "s_mov_b64 exec, 1\n"
    "global_store_dwordx2 v253, a[8:9], s[58:59] sc1\n"
    "s_mov_b64 exec, -1\n"
    "L_pa2_%=:\n"
    "v_mfma_f32_16x16x32_bf16 a[124:127], a[252:255], v[140:143], a[124:127]\n"
    "v_exp_f32 v127, v127\n"
    "v_mfma_f32_16x16x32_bf16 v[240:243], v[248:251], v[136:139], v[240:243]\n"
    "ds_read_b128 a[188:191], v199 offset:31744\n"
    "v_cvt_pk_bf16_f32 v158, v124, v125\n"
    "v_mfma_f32_16x16x32_bf16 v[244:247], v[248:251], v[140:143], v[244:247]\n"
    "v_cvt_pk_bf16_f32 v159, v126, v127\n"
    "v_add_u32 v199, s21, v198\n"
    "v_cndmask_b32 v184, v202, v194, s[44:45]\n"
    "v_cndmask_b32 v185, v202, v194, s[46:47]\n"
    "v_cndmask_b32 v186, v202, v194, s[48:49]\n"
    "v_cndmask_b32 v187, v202, v194, s[50:51]\n"
    "v_mfma_f32_16x16x32_bf16 v[104:107], a[128:131], v[32:35], v[168:171]\n"
    "ds_read_b128 a[192:195], v199 offset:0\n"
    "v_exp_f32 v72, v72\n"
    "v_mfma_f32_16x16x32_bf16 v[104:107], a[132:135], v[36:39], v[104:107]\n"
    "s_add_u32 s16, s16, 1\n"
    "s_add_u32 m0, s41, 0\n"
    "s_nop 0\n"
    "buffer_load_dwordx4 v204, s[36:39], s52 offen lds\n"
    "v_mfma_f32_16x16x32_bf16 v[104:107], a[136:139], v[40:43], v[104:107]\n"
    "ds_read_b128 a[196:199], v199 offset:1024\n"
    "v_exp_f32 v73, v73\n"
    "v_mfma_f32_16x16x32_bf16 v[104:107], a[140:143], v[44:47], v[104:107]\n"
    "v_cvt_pk_bf16_f32 v136, v72, v73\n"
    "v_mfma_f32_16x16x32_bf16 v[108:111], a[128:131], v[48:51], v[172:175]\n"
    "s_lshl_b32 s14, s16, 1\n"
    "s_cmp_gt_u32 s31, s14\n"
    "s_cselect_b64 vcc, -1, 0\n"
    "s_cmp_eq_u32 s31, s14\n"
    "s_cselect_b64 s[44:45], s[72:73], vcc\n"
    "s_cselect_b64 s[46:47], s[74:75], vcc\n"
    "s_cselect_b64 s[48:49], s[76:77], vcc\n"
    "s_cselect_b64 s[50:51], s[78:79], vcc\n"
    "ds_read_b128 a[200:203], v199 offset:2048\n"
    "v_exp_f32 v74, v74\n"
    "v_mfma_f32_16x16x32_bf16 v[108:111], a[132:135], v[52:55], v[108:111]\n"
    "s_add_u32 m0, s41, 1024\n"
    "s_nop 0\n"
    "buffer_load_dwordx4 v205, s[36:39], s52 offen lds\n"
    "v_mfma_f32_16x16x32_bf16 v[108:111], a[136:139], v[56:59], v[108:111]\n"
    "ds_read_b128 a[204:207], v199 offset:3072\n"
    "v_exp_f32 v75, v75\n"
    "v_mfma_f32_16x16x32_bf16 v[108:111], a[140:143], v[60:63], v[108:111]\n"
    "v_cvt_pk_bf16_f32 v137, v74, v75\n"
    "v_cndmask_b32 v172, v202, v195, s[44:45]\n"
    "v_cndmask_b32 v173, v202, v195, s[46:47]\n"
    "v_mfma_f32_16x16x32_bf16 a[64:67], a[160:163], v[152:155], a[64:67]\n"
    "ds_read_b128 a[224:227], v199 offset:16384\n"
    "v_exp_f32 v76, v76\n"
    "v_mfma_f32_16x16x32_bf16 a[96:99], a[160:163], v[156:159], a[96:99]\n"
    "s_add_u32 m0, s41, 2048\n"
    "s_nop 0\n"
    "buffer_load_dwordx4 v206, s[36:39], s52 offen lds\n"
    "v_cndmask_b32 v174, v202, v195, s[48:49]\n"
    "v_mfma_f32_16x16x32_bf16 a[68:71], a[164:167], v[152:155], a[68:71]\n"
    "s_waitcnt lgkmcnt(7)\n"
    "ds_read_b128 a[228:231], v199 offset:17408\n"
    "v_cndmask_b32 v175, v202, v195, s[50:51]\n"
    "v_mfma_f32_16x16x32_bf16 a[100:103], a[164:167], v[156:159], a[100:103]\n"
    "s_add_u32 s14, s14, 1\n"
    "s_cmp_gt_u32 s31, s14\n"
    "s_cselect_b64 vcc, -1, 0\n"
    "s_cmp_eq_u32 s31, s14\n"
    "s_cselect_b64 s[44:45], s[72:73], vcc\n"
    "s_cselect_b64 s[46:47], s[74:75], vcc\n"
    "s_cselect_b64 s[48:49], s[76:77], vcc\n"
    "s_cselect_b64 s[50:51], s[78:79], vcc\n"
    "v_exp_f32 v77, v77\n"
    "v_mfma_f32_16x16x32_bf16 a[72:75], a[168:171], v[152:155], a[72:75]\n"
    "ds_read_b128 a[232:235], v199 offset:18432\n"
    "v_exp_f32 v78, v78\n"
    "v_mfma_f32_16x16x32_bf16 a[104:107], a[168:171], v[156:159], a[104:107]\n"
    "s_add_u32 m0, s41, 3072\n"
    "s_nop 0\n"
    "buffer_load_dwordx4 v207, s[36:39], s52 offen lds\n"
    "s_add_u32 s52, s52, 65536\n"
    "s_and_b32 s52, s52, 0x7ffff\n"
    "s_add_u32 s41, s41, 16384\n"
    "s_and_b32 s41, s41, 0x1ffff\n"
    "v_cvt_pk_bf16_f32 v140, v76, v77\n"
    "v_mfma_f32_16x16x32_bf16 a[76:79], a[172:175], v[152:155], a[76:79]\n"
    "ds_read_b128 a[236:239], v199 offset:19456\n"
    "v_mfma_f32_16x16x32_bf16 a[108:111], a[172:175], v[156:159], a[108:111]\n"
    "v_exp_f32 v79, v79\n"
    "v_mfma_f32_16x16x32_bf16 a[80:83], a[176:179], v[152:155], a[80:83]\n"
    "v_exp_f32 v88, v88\n"
    "v_cvt_pk_bf16_f32 v141, v78, v79\n"
    "v_mfma_f32_16x16x32_bf16 v[120:123], a[144:147], v[32:35], v[184:187]\n"
    "ds_read_b128 a[208:211], v199 offset:4096\n"
    "s_add_u32 m0, s41, 0\n"
    "s_nop 0\n"
    "buffer_load_dwordx4 v204, s[36:39], s52 offen lds\n"
    "v_mfma_f32_16x16x32_bf16 v[120:123], a[148:151], v[36:39], v[120:123]\n"
    "v_mfma_f32_16x16x32_bf16 v[120:123], a[152:155], v[40:43], v[120:123]\n"
    "ds_read_b128 a[212:215], v199 offset:5120\n"
    "v_exp_f32 v89, v89\n"
    "v_mfma_f32_16x16x32_bf16 v[120:123], a[156:159], v[44:47], v[120:123]\n"
    "v_exp_f32 v90, v90\n"
    "v_cvt_pk_bf16_f32 v138, v88, v89\n"
    "v_mfma_f32_16x16x32_bf16 v[124:127], a[144:147], v[48:51], v[188:191]\n"
    "ds_read_b128 a[216:219], v199 offset:6144\n"
    "v_mfma_f32_16x16x32_bf16 v[124:127], a[148:151], v[52:55], v[124:127]\n"
    "s_add_u32 m0, s41, 1024\n"
    "s_nop 0\n"
    "buffer_load_dwordx4 v205, s[36:39], s52 offen lds\n"
    "v_mfma_f32_16x16x32_bf16 v[124:127], a[152:155], v[56:59], v[124:127]\n"
    "ds_read_b128 a[220:223], v199 offset:7168\n"
    "v_exp_f32 v91, v91\n"
    "v_mfma_f32_16x16x32_bf16 v[124:127], a[156:159], v[60:63], v[124:127]\n"
    "v_exp_f32 v92, v92\n"
    "v_cvt_pk_bf16_f32 v139, v90, v91\n"
    "v_mfma_f32_16x16x32_bf16 a[112:115], a[176:179], v[156:159], a[112:115]\n"
    "s_waitcnt lgkmcnt(6)\n"
    "ds_read_b128 a[240:243], v199 offset:20480\n"
    "v_cndmask_b32 v188, v202, v195, s[44:45]\n"
    "v_cndmask_b32 v189, v202, v195, s[46:47]\n"
    "v_mfma_f32_16x16x32_bf16 a[84:87], a[180:183], v[152:155], a[84:87]\n"
    "s_add_u32 m0, s41, 2048\n"
    "s_nop 0\n"
    "buffer_load_dwordx4 v206, s[36:39], s52 offen lds\n"
    "v_cndmask_b32 v190, v202, v195, s[48:49]\n"
    "v_mfma_f32_16x16x32_bf16 a[116:119], a[180:183], v[156:159], a[116:119]\n"
    "ds_read_b128 a[244:247], v199 offset:21504\n"
    "v_exp_f32 v93, v93\n"
    "v_mfma_f32_16x16x32_bf16 a[88:91], a[184:187], v[152:155], a[88:91]\n"
    "v_exp_f32 v94, v94\n"
    "v_cvt_pk_bf16_f32 v142, v92, v93\n"
    "v_mfma_f32_16x16x32_bf16 a[120:123], a[184:187], v[156:159], a[120:123]\n"
    "ds_read_b128 a[248:251], v199 offset:22528\n"
    "v_cndmask_b32 v191, v202, v195, s[50:51]\n"
    "v_mfma_f32_16x16x32_bf16 a[92:95], a[188:191], v[152:155], a[92:95]\n"
    "s_add_u32 m0, s41, 3072\n"
    "s_nop 0\n"
    "buffer_load_dwordx4 v207, s[36:39], s52 offen lds\n"
    "s_add_u32 s52, s52, 65536\n"
    "s_and_b32 s52, s52, 0x7ffff\n"
    "s_add_u32 s41, s41, 16384\n"
    "s_and_b32 s41, s41, 0x1ffff\n"
    "v_mfma_f32_16x16x32_bf16 a[124:127], a[188:191], v[156:159], a[124:127]\n"
    "ds_read_b128 a[252:255], v199 offset:23552\n"
    "v_exp_f32 v95, v95\n"
    "v_mfma_f32_16x16x32_bf16 v[240:243], v[248:251], v[152:155], v[240:243]\n"
    "v_cvt_pk_bf16_f32 v143, v94, v95\n"
    "v_mfma_f32_16x16x32_bf16 v[244:247], v[248:251], v[156:159], v[244:247]\n"
    "v_mfma_f32_16x16x32_bf16 v[76:79], a[192:195], v[48:51], v[172:175]\n"
    "s_waitcnt lgkmcnt(0)\n"
    "ds_read_b128 a[128:131], v199 offset:8192\n"
    "v_exp_f32 v104, v104\n"
    "v_mfma_f32_16x16x32_bf16 v[76:79], a[196:199], v[52:55], v[76:79]\n"
    "s_add_u32 s16, s16, 1\n"
    "ds_read_b128 a[132:135], v199 offset:9216\n"
    "v_exp_f32 v105, v105\n"
    "v_mfma_f32_16x16x32_bf16 v[76:79], a[200:203], v[56:59], v[76:79]\n"
    "s_add_u32 s21, s21, 32768\n"
    "v_cvt_pk_bf16_f32 v152, v104, v105\n"
    "v_mfma_f32_16x16x32_bf16 v[76:79], a[204:207], v[60:63], v[76:79]\n"
    "s_and_b32 s21, s21, 0x1ffff\n"
    "ds_read_b128 a[136:139], v199 offset:10240\n"
    "v_exp_f32 v106, v106\n"
    "v_mfma_f32_16x16x32_bf16 v[92:95], a[208:211], v[48:51], v[188:191]\n"
    "s_lshl_b32 s14, s16, 1\n"
    "s_cmp_gt_u32 s31, s14\n"
    "ds_read_b128 a[140:143], v199 offset:11264\n"
    "v_exp_f32 v107, v107\n"
    "v_mfma_f32_16x16x32_bf16 v[92:95], a[212:215], v[52:55], v[92:95]\n"
    "s_cselect_b64 vcc, -1, 0\n"
    "s_cmp_eq_u32 s31, s14\n"
    "v_cvt_pk_bf16_f32 v153, v106, v107\n"
    "v_mfma_f32_16x16x32_bf16 v[92:95], a[216:219], v[56:59], v[92:95]\n"
    "s_cselect_b64 s[44:45], s[72:73], vcc\n"
    "s_cselect_b64 s[46:47], s[74:75], vcc\n"
    "ds_read_b128 a[160:163], v199 offset:24576\n"
    "v_exp_f32 v108, v108\n"
    "v_mfma_f32_16x16x32_bf16 v[92:95], a[220:223], v[60:63], v[92:95]\n"
    "s_cselect_b64 s[48:49], s[76:77], vcc\n"
    "s_cselect_b64 s[50:51], s[78:79], vcc\n"
    "v_exp_f32 v109, v109\n"
    "v_mfma_f32_16x16x32_bf16 a[64:67], a[224:227], v[136:139], a[64:67]\n"
    "ds_read_b128 a[164:167], v199 offset:25600\n"
    "v_cvt_pk_bf16_f32 v156, v108, v109\n"
    "v_cndmask_b32 v172, v202, v195, s[44:45]\n"
    "v_mfma_f32_16x16x32_bf16 a[96:99], a[224:227], v[140:143], a[96:99]\n"
    "ds_read_b128 a[168:171], v199 offset:26624\n"
    "v_exp_f32 v110, v110\n"
    "v_mfma_f32_16x16x32_bf16 a[68:71], a[228:231], v[136:139], a[68:71]\n"
    "v_exp_f32 v111, v111\n"
    "v_cndmask_b32 v173, v202, v195, s[46:47]\n"
    "v_mfma_f32_16x16x32_bf16 a[100:103], a[228:231], v[140:143], a[100:103]\n"
    "ds_read_b128 a[172:175], v199 offset:27648\n"
    "v_cvt_pk_bf16_f32 v157, v110, v111\n"
    "v_cndmask_b32 v174, v202, v195, s[48:49]\n"
    "v_mfma_f32_16x16x32_bf16 a[72:75], a[232:235], v[136:139], a[72:75]\n"
    "v_exp_f32 v120, v120\n"
    "v_cndmask_b32 v175, v202, v195, s[50:51]\n"
    "v_mfma_f32_16x16x32_bf16 a[104:107], a[232:235], v[140:143], a[104:107]\n"
    "s_add_u32 s14, s14, 1\n"
    "s_cmp_gt_u32 s31, s14\n"
    "ds_read_b128 a[144:147], v199 offset:12288\n"
    "v_mfma_f32_16x16x32_bf16 a[76:79], a[236:239], v[136:139], a[76:79]\n"
    "s_cselect_b64 vcc, -1, 0\n"
    "s_cmp_eq_u32 s31, s14\n"
    "ds_read_b128 a[148:151], v199 offset:13312\n"
    "v_exp_f32 v121, v121\n"
    "v_mfma_f32_16x16x32_bf16 a[108:111], a[236:239], v[140:143], a[108:111]\n"
    "s_cselect_b64 s[44:45], s[72:73], vcc\n"
    "s_cselect_b64 s[46:47], s[74:75], vcc\n"
    "v_exp_f32 v122, v122\n"
    "v_cvt_pk_bf16_f32 v154, v120, v121\n"
    "v_mfma_f32_16x16x32_bf16 a[80:83], a[240:243], v[136:139], a[80:83]\n"
    "s_cselect_b64 s[48:49], s[76:77], vcc\n"
    "s_cselect_b64 s[50:51], s[78:79], vcc\n"
    "ds_read_b128 a[152:155], v199 offset:14336\n"
    "v_mfma_f32_16x16x32_bf16 a[112:115], a[240:243], v[140:143], a[112:115]\n"
    "ds_read_b128 a[156:159], v199 offset:15360\n"
    "v_exp_f32 v123, v123\n"
    "v_mfma_f32_16x16x32_bf16 a[84:87], a[244:247], v[136:139], a[84:87]\n"
    "v_exp_f32 v124, v124\n"
    "v_cvt_pk_bf16_f32 v155, v122, v123\n"
    "v_mfma_f32_16x16x32_bf16 a[116:119], a[244:247], v[140:143], a[116:119]\n"
    "ds_read_b128 a[176:179], v199 offset:28672\n"
    "v_cndmask_b32 v188, v202, v195, s[44:45]\n"
    "v_cndmask_b32 v189, v202, v195, s[46:47]\n"
    "v_mfma_f32_16x16x32_bf16 a[88:91], a[248:251], v[136:139], a[88:91]\n"
    "v_exp_f32 v125, v125\n"
    "v_cndmask_b32 v190, v202, v195, s[48:49]\n"
    "v_mfma_f32_16x16x32_bf16 a[120:123], a[248:251], v[140:143], a[120:123]\n"
    "ds_read_b128 a[180:183], v199 offset:29696\n"
    "v_exp_f32 v126, v126\n"
    "v_mfma_f32_16x16x32_bf16 a[92:95], a[252:255], v[136:139], a[92:95]\n"
    "s_waitcnt vmcnt(8)\n"
    "s_barrier\n"
    "ds_read_b128 a[184:187], v199 offset:30720\n"
    "v_cvt_pk_bf16_f32 v158, v124, v125\n"
    "v_cndmask_b32 v191, v202, v195, s[50:51]\n"
    "v_mfma_f32_16x16x32_bf16 a[124:127], a[252:255], v[140:143], a[124:127]\n"
    "v_exp_f32 v127, v127\n"
    "v_mfma_f32_16x16x32_bf16 v[240:243], v[248:251], v[136:139], v[240:243]\n"
    "ds_read_b128 a[188:191], v199 offset:31744\n"
    "v_cvt_pk_bf16_f32 v159, v126, v127\n"
    "v_mfma_f32_16x16x32_bf16 v[244:247], v[248:251], v[140:143], v[244:247]\n"
    "s_waitcnt lgkmcnt(8)\n"
    "v_add_u32 v199, s21, v198\n"
    "v_mfma_f32_16x16x32_bf16 v[108:111], a[128:131], v[48:51], v[172:175]\n"
    "v_exp_f32 v76, v76\n"
    "v_mfma_f32_16x16x32_bf16 v[108:111], a[132:135], v[52:55], v[108:111]\n"
    "s_add_u32 s16, s16, 1\n"
    "s_add_u32 m0, s41, 0\n"
    "s_nop 0\n"
    "buffer_load_dwordx4 v204, s[36:39], s52 offen lds\n"
    "v_mfma_f32_16x16x32_bf16 v[108:111], a[136:139], v[56:59], v[108:111]\n"
    "s_waitcnt lgkmcnt(0)\n"
    "ds_read_b128 a[224:227], v199 offset:16384\n"
    "v_mfma_f32_16x16x32_bf16 v[108:111], a[140:143], v[60:63], v[108:111]\n"
    "s_add_u32 m0, s41, 1024\n"
    "s_nop 0\n"
    "buffer_load_dwordx4 v205, s[36:39], s52 offen lds\n"
    "v_mfma_f32_16x16x32_bf16 v[124:127], a[144:147], v[48:51], v[188:191]\n"
    "v_exp_f32 v77, v77\n"
    "v_mfma_f32_16x16x32_bf16 v[124:127], a[148:151], v[52:55], v[124:127]\n"
    "ds_read_b128 a[228:231], v199 offset:17408\n"
    "v_cvt_pk_bf16_f32 v140, v76, v77\n"
    "v_mfma_f32_16x16x32_bf16 v[124:127], a[152:155], v[56:59], v[124:127]\n"
    "s_add_u32 m0, s41, 2048\n"
    "s_nop 0\n"
    "buffer_load_dwordx4 v206, s[36:39], s52 offen lds\n"
    "v_mfma_f32_16x16x32_bf16 v[124:127], a[156:159], v[60:63], v[124:127]\n"
    "v_exp_f32 v78, v78\n"
    "v_mfma_f32_16x16x32_bf16 a[64:67], a[160:163], v[152:155], a[64:67]\n"
    "ds_read_b128 a[232:235], v199 offset:18432\n"
    "v_mfma_f32_16x16x32_bf16 a[96:99], a[160:163], v[156:159], a[96:99]\n"
    "s_add_u32 m0, s41, 3072\n"
    "s_nop 0\n"
    "buffer_load_dwordx4 v207, s[36:39], s52 offen lds\n"
    "s_add_u32 s52, s52, 65536\n"
    "s_and_b32 s52, s52, 0x7ffff\n"
    "s_add_u32 s41, s41, 16384\n"
    "s_and_b32 s41, s41, 0x1ffff\n"
    "v_mfma_f32_16x16x32_bf16 a[68:71], a[164:167], v[152:155], a[68:71]\n"
    "v_exp_f32 v79, v79\n"
    "v_mfma_f32_16x16x32_bf16 a[100:103], a[164:167], v[156:159], a[100:103]\n"
    "ds_read_b128 a[236:239], v199 offset:19456\n"
    "v_cvt_pk_bf16_f32 v141, v78, v79\n"
    "v_mfma_f32_16x16x32_bf16 a[72:75], a[168:171], v[152:155], a[72:75]\n"
    "s_add_u32 m0, s41, 0\n"
    "s_nop 0\n"
    "buffer_load_dwordx4 v204, s[36:39], s52 offen lds\n"
    "v_mfma_f32_16x16x32_bf16 a[104:107], a[168:171], v[156:159], a[104:107]\n"
    "ds_read_b128 a[240:243], v199 offset:20480\n"
    "v_mfma_f32_16x16x32_bf16 a[76:79], a[172:175], v[152:155], a[76:79]\n"
    "v_exp_f32 v92, v92\n"
    "v_mfma_f32_16x16x32_bf16 a[108:111], a[172:175], v[156:159], a[108:111]\n"
    "s_add_u32 m0, s41, 1024\n"
    "s_nop 0\n"
    "buffer_load_dwordx4 v205, s[36:39], s52 offen lds\n"
    "v_mfma_f32_16x16x32_bf16 a[80:83], a[176:179], v[152:155], a[80:83]\n"
    "ds_read_b128 a[244:247], v199 offset:21504\n"
    "v_mfma_f32_16x16x32_bf16 a[112:115], a[176:179], v[156:159], a[112:115]\n"
    "v_exp_f32 v93, v93\n"
    "v_mfma_f32_16x16x32_bf16 a[84:87], a[180:183], v[152:155], a[84:87]\n"
    "s_add_u32 m0, s41, 2048\n"
    "s_nop 0\n"
    "buffer_load_dwordx4 v206, s[36:39], s52 offen lds\n"
    "v_mfma_f32_16x16x32_bf16 a[116:119], a[180:183], v[156:159], a[116:119]\n"
    "ds_read_b128 a[248:251], v199 offset:22528\n"
    "v_cvt_pk_bf16_f32 v142, v92, v93\n"
    "v_mfma_f32_16x16x32_bf16 a[88:91], a[184:187], v[152:155], a[88:91]\n"
    "v_exp_f32 v94, v94\n"
    "v_mfma_f32_16x16x32_bf16 a[120:123], a[184:187], v[156:159], a[120:123]\n"
    "s_add_u32 m0, s41, 3072\n"
    "s_nop 0\n"
    "buffer_load_dwordx4 v207, s[36:39], s52 offen lds\n"
    "s_add_u32 s52, s52, 65536\n"
    "s_and_b32 s52, s52, 0x7ffff\n"
    "s_add_u32 s41, s41, 16384\n"
    "s_and_b32 s41, s41, 0x1ffff\n"
    "v_mfma_f32_16x16x32_bf16 a[92:95], a[188:191], v[152:155], a[92:95]\n"
    "ds_read_b128 a[252:255], v199 offset:23552\n"
    "v_mfma_f32_16x16x32_bf16 a[124:127], a[188:191], v[156:159], a[124:127]\n"
    "v_exp_f32 v95, v95\n"
    "v_mfma_f32_16x16x32_bf16 v[240:243], v[248:251], v[152:155], v[240:243]\n"
    "v_cvt_pk_bf16_f32 v143, v94, v95\n"
    "v_mfma_f32_16x16x32_bf16 v[244:247], v[248:251], v[156:159], v[244:247]\n"
    "s_barrier\n"
    "L_att_D_%=:\n"
    "s_lshl_b32 s14, s13, 10\n"
    "s_lshl_b32 s15, s10, 7\n"
    "s_add_u32 s14, s14, s15\n"
    "s_lshl_b32 s15, s8, 3\n"
    "s_add_u32 s14, s14, s15\n"
    "s_add_u32 s14, s14, 0x3406000\n"
    "v_min_u32 v192, 3, v255\n"
    "v_lshlrev_b32 v192, 5, v192\n"
    "v_add_u32 v192, s14, v192\n"
    "global_load_dwordx2 v[194:195], v192, s[58:59] sc1\n"
    "s_waitcnt lgkmcnt(0)\n"
    "v_mfma_f32_16x16x32_bf16 a[96:99], a[224:227], v[140:143], a[96:99]\n"
    "v_exp_f32 v108, v108\n"
    "s_add_u32 m0, s41, 0\n"
    "s_nop 0\n"
    "buffer_load_dwordx4 v204, s[36:39], s52 offen lds\n"
    "v_mfma_f32_16x16x32_bf16 a[100:103], a[228:231], v[140:143], a[100:103]\n"
    "s_add_u32 s16, s16, 1\n"
    "ds_read_b128 a[164:167], v199 offset:25600\n"
    "ds_read_b128 a[160:163], v199 offset:24576\n"
    "v_exp_f32 v109, v109\n"
    "v_mfma_f32_16x16x32_bf16 a[104:107], a[232:235], v[140:143], a[104:107]\n"
    "v_exp_f32 v110, v110\n"
    "s_add_u32 m0, s41, 1024\n"
    "s_nop 0\n"
    "buffer_load_dwordx4 v205, s[36:39], s52 offen lds\n"
    "v_mfma_f32_16x16x32_bf16 a[108:111], a[236:239], v[140:143], a[108:111]\n"
    "ds_read_b128 a[172:175], v199 offset:27648\n"
    "ds_read_b128 a[168:171], v199 offset:26624\n"
    "v_exp_f32 v111, v111\n"
    "v_mfma_f32_16x16x32_bf16 a[112:115], a[240:243], v[140:143], a[112:115]\n"
    "v_exp_f32 v124, v124\n"
    "s_add_u32 m0, s41, 2048\n"
    "s_nop 0\n"
    "buffer_load_dwordx4 v206, s[36:39], s52 offen lds\n"
    "v_mfma_f32_16x16x32_bf16 a[116:119], a[244:247], v[140:143], a[116:119]\n"
    "ds_read_b128 a[180:183], v199 offset:29696\n"
    "ds_read_b128 a[176:179], v199 offset:28672\n"
    "v_exp_f32 v125, v125\n"
    "v_mfma_f32_16x16x32_bf16 a[120:123], a[248:251], v[140:143], a[120:123]\n"
    "v_exp_f32 v126, v126\n"
    "s_add_u32 m0, s41, 3072\n"
    "s_nop 0\n"
    "buffer_load_dwordx4 v207, s[36:39], s52 offen lds\n"
    "s_add_u32 s52, s52, 65536\n"
    "s_and_b32 s52, s52, 0x7ffff\n"
    "s_add_u32 s41, s41, 16384\n"
    "s_and_b32 s41, s41, 0x1ffff\n"
    "v_mfma_f32_16x16x32_bf16 a[124:127], a[252:255], v[140:143], a[124:127]\n"
    "ds_read_b128 a[188:191], v199 offset:31744\n"
    "ds_read_b128 a[184:187], v199 offset:30720\n"
    "v_exp_f32 v127, v127\n"
    "v_mfma_f32_16x16x32_bf16 v[244:247], v[248:251], v[140:143], v[244:247]\n"
    "s_waitcnt lgkmcnt(0)\n"
    "v_cvt_pk_bf16_f32 v156, v108, v109\n"
    "v_cvt_pk_bf16_f32 v157, v110, v111\n"
    "v_cvt_pk_bf16_f32 v158, v124, v125\n"
    "v_cvt_pk_bf16_f32 v159, v126, v127\n"
    "s_nop 1\n"
    "v_mfma_f32_16x16x32_bf16 a[96:99], a[160:163], v[156:159], a[96:99]\n"
    "s_add_u32 m0, s41, 0\n"
    "s_nop 0\n"
    "buffer_load_dwordx4 v204, s[36:39], s52 offen lds\n"
    "v_mfma_f32_16x16x32_bf16 a[100:103], a[164:167], v[156:159], a[100:103]\n"
    "s_add_u32 s16, s16, 1\n"
    "s_add_u32 m0, s41, 1024\n"
    "s_nop 0\n"
    "buffer_load_dwordx4 v205, s[36:39], s52 offen lds\n"
    "v_mfma_f32_16x16x32_bf16 a[104:107], a[168:171], v[156:159], a[104:107]\n"
    "s_add_u32 m0, s41, 2048\n"
    "s_nop 0\n"
    "buffer_load_dwordx4 v206, s[36:39], s52 offen lds\n"
    "v_mfma_f32_16x16x32_bf16 a[108:111], a[172:175], v[156:159], a[108:111]\n"
    "s_add_u32 m0, s41, 3072\n"
    "s_nop 0\n"
    "buffer_load_dwordx4 v207, s[36:39], s52 offen lds\n"
    "s_add_u32 s52, s52, 65536\n"
    "s_and_b32 s52, s52, 0x7ffff\n"
    "s_add_u32 s41, s41, 16384\n"
    "s_and_b32 s41, s41, 0x1ffff\n"
    "v_mfma_f32_16x16x32_bf16 a[112:115], a[176:179], v[156:159], a[112:115]\n"
    "s_add_u32 m0, s41, 0\n"
    "s_nop 0\n"
    "buffer_load_dwordx4 v204, s[36:39], s52 offen lds\n"
    "v_mfma_f32_16x16x32_bf16 a[116:119], a[180:183], v[156:159], a[116:119]\n"
    "s_add_u32 m0, s41, 1024\n"
    "s_nop 0\n"
    "buffer_load_dwordx4 v205, s[36:39], s52 offen lds\n"
    "v_mfma_f32_16x16x32_bf16 a[120:123], a[184:187], v[156:159], a[120:123]\n"
    "s_add_u32 m0, s41, 2048\n"
    "s_nop 0\n"
    "buffer_load_dwordx4 v206, s[36:39], s52 offen lds\n"
    "v_mfma_f32_16x16x32_bf16 a[124:127], a[188:191], v[156:159], a[124:127]\n"
    "s_add_u32 m0, s41, 3072\n"
    "s_nop 0\n"
    "buffer_load_dwordx4 v207, s[36:39], s52 offen lds\n"
    "s_add_u32 s52, s52, 65536\n"
    "s_and_b32 s52, s52, 0x7ffff\n"
    "s_add_u32 s41, s41, 16384\n"
    "s_and_b32 s41, s41, 0x1ffff\n"
    "v_mfma_f32_16x16x32_bf16 v[244:247], v[248:251], v[156:159], v[244:247]\n"
    "ds_read_b128 v[168:171], v254 offset:0\n"
    "ds_read_b128 v[172:175], v254 offset:1024\n"
    "ds_read_b128 v[184:187], v254 offset:2048\n"
    "ds_read_b128 v[188:191], v254 offset:3072\n"
    "v_cmp_nge_f32 vcc, s23, v240\n"
    "v_rcp_f32 v240, v240\n"
    "s_or_b64 s[26:27], s[26:27], vcc\n"
    "s_nop 0\n"
    "v_cmp_nge_f32 vcc, s23, v244\n"
    "v_rcp_f32 v244, v244\n"
    "s_or_b64 s[26:27], s[26:27], vcc\n"
    "s_lshl_b32 s15, s12, 12\n"
    "s_mul_i32 s25, s30, 131072\n"
    "s_add_u32 s25, s25, s15\n"
    "s_mul_i32 s35, s31, 131072\n"
    "s_add_u32 s35, s35, s15\n"
    "v_accvgpr_read_b32 v136, a64\n"
    "v_accvgpr_read_b32 v137, a65\n"
    "v_accvgpr_read_b32 v138, a66\n"
    "v_accvgpr_read_b32 v139, a67\n"
    "v_accvgpr_read_b32 v140, a68\n"
    "v_accvgpr_read_b32 v141, a69\n"
    "v_accvgpr_read_b32 v142, a70\n"
    "v_accvgpr_read_b32 v143, a71\n"
    "v_pk_mul_f32 v[136:137], v[136:137], v[240:241] op_sel:[0,0] op_sel_hi:[1,0]\n"
    "v_pk_mul_f32 v[138:139], v[138:139], v[240:241] op_sel:[0,0] op_sel_hi:[1,0]\n"
    "v_pk_mul_f32 v[140:141], v[140:141], v[240:241] op_sel:[0,0] op_sel_hi:[1,0]\n"
    "v_pk_mul_f32 v[142:143], v[142:143], v[240:241] op_sel:[0,0] op_sel_hi:[1,0]\n"
    "v_cvt_pk_bf16_f32 v72, v136, v137\n"
    "v_cvt_pk_bf16_f32 v73, v138, v139\n"
    "v_cvt_pk_bf16_f32 v74, v140, v141\n"
    "v_cvt_pk_bf16_f32 v75, v142, v143\n"
    "buffer_store_dwordx4 v[72:75], v198, s[68:71], s25 offen offset:0 sc1\n"
    "v_accvgpr_read_b32 v152, a72\n"
    "v_accvgpr_read_b32 v153, a73\n"
    "v_accvgpr_read_b32 v154, a74\n"
    "v_accvgpr_read_b32 v155, a75\n"
    "v_accvgpr_read_b32 v156, a76\n"
    "v_accvgpr_read_b32 v157, a77\n"
    "v_accvgpr_read_b32 v158, a78\n"
    "v_accvgpr_read_b32 v159, a79\n"
    "v_pk_mul_f32 v[152:153], v[152:153], v[240:241] op_sel:[0,0] op_sel_hi:[1,0]\n"
    "v_pk_mul_f32 v[154:155], v[154:155], v[240:241] op_sel:[0,0] op_sel_hi:[1,0]\n"
    "v_pk_mul_f32 v[156:157], v[156:157], v[240:241] op_sel:[0,0] op_sel_hi:[1,0]\n"
    "v_pk_mul_f32 v[158:159], v[158:159], v[240:241] op_sel:[0,0] op_sel_hi:[1,0]\n"
    "v_cvt_pk_bf16_f32 v76, v152, v153\n"
    "v_cvt_pk_bf16_f32 v77, v154, v155\n"
    "v_cvt_pk_bf16_f32 v78, v156, v157\n"
    "v_cvt_pk_bf16_f32 v79, v158, v159\n"
    "buffer_store_dwordx4 v[76:79], v198, s[68:71], s25 offen offset:1024 sc1\n"
    "v_accvgpr_read_b32 v136, a80\n"
    "v_accvgpr_read_b32 v137, a81\n"
    "v_accvgpr_read_b32 v138, a82\n"
    "v_accvgpr_read_b32 v139, a83\n"
    "v_accvgpr_read_b32 v140, a84\n"
    "v_accvgpr_read_b32 v141, a85\n"
    "v_accvgpr_read_b32 v142, a86\n"
    "v_accvgpr_read_b32 v143, a87\n"
    "v_pk_mul_f32 v[136:137], v[136:137], v[240:241] op_sel:[0,0] op_sel_hi:[1,0]\n"
    "v_pk_mul_f32 v[138:139], v[138:139], v[240:241] op_sel:[0,0] op_sel_hi:[1,0]\n"
    "v_pk_mul_f32 v[140:141], v[140:141], v[240:241] op_sel:[0,0] op_sel_hi:[1,0]\n"
    "v_pk_mul_f32 v[142:143], v[142:143], v[240:241] op_sel:[0,0] op_sel_hi:[1,0]\n"
    "v_cvt_pk_bf16_f32 v88, v136, v137\n"
    "v_cvt_pk_bf16_f32 v89, v138, v139\n"
    "v_cvt_pk_bf16_f32 v90, v140, v141\n"
    "v_cvt_pk_bf16_f32 v91, v142, v143\n"
    "buffer_store_dwordx4 v[88:91], v198, s[68:71], s25 offen offset:2048 sc1\n"
    "v_accvgpr_read_b32 v152, a88\n"
    "v_accvgpr_read_b32 v153, a89\n"
    "v_accvgpr_read_b32 v154, a90\n"
    "v_accvgpr_read_b32 v155, a91\n"
    "v_accvgpr_read_b32 v156, a92\n"
    "v_accvgpr_read_b32 v157, a93\n"
    "v_accvgpr_read_b32 v158, a94\n"
    "v_accvgpr_read_b32 v159, a95\n"
    "v_pk_mul_f32 v[152:153], v[152:153], v[240:241] op_sel:[0,0] op_sel_hi:[1,0]\n"
    "v_pk_mul_f32 v[154:155], v[154:155], v[240:241] op_sel:[0,0] op_sel_hi:[1,0]\n"
    "v_pk_mul_f32 v[156:157], v[156:157], v[240:241] op_sel:[0,0] op_sel_hi:[1,0]\n"
    "v_pk_mul_f32 v[158:159], v[158:159], v[240:241] op_sel:[0,0] op_sel_hi:[1,0]\n"
    "v_cvt_pk_bf16_f32 v92, v152, v153\n"
    "v_cvt_pk_bf16_f32 v93, v154, v155\n"
    "v_cvt_pk_bf16_f32 v94, v156, v157\n"
    "v_cvt_pk_bf16_f32 v95, v158, v159\n"
    "buffer_store_dwordx4 v[92:95], v198, s[68:71], s25 offen offset:3072 sc1\n"
    "v_accvgpr_read_b32 v136, a96\n"
    "v_accvgpr_read_b32 v137, a97\n"
    "v_accvgpr_read_b32 v138, a98\n"
    "v_accvgpr_read_b32 v139, a99\n"
    "v_accvgpr_read_b32 v140, a100\n"
    "v_accvgpr_read_b32 v141, a101\n"
    "v_accvgpr_read_b32 v142, a102\n"
    "v_accvgpr_read_b32 v143, a103\n"
    "v_pk_mul_f32 v[136:137], v[136:137], v[244:245] op_sel:[0,0] op_sel_hi:[1,0]\n"
    "v_pk_mul_f32 v[138:139], v[138:139], v[244:245] op_sel:[0,0] op_sel_hi:[1,0]\n"
    "v_pk_mul_f32 v[140:141], v[140:141], v[244:245] op_sel:[0,0] op_sel_hi:[1,0]\n"
    "v_pk_mul_f32 v[142:143], v[142:143], v[244:245] op_sel:[0,0] op_sel_hi:[1,0]\n"
    "v_cvt_pk_bf16_f32 v104, v136, v137\n"
    "v_cvt_pk_bf16_f32 v105, v138, v139\n"
    "v_cvt_pk_bf16_f32 v106, v140, v141\n"
    "v_cvt_pk_bf16_f32 v107, v142, v143\n"
    "buffer_store_dwordx4 v[104:107], v198, s[68:71], s35 offen offset:0 sc1\n"
    "v_accvgpr_read_b32 v152, a104\n"
    "v_accvgpr_read_b32 v153, a105\n"
    "v_accvgpr_read_b32 v154, a106\n"
    "v_accvgpr_read_b32 v155, a107\n"
    "v_accvgpr_read_b32 v156, a108\n"
    "v_accvgpr_read_b32 v157, a109\n"
    "v_accvgpr_read_b32 v158, a110\n"
    "v_accvgpr_read_b32 v159, a111\n"
    "v_pk_mul_f32 v[152:153], v[152:153], v[244:245] op_sel:[0,0] op_sel_hi:[1,0]\n"
    "v_pk_mul_f32 v[154:155], v[154:155], v[244:245] op_sel:[0,0] op_sel_hi:[1,0]\n"
    "v_pk_mul_f32 v[156:157], v[156:157], v[244:245] op_sel:[0,0] op_sel_hi:[1,0]\n"
    "v_pk_mul_f32 v[158:159], v[158:159], v[244:245] op_sel:[0,0] op_sel_hi:[1,0]\n"
    "v_cvt_pk_bf16_f32 v108, v152, v153\n"
    "v_cvt_pk_bf16_f32 v109, v154, v155\n"
    "v_cvt_pk_bf16_f32 v110, v156, v157\n"
    "v_cvt_pk_bf16_f32 v111, v158, v159\n"
    "buffer_store_dwordx4 v[108:111], v198, s[68:71], s35 offen offset:1024 sc1\n"
    "v_accvgpr_read_b32 v136, a112\n"
    "v_accvgpr_read_b32 v137, a113\n"
    "v_accvgpr_read_b32 v138, a114\n"
    "v_accvgpr_read_b32 v139, a115\n"
    "v_accvgpr_read_b32 v140, a116\n"
    "v_accvgpr_read_b32 v141, a117\n"
    "v_accvgpr_read_b32 v142, a118\n"
    "v_accvgpr_read_b32 v143, a119\n"
    "v_pk_mul_f32 v[136:137], v[136:137], v[244:245] op_sel:[0,0] op_sel_hi:[1,0]\n"
    "v_pk_mul_f32 v[138:139], v[138:139], v[244:245] op_sel:[0,0] op_sel_hi:[1,0]\n"
    "v_pk_mul_f32 v[140:141], v[140:141], v[244:245] op_sel:[0,0] op_sel_hi:[1,0]\n"
    "v_pk_mul_f32 v[142:143], v[142:143], v[244:245] op_sel:[0,0] op_sel_hi:[1,0]\n"
    "v_cvt_pk_bf16_f32 v120, v136, v137\n"
    "v_cvt_pk_bf16_f32 v121, v138, v139\n"
    "v_cvt_pk_bf16_f32 v122, v140, v141\n"
    "v_cvt_pk_bf16_f32 v123, v142, v143\n"
    "buffer_store_dwordx4 v[120:123], v198, s[68:71], s35 offen offset:2048 sc1\n"
    "v_accvgpr_read_b32 v152, a120\n"
    "v_accvgpr_read_b32 v153, a121\n"
    "v_accvgpr_read_b32 v154, a122\n"
    "v_accvgpr_read_b32 v155, a123\n"
    "v_accvgpr_read_b32 v156, a124\n"
    "v_accvgpr_read_b32 v157, a125\n"
    "v_accvgpr_read_b32 v158, a126\n"
    "v_accvgpr_read_b32 v159, a127\n"
    "v_pk_mul_f32 v[152:153], v[152:153], v[244:245] op_sel:[0,0] op_sel_hi:[1,0]\n"
    "v_pk_mul_f32 v[154:155], v[154:155], v[244:245] op_sel:[0,0] op_sel_hi:[1,0]\n"
    "v_pk_mul_f32 v[156:157], v[156:157], v[244:245] op_sel:[0,0] op_sel_hi:[1,0]\n"
    "v_pk_mul_f32 v[158:159], v[158:159], v[244:245] op_sel:[0,0] op_sel_hi:[1,0]\n"
    "v_cvt_pk_bf16_f32 v124, v152, v153\n"
    "v_cvt_pk_bf16_f32 v125, v154, v155\n"
    "v_cvt_pk_bf16_f32 v126, v156, v157\n"
    "v_cvt_pk_bf16_f32 v127, v158, v159\n"
    "buffer_store_dwordx4 v[124:127], v198, s[68:71], s35 offen offset:3072 sc1\n"
    "s_waitcnt vmcnt(21)\n"
    "v_accvgpr_read_b32 v136, a0\n"
    "v_accvgpr_read_b32 v137, a1\n"
    "v_accvgpr_read_b32 v138, a2\n"
    "v_accvgpr_read_b32 v139, a3\n"
    "v_pk_add_f32 v[64:65], v[64:65], v[128:129]\n"
    "v_pk_add_f32 v[66:67], v[66:67], v[130:131]\n"
    "v_pk_add_f32 v[68:69], v[68:69], v[132:133]\n"
    "v_pk_add_f32 v[70:71], v[70:71], v[134:135]\n"
    "v_pk_add_f32 v[80:81], v[80:81], v[144:145]\n"
    "v_pk_add_f32 v[82:83], v[82:83], v[146:147]\n"
    "v_pk_add_f32 v[84:85], v[84:85], v[148:149]\n"
    "v_pk_add_f32 v[86:87], v[86:87], v[150:151]\n"
    "v_pk_add_f32 v[96:97], v[96:97], v[160:161]\n"
    "v_pk_add_f32 v[98:99], v[98:99], v[162:163]\n"
    "v_pk_add_f32 v[100:101], v[100:101], v[164:165]\n"
    "v_pk_add_f32 v[102:103], v[102:103], v[166:167]\n"
    "v_pk_add_f32 v[112:113], v[112:113], v[176:177]\n"
    "v_pk_add_f32 v[114:115], v[114:115], v[178:179]\n"
    "v_pk_add_f32 v[116:117], v[116:117], v[180:181]\n"
    "v_pk_add_f32 v[118:119], v[118:119], v[182:183]\n"
    "v_pk_add_f32 v[64:65], v[64:65], v[96:97]\n"
    "v_pk_add_f32 v[66:67], v[66:67], v[98:99]\n"
    "v_pk_add_f32 v[68:69], v[68:69], v[100:101]\n"
    "v_pk_add_f32 v[70:71], v[70:71], v[102:103]\n"
    "v_pk_add_f32 v[80:81], v[80:81], v[112:113]\n"
    "v_pk_add_f32 v[82:83], v[82:83], v[114:115]\n"
    "v_pk_add_f32 v[84:85], v[84:85], v[116:117]\n"
    "v_pk_add_f32 v[86:87], v[86:87], v[118:119]\n"
    "v_pk_add_f32 v[64:65], v[64:65], v[80:81]\n"
    "v_pk_add_f32 v[66:67], v[66:67], v[82:83]\n"
    "v_pk_add_f32 v[68:69], v[68:69], v[84:85]\n"
    "v_pk_add_f32 v[70:71], v[70:71], v[86:87]\n"
    "v_pk_add_f32 v[64:65], v[64:65], v[68:69]\n"
    "v_pk_add_f32 v[66:67], v[66:67], v[70:71]\n"
    "v_add_f32 v136, v136, v136 row_ror:8\n"
    "v_add_f32 v137, v137, v137 row_ror:8\n"
    "v_add_f32 v138, v138, v138 row_ror:8\n"
    "v_add_f32 v139, v139, v139 row_ror:8\n"
    "v_add_f32 v136, v136, v136 row_ror:4\n"
    "v_add_f32 v137, v137, v137 row_ror:4\n"
    "v_add_f32 v138, v138, v138 row_ror:4\n"
    "v_add_f32 v139, v139, v139 row_ror:4\n"
    "v_add_f32 v136, v136, v136 row_ror:2\n"
    "v_add_f32 v137, v137, v137 row_ror:2\n"
    "v_add_f32 v138, v138, v138 row_ror:2\n"
    "v_add_f32 v139, v139, v139 row_ror:2\n"
    "v_add_f32 v136, v136, v136 row_ror:1\n"
    "v_add_f32 v137, v137, v137 row_ror:1\n"
    "v_add_f32 v138, v138, v138 row_ror:1\n"
    "v_add_f32 v139, v139, v139 row_ror:1\n"
    "v_cvt_pk_bf16_f32 v64, v64, v65\n"
    "v_cvt_pk_bf16_f32 v65, v66, v67\n"
    "v_cvt_pk_bf16_f32 v66, v136, v137\n"
    "v_cvt_pk_bf16_f32 v67, v138, v139\n"
    "s_cmp_lg_u64 s[26:27], 0\n"
    "s_cselect_b32 s14, 1, 0\n"
    "s_lshl_b32 s15, s8, 2\n"
    "s_add_u32 s15, s15, 131072\n"
    "v_mov_b32 v203, s14\n"
    "v_mov_b32 v152, s15\n"
    "ds_write_b32 v152, v203\n"
    "v_mov_b32 v152, 131072\n"
    "v_lshlrev_b32 v253, 3, v255\n"
    "v_add_u32 v253, 132096, v253\n"
    "s_lshl_b32 s15, s8, 10\n"
    "v_add_u32 v252, s15, v253\n"
    "ds_write_b64 v252, v[64:65]\n"
    "ds_write_b64 v252, v[66:67] offset:512\n"
    "s_waitcnt vmcnt(12) lgkmcnt(0)\n"
    "s_barrier\n"
    "ds_read_b128 v[96:99], v152\n"
    "ds_read_b64 v[64:65], v253 offset:0\n"
    "ds_read_b64 v[80:81], v253 offset:512\n"
    "ds_read_b64 v[66:67], v253 offset:1024\n"
    "ds_read_b64 v[82:83], v253 offset:1536\n"
    "ds_read_b64 v[68:69], v253 offset:2048\n"
    "ds_read_b64 v[84:85], v253 offset:2560\n"
    "ds_read_b64 v[70:71], v253 offset:3072\n"
    "ds_read_b64 v[86:87], v253 offset:3584\n"
    "s_waitcnt lgkmcnt(8)\n"
    "v_or3_b32 v203, v96, v97, v98\n"
    "v_or_b32 v203, v203, v99\n"
    "s_nop 0\n"
    "v_readfirstlane_b32 s14, v203\n"
    "s_nop 0\n"
    "s_cmp_gt_u32 s14, s24\n"
    "s_cbranch_scc1 L_att_fallback_%=\n"
    "s_lshl_b32 s15, s13, 10\n"
    "s_lshl_b32 s98, s12, 5\n"
    "s_add_u32 s15, s15, s98\n"
    "s_lshl_b32 s98, s8, 3\n"
    "s_add_u32 s15, s15, s98\n"
    "s_add_u32 s15, s15, 0x3406000\n"
    "v_accvgpr_write_b32 a252, s28\n"
    "v_accvgpr_write_b32 a253, s29\n"
    "v_mov_b32 v251, s15\n"
    "s_mov_b64 exec, 1\n"
    "global_store_dwordx2 v251, a[252:253], s[58:59] sc1\n"
    "s_mov_b64 exec, -1\n"
    "v_cmp_ne_u64 s[98:99], s[28:29], v[194:195]\n"
    "s_nop 0\n"
    "s_cmp_lg_u64 s[98:99], 0\n"
    "s_cbranch_scc1 L_spin11_%=\n"
    "L_ok12_%=:\n"
    "v_mov_b32 v201, v192\n"
    "v_bfe_u32 v248, v255, 2, 3\n"
    "v_lshrrev_b32 v249, 2, v255\n"
    "v_lshlrev_b32 v249, 8, v249\n"
    "v_lshl_add_u32 v248, v248, 5, v249\n"
    "v_and_b32 v249, 3, v255\n"
    "v_lshl_add_u32 v192, v249, 3, v248\n"
    "v_xor_b32 v193, 32, v192\n"
    "v_xor_b32 v194, 64, v192\n"
    "v_xor_b32 v195, 96, v192\n"
    "v_xor_b32 v196, 128, v192\n"
    "v_xor_b32 v197, 160, v192\n"
    "v_xor_b32 v199, 192, v192\n"
    "v_xor_b32 v200, 224, v192\n"
    "s_add_u32 s19, s53, 0\n"
    "s_and_b32 s19, s19, 0x1ffff\n"
    "v_add_u32 v240, s19, v192\n"
    "v_add_u32 v241, s19, v193\n"
    "v_add_u32 v242, s19, v194\n"
    "v_add_u32 v243, s19, v195\n"
    "v_add_u32 v244, s19, v196\n"
    "v_add_u32 v245, s19, v197\n"
    "v_add_u32 v246, s19, v199\n"
    "v_add_u32 v247, s19, v200\n"
    "s_waitcnt lgkmcnt(0)\n"
    "v_mfma_f32_16x16x32_bf16 v[96:99], v[64:67], v[168:171], 0\n"
    "v_mfma_f32_16x16x32_bf16 a[16:19], v[80:83], v[168:171], 0\n"
    "v_mfma_f32_16x16x32_bf16 v[100:103], v[64:67], v[172:175], 0\n"
    "v_mfma_f32_16x16x32_bf16 a[20:23], v[80:83], v[172:175], 0\n"
    "v_mfma_f32_16x16x32_bf16 v[112:115], v[68:71], v[184:187], 0\n"
    "v_mfma_f32_16x16x32_bf16 a[24:27], v[84:87], v[184:187], 0\n"
    "v_mfma_f32_16x16x32_bf16 v[116:119], v[68:71], v[188:191], 0\n"
    "v_mfma_f32_16x16x32_bf16 a[28:31], v[84:87], v[188:191], 0\n"
    "ds_read_b64_tr_b16 v[128:129], v240 offset:0\n"
    "ds_read_b64_tr_b16 v[130:131], v240 offset:4096\n"
    "ds_read_b64_tr_b16 v[132:133], v241 offset:0\n"
    "ds_read_b64_tr_b16 v[134:135], v241 offset:4096\n"
    "ds_read_b64_tr_b16 v[136:137], v242 offset:0\n"
    "ds_read_b64_tr_b16 v[138:139], v242 offset:4096\n"
    "ds_read_b64_tr_b16 v[140:141], v243 offset:0\n"
    "ds_read_b64_tr_b16 v[142:143], v243 offset:4096\n"
    "ds_read_b64_tr_b16 v[144:145], v244 offset:0\n"
    "ds_read_b64_tr_b16 v[146:147], v244 offset:4096\n"
    "ds_read_b64_tr_b16 v[148:149], v245 offset:0\n"
    "ds_read_b64_tr_b16 v[150:151], v245 offset:4096\n"
    "ds_read_b64_tr_b16 v[152:153], v246 offset:0\n"
    "ds_read_b64_tr_b16 v[154:155], v246 offset:4096\n"
    "ds_read_b64_tr_b16 v[156:157], v247 offset:0\n"
    "ds_read_b64_tr_b16 v[158:159], v247 offset:4096\n"
    "s_waitcnt lgkmcnt(11)\n"
    "v_mfma_f32_16x16x32_bf16 v[0:3], v[128:131], v[72:75], 0\n"
    "s_add_u32 s35, s40, 1\n"
    "s_and_b32 s35, s35, 3\n"
    "s_lshl_b32 s35, s35, 12\n"
    "s_lshl_b32 s21, s10, 14\n"
    "s_add_u32 s35, s35, s21\n"
    "s_mul_i32 s21, s17, 131072\n"
    "s_add_u32 s21, s21, s35\n"
    "s_mul_i32 s96, s18, 131072\n"
    "s_add_u32 s35, s35, s96\n"
    "buffer_load_dwordx4 a[32:35], v198, s[68:71], s21 offen offset:0 sc1\n"
    "v_mfma_f32_16x16x32_bf16 v[8:11], v[128:131], v[104:107], 0\n"
    "ds_read_b64_tr_b16 v[160:161], v240 offset:8192\n"
    "ds_read_b64_tr_b16 v[162:163], v240 offset:12288\n"
    "v_mfma_f32_16x16x32_bf16 v[4:7], v[132:135], v[72:75], 0\n"
    "ds_read_b64_tr_b16 v[164:165], v241 offset:8192\n"
    "s_lshl_b32 s14, s33, 3\n"
    "s_lshl_b32 s15, s8, 1\n"
    "s_add_u32 s14, s14, s15\n"
    "s_lshl_b32 s14, s14, 14\n"
    "s_lshl_b32 s15, s34, 10\n"
    "s_add_u32 s15, s15, s14\n"
    "s_add_u32 s72, s58, s15\n"
    "s_addc_u32 s73, s59, 0\n"
    "s_add_u32 s72, s72, 0x3000000\n"
    "s_addc_u32 s73, s73, 0\n"
    "s_and_b32 s73, s73, 0xffff\n"
    "s_mov_b32 s74, 0x8000\n"
    "s_mov_b32 s75, 0x20000\n"
    "v_add_u32 v201, 8192, v201\n"
    "v_accvgpr_read_b32 v80, a16\n"
    "v_mfma_f32_16x16x32_bf16 v[12:15], v[132:135], v[104:107], 0\n"
    "s_waitcnt lgkmcnt(10)\n"
    "ds_read_b64_tr_b16 v[166:167], v241 offset:12288\n"
    "v_accvgpr_read_b32 v81, a20\n"
    "v_mfma_f32_16x16x32_bf16 v[16:19], v[136:139], v[72:75], 0\n"
    "ds_read_b64_tr_b16 v[168:169], v242 offset:8192\n"
    "ds_read_b64_tr_b16 v[170:171], v242 offset:12288\n"
    "v_mfma_f32_16x16x32_bf16 v[24:27], v[136:139], v[104:107], 0\n"
    "buffer_load_dwordx4 a[80:83], v198, s[68:71], s35 offen offset:0 sc1\n"
    "v_mfma_f32_16x16x32_bf16 v[20:23], v[140:143], v[72:75], 0\n"
    "ds_read_b64_tr_b16 v[172:173], v243 offset:8192\n"
    "v_accvgpr_read_b32 v82, a24\n"
    "v_mfma_f32_16x16x32_bf16 v[28:31], v[140:143], v[104:107], 0\n"
    "s_waitcnt lgkmcnt(10)\n"
    "ds_read_b64_tr_b16 v[174:175], v243 offset:12288\n"
    "v_accvgpr_read_b32 v83, a28\n"
    "v_mfma_f32_16x16x32_bf16 v[32:35], v[144:147], v[72:75], 0\n"
    "ds_read_b64_tr_b16 v[176:177], v244 offset:8192\n"
    "ds_read_b64_tr_b16 v[178:179], v244 offset:12288\n"
    "v_mfma_f32_16x16x32_bf16 v[40:43], v[144:147], v[104:107], 0\n"
    "ds_read_b64_tr_b16 v[180:181], v245 offset:8192\n"
    "v_add_f32 v80, 0x358637bd, v80\n"
    "v_add_f32 v81, 0x358637bd, v81\n"
    "v_mfma_f32_16x16x32_bf16 v[36:39], v[148:151], v[72:75], 0\n"
    "ds_read_b64_tr_b16 v[182:183], v245 offset:12288\n"
    "v_add_f32 v82, 0x358637bd, v82\n"
    "v_mfma_f32_16x16x32_bf16 v[44:47], v[148:151], v[104:107], 0\n"
    "s_waitcnt lgkmcnt(11)\n"
    "buffer_load_dwordx4 a[36:39], v198, s[68:71], s21 offen offset:1024 sc1\n"
    "v_mfma_f32_16x16x32_bf16 v[48:51], v[152:155], v[72:75], 0\n"
    "ds_read_b64_tr_b16 v[184:185], v246 offset:8192\n"
    "ds_read_b64_tr_b16 v[186:187], v246 offset:12288\n"
    "v_mfma_f32_16x16x32_bf16 v[56:59], v[152:155], v[104:107], 0\n"
    "ds_read_b64_tr_b16 v[188:189], v247 offset:8192\n"
    "v_add_f32 v83, 0x358637bd, v83\n"
    "v_mfma_f32_16x16x32_bf16 v[52:55], v[156:159], v[72:75], 0\n"
    "ds_read_b64_tr_b16 v[190:191], v247 offset:12288\n"
    "v_rcp_f32 v80, v80\n"
    "v_mfma_f32_16x16x32_bf16 v[60:63], v[156:159], v[104:107], 0\n"
    "s_waitcnt lgkmcnt(11)\n"
    "v_rcp_f32 v81, v81\n"
    "v_mfma_f32_16x16x32_bf16 v[0:3], v[160:163], v[76:79], v[0:3]\n"
    "ds_read_b64_tr_b16 v[128:129], v240 offset:16384\n"
    "ds_read_b64_tr_b16 v[130:131], v240 offset:20480\n"
    "v_mfma_f32_16x16x32_bf16 v[8:11], v[160:163], v[108:111], v[8:11]\n"
    "buffer_load_dwordx4 a[84:87], v198, s[68:71], s35 offen offset:1024 sc1\n"
    "v_mfma_f32_16x16x32_bf16 v[4:7], v[164:167], v[76:79], v[4:7]\n"
    "ds_read_b64_tr_b16 v[132:133], v241 offset:16384\n"
    "v_rcp_f32 v82, v82\n"
    "v_mfma_f32_16x16x32_bf16 v[12:15], v[164:167], v[108:111], v[12:15]\n"
    "s_waitcnt lgkmcnt(10)\n"
    "ds_read_b64_tr_b16 v[134:135], v241 offset:20480\n"
    "v_mfma_f32_16x16x32_bf16 v[16:19], v[168:171], v[76:79], v[16:19]\n"
    "ds_read_b64_tr_b16 v[136:137], v242 offset:16384\n"
    "ds_read_b64_tr_b16 v[138:139], v242 offset:20480\n"
    "v_mfma_f32_16x16x32_bf16 v[24:27], v[168:171], v[108:111], v[24:27]\n"
    "ds_read_b64_tr_b16 v[140:141], v243 offset:16384\n"
    "v_rcp_f32 v83, v83\n"
    "v_mfma_f32_16x16x32_bf16 v[20:23], v[172:175], v[76:79], v[20:23]\n"
    "ds_read_b64_tr_b16 v[142:143], v243 offset:20480\n"
    "v_mul_f32 v96, v96, v80\n"
    "v_mfma_f32_16x16x32_bf16 v[28:31], v[172:175], v[108:111], v[28:31]\n"
    "s_waitcnt lgkmcnt(11)\n"
    "buffer_load_dwordx4 a[40:43], v198, s[68:71], s21 offen offset:2048 sc1\n"
    "v_mfma_f32_16x16x32_bf16 v[32:35], v[176:179], v[76:79], v[32:35]\n"
    "ds_read_b64_tr_b16 v[144:145], v244 offset:16384\n"
    "ds_read_b64_tr_b16 v[146:147], v244 offset:20480\n"
    "v_mfma_f32_16x16x32_bf16 v[40:43], v[176:179], v[108:111], v[40:43]\n"
    "ds_read_b64_tr_b16 v[148:149], v245 offset:16384\n"
    "v_mul_f32 v97, v97, v80\n"
    "v_mfma_f32_16x16x32_bf16 v[36:39], v[180:183], v[76:79], v[36:39]\n"
    "ds_read_b64_tr_b16 v[150:151], v245 offset:20480\n"
    "v_mul_f32 v98, v98, v80\n"
    "v_mfma_f32_16x16x32_bf16 v[44:47], v[180:183], v[108:111], v[44:47]\n"
    "s_waitcnt lgkmcnt(11)\n"
    "ds_read_b64_tr_b16 v[152:153], v246 offset:16384\n"
    "ds_read_b64_tr_b16 v[154:155], v246 offset:20480\n"
    "v_mfma_f32_16x16x32_bf16 v[48:51], v[184:187], v[76:79], v[48:51]\n"
    "ds_read_b64_tr_b16 v[156:157], v247 offset:16384\n"
    "v_mul_f32 v99, v99, v80\n"
    "v_mfma_f32_16x16x32_bf16 v[56:59], v[184:187], v[108:111], v[56:59]\n"
    "buffer_load_dwordx4 a[88:91], v198, s[68:71], s35 offen offset:2048 sc1\n"
    "v_mfma_f32_16x16x32_bf16 v[52:55], v[188:191], v[76:79], v[52:55]\n"
    "ds_read_b64_tr_b16 v[158:159], v247 offset:20480\n"
    "v_mul_f32 v100, v100, v81\n"
    "v_mfma_f32_16x16x32_bf16 v[60:63], v[188:191], v[108:111], v[60:63]\n"
    "s_waitcnt lgkmcnt(11)\n"
    "v_mul_f32 v101, v101, v81\n"
    "v_mul_f32 v102, v102, v81\n"
    "v_mul_f32 v103, v103, v81\n"
    "v_mfma_f32_16x16x32_bf16 v[0:3], v[128:131], v[88:91], v[0:3]\n"
    "ds_read_b64_tr_b16 v[160:161], v240 offset:24576\n"
    "ds_read_b64_tr_b16 v[162:163], v240 offset:28672\n"
    "v_mfma_f32_16x16x32_bf16 v[8:11], v[128:131], v[120:123], v[8:11]\n"
    "ds_read_b64_tr_b16 v[164:165], v241 offset:24576\n"
    "v_mul_f32 v112, v112, v82\n"
    "v_mfma_f32_16x16x32_bf16 v[4:7], v[132:135], v[88:91], v[4:7]\n"
    "ds_read_b64_tr_b16 v[166:167], v241 offset:28672\n"
    "v_mul_f32 v113, v113, v82\n"
    "v_mfma_f32_16x16x32_bf16 v[12:15], v[132:135], v[120:123], v[12:15]\n"
    "s_waitcnt lgkmcnt(11)\n"
    "buffer_load_dwordx4 a[44:47], v198, s[68:71], s21 offen offset:3072 sc1\n"
    "v_mfma_f32_16x16x32_bf16 v[16:19], v[136:139], v[88:91], v[16:19]\n"
    "ds_read_b64_tr_b16 v[168:169], v242 offset:24576\n"
    "ds_read_b64_tr_b16 v[170:171], v242 offset:28672\n"
    "v_mfma_f32_16x16x32_bf16 v[24:27], v[136:139], v[120:123], v[24:27]\n"
    "ds_read_b64_tr_b16 v[172:173], v243 offset:24576\n"
    "v_mul_f32 v114, v114, v82\n"
    "v_mfma_f32_16x16x32_bf16 v[20:23], v[140:143], v[88:91], v[20:23]\n"
    "ds_read_b64_tr_b16 v[174:175], v243 offset:28672\n"
    "v_mul_f32 v115, v115, v82\n"
    "v_mul_f32 v116, v116, v83\n"
    "v_mfma_f32_16x16x32_bf16 v[28:31], v[140:143], v[120:123], v[28:31]\n"
    "s_waitcnt lgkmcnt(11)\n"
    "ds_read_b64_tr_b16 v[176:177], v244 offset:24576\n"
    "ds_read_b64_tr_b16 v[178:179], v244 offset:28672\n"
    "v_mfma_f32_16x16x32_bf16 v[32:35], v[144:147], v[88:91], v[32:35]\n"
    "buffer_load_dwordx4 a[92:95], v198, s[68:71], s35 offen offset:3072 sc1\n"
    "v_mfma_f32_16x16x32_bf16 v[40:43], v[144:147], v[120:123], v[40:43]\n"
    "ds_read_b64_tr_b16 v[180:181], v245 offset:24576\n"
    "v_mul_f32 v117, v117, v83\n"
    "v_mfma_f32_16x16x32_bf16 v[36:39], v[148:151], v[88:91], v[36:39]\n"
    "ds_read_b64_tr_b16 v[182:183], v245 offset:28672\n"
    "v_mul_f32 v118, v118, v83\n"
    "v_mfma_f32_16x16x32_bf16 v[44:47], v[148:151], v[120:123], v[44:47]\n"
    "s_waitcnt lgkmcnt(11)\n"
    "ds_read_b64_tr_b16 v[184:185], v246 offset:24576\n"
    "ds_read_b64_tr_b16 v[186:187], v246 offset:28672\n"
    "v_mfma_f32_16x16x32_bf16 v[48:51], v[152:155], v[88:91], v[48:51]\n"
    "ds_read_b64_tr_b16 v[188:189], v247 offset:24576\n"
    "v_mul_f32 v119, v119, v83\n"
    "s_mov_b32 s16, 16384\n"
    "v_mfma_f32_16x16x32_bf16 v[56:59], v[152:155], v[120:123], v[56:59]\n"
    "ds_read_b64_tr_b16 v[190:191], v247 offset:28672\n"
    "s_add_u32 m0, s41, 0\n"
    "v_cvt_pk_bf16_f32 v64, v96, v97\n"
    "v_cvt_pk_bf16_f32 v65, v98, v99\n"
    "v_mfma_f32_16x16x32_bf16 v[52:55], v[156:159], v[88:91], v[52:55]\n"
    "buffer_load_dwordx4 v204, s[36:39], s52 offen lds\n"
    "v_mfma_f32_16x16x32_bf16 v[60:63], v[156:159], v[120:123], v[60:63]\n"
    "s_waitcnt lgkmcnt(11)\n"
    "v_cvt_pk_bf16_f32 v66, v112, v113\n"
    "v_cvt_pk_bf16_f32 v67, v114, v115\n"
    "v_mfma_f32_16x16x32_bf16 v[0:3], v[160:163], v[92:95], v[0:3]\n"
    "ds_read_b64_tr_b16 v[128:129], v240 offset:0\n"
    "ds_read_b64_tr_b16 v[130:131], v240 offset:4096\n"
    "v_mfma_f32_16x16x32_bf16 v[8:11], v[160:163], v[124:127], v[8:11]\n"
    "ds_read_b64_tr_b16 v[132:133], v241 offset:0\n"
    "buffer_store_dwordx4 v[64:67], v198, s[72:75], 0 offen sc1\n"
    "v_mfma_f32_16x16x32_bf16 v[4:7], v[164:167], v[92:95], v[4:7]\n"
    "ds_read_b64_tr_b16 v[134:135], v241 offset:4096\n"
    "v_mfma_f32_16x16x32_bf16 v[12:15], v[164:167], v[124:127], v[12:15]\n"
    "s_waitcnt lgkmcnt(11)\n"
    "ds_read_b64_tr_b16 v[136:137], v242 offset:0\n"
    "s_add_u32 m0, s41, 1024\n"
    "v_cvt_pk_bf16_f32 v68, v100, v101\n"
    "v_cvt_pk_bf16_f32 v69, v102, v103\n"
    "v_mfma_f32_16x16x32_bf16 v[16:19], v[168:171], v[92:95], v[16:19]\n"
    "buffer_load_dwordx4 v205, s[36:39], s52 offen lds\n"
    "v_mfma_f32_16x16x32_bf16 v[24:27], v[168:171], v[124:127], v[24:27]\n"
    "ds_read_b64_tr_b16 v[138:139], v242 offset:4096\n"
    "ds_read_b64_tr_b16 v[140:141], v243 offset:0\n"
    "v_mfma_f32_16x16x32_bf16 v[20:23], v[172:175], v[92:95], v[20:23]\n"
    "ds_read_b64_tr_b16 v[142:143], v243 offset:4096\n"
    "v_cvt_pk_bf16_f32 v70, v116, v117\n"
    "v_mfma_f32_16x16x32_bf16 v[28:31], v[172:175], v[124:127], v[28:31]\n"
    "s_waitcnt lgkmcnt(11)\n"
    "ds_read_b64_tr_b16 v[144:145], v244 offset:0\n"
    "v_cvt_pk_bf16_f32 v71, v118, v119\n"
    "v_mfma_f32_16x16x32_bf16 v[32:35], v[176:179], v[92:95], v[32:35]\n"
    "ds_read_b64_tr_b16 v[146:147], v244 offset:4096\n"
    "buffer_store_dwordx4 v[68:71], v198, s[72:75], s16 offen sc1\n"
    "v_mfma_f32_16x16x32_bf16 v[40:43], v[176:179], v[124:127], v[40:43]\n"
    "ds_read_b64_tr_b16 v[148:149], v245 offset:0\n"
    "s_add_u32 m0, s41, 2048\n"
    "ds_read_b64_tr_b16 v[150:151], v245 offset:4096\n"
    "v_mfma_f32_16x16x32_bf16 v[36:39], v[180:183], v[92:95], v[36:39]\n"
    "buffer_load_dwordx4 v206, s[36:39], s52 offen lds\n"
    "v_mfma_f32_16x16x32_bf16 v[44:47], v[180:183], v[124:127], v[44:47]\n"
    "s_waitcnt lgkmcnt(11)\n"
    "ds_read_b64_tr_b16 v[152:153], v246 offset:0\n"
    "v_mfma_f32_16x16x32_bf16 v[48:51], v[184:187], v[92:95], v[48:51]\n"
    "ds_read_b64_tr_b16 v[154:155], v246 offset:4096\n"
    "v_mfma_f32_16x16x32_bf16 v[56:59], v[184:187], v[124:127], v[56:59]\n"
    "ds_read_b64_tr_b16 v[156:157], v247 offset:0\n"
    "v_mfma_f32_16x16x32_bf16 v[52:55], v[188:191], v[92:95], v[52:55]\n"
    "ds_read_b64_tr_b16 v[158:159], v247 offset:4096\n"
    "v_mfma_f32_16x16x32_bf16 v[60:63], v[188:191], v[124:127], v[60:63]\n"
    "s_waitcnt lgkmcnt(11)\n"
    "s_add_u32 m0, s41, 3072\n"
    "v_mfma_f32_16x16x32_bf16 v[64:67], v[128:131], v[208:211], 0\n"
    "buffer_load_dwordx4 v207, s[36:39], s52 offen lds\n"
    "v_mfma_f32_16x16x32_bf16 v[72:75], v[128:131], v[224:227], 0\n"
    "ds_read_b64_tr_b16 v[160:161], v240 offset:8192\n"
    "s_add_u32 s52, s52, 65536\n"
    "s_and_b32 s52, s52, 0x7ffff\n"
    "s_add_u32 s41, s41, 16384\n"
    "s_and_b32 s41, s41, 0x1ffff\n"
    "ds_read_b64_tr_b16 v[162:163], v240 offset:12288\n"
    "v_mfma_f32_16x16x32_bf16 v[68:71], v[132:135], v[208:211], 0\n"
    "ds_read_b64_tr_b16 v[164:165], v241 offset:8192\n"
    "v_mfma_f32_16x16x32_bf16 v[76:79], v[132:135], v[224:227], 0\n"
    "s_waitcnt lgkmcnt(10)\n"
    "ds_read_b64_tr_b16 v[166:167], v241 offset:12288\n"
    "ds_read_b64_tr_b16 v[168:169], v242 offset:8192\n"
    "v_mfma_f32_16x16x32_bf16 v[80:83], v[136:139], v[208:211], 0\n"
    "ds_read_b64_tr_b16 v[170:171], v242 offset:12288\n"
    "v_mfma_f32_16x16x32_bf16 v[88:91], v[136:139], v[224:227], 0\n"
    "ds_read_b64_tr_b16 v[172:173], v243 offset:8192\n"
    "ds_read_b64_tr_b16 v[174:175], v243 offset:12288\n"
    "v_mfma_f32_16x16x32_bf16 v[84:87], v[140:143], v[208:211], 0\n"
    "s_waitcnt vmcnt(14)\n"
    "s_lshl_b32 s15, s13, 10\n"
    "s_lshl_b32 s98, s12, 5\n"
    "s_add_u32 s15, s15, s98\n"
    "s_lshl_b32 s98, s8, 3\n"
    "s_add_u32 s15, s15, s98\n"
    "s_add_u32 s15, s15, 0x3408000\n"
    "v_accvgpr_write_b32 a252, s28\n"
    "v_accvgpr_write_b32 a253, s29\n"
    "v_mov_b32 v251, s15\n"
    "s_mov_b64 exec, 1\n"
    "global_store_dwordx2 v251, a[252:253], s[58:59] sc1\n"
    "s_mov_b64 exec, -1\n"
    "v_mfma_f32_16x16x32_bf16 v[92:95], v[140:143], v[224:227], 0\n"
    "s_waitcnt lgkmcnt(11)\n"
    "s_add_u32 s35, s40, 2\n"
    "s_and_b32 s35, s35, 3\n"
    "s_lshl_b32 s35, s35, 12\n"
    "s_lshl_b32 s21, s10, 14\n"
    "s_add_u32 s35, s35, s21\n"
    "s_mul_i32 s21, s17, 131072\n"
    "s_add_u32 s21, s21, s35\n"
    "s_mul_i32 s96, s18, 131072\n"
    "s_add_u32 s35, s35, s96\n"
    "buffer_load_dwordx4 a[48:51], v198, s[68:71], s21 offen offset:0 sc1\n"
    "v_mfma_f32_16x16x32_bf16 v[96:99], v[144:147], v[208:211], 0\n"
    "ds_read_b64_tr_b16 v[176:177], v244 offset:8192\n"
    "v_mfma_f32_16x16x32_bf16 v[104:107], v[144:147], v[224:227], 0\n"
    "ds_read_b64_tr_b16 v[178:179], v244 offset:12288\n"
    "ds_read_b64_tr_b16 v[180:181], v245 offset:8192\n"
    "v_mfma_f32_16x16x32_bf16 v[100:103], v[148:151], v[208:211], 0\n"
    "ds_read_b64_tr_b16 v[182:183], v245 offset:12288\n"
    "v_mfma_f32_16x16x32_bf16 v[108:111], v[148:151], v[224:227], 0\n"
    "s_waitcnt lgkmcnt(11)\n"
    "ds_read_b64_tr_b16 v[184:185], v246 offset:8192\n"
    "ds_read_b64_tr_b16 v[186:187], v246 offset:12288\n"
    "v_mfma_f32_16x16x32_bf16 v[112:115], v[152:155], v[208:211], 0\n"
    "ds_read_b64_tr_b16 v[188:189], v247 offset:8192\n"
    "v_mfma_f32_16x16x32_bf16 v[120:123], v[152:155], v[224:227], 0\n"
    "buffer_load_dwordx4 a[96:99], v198, s[68:71], s35 offen offset:0 sc1\n"
    "v_mfma_f32_16x16x32_bf16 v[116:119], v[156:159], v[208:211], 0\n"
    "ds_read_b64_tr_b16 v[190:191], v247 offset:12288\n"
    "v_mfma_f32_16x16x32_bf16 v[124:127], v[156:159], v[224:227], 0\n"
    "s_waitcnt lgkmcnt(11)\n"
    "v_mfma_f32_16x16x32_bf16 v[64:67], v[160:163], v[212:215], v[64:67]\n"
    "ds_read_b64_tr_b16 v[128:129], v240 offset:16384\n"
    "ds_read_b64_tr_b16 v[130:131], v240 offset:20480\n"
    "v_mfma_f32_16x16x32_bf16 v[72:75], v[160:163], v[228:231], v[72:75]\n"
    "ds_read_b64_tr_b16 v[132:133], v241 offset:16384\n"
    "v_mfma_f32_16x16x32_bf16 v[68:71], v[164:167], v[212:215], v[68:71]\n"
    "ds_read_b64_tr_b16 v[134:135], v241 offset:20480\n"
    "v_mfma_f32_16x16x32_bf16 v[76:79], v[164:167], v[228:231], v[76:79]\n"
    "s_waitcnt lgkmcnt(11)\n"
    "ds_read_b64_tr_b16 v[136:137], v242 offset:16384\n"
    "v_mfma_f32_16x16x32_bf16 v[80:83], v[168:171], v[212:215], v[80:83]\n"
    "buffer_load_dwordx4 a[52:55], v198, s[68:71], s21 offen offset:1024 sc1\n"
    "v_mfma_f32_16x16x32_bf16 v[88:91], v[168:171], v[228:231], v[88:91]\n"
    "ds_read_b64_tr_b16 v[138:139], v242 offset:20480\n"
    "ds_read_b64_tr_b16 v[140:141], v243 offset:16384\n"
    "v_mfma_f32_16x16x32_bf16 v[84:87], v[172:175], v[212:215], v[84:87]\n"
    "ds_read_b64_tr_b16 v[142:143], v243 offset:20480\n"
    "v_mfma_f32_16x16x32_bf16 v[92:95], v[172:175], v[228:231], v[92:95]\n"
    "s_waitcnt lgkmcnt(11)\n"
    "ds_read_b64_tr_b16 v[144:145], v244 offset:16384\n"
    "v_mfma_f32_16x16x32_bf16 v[96:99], v[176:179], v[212:215], v[96:99]\n"
    "ds_read_b64_tr_b16 v[146:147], v244 offset:20480\n"
    "v_mfma_f32_16x16x32_bf16 v[104:107], v[176:179], v[228:231], v[104:107]\n"
    "ds_read_b64_tr_b16 v[148:149], v245 offset:16384\n"
    "ds_read_b64_tr_b16 v[150:151], v245 offset:20480\n"
    "v_mfma_f32_16x16x32_bf16 v[100:103], v[180:183], v[212:215], v[100:103]\n"
    "buffer_load_dwordx4 a[100:103], v198, s[68:71], s35 offen offset:1024 sc1\n"
    "v_mfma_f32_16x16x32_bf16 v[108:111], v[180:183], v[228:231], v[108:111]\n"
    "s_waitcnt lgkmcnt(11)\n"
    "ds_read_b64_tr_b16 v[152:153], v246 offset:16384\n"
    "v_mfma_f32_16x16x32_bf16 v[112:115], v[184:187], v[212:215], v[112:115]\n"
    "ds_read_b64_tr_b16 v[154:155], v246 offset:20480\n"
    "v_mfma_f32_16x16x32_bf16 v[120:123], v[184:187], v[228:231], v[120:123]\n"
    "ds_read_b64_tr_b16 v[156:157], v247 offset:16384\n"
    "v_mfma_f32_16x16x32_bf16 v[116:119], v[188:191], v[212:215], v[116:119]\n"
    "ds_read_b64_tr_b16 v[158:159], v247 offset:20480\n"
    "v_mfma_f32_16x16x32_bf16 v[124:127], v[188:191], v[228:231], v[124:127]\n"
    "s_waitcnt lgkmcnt(11)\n"
    "v_mfma_f32_16x16x32_bf16 v[64:67], v[128:131], v[216:219], v[64:67]\n"
    "ds_read_b64_tr_b16 v[160:161], v240 offset:24576\n"
    "ds_read_b64_tr_b16 v[162:163], v240 offset:28672\n"
    "v_mfma_f32_16x16x32_bf16 v[72:75], v[128:131], v[232:235], v[72:75]\n"
    "buffer_load_dwordx4 a[56:59], v198, s[68:71], s21 offen offset:2048 sc1\n"
    "v_mfma_f32_16x16x32_bf16 v[68:71], v[132:135], v[216:219], v[68:71]\n"
    "ds_read_b64_tr_b16 v[164:165], v241 offset:24576\n"
    "v_mfma_f32_16x16x32_bf16 v[76:79], v[132:135], v[232:235], v[76:79]\n"
    "s_waitcnt lgkmcnt(10)\n"
    "ds_read_b64_tr_b16 v[166:167], v241 offset:28672\n"
    "v_mfma_f32_16x16x32_bf16 v[80:83], v[136:139], v[216:219], v[80:83]\n"
    "ds_read_b64_tr_b16 v[168:169], v242 offset:24576\n"
    "ds_read_b64_tr_b16 v[170:171], v242 offset:28672\n"
    "v_mfma_f32_16x16x32_bf16 v[88:91], v[136:139], v[232:235], v[88:91]\n"
    "ds_read_b64_tr_b16 v[172:173], v243 offset:24576\n"
    "v_mfma_f32_16x16x32_bf16 v[84:87], v[140:143], v[216:219], v[84:87]\n"
    "ds_read_b64_tr_b16 v[174:175], v243 offset:28672\n"
    "v_mfma_f32_16x16x32_bf16 v[92:95], v[140:143], v[232:235], v[92:95]\n"
    "s_waitcnt lgkmcnt(11)\n"
    "buffer_load_dwordx4 a[104:107], v198, s[68:71], s35 offen offset:2048 sc1\n"
    "v_mfma_f32_16x16x32_bf16 v[96:99], v[144:147], v[216:219], v[96:99]\n"
    "ds_read_b64_tr_b16 v[176:177], v244 offset:24576\n"
    "ds_read_b64_tr_b16 v[178:179], v244 offset:28672\n"
    "v_mfma_f32_16x16x32_bf16 v[104:107], v[144:147], v[232:235], v[104:107]\n"
    "ds_read_b64_tr_b16 v[180:181], v245 offset:24576\n"
    "v_mfma_f32_16x16x32_bf16 v[100:103], v[148:151], v[216:219], v[100:103]\n"
    "ds_read_b64_tr_b16 v[182:183], v245 offset:28672\n"
    "v_mfma_f32_16x16x32_bf16 v[108:111], v[148:151], v[232:235], v[108:111]\n"
    "s_waitcnt lgkmcnt(11)\n"
    "ds_read_b64_tr_b16 v[184:185], v246 offset:24576\n"
    "ds_read_b64_tr_b16 v[186:187], v246 offset:28672\n"
    "v_mfma_f32_16x16x32_bf16 v[112:115], v[152:155], v[216:219], v[112:115]\n"
    "ds_read_b64_tr_b16 v[188:189], v247 offset:24576\n"
    "v_mfma_f32_16x16x32_bf16 v[120:123], v[152:155], v[232:235], v[120:123]\n"
    "ds_read_b64_tr_b16 v[190:191], v247 offset:28672\n"
    "v_mfma_f32_16x16x32_bf16 v[116:119], v[156:159], v[216:219], v[116:119]\n"
    "buffer_load_dwordx4 a[60:63], v198, s[68:71], s21 offen offset:3072 sc1\n"
    "v_mfma_f32_16x16x32_bf16 v[124:127], v[156:159], v[232:235], v[124:127]\n"
    "s_waitcnt lgkmcnt(11)\n"
    "v_mfma_f32_16x16x32_bf16 v[64:67], v[160:163], v[220:223], v[64:67]\n"
    "s_add_u32 s19, s53, 32768\n"
    "s_and_b32 s19, s19, 0x1ffff\n"
    "v_add_u32 v240, s19, v192\n"
    "v_add_u32 v241, s19, v193\n"
    "v_mfma_f32_16x16x32_bf16 v[72:75], v[160:163], v[236:239], v[72:75]\n"
    "v_add_u32 v242, s19, v194\n"
    "v_add_u32 v243, s19, v195\n"
    "v_mfma_f32_16x16x32_bf16 v[68:71], v[164:167], v[220:223], v[68:71]\n"
    "v_add_u32 v244, s19, v196\n"
    "v_add_u32 v245, s19, v197\n"
    "v_mfma_f32_16x16x32_bf16 v[76:79], v[164:167], v[236:239], v[76:79]\n"
    "s_waitcnt lgkmcnt(7)\n"
    "v_add_u32 v246, s19, v199\n"
    "v_add_u32 v247, s19, v200\n"
    "v_mfma_f32_16x16x32_bf16 v[80:83], v[168:171], v[220:223], v[80:83]\n"
    "buffer_load_dwordx4 a[108:111], v198, s[68:71], s35 offen offset:3072 sc1\n"
    "s_add_u32 s35, s40, 3\n"
    "s_and_b32 s35, s35, 3\n"
    "s_lshl_b32 s35, s35, 12\n"
    "s_lshl_b32 s21, s10, 14\n"
    "s_add_u32 s35, s35, s21\n"
    "s_mul_i32 s21, s17, 131072\n"
    "s_add_u32 s21, s21, s35\n"
    "s_mul_i32 s96, s18, 131072\n"
    "s_add_u32 s35, s35, s96\n"
    "v_mfma_f32_16x16x32_bf16 v[88:91], v[168:171], v[236:239], v[88:91]\n"
    "ds_read_b64_tr_b16 v[128:129], v240 offset:0\n"
    "ds_read_b64_tr_b16 v[130:131], v240 offset:4096\n"
    "v_mfma_f32_16x16x32_bf16 v[84:87], v[172:175], v[220:223], v[84:87]\n"
    "ds_read_b64_tr_b16 v[132:133], v241 offset:0\n"
    "ds_read_b64_tr_b16 v[134:135], v241 offset:4096\n"
    "v_mfma_f32_16x16x32_bf16 v[92:95], v[172:175], v[236:239], v[92:95]\n"
    "s_waitcnt lgkmcnt(7)\n"
    "ds_read_b64_tr_b16 v[136:137], v242 offset:0\n"
    "v_mfma_f32_16x16x32_bf16 v[96:99], v[176:179], v[220:223], v[96:99]\n"
    "ds_read_b64_tr_b16 v[138:139], v242 offset:4096\n"
    "ds_read_b64_tr_b16 v[140:141], v243 offset:0\n"
    "v_mfma_f32_16x16x32_bf16 v[104:107], v[176:179], v[236:239], v[104:107]\n"
    "ds_read_b64_tr_b16 v[142:143], v243 offset:4096\n"
    "ds_read_b64_tr_b16 v[144:145], v244 offset:0\n"
    "v_mfma_f32_16x16x32_bf16 v[100:103], v[180:183], v[220:223], v[100:103]\n"
    "ds_read_b64_tr_b16 v[146:147], v244 offset:4096\n"
    "ds_read_b64_tr_b16 v[148:149], v245 offset:0\n"
    "v_mfma_f32_16x16x32_bf16 v[108:111], v[180:183], v[236:239], v[108:111]\n"
    "s_waitcnt lgkmcnt(10)\n"
    "buffer_load_dwordx4 a[64:67], v198, s[68:71], s21 offen offset:0 sc1\n"
    "v_mfma_f32_16x16x32_bf16 v[112:115], v[184:187], v[220:223], v[112:115]\n"
    "ds_read_b64_tr_b16 v[150:151], v245 offset:4096\n"
    "ds_read_b64_tr_b16 v[152:153], v246 offset:0\n"
    "v_mfma_f32_16x16x32_bf16 v[120:123], v[184:187], v[236:239], v[120:123]\n"
    "ds_read_b64_tr_b16 v[154:155], v246 offset:4096\n"
    "ds_read_b64_tr_b16 v[156:157], v247 offset:0\n"
    "v_mfma_f32_16x16x32_bf16 v[116:119], v[188:191], v[220:223], v[116:119]\n"
    "ds_read_b64_tr_b16 v[158:159], v247 offset:4096\n"
    "v_mfma_f32_16x16x32_bf16 v[124:127], v[188:191], v[236:239], v[124:127]\n"
    "s_waitcnt vmcnt(15)\n"
    "s_waitcnt lgkmcnt(11)\n"
    "v_mfma_f32_16x16x32_bf16 v[64:67], v[128:131], a[32:35], v[64:67]\n"
    "ds_read_b64_tr_b16 v[160:161], v240 offset:8192\n"
    "ds_read_b64_tr_b16 v[162:163], v240 offset:12288\n"
    "s_lshl_b32 s14, s13, 2\n"
    "s_add_u32 s14, s14, s40\n"
    "s_mul_i32 s15, s14, 548864\n"
    "s_add_u32 s60, s58, s15\n"
    "s_addc_u32 s61, s59, 0\n"
    "s_add_u32 s60, s60, 0x4000000\n"
    "s_addc_u32 s61, s61, 0\n"
    "s_and_b32 s61, s61, 0xffff\n"
    "s_mov_b32 s62, 0x86000\n"
    "s_mov_b32 s63, 0x20000\n"
    "s_mul_i32 s22, s8, 68608\n"
    "s_mul_i32 s14, s10, 8192\n"
    "s_add_u32 s22, s22, s14\n"
    "s_lshl_b32 s23, s10, 2\n"
    "s_and_b32 s14, s11, 3\n"
    "s_add_u32 s23, s23, s14\n"
    "s_lshr_b32 s24, s11, 2\n"
    "s_lshl_b32 s15, s23, 16\n"
    "s_add_u32 s72, s58, s15\n"
    "s_addc_u32 s73, s59, 0\n"
    "s_add_u32 s72, s72, 0x3000000\n"
    "s_addc_u32 s73, s73, 0\n"
    "s_and_b32 s73, s73, 0xffff\n"
    "s_mov_b32 s74, 0x10000\n"
    "s_mov_b32 s75, 0x20000\n"
    "s_lshl_b32 s15, s24, 7\n"
    "s_add_u32 s76, s42, s15\n"
    "s_addc_u32 s77, s43, 0\n"
    "s_and_b32 s77, s77, 0xffff\n"
    "s_mov_b32 s78, 0x80000\n"
    "s_mov_b32 s79, 0x20000\n"
    "s_sub_u32 s78, s78, s15\n"
    "s_lshl_b32 s25, s8, 12\n"
    "s_lshl_b32 s26, s8, 17\n"
    "s_lshr_b32 s14, s23, 1\n"
    "s_lshl_b32 s14, s14, 7\n"
    "s_add_u32 s14, s14, 12288\n"
    "s_add_u32 s14, s14, 0x3400000\n"
    "v_mfma_f32_16x16x32_bf16 v[72:75], v[128:131], a[80:83], v[72:75]\n"
    "ds_read_b64_tr_b16 v[164:165], v241 offset:8192\n"
    "v_min_u32 v250, 15, v255\n"
    "v_mfma_f32_16x16x32_bf16 v[68:71], v[132:135], a[32:35], v[68:71]\n"
    "buffer_load_dwordx4 a[112:115], v198, s[68:71], s35 offen offset:0 sc1\n"
    "v_mfma_f32_16x16x32_bf16 v[76:79], v[132:135], a[80:83], v[76:79]\n"
    "s_waitcnt lgkmcnt(10)\n"
    "ds_read_b64_tr_b16 v[166:167], v241 offset:12288\n"
    "v_lshlrev_b32 v250, 3, v250\n"
    "v_mfma_f32_16x16x32_bf16 v[80:83], v[136:139], a[32:35], v[80:83]\n"
    "ds_read_b64_tr_b16 v[168:169], v242 offset:8192\n"
    "v_add_u32 v250, s14, v250\n"
    "v_mfma_f32_16x16x32_bf16 v[88:91], v[136:139], a[80:83], v[88:91]\n"
    "ds_read_b64_tr_b16 v[170:171], v242 offset:12288\n"
    "ds_read_b64_tr_b16 v[172:173], v243 offset:8192\n"
    "v_mfma_f32_16x16x32_bf16 v[84:87], v[140:143], a[32:35], v[84:87]\n"
    "ds_read_b64_tr_b16 v[174:175], v243 offset:12288\n"
    "v_add_u32 v208, 0, v198\n"
    "v_mfma_f32_16x16x32_bf16 v[92:95], v[140:143], a[80:83], v[92:95]\n"
    "s_waitcnt lgkmcnt(11)\n"
    "ds_read_b64_tr_b16 v[176:177], v244 offset:8192\n"
    "v_mfma_f32_16x16x32_bf16 v[96:99], v[144:147], a[32:35], v[96:99]\n"
    "buffer_load_dwordx4 a[68:71], v198, s[68:71], s21 offen offset:1024 sc1\n"
    "v_mfma_f32_16x16x32_bf16 v[104:107], v[144:147], a[80:83], v[104:107]\n"
    "ds_read_b64_tr_b16 v[178:179], v244 offset:12288\n"
    "v_add_u32 v209, 16384, v198\n"
    "v_mfma_f32_16x16x32_bf16 v[100:103], v[148:151], a[32:35], v[100:103]\n"
    "ds_read_b64_tr_b16 v[180:181], v245 offset:8192\n"
    "ds_read_b64_tr_b16 v[182:183], v245 offset:12288\n"
    "v_mfma_f32_16x16x32_bf16 v[108:111], v[148:151], a[80:83], v[108:111]\n"
    "s_waitcnt lgkmcnt(11)\n"
    "ds_read_b64_tr_b16 v[184:185], v246 offset:8192\n"
    "v_add_u32 v210, 32768, v198\n"
    "v_mfma_f32_16x16x32_bf16 v[112:115], v[152:155], a[32:35], v[112:115]\n"
    "ds_read_b64_tr_b16 v[186:187], v246 offset:12288\n"
    "v_add_u32 v211, 49152, v198\n"
    "v_mfma_f32_16x16x32_bf16 v[120:123], v[152:155], a[80:83], v[120:123]\n"
    "ds_read_b64_tr_b16 v[188:189], v247 offset:8192\n"
    "v_bfe_u32 v204, v255, 4, 2\n"
    "v_mfma_f32_16x16x32_bf16 v[116:119], v[156:159], a[32:35], v[116:119]\n"
    "ds_read_b64_tr_b16 v[190:191], v247 offset:12288\n"
    "v_mfma_f32_16x16x32_bf16 v[124:127], v[156:159], a[80:83], v[124:127]\n"
    "s_waitcnt lgkmcnt(11)\n"
    "buffer_load_dwordx4 a[116:119], v198, s[68:71], s35 offen offset:1024 sc1\n"
    "v_mfma_f32_16x16x32_bf16 v[64:67], v[160:163], a[36:39], v[64:67]\n"
    "ds_read_b64_tr_b16 v[128:129], v240 offset:16384\n"
    "ds_read_b64_tr_b16 v[130:131], v240 offset:20480\n"
    "v_mfma_f32_16x16x32_bf16 v[72:75], v[160:163], a[84:87], v[72:75]\n"
    "ds_read_b64_tr_b16 v[132:133], v241 offset:16384\n"
    "v_lshlrev_b32 v204, 1, v204\n"
    "v_mfma_f32_16x16x32_bf16 v[68:71], v[164:167], a[36:39], v[68:71]\n"
    "ds_read_b64_tr_b16 v[134:135], v241 offset:20480\n"
    "v_and_b32 v205, 7, v255\n"
    "v_mfma_f32_16x16x32_bf16 v[76:79], v[164:167], a[84:87], v[76:79]\n"
    "s_waitcnt lgkmcnt(11)\n"
    "ds_read_b64_tr_b16 v[136:137], v242 offset:16384\n"
    "v_xor_b32 v204, v204, v205\n"
    "v_mfma_f32_16x16x32_bf16 v[80:83], v[168:171], a[36:39], v[80:83]\n"
    "ds_read_b64_tr_b16 v[138:139], v242 offset:20480\n"
    "ds_read_b64_tr_b16 v[140:141], v243 offset:16384\n"
    "v_mfma_f32_16x16x32_bf16 v[88:91], v[168:171], a[84:87], v[88:91]\n"
    "buffer_load_dwordx4 a[72:75], v198, s[68:71], s21 offen offset:2048 sc1\n"
    "v_mfma_f32_16x16x32_bf16 v[84:87], v[172:175], a[36:39], v[84:87]\n"
    "ds_read_b64_tr_b16 v[142:143], v243 offset:20480\n"
    "v_lshrrev_b32 v205, 3, v255\n"
    "v_mfma_f32_16x16x32_bf16 v[92:95], v[172:175], a[84:87], v[92:95]\n"
    "s_waitcnt lgkmcnt(11)\n"
    "ds_read_b64_tr_b16 v[144:145], v244 offset:16384\n"
    "v_mfma_f32_16x16x32_bf16 v[96:99], v[176:179], a[36:39], v[96:99]\n"
    "ds_read_b64_tr_b16 v[146:147], v244 offset:20480\n"
    "v_lshlrev_b32 v205, 10, v205\n"
    "v_mfma_f32_16x16x32_bf16 v[104:107], v[176:179], a[84:87], v[104:107]\n"
    "ds_read_b64_tr_b16 v[148:149], v245 offset:16384\n"
    "ds_read_b64_tr_b16 v[150:151], v245 offset:20480\n"
    "v_mfma_f32_16x16x32_bf16 v[100:103], v[180:183], a[36:39], v[100:103]\n"
    "ds_read_b64_tr_b16 v[152:153], v246 offset:16384\n"
    "v_lshl_add_u32 v212, v204, 4, v205\n"
    "v_mfma_f32_16x16x32_bf16 v[108:111], v[180:183], a[84:87], v[108:111]\n"
    "s_waitcnt lgkmcnt(12)\n"
    "ds_read_b64_tr_b16 v[154:155], v246 offset:20480\n"
    "v_add_u32 v213, 8192, v212\n"
    "v_mfma_f32_16x16x32_bf16 v[112:115], v[184:187], a[36:39], v[112:115]\n"
    "buffer_load_dwordx4 a[120:123], v198, s[68:71], s35 offen offset:2048 sc1\n"
    "v_mfma_f32_16x16x32_bf16 v[120:123], v[184:187], a[84:87], v[120:123]\n"
    "ds_read_b64_tr_b16 v[156:157], v247 offset:16384\n"
    "v_add_u32 v214, 16384, v212\n"
    "v_mfma_f32_16x16x32_bf16 v[116:119], v[188:191], a[36:39], v[116:119]\n"
    "ds_read_b64_tr_b16 v[158:159], v247 offset:20480\n"
    "v_mfma_f32_16x16x32_bf16 v[124:127], v[188:191], a[84:87], v[124:127]\n"
    "s_waitcnt lgkmcnt(11)\n"
    "v_add_u32 v215, 24576, v212\n"
    "v_bfe_u32 v204, v255, 3, 2\n"
    "v_mfma_f32_16x16x32_bf16 v[64:67], v[128:131], a[40:43], v[64:67]\n"
    "ds_read_b64_tr_b16 v[160:161], v240 offset:24576\n"
    "ds_read_b64_tr_b16 v[162:163], v240 offset:28672\n"
    "v_mfma_f32_16x16x32_bf16 v[72:75], v[128:131], a[88:91], v[72:75]\n"
    "ds_read_b64_tr_b16 v[164:165], v241 offset:24576\n"
    "v_lshrrev_b32 v205, 2, v255\n"
    "v_mfma_f32_16x16x32_bf16 v[68:71], v[132:135], a[40:43], v[68:71]\n"
    "buffer_load_dwordx4 a[76:79], v198, s[68:71], s21 offen offset:3072 sc1\n"
    "v_mfma_f32_16x16x32_bf16 v[76:79], v[132:135], a[88:91], v[76:79]\n"
    "s_waitcnt lgkmcnt(10)\n"
    "ds_read_b64_tr_b16 v[166:167], v241 offset:28672\n"
    "v_lshlrev_b32 v205, 7, v205\n"
    "v_mfma_f32_16x16x32_bf16 v[80:83], v[136:139], a[40:43], v[80:83]\n"
    "ds_read_b64_tr_b16 v[168:169], v242 offset:24576\n"
    "v_mfma_f32_16x16x32_bf16 v[88:91], v[136:139], a[88:91], v[88:91]\n"
    "ds_read_b64_tr_b16 v[170:171], v242 offset:28672\n"
    "ds_read_b64_tr_b16 v[172:173], v243 offset:24576\n"
    "v_mfma_f32_16x16x32_bf16 v[84:87], v[140:143], a[40:43], v[84:87]\n"
    "ds_read_b64_tr_b16 v[174:175], v243 offset:28672\n"
    "v_lshl_add_u32 v204, v204, 5, v205\n"
    "v_mfma_f32_16x16x32_bf16 v[92:95], v[140:143], a[88:91], v[92:95]\n"
    "s_waitcnt lgkmcnt(11)\n"
    "ds_read_b64_tr_b16 v[176:177], v244 offset:24576\n"
    "v_and_b32 v205, 3, v255\n"
    "v_mfma_f32_16x16x32_bf16 v[96:99], v[144:147], a[40:43], v[96:99]\n"
    "ds_read_b64_tr_b16 v[178:179], v244 offset:28672\n"
    "v_lshl_add_u32 v204, v205, 3, v204\n"
    "s_lshl_b32 s14, s8, 14\n"
    "s_add_u32 s16, s14, s53\n"
    "s_add_u32 s15, s14, 98304\n"
    "s_cmp_lt_u32 s8, 2\n"
    "s_cselect_b32 s16, s16, s15\n"
    "v_mfma_f32_16x16x32_bf16 v[104:107], v[144:147], a[88:91], v[104:107]\n"
    "buffer_load_dwordx4 a[124:127], v198, s[68:71], s35 offen offset:3072 sc1\n"
    "v_mfma_f32_16x16x32_bf16 v[100:103], v[148:151], a[40:43], v[100:103]\n"
    "ds_read_b64_tr_b16 v[180:181], v245 offset:24576\n"
    "ds_read_b64_tr_b16 v[182:183], v245 offset:28672\n"
    "v_mfma_f32_16x16x32_bf16 v[108:111], v[148:151], a[88:91], v[108:111]\n"
    "s_waitcnt lgkmcnt(11)\n"
    "ds_read_b64_tr_b16 v[184:185], v246 offset:24576\n"
    "v_add_u32 v216, s16, v204\n"
    "v_mfma_f32_16x16x32_bf16 v[112:115], v[152:155], a[40:43], v[112:115]\n"
    "ds_read_b64_tr_b16 v[186:187], v246 offset:28672\n"
    "v_mfma_f32_16x16x32_bf16 v[120:123], v[152:155], a[88:91], v[120:123]\n"
    "ds_read_b64_tr_b16 v[188:189], v247 offset:24576\n"
    "v_xor_b32 v217, 32, v216\n"
    "v_mfma_f32_16x16x32_bf16 v[116:119], v[156:159], a[40:43], v[116:119]\n"
    "ds_read_b64_tr_b16 v[190:191], v247 offset:28672\n"
    "v_xor_b32 v218, 64, v216\n"
    "v_mfma_f32_16x16x32_bf16 v[124:127], v[156:159], a[88:91], v[124:127]\n"
    "s_waitcnt lgkmcnt(11)\n"
    "v_xor_b32 v219, 96, v216\n"
    "v_mfma_f32_16x16x32_bf16 v[64:67], v[160:163], a[44:47], v[64:67]\n"
    "v_mfma_f32_16x16x32_bf16 v[72:75], v[160:163], a[92:95], v[72:75]\n"
    "s_waitcnt vmcnt(16)\n"
    "s_barrier\n"
    "s_cmp_lg_u32 s8, 0\n"
    "s_cbranch_scc1 L_pub17_%=\n"
    "s_lshl_b32 s14, s33, 7\n"
    "s_lshl_b32 s15, s34, 3\n"
    "s_add_u32 s14, s14, s15\n"
    "s_add_u32 s14, s14, 12288\n"
    "s_add_u32 s14, s14, 0x3400000\n"
    "v_accvgpr_write_b32 a252, s28\n"
    "v_accvgpr_write_b32 a253, s29\n"
    "v_mov_b32 v251, s14\n"
    "s_mov_b64 exec, 1\n"
    "global_store_dwordx2 v251, a[252:253], s[58:59] sc1\n"
    "s_mov_b64 exec, -1\n"
    "L_pub17_%=:\n"
    "v_mfma_f32_16x16x32_bf16 v[68:71], v[164:167], a[44:47], v[68:71]\n"
    "s_add_u32 s19, s53, 65536\n"
    "s_and_b32 s19, s19, 0x1ffff\n"
    "v_add_u32 v240, s19, v192\n"
    "v_add_u32 v241, s19, v193\n"
    "v_add_u32 v242, s19, v194\n"
    "s_lshl_b32 s14, s8, 14\n"
    "v_mfma_f32_16x16x32_bf16 v[76:79], v[164:167], a[92:95], v[76:79]\n"
    "s_waitcnt lgkmcnt(7)\n"
    "v_add_u32 v243, s19, v195\n"
    "v_add_u32 v244, s19, v196\n"
    "v_mfma_f32_16x16x32_bf16 v[80:83], v[168:171], a[44:47], v[80:83]\n"
    "global_load_dwordx2 v[202:203], v201, s[58:59] sc1\n"
    "v_mfma_f32_16x16x32_bf16 v[88:91], v[168:171], a[92:95], v[88:91]\n"
    "v_add_u32 v245, s19, v197\n"
    "v_add_u32 v246, s19, v199\n"
    "v_add_u32 v247, s19, v200\n"
    "v_mfma_f32_16x16x32_bf16 v[84:87], v[172:175], a[44:47], v[84:87]\n"
    "ds_read_b64_tr_b16 v[128:129], v240 offset:0\n"
    "ds_read_b64_tr_b16 v[130:131], v240 offset:4096\n"
    "v_mfma_f32_16x16x32_bf16 v[92:95], v[172:175], a[92:95], v[92:95]\n"
    "s_waitcnt lgkmcnt(5)\n"
    "ds_read_b64_tr_b16 v[132:133], v241 offset:0\n"
    "s_mov_b32 m0, s16\n"
    "ds_read_b64_tr_b16 v[134:135], v241 offset:4096\n"
    "v_mfma_f32_16x16x32_bf16 v[96:99], v[176:179], a[44:47], v[96:99]\n"
    "buffer_load_dwordx4 v212, s[76:79], s26 offen lds\n"
    "v_mfma_f32_16x16x32_bf16 v[104:107], v[176:179], a[92:95], v[104:107]\n"
    "ds_read_b64_tr_b16 v[136:137], v242 offset:0\n"
    "ds_read_b64_tr_b16 v[138:139], v242 offset:4096\n"
    "ds_read_b64_tr_b16 v[140:141], v243 offset:0\n"
    "v_mfma_f32_16x16x32_bf16 v[100:103], v[180:183], a[44:47], v[100:103]\n"
    "ds_read_b64_tr_b16 v[142:143], v243 offset:4096\n"
    "ds_read_b64_tr_b16 v[144:145], v244 offset:0\n"
    "v_mfma_f32_16x16x32_bf16 v[108:111], v[180:183], a[92:95], v[108:111]\n"
    "s_waitcnt lgkmcnt(8)\n"
    "ds_read_b64_tr_b16 v[146:147], v244 offset:4096\n"
    "s_add_u32 m0, m0, 1024\n"
    "ds_read_b64_tr_b16 v[148:149], v245 offset:0\n"
    "ds_read_b64_tr_b16 v[150:151], v245 offset:4096\n"
    "v_mfma_f32_16x16x32_bf16 v[112:115], v[184:187], a[44:47], v[112:115]\n"
    "buffer_load_dwordx4 v213, s[76:79], s26 offen lds\n"
    "v_mfma_f32_16x16x32_bf16 v[120:123], v[184:187], a[92:95], v[120:123]\n"
    "ds_read_b64_tr_b16 v[152:153], v246 offset:0\n"
    "ds_read_b64_tr_b16 v[154:155], v246 offset:4096\n"
    "v_mfma_f32_16x16x32_bf16 v[116:119], v[188:191], a[44:47], v[116:119]\n"
    "ds_read_b64_tr_b16 v[156:157], v247 offset:0\n"
    "ds_read_b64_tr_b16 v[158:159], v247 offset:4096\n"
    "v_mfma_f32_16x16x32_bf16 v[124:127], v[188:191], a[92:95], v[124:127]\n"
    "s_waitcnt vmcnt(11)\n"
    "s_add_u32 m0, m0, 1024\n"
    "v_and_b32 v204, 15, v255\n"
    "v_lshrrev_b32 v205, 4, v255\n"
    "v_xor_b32 v205, v205, v204\n"
    "s_waitcnt lgkmcnt(11)\n"
    "v_mfma_f32_16x16x32_bf16 v[64:67], v[128:131], a[48:51], v[64:67]\n"
    "buffer_load_dwordx4 v214, s[76:79], s26 offen lds\n"
    "v_mfma_f32_16x16x32_bf16 v[72:75], v[128:131], a[96:99], v[72:75]\n"
    "ds_read_b64_tr_b16 v[160:161], v240 offset:8192\n"
    "ds_read_b64_tr_b16 v[162:163], v240 offset:12288\n"
    "v_mfma_f32_16x16x32_bf16 v[68:71], v[132:135], a[48:51], v[68:71]\n"
    "ds_read_b64_tr_b16 v[164:165], v241 offset:8192\n"
    "v_lshlrev_b32 v204, 8, v204\n"
    "v_mfma_f32_16x16x32_bf16 v[76:79], v[132:135], a[96:99], v[76:79]\n"
    "s_waitcnt lgkmcnt(10)\n"
    "ds_read_b64_tr_b16 v[166:167], v241 offset:12288\n"
    "s_add_u32 m0, m0, 1024\n"
    "ds_read_b64_tr_b16 v[168:169], v242 offset:8192\n"
    "v_mfma_f32_16x16x32_bf16 v[80:83], v[136:139], a[48:51], v[80:83]\n"
    "buffer_load_dwordx4 v215, s[76:79], s26 offen lds\n"
    "v_mfma_f32_16x16x32_bf16 v[88:91], v[136:139], a[96:99], v[88:91]\n"
    "ds_read_b64_tr_b16 v[170:171], v242 offset:12288\n"
    "s_add_u32 s26, s26, 32768\n"
    "v_add_u32 v204, s14, v204\n"
    "v_mfma_f32_16x16x32_bf16 v[84:87], v[140:143], a[48:51], v[84:87]\n"
    "ds_read_b64_tr_b16 v[172:173], v243 offset:8192\n"
    "ds_read_b64_tr_b16 v[174:175], v243 offset:12288\n"
    "v_mfma_f32_16x16x32_bf16 v[92:95], v[140:143], a[96:99], v[92:95]\n"
    "s_waitcnt lgkmcnt(11)\n"
    "ds_read_b64_tr_b16 v[176:177], v244 offset:8192\n"
    "s_add_u32 m0, m0, 1024\n"
    "v_lshl_add_u32 v220, v205, 4, v204\n"
    "v_mfma_f32_16x16x32_bf16 v[96:99], v[144:147], a[48:51], v[96:99]\n"
    "buffer_load_dwordx4 v212, s[76:79], s26 offen lds\n"
    "v_mfma_f32_16x16x32_bf16 v[104:107], v[144:147], a[96:99], v[104:107]\n"
    "ds_read_b64_tr_b16 v[178:179], v244 offset:12288\n"
    "ds_read_b64_tr_b16 v[180:181], v245 offset:8192\n"
    "v_mfma_f32_16x16x32_bf16 v[100:103], v[148:151], a[48:51], v[100:103]\n"
    "ds_read_b64_tr_b16 v[182:183], v245 offset:12288\n"
    "v_xor_b32 v221, 64, v220\n"
    "v_mfma_f32_16x16x32_bf16 v[108:111], v[148:151], a[96:99], v[108:111]\n"
    "s_waitcnt lgkmcnt(11)\n"
    "ds_read_b64_tr_b16 v[184:185], v246 offset:8192\n"
    "s_add_u32 m0, m0, 1024\n"
    "ds_read_b64_tr_b16 v[186:187], v246 offset:12288\n"
    "v_mfma_f32_16x16x32_bf16 v[112:115], v[152:155], a[48:51], v[112:115]\n"
    "buffer_load_dwordx4 v213, s[76:79], s26 offen lds\n"
    "v_mfma_f32_16x16x32_bf16 v[120:123], v[152:155], a[96:99], v[120:123]\n"
    "ds_read_b64_tr_b16 v[188:189], v247 offset:8192\n"
    "v_xor_b32 v222, 128, v220\n"
    "v_mfma_f32_16x16x32_bf16 v[116:119], v[156:159], a[48:51], v[116:119]\n"
    "ds_read_b64_tr_b16 v[190:191], v247 offset:12288\n"
    "v_xor_b32 v223, 192, v220\n"
    "v_mfma_f32_16x16x32_bf16 v[124:127], v[156:159], a[96:99], v[124:127]\n"
    "s_waitcnt lgkmcnt(11)\n"
    "s_add_u32 m0, m0, 1024\n"
    "v_lshrrev_b32 v204, 2, v255\n"
    "v_and_b32 v205, 3, v255\n"
    "v_mfma_f32_16x16x32_bf16 v[64:67], v[160:163], a[52:55], v[64:67]\n"
    "buffer_load_dwordx4 v214, s[76:79], s26 offen lds\n"
    "v_mfma_f32_16x16x32_bf16 v[72:75], v[160:163], a[100:103], v[72:75]\n"
    "ds_read_b64_tr_b16 v[128:129], v240 offset:16384\n"
    "ds_read_b64_tr_b16 v[130:131], v240 offset:20480\n"
    "v_mfma_f32_16x16x32_bf16 v[68:71], v[164:167], a[52:55], v[68:71]\n"
    "ds_read_b64_tr_b16 v[132:133], v241 offset:16384\n"
    "ds_read_b64_tr_b16 v[134:135], v241 offset:20480\n"
    "v_mfma_f32_16x16x32_bf16 v[76:79], v[164:167], a[100:103], v[76:79]\n"
    "s_waitcnt lgkmcnt(11)\n"
    "ds_read_b64_tr_b16 v[136:137], v242 offset:16384\n"
    "s_add_u32 m0, m0, 1024\n"
    "v_lshlrev_b32 v206, 2, v205\n"
    "v_mfma_f32_16x16x32_bf16 v[80:83], v[168:171], a[52:55], v[80:83]\n"
    "buffer_load_dwordx4 v215, s[76:79], s26 offen lds\n"
    "v_mfma_f32_16x16x32_bf16 v[88:91], v[168:171], a[100:103], v[88:91]\n"
    "ds_read_b64_tr_b16 v[138:139], v242 offset:20480\n"
    "s_add_u32 s26, s26, 32768\n"
    "ds_read_b64_tr_b16 v[140:141], v243 offset:16384\n"
    "v_mfma_f32_16x16x32_bf16 v[84:87], v[172:175], a[52:55], v[84:87]\n"
    "ds_read_b64_tr_b16 v[142:143], v243 offset:20480\n"
    "v_xor_b32 v206, v206, v204\n"
    "v_mfma_f32_16x16x32_bf16 v[92:95], v[172:175], a[100:103], v[92:95]\n"
    "s_waitcnt lgkmcnt(11)\n"
    "ds_read_b64_tr_b16 v[144:145], v244 offset:16384\n"
    "ds_read_b64_tr_b16 v[146:147], v244 offset:20480\n"
    "v_mfma_f32_16x16x32_bf16 v[96:99], v[176:179], a[52:55], v[96:99]\n"
    "s_waitcnt vmcnt(8)\n"
    "v_cmp_ne_u64 s[98:99], s[28:29], v[202:203]\n"
    "s_nop 0\n"
    "s_cmp_lg_u64 s[98:99], 0\n"
    "s_cbranch_scc1 L_spin14_%=\n"
    "L_ok15_%=:\n"
    "v_lshlrev_b32 v228, 11, v204\n"
    "v_mfma_f32_16x16x32_bf16 v[104:107], v[176:179], a[100:103], v[104:107]\n"
    "s_add_u32 s35, s40, 1\n"
    "s_and_b32 s35, s35, 3\n"
    "s_lshl_b32 s35, s35, 12\n"
    "s_lshl_b32 s21, s10, 14\n"
    "s_add_u32 s35, s35, s21\n"
    "s_mul_i32 s21, s30, 131072\n"
    "s_add_u32 s21, s21, s35\n"
    "s_mul_i32 s96, s31, 131072\n"
    "s_add_u32 s35, s35, s96\n"
    "buffer_load_dwordx4 a[128:131], v198, s[68:71], s21 offen offset:0 sc1\n"
    "v_mfma_f32_16x16x32_bf16 v[100:103], v[180:183], a[52:55], v[100:103]\n"
    "ds_read_b64_tr_b16 v[148:149], v245 offset:16384\n"
    "ds_read_b64_tr_b16 v[150:151], v245 offset:20480\n"
    "v_mfma_f32_16x16x32_bf16 v[108:111], v[180:183], a[100:103], v[108:111]\n"
    "s_waitcnt lgkmcnt(11)\n"
    "ds_read_b64_tr_b16 v[152:153], v246 offset:16384\n"
    "v_lshl_add_u32 v228, v205, 5, v228\n"
    "v_mfma_f32_16x16x32_bf16 v[112:115], v[184:187], a[52:55], v[112:115]\n"
    "ds_read_b64_tr_b16 v[154:155], v246 offset:20480\n"
    "ds_read_b64_tr_b16 v[156:157], v247 offset:16384\n"
    "v_mfma_f32_16x16x32_bf16 v[120:123], v[184:187], a[100:103], v[120:123]\n"
    "buffer_load_dwordx4 a[176:179], v198, s[68:71], s35 offen offset:0 sc1\n"
    "v_mfma_f32_16x16x32_bf16 v[116:119], v[188:191], a[52:55], v[116:119]\n"
    "ds_read_b64_tr_b16 v[158:159], v247 offset:20480\n"
    "v_lshlrev_b32 v204, 8, v204\n"
    "s_lshl_b32 s14, s8, 12\n"
    "v_mfma_f32_16x16x32_bf16 v[124:127], v[188:191], a[100:103], v[124:127]\n"
    "s_waitcnt lgkmcnt(11)\n"
    "v_add_u32 v204, s14, v204\n"
    "v_lshl_add_u32 v224, v206, 4, v204\n"
    "v_mfma_f32_16x16x32_bf16 v[64:67], v[128:131], a[56:59], v[64:67]\n"
    "ds_read_b64_tr_b16 v[160:161], v240 offset:24576\n"
    "ds_read_b64_tr_b16 v[162:163], v240 offset:28672\n"
    "v_mfma_f32_16x16x32_bf16 v[72:75], v[128:131], a[104:107], v[72:75]\n"
    "buffer_load_dwordx4 a[132:135], v198, s[68:71], s21 offen offset:1024 sc1\n"
    "v_mfma_f32_16x16x32_bf16 v[68:71], v[132:135], a[56:59], v[68:71]\n"
    "ds_read_b64_tr_b16 v[164:165], v241 offset:24576\n"
    "v_xor_b32 v225, 16, v224\n"
    "v_mfma_f32_16x16x32_bf16 v[76:79], v[132:135], a[104:107], v[76:79]\n"
    "s_waitcnt lgkmcnt(10)\n"
    "ds_read_b64_tr_b16 v[166:167], v241 offset:28672\n"
    "ds_read_b64_tr_b16 v[168:169], v242 offset:24576\n"
    "v_mfma_f32_16x16x32_bf16 v[80:83], v[136:139], a[56:59], v[80:83]\n"
    "ds_read_b64_tr_b16 v[170:171], v242 offset:28672\n"
    "v_xor_b32 v226, 32, v224\n"
    "v_mfma_f32_16x16x32_bf16 v[88:91], v[136:139], a[104:107], v[88:91]\n"
    "buffer_load_dwordx4 a[180:183], v198, s[68:71], s35 offen offset:1024 sc1\n"
    "v_mfma_f32_16x16x32_bf16 v[84:87], v[140:143], a[56:59], v[84:87]\n"
    "ds_read_b64_tr_b16 v[172:173], v243 offset:24576\n"
    "ds_read_b64_tr_b16 v[174:175], v243 offset:28672\n"
    "v_mfma_f32_16x16x32_bf16 v[92:95], v[140:143], a[104:107], v[92:95]\n"
    "s_waitcnt lgkmcnt(11)\n"
    "ds_read_b64_tr_b16 v[176:177], v244 offset:24576\n"
    "v_xor_b32 v227, 48, v224\n"
    "s_lshl_b32 s14, s23, 17\n"
    "s_lshl_b32 s15, s8, 15\n"
    "s_add_u32 s14, s14, s15\n"
    "s_lshl_b32 s15, s24, 7\n"
    "s_add_u32 s14, s14, s15\n"
    "v_mfma_f32_16x16x32_bf16 v[96:99], v[144:147], a[56:59], v[96:99]\n"
    "ds_read_b64_tr_b16 v[178:179], v244 offset:28672\n"
    "ds_read_b64_tr_b16 v[180:181], v245 offset:24576\n"
    "v_mfma_f32_16x16x32_bf16 v[104:107], v[144:147], a[104:107], v[104:107]\n"
    "buffer_load_dwordx4 a[136:139], v198, s[68:71], s21 offen offset:2048 sc1\n"
    "v_mfma_f32_16x16x32_bf16 v[100:103], v[148:151], a[56:59], v[100:103]\n"
    "ds_read_b64_tr_b16 v[182:183], v245 offset:28672\n"
    "v_add_u32 v228, s14, v228\n"
    "s_sub_u32 s14, 15, s13\n"
    "s_and_b32 s15, s10, 3\n"
    "s_cmp_lt_u32 s10, 4\n"
    "s_cselect_b32 s14, s13, s14\n"
    "s_lshl_b32 s14, s14, 3\n"
    "s_add_u32 s14, s14, s15\n"
    "s_lshl_b32 s15, s15, 1\n"
    "s_sub_u32 s97, 7, s15\n"
    "s_lshl_b32 s97, s97, 15\n"
    "s_lshl_b32 s14, s14, 15\n"
    "s_lshl_b32 s15, s40, 8\n"
    "s_add_u32 s14, s14, s15\n"
    "s_lshl_b32 s15, s8, 6\n"
    "s_add_u32 s14, s14, s15\n"
    "s_add_u32 s14, s14, 1024\n"
    "v_mfma_f32_16x16x32_bf16 v[108:111], v[148:151], a[104:107], v[108:111]\n"
    "s_waitcnt lgkmcnt(11)\n"
    "ds_read_b64_tr_b16 v[184:185], v246 offset:24576\n"
    "ds_read_b64_tr_b16 v[186:187], v246 offset:28672\n"
    "v_mfma_f32_16x16x32_bf16 v[112:115], v[152:155], a[56:59], v[112:115]\n"
    "ds_read_b64_tr_b16 v[188:189], v247 offset:24576\n"
    "v_and_b32 v204, 15, v255\n"
    "v_mfma_f32_16x16x32_bf16 v[120:123], v[152:155], a[104:107], v[120:123]\n"
    "buffer_load_dwordx4 a[184:187], v198, s[68:71], s35 offen offset:2048 sc1\n"
    "v_mfma_f32_16x16x32_bf16 v[116:119], v[156:159], a[56:59], v[116:119]\n"
    "ds_read_b64_tr_b16 v[190:191], v247 offset:28672\n"
    "v_lshlrev_b32 v204, 11, v204\n"
    "v_mfma_f32_16x16x32_bf16 v[124:127], v[156:159], a[104:107], v[124:127]\n"
    "s_waitcnt lgkmcnt(11)\n"
    "v_and_b32 v205, 48, v255\n"
    "v_add3_u32 v229, v204, v205, s14\n"
    "v_mfma_f32_16x16x32_bf16 v[64:67], v[160:163], a[60:63], v[64:67]\n"
    "s_add_u32 s19, s53, 98304\n"
    "s_and_b32 s19, s19, 0x1ffff\n"
    "v_add_u32 v240, s19, v192\n"
    "v_add_u32 v241, s19, v193\n"
    "v_add_u32 v242, s19, v194\n"
    "v_mfma_f32_16x16x32_bf16 v[72:75], v[160:163], a[108:111], v[72:75]\n"
    "buffer_load_dwordx4 a[140:143], v198, s[68:71], s21 offen offset:3072 sc1\n"
    "v_mfma_f32_16x16x32_bf16 v[68:71], v[164:167], a[60:63], v[68:71]\n"
    "v_add_u32 v243, s19, v195\n"
    "v_add_u32 v244, s19, v196\n"
    "v_mfma_f32_16x16x32_bf16 v[76:79], v[164:167], a[108:111], v[76:79]\n"
    "s_waitcnt lgkmcnt(7)\n"
    "v_add_u32 v245, s19, v197\n"
    "v_add_u32 v246, s19, v199\n"
    "v_mfma_f32_16x16x32_bf16 v[80:83], v[168:171], a[60:63], v[80:83]\n"
    "v_add_u32 v247, s19, v200\n"
    "ds_read_b64_tr_b16 v[128:129], v240 offset:0\n"
    "v_mfma_f32_16x16x32_bf16 v[88:91], v[168:171], a[108:111], v[88:91]\n"
    "buffer_load_dwordx4 a[188:191], v198, s[68:71], s35 offen offset:3072 sc1\n"
    "s_add_u32 s35, s40, 2\n"
    "s_and_b32 s35, s35, 3\n"
    "s_lshl_b32 s35, s35, 12\n"
    "s_lshl_b32 s21, s10, 14\n"
    "s_add_u32 s35, s35, s21\n"
    "s_mul_i32 s21, s30, 131072\n"
    "s_add_u32 s21, s21, s35\n"
    "s_mul_i32 s96, s31, 131072\n"
    "s_add_u32 s35, s35, s96\n"
    "v_mfma_f32_16x16x32_bf16 v[84:87], v[172:175], a[60:63], v[84:87]\n"
    "ds_read_b64_tr_b16 v[130:131], v240 offset:4096\n"
    "ds_read_b64_tr_b16 v[132:133], v241 offset:0\n"
    "v_mfma_f32_16x16x32_bf16 v[92:95], v[172:175], a[108:111], v[92:95]\n"
    "s_waitcnt lgkmcnt(6)\n"
    "ds_read_b64_tr_b16 v[134:135], v241 offset:4096\n"
    "ds_read_b64_tr_b16 v[136:137], v242 offset:0\n"
    "ds_read_b64_tr_b16 v[138:139], v242 offset:4096\n"
    "v_mfma_f32_16x16x32_bf16 v[96:99], v[176:179], a[60:63], v[96:99]\n"
    "ds_read_b64_tr_b16 v[140:141], v243 offset:0\n"
    "ds_read_b64_tr_b16 v[142:143], v243 offset:4096\n"
    "v_mfma_f32_16x16x32_bf16 v[104:107], v[176:179], a[108:111], v[104:107]\n"
    "buffer_load_dwordx4 a[144:147], v198, s[68:71], s21 offen offset:0 sc1\n"
    "v_mfma_f32_16x16x32_bf16 v[100:103], v[180:183], a[60:63], v[100:103]\n"
    "ds_read_b64_tr_b16 v[144:145], v244 offset:0\n"
    "ds_read_b64_tr_b16 v[146:147], v244 offset:4096\n"
    "v_mfma_f32_16x16x32_bf16 v[108:111], v[180:183], a[108:111], v[108:111]\n"
    "s_waitcnt lgkmcnt(9)\n"
    "ds_read_b64_tr_b16 v[148:149], v245 offset:0\n"
    "ds_read_b64_tr_b16 v[150:151], v245 offset:4096\n"
    "v_mfma_f32_16x16x32_bf16 v[112:115], v[184:187], a[60:63], v[112:115]\n"
    "ds_read_b64_tr_b16 v[152:153], v246 offset:0\n"
    "ds_read_b64_tr_b16 v[154:155], v246 offset:4096\n"
    "v_mfma_f32_16x16x32_bf16 v[120:123], v[184:187], a[108:111], v[120:123]\n"
    "ds_read_b64_tr_b16 v[156:157], v247 offset:0\n"
    "ds_read_b64_tr_b16 v[158:159], v247 offset:4096\n"
    "v_mfma_f32_16x16x32_bf16 v[116:119], v[188:191], a[60:63], v[116:119]\n"
    "buffer_load_dwordx4 a[192:195], v198, s[68:71], s35 offen offset:0 sc1\n"
    "v_mfma_f32_16x16x32_bf16 v[124:127], v[188:191], a[108:111], v[124:127]\n"
    "s_waitcnt vmcnt(19)\n"
    "v_and_b32 v204, 1, v255\n"
    "v_lshlrev_b32 v204, 4, v204\n"
    "s_waitcnt lgkmcnt(11)\n"
    "v_mfma_f32_16x16x32_bf16 v[64:67], v[128:131], a[64:67], v[64:67]\n"
    "ds_read_b64_tr_b16 v[160:161], v240 offset:8192\n"
    "ds_read_b64_tr_b16 v[162:163], v240 offset:12288\n"
    "v_mfma_f32_16x16x32_bf16 v[72:75], v[128:131], a[112:115], v[72:75]\n"
    "ds_read_b64_tr_b16 v[164:165], v241 offset:8192\n"
    "v_mov_b32 v205, 0x3f80\n"
    "v_mfma_f32_16x16x32_bf16 v[68:71], v[132:135], a[64:67], v[68:71]\n"
    "buffer_load_dwordx4 a[148:151], v198, s[68:71], s21 offen offset:1024 sc1\n"
    "v_mfma_f32_16x16x32_bf16 v[76:79], v[132:135], a[112:115], v[76:79]\n"
    "s_waitcnt lgkmcnt(10)\n"
    "ds_read_b64_tr_b16 v[166:167], v241 offset:12288\n"
    "ds_read_b64_tr_b16 v[168:169], v242 offset:8192\n"
    "v_mfma_f32_16x16x32_bf16 v[80:83], v[136:139], a[64:67], v[80:83]\n"
    "ds_read_b64_tr_b16 v[170:171], v242 offset:12288\n"
    "v_lshlrev_b32 v204, v204, v205\n"
    "v_mfma_f32_16x16x32_bf16 v[88:91], v[136:139], a[112:115], v[88:91]\n"
    "ds_read_b64_tr_b16 v[172:173], v243 offset:8192\n"
    "ds_read_b64_tr_b16 v[174:175], v243 offset:12288\n"
    "v_mfma_f32_16x16x32_bf16 v[84:87], v[140:143], a[64:67], v[84:87]\n"
    "buffer_load_dwordx4 a[196:199], v198, s[68:71], s35 offen offset:1024 sc1\n"
    "v_mfma_f32_16x16x32_bf16 v[92:95], v[140:143], a[112:115], v[92:95]\n"
    "s_waitcnt lgkmcnt(11)\n"
    "ds_read_b64_tr_b16 v[176:177], v244 offset:8192\n"
    "v_bfe_u32 v205, v255, 3, 1\n"
    "v_mfma_f32_16x16x32_bf16 v[96:99], v[144:147], a[64:67], v[96:99]\n"
    "ds_read_b64_tr_b16 v[178:179], v244 offset:12288\n"
    "ds_read_b64_tr_b16 v[180:181], v245 offset:8192\n"
    "v_mfma_f32_16x16x32_bf16 v[104:107], v[144:147], a[112:115], v[104:107]\n"
    "ds_read_b64_tr_b16 v[182:183], v245 offset:12288\n"
    "v_bfe_u32 v206, v255, 1, 1\n"
    "v_mfma_f32_16x16x32_bf16 v[100:103], v[148:151], a[64:67], v[100:103]\n"
    "buffer_load_dwordx4 a[152:155], v198, s[68:71], s21 offen offset:2048 sc1\n"
    "v_mfma_f32_16x16x32_bf16 v[108:111], v[148:151], a[112:115], v[108:111]\n"
    "s_waitcnt lgkmcnt(11)\n"
    "ds_read_b64_tr_b16 v[184:185], v246 offset:8192\n"
    "ds_read_b64_tr_b16 v[186:187], v246 offset:12288\n"
    "v_mfma_f32_16x16x32_bf16 v[112:115], v[152:155], a[64:67], v[112:115]\n"
    "ds_read_b64_tr_b16 v[188:189], v247 offset:8192\n"
    "v_lshl_add_u32 v205, v205, 1, v206\n"
    "v_mfma_f32_16x16x32_bf16 v[120:123], v[152:155], a[112:115], v[120:123]\n"
    "ds_read_b64_tr_b16 v[190:191], v247 offset:12288\n"
    "v_bfe_u32 v206, v255, 2, 1\n"
    "v_mfma_f32_16x16x32_bf16 v[116:119], v[156:159], a[64:67], v[116:119]\n"
    "buffer_load_dwordx4 a[200:203], v198, s[68:71], s35 offen offset:2048 sc1\n"
    "v_mfma_f32_16x16x32_bf16 v[124:127], v[156:159], a[112:115], v[124:127]\n"
    "s_waitcnt lgkmcnt(11)\n"
    "v_lshlrev_b32 v206, 1, v206\n"
    "v_lshrrev_b32 v207, 4, v255\n"
    "v_mfma_f32_16x16x32_bf16 v[64:67], v[160:163], a[68:71], v[64:67]\n"
    "ds_read_b64_tr_b16 v[128:129], v240 offset:16384\n"
    "ds_read_b64_tr_b16 v[130:131], v240 offset:20480\n"
    "v_mfma_f32_16x16x32_bf16 v[72:75], v[160:163], a[116:119], v[72:75]\n"
    "ds_read_b64_tr_b16 v[132:133], v241 offset:16384\n"
    "v_sub_u32 v206, v207, v206\n"
    "v_mfma_f32_16x16x32_bf16 v[68:71], v[164:167], a[68:71], v[68:71]\n"
    "buffer_load_dwordx4 a[156:159], v198, s[68:71], s21 offen offset:3072 sc1\n"
    "v_mfma_f32_16x16x32_bf16 v[76:79], v[164:167], a[116:119], v[76:79]\n"
    "s_waitcnt lgkmcnt(10)\n"
    "ds_read_b64_tr_b16 v[134:135], v241 offset:20480\n"
    "v_lshl_add_u32 v205, v206, 2, v205\n"
    "v_mfma_f32_16x16x32_bf16 v[80:83], v[168:171], a[68:71], v[80:83]\n"
    "ds_read_b64_tr_b16 v[136:137], v242 offset:16384\n"
    "ds_read_b64_tr_b16 v[138:139], v242 offset:20480\n"
    "v_mfma_f32_16x16x32_bf16 v[88:91], v[168:171], a[116:119], v[88:91]\n"
    "ds_read_b64_tr_b16 v[140:141], v243 offset:16384\n"
    "v_cmp_eq_u32 vcc, 0, v205\n"
    "v_mfma_f32_16x16x32_bf16 v[84:87], v[172:175], a[68:71], v[84:87]\n"
    "buffer_load_dwordx4 a[204:207], v198, s[68:71], s35 offen offset:3072 sc1\n"
    "s_add_u32 s35, s40, 3\n"
    "s_and_b32 s35, s35, 3\n"
    "s_lshl_b32 s35, s35, 12\n"
    "s_lshl_b32 s21, s10, 14\n"
    "s_add_u32 s35, s35, s21\n"
    "s_mul_i32 s21, s30, 131072\n"
    "s_add_u32 s21, s21, s35\n"
    "s_mul_i32 s96, s31, 131072\n"
    "s_add_u32 s35, s35, s96\n"
    "v_mfma_f32_16x16x32_bf16 v[92:95], v[172:175], a[116:119], v[92:95]\n"
    "s_waitcnt lgkmcnt(10)\n"
    "ds_read_b64_tr_b16 v[142:143], v243 offset:20480\n"
    "v_cndmask_b32 v230, 0, v204, vcc\n"
    "v_mfma_f32_16x16x32_bf16 v[96:99], v[176:179], a[68:71], v[96:99]\n"
    "ds_read_b64_tr_b16 v[144:145], v244 offset:16384\n"
    "ds_read_b64_tr_b16 v[146:147], v244 offset:20480\n"
    "v_mfma_f32_16x16x32_bf16 v[104:107], v[176:179], a[116:119], v[104:107]\n"
    "ds_read_b64_tr_b16 v[148:149], v245 offset:16384\n"
    "v_cmp_eq_u32 vcc, 1, v205\n"
    "v_mfma_f32_16x16x32_bf16 v[100:103], v[180:183], a[68:71], v[100:103]\n"
    "buffer_load_dwordx4 a[160:163], v198, s[68:71], s21 offen offset:0 sc1\n"
    "v_mfma_f32_16x16x32_bf16 v[108:111], v[180:183], a[116:119], v[108:111]\n"
    "s_waitcnt lgkmcnt(10)\n"
    "ds_read_b64_tr_b16 v[150:151], v245 offset:20480\n"
    "v_cndmask_b32 v231, 0, v204, vcc\n"
    "v_mfma_f32_16x16x32_bf16 v[112:115], v[184:187], a[68:71], v[112:115]\n"
    "ds_read_b64_tr_b16 v[152:153], v246 offset:16384\n"
    "ds_read_b64_tr_b16 v[154:155], v246 offset:20480\n"
    "v_mfma_f32_16x16x32_bf16 v[120:123], v[184:187], a[116:119], v[120:123]\n"
    "ds_read_b64_tr_b16 v[156:157], v247 offset:16384\n"
    "v_cmp_eq_u32 vcc, 2, v205\n"
    "v_mfma_f32_16x16x32_bf16 v[116:119], v[188:191], a[68:71], v[116:119]\n"
    "ds_read_b64_tr_b16 v[158:159], v247 offset:20480\n"
    "v_cndmask_b32 v232, 0, v204, vcc\n"
    "v_mfma_f32_16x16x32_bf16 v[124:127], v[188:191], a[116:119], v[124:127]\n"
    "s_waitcnt lgkmcnt(11)\n"
    "buffer_load_dwordx4 a[208:211], v198, s[68:71], s35 offen offset:0 sc1\n"
    "v_mfma_f32_16x16x32_bf16 v[64:67], v[128:131], a[72:75], v[64:67]\n"
    "ds_read_b64_tr_b16 v[160:161], v240 offset:24576\n"
    "ds_read_b64_tr_b16 v[162:163], v240 offset:28672\n"
    "v_mfma_f32_16x16x32_bf16 v[72:75], v[128:131], a[120:123], v[72:75]\n"
    "ds_read_b64_tr_b16 v[164:165], v241 offset:24576\n"
    "v_cmp_eq_u32 vcc, 3, v205\n"
    "v_mfma_f32_16x16x32_bf16 v[68:71], v[132:135], a[72:75], v[68:71]\n"
    "ds_read_b64_tr_b16 v[166:167], v241 offset:28672\n"
    "v_cndmask_b32 v233, 0, v204, vcc\n"
    "v_mfma_f32_16x16x32_bf16 v[76:79], v[132:135], a[120:123], v[76:79]\n"
    "s_waitcnt lgkmcnt(11)\n"
    "buffer_load_dwordx4 a[164:167], v198, s[68:71], s21 offen offset:1024 sc1\n"
    "v_mfma_f32_16x16x32_bf16 v[80:83], v[136:139], a[72:75], v[80:83]\n"
    "ds_read_b64_tr_b16 v[168:169], v242 offset:24576\n"
    "ds_read_b64_tr_b16 v[170:171], v242 offset:28672\n"
    "v_mfma_f32_16x16x32_bf16 v[88:91], v[136:139], a[120:123], v[88:91]\n"
    "ds_read_b64_tr_b16 v[172:173], v243 offset:24576\n"
    "v_cmp_eq_u32 vcc, 4, v205\n"
    "v_mfma_f32_16x16x32_bf16 v[84:87], v[140:143], a[72:75], v[84:87]\n"
    "ds_read_b64_tr_b16 v[174:175], v243 offset:28672\n"
    "v_cndmask_b32 v234, 0, v204, vcc\n"
    "v_mfma_f32_16x16x32_bf16 v[92:95], v[140:143], a[120:123], v[92:95]\n"
    "s_waitcnt lgkmcnt(11)\n"
    "buffer_load_dwordx4 a[212:215], v198, s[68:71], s35 offen offset:1024 sc1\n"
    "v_mfma_f32_16x16x32_bf16 v[96:99], v[144:147], a[72:75], v[96:99]\n"
    "ds_read_b64_tr_b16 v[176:177], v244 offset:24576\n"
    "ds_read_b64_tr_b16 v[178:179], v244 offset:28672\n"
    "v_mfma_f32_16x16x32_bf16 v[104:107], v[144:147], a[120:123], v[104:107]\n"
    "ds_read_b64_tr_b16 v[180:181], v245 offset:24576\n"
    "v_cmp_eq_u32 vcc, 5, v205\n"
    "v_mfma_f32_16x16x32_bf16 v[100:103], v[148:151], a[72:75], v[100:103]\n"
    "ds_read_b64_tr_b16 v[182:183], v245 offset:28672\n"
    "v_cndmask_b32 v235, 0, v204, vcc\n"
    "v_mfma_f32_16x16x32_bf16 v[108:111], v[148:151], a[120:123], v[108:111]\n"
    "s_waitcnt lgkmcnt(11)\n"
    "buffer_load_dwordx4 a[168:171], v198, s[68:71], s21 offen offset:2048 sc1\n"
    "v_mfma_f32_16x16x32_bf16 v[112:115], v[152:155], a[72:75], v[112:115]\n"
    "ds_read_b64_tr_b16 v[184:185], v246 offset:24576\n"
    "ds_read_b64_tr_b16 v[186:187], v246 offset:28672\n"
    "v_mfma_f32_16x16x32_bf16 v[120:123], v[152:155], a[120:123], v[120:123]\n"
    "ds_read_b64_tr_b16 v[188:189], v247 offset:24576\n"
    "v_cmp_eq_u32 vcc, 6, v205\n"
    "v_mfma_f32_16x16x32_bf16 v[116:119], v[156:159], a[72:75], v[116:119]\n"
    "ds_read_b64_tr_b16 v[190:191], v247 offset:28672\n"
    "v_cndmask_b32 v236, 0, v204, vcc\n"
    "v_mfma_f32_16x16x32_bf16 v[124:127], v[156:159], a[120:123], v[124:127]\n"
    "s_waitcnt lgkmcnt(11)\n"
    "buffer_load_dwordx4 a[216:219], v198, s[68:71], s35 offen offset:2048 sc1\n"
    "v_mfma_f32_16x16x32_bf16 v[64:67], v[160:163], a[76:79], v[64:67]\n"
    "s_add_u32 s19, s53, 32768\n"
    "s_and_b32 s19, s19, 0x1ffff\n"
    "v_add_u32 v240, s19, v192\n"
    "v_add_u32 v241, s19, v193\n"
    "v_mfma_f32_16x16x32_bf16 v[72:75], v[160:163], a[124:127], v[72:75]\n"
    "v_add_u32 v242, s19, v194\n"
    "v_add_u32 v243, s19, v195\n"
    "v_mfma_f32_16x16x32_bf16 v[68:71], v[164:167], a[76:79], v[68:71]\n"
    "v_add_u32 v244, s19, v196\n"
    "v_add_u32 v245, s19, v197\n"
    "v_mfma_f32_16x16x32_bf16 v[76:79], v[164:167], a[124:127], v[76:79]\n"
    "s_waitcnt lgkmcnt(7)\n"
    "buffer_load_dwordx4 a[172:175], v198, s[68:71], s21 offen offset:3072 sc1\n"
    "v_mfma_f32_16x16x32_bf16 v[80:83], v[168:171], a[76:79], v[80:83]\n"
    "v_add_u32 v246, s19, v199\n"
    "v_add_u32 v247, s19, v200\n"
    "v_mfma_f32_16x16x32_bf16 v[88:91], v[168:171], a[124:127], v[88:91]\n"
    "ds_read_b64_tr_b16 v[128:129], v240 offset:0\n"
    "ds_read_b64_tr_b16 v[130:131], v240 offset:4096\n"
    "v_mfma_f32_16x16x32_bf16 v[84:87], v[172:175], a[76:79], v[84:87]\n"
    "ds_read_b64_tr_b16 v[132:133], v241 offset:0\n"
    "ds_read_b64_tr_b16 v[134:135], v241 offset:4096\n"
    "v_mfma_f32_16x16x32_bf16 v[92:95], v[172:175], a[124:127], v[92:95]\n"
    "s_waitcnt lgkmcnt(7)\n"
    "buffer_load_dwordx4 a[220:223], v198, s[68:71], s35 offen offset:3072 sc1\n"
    "v_mfma_f32_16x16x32_bf16 v[96:99], v[176:179], a[76:79], v[96:99]\n"
    "ds_read_b64_tr_b16 v[136:137], v242 offset:0\n"
    "ds_read_b64_tr_b16 v[138:139], v242 offset:4096\n"
    "v_mfma_f32_16x16x32_bf16 v[104:107], v[176:179], a[124:127], v[104:107]\n"
    "ds_read_b64_tr_b16 v[140:141], v243 offset:0\n"
    "ds_read_b64_tr_b16 v[142:143], v243 offset:4096\n"
    "v_mfma_f32_16x16x32_bf16 v[100:103], v[180:183], a[76:79], v[100:103]\n"
    "ds_read_b64_tr_b16 v[144:145], v240 offset:8192\n"
    "ds_read_b64_tr_b16 v[146:147], v240 offset:12288\n"
    "v_mfma_f32_16x16x32_bf16 v[108:111], v[180:183], a[124:127], v[108:111]\n"
    "s_waitcnt lgkmcnt(9)\n"
    "global_load_dwordx2 v[252:253], v250, s[58:59] sc1\n"
    "v_mfma_f32_16x16x32_bf16 v[112:115], v[184:187], a[76:79], v[112:115]\n"
    "ds_read_b64_tr_b16 v[148:149], v241 offset:8192\n"
    "ds_read_b64_tr_b16 v[150:151], v241 offset:12288\n"
    "v_mfma_f32_16x16x32_bf16 v[120:123], v[184:187], a[124:127], v[120:123]\n"
    "ds_read_b64_tr_b16 v[152:153], v242 offset:8192\n"
    "ds_read_b64_tr_b16 v[154:155], v242 offset:12288\n"
    "v_mfma_f32_16x16x32_bf16 v[116:119], v[188:191], a[76:79], v[116:119]\n"
    "ds_read_b64_tr_b16 v[156:157], v243 offset:8192\n"
    "ds_read_b64_tr_b16 v[158:159], v243 offset:12288\n"
    "v_mfma_f32_16x16x32_bf16 v[124:127], v[188:191], a[124:127], v[124:127]\n"
    "s_waitcnt vmcnt(17)\n"
    "s_add_u32 m0, m0, 1024\n"
    "v_cmp_eq_u32 vcc, 7, v205\n"
    "s_nop 1\n"
    "v_cndmask_b32 v237, 0, v204, vcc\n"
    "s_waitcnt lgkmcnt(11)\n"
    "v_mfma_f32_16x16x32_bf16 v[0:3], v[128:131], a[128:131], v[0:3]\n"
    "s_waitcnt lgkmcnt(3)\n"
    "buffer_load_dwordx4 v212, s[76:79], s26 offen lds\n"
    "v_mfma_f32_16x16x32_bf16 v[0:3], v[144:147], a[132:135], v[0:3]\n"
    "ds_read_b64_tr_b16 v[160:161], v240 offset:16384\n"
    "ds_read_b64_tr_b16 v[162:163], v240 offset:20480\n"
    "s_add_u32 s27, s22, 0\n"
    "v_mfma_f32_16x16x32_bf16 v[8:11], v[128:131], a[176:179], v[8:11]\n"
    "ds_read_b64_tr_b16 v[164:165], v241 offset:16384\n"
    "v_cvt_pk_bf16_f32 v64, v64, v65\n"
    "v_mfma_f32_16x16x32_bf16 v[8:11], v[144:147], a[180:183], v[8:11]\n"
    "ds_read_b64_tr_b16 v[166:167], v241 offset:20480\n"
    "s_add_u32 m0, m0, 1024\n"
    "ds_read_b64_tr_b16 v[168:169], v242 offset:16384\n"
    "v_mfma_f32_16x16x32_bf16 v[4:7], v[132:135], a[128:131], v[4:7]\n"
    "buffer_load_dwordx4 v213, s[76:79], s26 offen lds\n"
    "v_mfma_f32_16x16x32_bf16 v[4:7], v[148:151], a[132:135], v[4:7]\n"
    "ds_read_b64_tr_b16 v[170:171], v242 offset:20480\n"
    "v_cvt_pk_bf16_f32 v65, v66, v67\n"
    "v_mfma_f32_16x16x32_bf16 v[12:15], v[132:135], a[176:179], v[12:15]\n"
    "ds_read_b64_tr_b16 v[172:173], v243 offset:16384\n"
    "ds_read_b64_tr_b16 v[174:175], v243 offset:20480\n"
    "v_mfma_f32_16x16x32_bf16 v[12:15], v[148:151], a[180:183], v[12:15]\n"
    "ds_read_b64_tr_b16 v[176:177], v240 offset:24576\n"
    "s_add_u32 m0, m0, 1024\n"
    "v_cvt_pk_bf16_f32 v66, v68, v69\n"
    "v_mfma_f32_16x16x32_bf16 v[16:19], v[136:139], a[128:131], v[16:19]\n"
    "s_waitcnt lgkmcnt(8)\n"
    "buffer_load_dwordx4 v214, s[76:79], s26 offen lds\n"
    "v_mfma_f32_16x16x32_bf16 v[16:19], v[152:155], a[132:135], v[16:19]\n"
    "ds_read_b64_tr_b16 v[178:179], v240 offset:28672\n"
    "ds_read_b64_tr_b16 v[180:181], v241 offset:24576\n"
    "v_mfma_f32_16x16x32_bf16 v[24:27], v[136:139], a[176:179], v[24:27]\n"
    "ds_read_b64_tr_b16 v[182:183], v241 offset:28672\n"
    "v_cvt_pk_bf16_f32 v67, v70, v71\n"
    "v_mfma_f32_16x16x32_bf16 v[24:27], v[152:155], a[180:183], v[24:27]\n"
    "ds_read_b64_tr_b16 v[184:185], v242 offset:24576\n"
    "s_add_u32 m0, m0, 1024\n"
    "ds_read_b64_tr_b16 v[186:187], v242 offset:28672\n"
    "v_mfma_f32_16x16x32_bf16 v[20:23], v[140:143], a[128:131], v[20:23]\n"
    "buffer_load_dwordx4 v215, s[76:79], s26 offen lds\n"
    "v_mfma_f32_16x16x32_bf16 v[20:23], v[156:159], a[132:135], v[20:23]\n"
    "ds_read_b64_tr_b16 v[188:189], v243 offset:24576\n"
    "s_add_u32 s26, s26, 32768\n"
    "v_mfma_f32_16x16x32_bf16 v[28:31], v[140:143], a[176:179], v[28:31]\n"
    "ds_read_b64_tr_b16 v[190:191], v243 offset:28672\n"
    "v_mfma_f32_16x16x32_bf16 v[28:31], v[156:159], a[180:183], v[28:31]\n"
    "s_waitcnt lgkmcnt(11)\n"
    "s_add_u32 m0, m0, 1024\n"
    "buffer_store_dwordx4 v[64:67], v198, s[60:63], s27 offen offset:0 sc1\n"
    "v_cvt_pk_bf16_f32 v72, v72, v73\n"
    "v_mfma_f32_16x16x32_bf16 v[0:3], v[160:163], a[136:139], v[0:3]\n"
    "s_waitcnt lgkmcnt(3)\n"
    "buffer_load_dwordx4 v212, s[76:79], s26 offen lds\n"
    "s_add_u32 s19, s53, 65536\n"
    "s_and_b32 s19, s19, 0x1ffff\n"
    "v_mfma_f32_16x16x32_bf16 v[0:3], v[176:179], a[140:143], v[0:3]\n"
    "v_add_u32 v240, s19, v192\n"
    "v_add_u32 v241, s19, v193\n"
    "v_add_u32 v242, s19, v194\n"
    "v_mfma_f32_16x16x32_bf16 v[8:11], v[160:163], a[184:187], v[8:11]\n"
    "v_add_u32 v243, s19, v195\n"
    "v_add_u32 v244, s19, v196\n"
    "v_mfma_f32_16x16x32_bf16 v[8:11], v[176:179], a[188:191], v[8:11]\n"
    "s_add_u32 m0, m0, 1024\n"
    "v_add_u32 v245, s19, v197\n"
    "v_add_u32 v246, s19, v199\n"
    "v_mfma_f32_16x16x32_bf16 v[4:7], v[164:167], a[136:139], v[4:7]\n"
    "buffer_load_dwordx4 v213, s[76:79], s26 offen lds\n"
    "v_mfma_f32_16x16x32_bf16 v[4:7], v[180:183], a[140:143], v[4:7]\n"
    "v_add_u32 v247, s19, v200\n"
    "ds_read_b64_tr_b16 v[128:129], v240 offset:0\n"
    "v_mfma_f32_16x16x32_bf16 v[12:15], v[164:167], a[184:187], v[12:15]\n"
    "ds_read_b64_tr_b16 v[130:131], v240 offset:4096\n"
    "ds_read_b64_tr_b16 v[132:133], v241 offset:0\n"
    "v_mfma_f32_16x16x32_bf16 v[12:15], v[180:183], a[188:191], v[12:15]\n"
    "ds_read_b64_tr_b16 v[134:135], v241 offset:4096\n"
    "s_add_u32 m0, m0, 1024\n"
    "ds_read_b64_tr_b16 v[136:137], v242 offset:0\n"
    "ds_read_b64_tr_b16 v[138:139], v242 offset:4096\n"
    "v_mfma_f32_16x16x32_bf16 v[16:19], v[168:171], a[136:139], v[16:19]\n"
    "s_waitcnt lgkmcnt(5)\n"
    "buffer_load_dwordx4 v214, s[76:79], s26 offen lds\n"
    "v_mfma_f32_16x16x32_bf16 v[16:19], v[184:187], a[140:143], v[16:19]\n"
    "ds_read_b64_tr_b16 v[140:141], v243 offset:0\n"
    "ds_read_b64_tr_b16 v[142:143], v243 offset:4096\n"
    "v_mfma_f32_16x16x32_bf16 v[24:27], v[168:171], a[184:187], v[24:27]\n"
    "ds_read_b64_tr_b16 v[144:145], v240 offset:8192\n"
    "ds_read_b64_tr_b16 v[146:147], v240 offset:12288\n"
    "v_mfma_f32_16x16x32_bf16 v[24:27], v[184:187], a[188:191], v[24:27]\n"
    "ds_read_b64_tr_b16 v[148:149], v241 offset:8192\n"
    "s_add_u32 m0, m0, 1024\n"
    "ds_read_b64_tr_b16 v[150:151], v241 offset:12288\n"
    "v_mfma_f32_16x16x32_bf16 v[20:23], v[172:175], a[136:139], v[20:23]\n"
    "buffer_load_dwordx4 v215, s[76:79], s26 offen lds\n"
    "v_mfma_f32_16x16x32_bf16 v[20:23], v[188:191], a[140:143], v[20:23]\n"
    "ds_read_b64_tr_b16 v[152:153], v242 offset:8192\n"
    "s_add_u32 s26, s26, 32768\n"
    "ds_read_b64_tr_b16 v[154:155], v242 offset:12288\n"
    "v_mfma_f32_16x16x32_bf16 v[28:31], v[172:175], a[184:187], v[28:31]\n"
    "ds_read_b64_tr_b16 v[156:157], v243 offset:8192\n"
    "ds_read_b64_tr_b16 v[158:159], v243 offset:12288\n"
    "v_mfma_f32_16x16x32_bf16 v[28:31], v[188:191], a[188:191], v[28:31]\n"
    "s_waitcnt vmcnt(18)\n"
    "v_cvt_pk_bf16_f32 v73, v74, v75\n"
    "v_cvt_pk_bf16_f32 v74, v76, v77\n"
    "s_waitcnt lgkmcnt(11)\n"
    "v_mfma_f32_16x16x32_bf16 v[0:3], v[128:131], a[144:147], v[0:3]\n"
    "s_waitcnt vmcnt(9)\n"
    "v_cmp_ne_u64 s[98:99], s[28:29], v[252:253]\n"
    "s_nop 0\n"
    "s_cmp_lg_u64 s[98:99], 0\n"
    "s_cbranch_scc1 L_spin18_%=\n"
    "L_ok19_%=:\n"
    "ds_read_b64_tr_b16 v[160:161], v240 offset:16384\n"
    "s_waitcnt lgkmcnt(4)\n"
    "v_mfma_f32_16x16x32_bf16 v[0:3], v[144:147], a[148:151], v[0:3]\n"
    "buffer_load_dwordx4 a[32:35], v208, s[72:75], s25 offen offset:0 sc1\n"
    "v_mfma_f32_16x16x32_bf16 v[8:11], v[128:131], a[192:195], v[8:11]\n"
    "ds_read_b64_tr_b16 v[162:163], v240 offset:20480\n"
    "ds_read_b64_tr_b16 v[164:165], v241 offset:16384\n"
    "v_mfma_f32_16x16x32_bf16 v[8:11], v[144:147], a[196:199], v[8:11]\n"
    "ds_read_b64_tr_b16 v[166:167], v241 offset:20480\n"
    "v_cvt_pk_bf16_f32 v75, v78, v79\n"
    "v_mfma_f32_16x16x32_bf16 v[4:7], v[132:135], a[144:147], v[4:7]\n"
    "ds_read_b64_tr_b16 v[168:169], v242 offset:16384\n"
    "v_mfma_f32_16x16x32_bf16 v[4:7], v[148:151], a[148:151], v[4:7]\n"
    "ds_read_b64_tr_b16 v[170:171], v242 offset:20480\n"
    "ds_read_b64_tr_b16 v[172:173], v243 offset:16384\n"
    "v_mfma_f32_16x16x32_bf16 v[12:15], v[132:135], a[192:195], v[12:15]\n"
    "ds_read_b64_tr_b16 v[174:175], v243 offset:20480\n"
    "v_mfma_f32_16x16x32_bf16 v[12:15], v[148:151], a[196:199], v[12:15]\n"
    "buffer_load_dwordx4 a[36:39], v209, s[72:75], s25 offen offset:0 sc1\n"
    "v_mfma_f32_16x16x32_bf16 v[16:19], v[136:139], a[144:147], v[16:19]\n"
    "s_waitcnt lgkmcnt(7)\n"
    "ds_read_b64_tr_b16 v[176:177], v240 offset:24576\n"
    "ds_read_b64_tr_b16 v[178:179], v240 offset:28672\n"
    "v_mfma_f32_16x16x32_bf16 v[16:19], v[152:155], a[148:151], v[16:19]\n"
    "ds_read_b64_tr_b16 v[180:181], v241 offset:24576\n"
    "v_mfma_f32_16x16x32_bf16 v[24:27], v[136:139], a[192:195], v[24:27]\n"
    "ds_read_b64_tr_b16 v[182:183], v241 offset:28672\n"
    "v_mfma_f32_16x16x32_bf16 v[24:27], v[152:155], a[196:199], v[24:27]\n"
    "ds_read_b64_tr_b16 v[184:185], v242 offset:24576\n"
    "ds_read_b64_tr_b16 v[186:187], v242 offset:28672\n"
    "v_mfma_f32_16x16x32_bf16 v[20:23], v[140:143], a[144:147], v[20:23]\n"
    "ds_read_b64_tr_b16 v[188:189], v243 offset:24576\n"
    "v_mfma_f32_16x16x32_bf16 v[20:23], v[156:159], a[148:151], v[20:23]\n"
    "ds_read_b64_tr_b16 v[190:191], v243 offset:28672\n"
    "v_mfma_f32_16x16x32_bf16 v[28:31], v[140:143], a[192:195], v[28:31]\n"
    "buffer_load_dwordx4 a[40:43], v210, s[72:75], s25 offen offset:0 sc1\n"
    "v_mfma_f32_16x16x32_bf16 v[28:31], v[156:159], a[196:199], v[28:31]\n"
    "s_waitcnt lgkmcnt(11)\n"
    "buffer_store_dwordx4 v[72:75], v198, s[60:63], s27 offen offset:1024 sc1\n"
    "v_cvt_pk_bf16_f32 v80, v80, v81\n"
    "v_mfma_f32_16x16x32_bf16 v[0:3], v[160:163], a[152:155], v[0:3]\n"
    "s_waitcnt lgkmcnt(3)\n"
    "s_add_u32 s19, s53, 98304\n"
    "s_and_b32 s19, s19, 0x1ffff\n"
    "v_add_u32 v240, s19, v192\n"
    "v_add_u32 v241, s19, v193\n"
    "v_cvt_pk_bf16_f32 v81, v82, v83\n"
    "v_mfma_f32_16x16x32_bf16 v[0:3], v[176:179], a[156:159], v[0:3]\n"
    "v_add_u32 v242, s19, v194\n"
    "v_add_u32 v243, s19, v195\n"
    "v_cvt_pk_bf16_f32 v82, v84, v85\n"
    "v_mfma_f32_16x16x32_bf16 v[8:11], v[160:163], a[200:203], v[8:11]\n"
    "v_add_u32 v244, s19, v196\n"
    "v_add_u32 v245, s19, v197\n"
    "v_cvt_pk_bf16_f32 v83, v86, v87\n"
    "v_mfma_f32_16x16x32_bf16 v[8:11], v[176:179], a[204:207], v[8:11]\n"
    "v_add_u32 v246, s19, v199\n"
    "v_add_u32 v247, s19, v200\n"
    "v_mfma_f32_16x16x32_bf16 v[4:7], v[164:167], a[152:155], v[4:7]\n"
    "ds_read_b64_tr_b16 v[128:129], v240 offset:0\n"
    "ds_read_b64_tr_b16 v[130:131], v240 offset:4096\n"
    "v_mfma_f32_16x16x32_bf16 v[4:7], v[180:183], a[156:159], v[4:7]\n"
    "buffer_load_dwordx4 a[44:47], v211, s[72:75], s25 offen offset:0 sc1\n"
    "v_mfma_f32_16x16x32_bf16 v[12:15], v[164:167], a[200:203], v[12:15]\n"
    "ds_read_b64_tr_b16 v[132:133], v241 offset:0\n"
    "ds_read_b64_tr_b16 v[134:135], v241 offset:4096\n"
    "v_mfma_f32_16x16x32_bf16 v[12:15], v[180:183], a[204:207], v[12:15]\n"
    "ds_read_b64_tr_b16 v[136:137], v242 offset:0\n"
    "buffer_store_dwordx4 v[80:83], v198, s[60:63], s27 offen offset:2048 sc1\n"
    "v_mfma_f32_16x16x32_bf16 v[16:19], v[168:171], a[152:155], v[16:19]\n"
    "s_waitcnt lgkmcnt(4)\n"
    "ds_read_b64_tr_b16 v[138:139], v242 offset:4096\n"
    "ds_read_b64_tr_b16 v[140:141], v243 offset:0\n"
    "v_cvt_pk_bf16_f32 v88, v88, v89\n"
    "v_mfma_f32_16x16x32_bf16 v[16:19], v[184:187], a[156:159], v[16:19]\n"
    "ds_read_b64_tr_b16 v[142:143], v243 offset:4096\n"
    "ds_read_b64_tr_b16 v[144:145], v240 offset:8192\n"
    "v_mfma_f32_16x16x32_bf16 v[24:27], v[168:171], a[200:203], v[24:27]\n"
    "ds_read_b64_tr_b16 v[146:147], v240 offset:12288\n"
    "ds_read_b64_tr_b16 v[148:149], v241 offset:8192\n"
    "v_mfma_f32_16x16x32_bf16 v[24:27], v[184:187], a[204:207], v[24:27]\n"
    "ds_read_b64_tr_b16 v[150:151], v241 offset:12288\n"
    "ds_read_b64_tr_b16 v[152:153], v242 offset:8192\n"
    "v_mfma_f32_16x16x32_bf16 v[20:23], v[172:175], a[152:155], v[20:23]\n"
    "buffer_load_dwordx4 a[48:51], v208, s[72:75], s25 offen offset:1024 sc1\n"
    "v_mfma_f32_16x16x32_bf16 v[20:23], v[188:191], a[156:159], v[20:23]\n"
    "ds_read_b64_tr_b16 v[154:155], v242 offset:12288\n"
    "ds_read_b64_tr_b16 v[156:157], v243 offset:8192\n"
    "v_mfma_f32_16x16x32_bf16 v[28:31], v[172:175], a[200:203], v[28:31]\n"
    "ds_read_b64_tr_b16 v[158:159], v243 offset:12288\n"
    "v_cvt_pk_bf16_f32 v89, v90, v91\n"
    "v_mfma_f32_16x16x32_bf16 v[28:31], v[188:191], a[204:207], v[28:31]\n"
    "s_waitcnt vmcnt(17)\n"
    "v_cvt_pk_bf16_f32 v90, v92, v93\n"
    "v_cvt_pk_bf16_f32 v91, v94, v95\n"
    "s_waitcnt lgkmcnt(11)\n"
    "v_mfma_f32_16x16x32_bf16 v[0:3], v[128:131], a[160:163], v[0:3]\n"
    "s_waitcnt lgkmcnt(3)\n"
    "ds_read_b64_tr_b16 v[160:161], v240 offset:16384\n"
    "ds_read_b64_tr_b16 v[162:163], v240 offset:20480\n"
    "v_mfma_f32_16x16x32_bf16 v[0:3], v[144:147], a[164:167], v[0:3]\n"
    "ds_read_b64_tr_b16 v[164:165], v241 offset:16384\n"
    "v_mfma_f32_16x16x32_bf16 v[8:11], v[128:131], a[208:211], v[8:11]\n"
    "ds_read_b64_tr_b16 v[166:167], v241 offset:20480\n"
    "v_mfma_f32_16x16x32_bf16 v[8:11], v[144:147], a[212:215], v[8:11]\n"
    "buffer_load_dwordx4 a[52:55], v209, s[72:75], s25 offen offset:1024 sc1\n"
    "v_mfma_f32_16x16x32_bf16 v[4:7], v[132:135], a[160:163], v[4:7]\n"
    "ds_read_b64_tr_b16 v[168:169], v242 offset:16384\n"
    "v_mfma_f32_16x16x32_bf16 v[4:7], v[148:151], a[164:167], v[4:7]\n"
    "ds_read_b64_tr_b16 v[170:171], v242 offset:20480\n"
    "ds_read_b64_tr_b16 v[172:173], v243 offset:16384\n"
    "v_mfma_f32_16x16x32_bf16 v[12:15], v[132:135], a[208:211], v[12:15]\n"
    "ds_read_b64_tr_b16 v[174:175], v243 offset:20480\n"
    "v_mfma_f32_16x16x32_bf16 v[12:15], v[148:151], a[212:215], v[12:15]\n"
    "ds_read_b64_tr_b16 v[176:177], v240 offset:24576\n"
    "v_mfma_f32_16x16x32_bf16 v[16:19], v[136:139], a[160:163], v[16:19]\n"
    "s_waitcnt lgkmcnt(8)\n"
    "ds_read_b64_tr_b16 v[178:179], v240 offset:28672\n"
    "v_mfma_f32_16x16x32_bf16 v[16:19], v[152:155], a[164:167], v[16:19]\n"
    "ds_read_b64_tr_b16 v[180:181], v241 offset:24576\n"
    "ds_read_b64_tr_b16 v[182:183], v241 offset:28672\n"
    "v_mfma_f32_16x16x32_bf16 v[24:27], v[136:139], a[208:211], v[24:27]\n"
    "buffer_load_dwordx4 a[56:59], v210, s[72:75], s25 offen offset:1024 sc1\n"
    "v_mfma_f32_16x16x32_bf16 v[24:27], v[152:155], a[212:215], v[24:27]\n"
    "ds_read_b64_tr_b16 v[184:185], v242 offset:24576\n"
    "v_mfma_f32_16x16x32_bf16 v[20:23], v[140:143], a[160:163], v[20:23]\n"
    "ds_read_b64_tr_b16 v[186:187], v242 offset:28672\n"
    "v_mfma_f32_16x16x32_bf16 v[20:23], v[156:159], a[164:167], v[20:23]\n"
    "ds_read_b64_tr_b16 v[188:189], v243 offset:24576\n"
    "v_mfma_f32_16x16x32_bf16 v[28:31], v[140:143], a[208:211], v[28:31]\n"
    "ds_read_b64_tr_b16 v[190:191], v243 offset:28672\n"
    "v_mfma_f32_16x16x32_bf16 v[28:31], v[156:159], a[212:215], v[28:31]\n"
    "s_waitcnt lgkmcnt(11)\n"
    "buffer_store_dwordx4 v[88:91], v198, s[60:63], s27 offen offset:3072 sc1\n"
    "s_add_u32 s27, s22, 4096\n"
    "v_cvt_pk_bf16_f32 v96, v96, v97\n"
    "v_mfma_f32_16x16x32_bf16 v[0:3], v[160:163], a[168:171], v[0:3]\n"
    "s_waitcnt lgkmcnt(3)\n"
    "s_add_u32 s19, s53, 32768\n"
    "s_and_b32 s19, s19, 0x1ffff\n"
    "v_add_u32 v240, s19, v192\n"
    "v_add_u32 v241, s19, v193\n"
    "v_cvt_pk_bf16_f32 v97, v98, v99\n"
    "v_mfma_f32_16x16x32_bf16 v[0:3], v[176:179], a[172:175], v[0:3]\n"
    "buffer_load_dwordx4 a[60:63], v211, s[72:75], s25 offen offset:1024 sc1\n"
    "v_cvt_pk_bf16_f32 v98, v100, v101\n"
    "v_mfma_f32_16x16x32_bf16 v[8:11], v[160:163], a[216:219], v[8:11]\n"
    "v_add_u32 v242, s19, v194\n"
    "v_add_u32 v243, s19, v195\n"
    "v_cvt_pk_bf16_f32 v99, v102, v103\n"
    "v_mfma_f32_16x16x32_bf16 v[8:11], v[176:179], a[220:223], v[8:11]\n"
    "v_add_u32 v244, s19, v196\n"
    "v_add_u32 v245, s19, v197\n"
    "v_mfma_f32_16x16x32_bf16 v[4:7], v[164:167], a[168:171], v[4:7]\n"
    "v_add_u32 v246, s19, v199\n"
    "v_add_u32 v247, s19, v200\n"
    "v_mfma_f32_16x16x32_bf16 v[4:7], v[180:183], a[172:175], v[4:7]\n"
    "ds_read_b64_tr_b16 v[128:129], v244 offset:0\n"
    "ds_read_b64_tr_b16 v[130:131], v244 offset:4096\n"
    "v_mfma_f32_16x16x32_bf16 v[12:15], v[164:167], a[216:219], v[12:15]\n"
    "ds_read_b64_tr_b16 v[132:133], v245 offset:0\n"
    "ds_read_b64_tr_b16 v[134:135], v245 offset:4096\n"
    "v_mfma_f32_16x16x32_bf16 v[12:15], v[180:183], a[220:223], v[12:15]\n"
    "ds_read_b64_tr_b16 v[136:137], v246 offset:0\n"
    "buffer_store_dwordx4 v[96:99], v198, s[60:63], s27 offen offset:0 sc1\n"
    "v_mfma_f32_16x16x32_bf16 v[16:19], v[168:171], a[168:171], v[16:19]\n"
    "s_waitcnt lgkmcnt(4)\n"
    "buffer_load_dwordx4 a[64:67], v208, s[72:75], s25 offen offset:2048 sc1\n"
    "v_cvt_pk_bf16_f32 v104, v104, v105\n"
    "v_mfma_f32_16x16x32_bf16 v[16:19], v[184:187], a[172:175], v[16:19]\n"
    "ds_read_b64_tr_b16 v[138:139], v246 offset:4096\n"
    "ds_read_b64_tr_b16 v[140:141], v247 offset:0\n"
    "v_cvt_pk_bf16_f32 v105, v106, v107\n"
    "v_mfma_f32_16x16x32_bf16 v[24:27], v[168:171], a[216:219], v[24:27]\n"
    "ds_read_b64_tr_b16 v[142:143], v247 offset:4096\n"
    "ds_read_b64_tr_b16 v[144:145], v244 offset:8192\n"
    "v_cvt_pk_bf16_f32 v106, v108, v109\n"
    "v_mfma_f32_16x16x32_bf16 v[24:27], v[184:187], a[220:223], v[24:27]\n"
    "ds_read_b64_tr_b16 v[146:147], v244 offset:12288\n"
    "ds_read_b64_tr_b16 v[148:149], v245 offset:8192\n"
    "v_cvt_pk_bf16_f32 v107, v110, v111\n"
    "v_mfma_f32_16x16x32_bf16 v[20:23], v[172:175], a[168:171], v[20:23]\n"
    "ds_read_b64_tr_b16 v[150:151], v245 offset:12288\n"
    "ds_read_b64_tr_b16 v[152:153], v246 offset:8192\n"
    "v_mfma_f32_16x16x32_bf16 v[20:23], v[188:191], a[172:175], v[20:23]\n"
    "ds_read_b64_tr_b16 v[154:155], v246 offset:12288\n"
    "ds_read_b64_tr_b16 v[156:157], v247 offset:8192\n"
    "v_mfma_f32_16x16x32_bf16 v[28:31], v[172:175], a[216:219], v[28:31]\n"
    "ds_read_b64_tr_b16 v[158:159], v247 offset:12288\n"
    "buffer_store_dwordx4 v[104:107], v198, s[60:63], s27 offen offset:1024 sc1\n"
    "v_mfma_f32_16x16x32_bf16 v[28:31], v[188:191], a[220:223], v[28:31]\n"
    "s_waitcnt lgkmcnt(11)\n"
    "buffer_load_dwordx4 a[68:71], v209, s[72:75], s25 offen offset:2048 sc1\n"
    "v_mfma_f32_16x16x32_bf16 v[32:35], v[128:131], a[128:131], v[32:35]\n"
    "s_waitcnt lgkmcnt(3)\n"
    "ds_read_b64_tr_b16 v[160:161], v244 offset:16384\n"
    "ds_read_b64_tr_b16 v[162:163], v244 offset:20480\n"
    "v_mfma_f32_16x16x32_bf16 v[32:35], v[144:147], a[132:135], v[32:35]\n"
    "ds_read_b64_tr_b16 v[164:165], v245 offset:16384\n"
    "v_cvt_pk_bf16_f32 v112, v112, v113\n"
    "v_mfma_f32_16x16x32_bf16 v[40:43], v[128:131], a[176:179], v[40:43]\n"
    "ds_read_b64_tr_b16 v[166:167], v245 offset:20480\n"
    "v_cvt_pk_bf16_f32 v113, v114, v115\n"
    "v_mfma_f32_16x16x32_bf16 v[40:43], v[144:147], a[180:183], v[40:43]\n"
    "ds_read_b64_tr_b16 v[168:169], v246 offset:16384\n"
    "v_cvt_pk_bf16_f32 v114, v116, v117\n"
    "v_mfma_f32_16x16x32_bf16 v[36:39], v[132:135], a[128:131], v[36:39]\n"
    "ds_read_b64_tr_b16 v[170:171], v246 offset:20480\n"
    "ds_read_b64_tr_b16 v[172:173], v247 offset:16384\n"
    "v_mfma_f32_16x16x32_bf16 v[36:39], v[148:151], a[132:135], v[36:39]\n"
    "ds_read_b64_tr_b16 v[174:175], v247 offset:20480\n"
    "v_cvt_pk_bf16_f32 v115, v118, v119\n"
    "v_mfma_f32_16x16x32_bf16 v[44:47], v[132:135], a[176:179], v[44:47]\n"
    "buffer_load_dwordx4 a[72:75], v210, s[72:75], s25 offen offset:2048 sc1\n"
    "v_mfma_f32_16x16x32_bf16 v[44:47], v[148:151], a[180:183], v[44:47]\n"
    "ds_read_b64_tr_b16 v[176:177], v244 offset:24576\n"
    "v_mfma_f32_16x16x32_bf16 v[48:51], v[136:139], a[128:131], v[48:51]\n"
    "s_waitcnt lgkmcnt(8)\n"
    "ds_read_b64_tr_b16 v[178:179], v244 offset:28672\n"
    "v_mfma_f32_16x16x32_bf16 v[48:51], v[152:155], a[132:135], v[48:51]\n"
    "ds_read_b64_tr_b16 v[180:181], v245 offset:24576\n"
    "ds_read_b64_tr_b16 v[182:183], v245 offset:28672\n"
    "v_mfma_f32_16x16x32_bf16 v[56:59], v[136:139], a[176:179], v[56:59]\n"
    "ds_read_b64_tr_b16 v[184:185], v246 offset:24576\n"
    "v_mfma_f32_16x16x32_bf16 v[56:59], v[152:155], a[180:183], v[56:59]\n"
    "ds_read_b64_tr_b16 v[186:187], v246 offset:28672\n"
    "v_mfma_f32_16x16x32_bf16 v[52:55], v[140:143], a[128:131], v[52:55]\n"
    "ds_read_b64_tr_b16 v[188:189], v247 offset:24576\n"
    "v_mfma_f32_16x16x32_bf16 v[52:55], v[156:159], a[132:135], v[52:55]\n"
    "buffer_load_dwordx4 a[76:79], v211, s[72:75], s25 offen offset:2048 sc1\n"
    "v_mfma_f32_16x16x32_bf16 v[60:63], v[140:143], a[176:179], v[60:63]\n"
    "ds_read_b64_tr_b16 v[190:191], v247 offset:28672\n"
    "v_mfma_f32_16x16x32_bf16 v[60:63], v[156:159], a[180:183], v[60:63]\n"
    "s_waitcnt lgkmcnt(11)\n"
    "buffer_store_dwordx4 v[112:115], v198, s[60:63], s27 offen offset:2048 sc1\n"
    "v_cvt_pk_bf16_f32 v120, v120, v121\n"
    "v_cvt_pk_bf16_f32 v121, v122, v123\n"
    "v_cvt_pk_bf16_f32 v122, v124, v125\n"
    "v_cvt_pk_bf16_f32 v123, v126, v127\n"
    "buffer_store_dwordx4 v[120:123], v198, s[60:63], s27 offen offset:3072 sc1\n"
    "v_mfma_f32_16x16x32_bf16 v[32:35], v[160:163], a[136:139], v[32:35]\n"
    "s_waitcnt lgkmcnt(3)\n"
    "s_add_u32 s19, s53, 65536\n"
    "s_and_b32 s19, s19, 0x1ffff\n"
    "v_add_u32 v240, s19, v192\n"
    "v_add_u32 v241, s19, v193\n"
    "s_add_u32 s27, s22, 274432\n"
    "v_mfma_f32_16x16x32_bf16 v[32:35], v[176:179], a[140:143], v[32:35]\n"
    "v_add_u32 v242, s19, v194\n"
    "v_add_u32 v243, s19, v195\n"
    "v_mfma_f32_16x16x32_bf16 v[40:43], v[160:163], a[184:187], v[40:43]\n"
    "v_add_u32 v244, s19, v196\n"
    "v_add_u32 v245, s19, v197\n"
    "v_mfma_f32_16x16x32_bf16 v[40:43], v[176:179], a[188:191], v[40:43]\n"
    "v_add_u32 v246, s19, v199\n"
    "v_add_u32 v247, s19, v200\n"
    "v_mfma_f32_16x16x32_bf16 v[36:39], v[164:167], a[136:139], v[36:39]\n"
    "buffer_load_dwordx4 a[80:83], v208, s[72:75], s25 offen offset:3072 sc1\n"
    "v_mfma_f32_16x16x32_bf16 v[36:39], v[180:183], a[140:143], v[36:39]\n"
    "ds_read_b64_tr_b16 v[128:129], v244 offset:0\n"
    "ds_read_b64_tr_b16 v[130:131], v244 offset:4096\n"
    "v_mfma_f32_16x16x32_bf16 v[44:47], v[164:167], a[184:187], v[44:47]\n"
    "ds_read_b64_tr_b16 v[132:133], v245 offset:0\n"
    "ds_read_b64_tr_b16 v[134:135], v245 offset:4096\n"
    "v_mfma_f32_16x16x32_bf16 v[44:47], v[180:183], a[188:191], v[44:47]\n"
    "ds_read_b64_tr_b16 v[136:137], v246 offset:0\n"
    "v_cvt_pk_bf16_f32 v0, v0, v1\n"
    "v_mfma_f32_16x16x32_bf16 v[48:51], v[168:171], a[136:139], v[48:51]\n"
    "s_waitcnt lgkmcnt(4)\n"
    "ds_read_b64_tr_b16 v[138:139], v246 offset:4096\n"
    "ds_read_b64_tr_b16 v[140:141], v247 offset:0\n"
    "v_mfma_f32_16x16x32_bf16 v[48:51], v[184:187], a[140:143], v[48:51]\n"
    "ds_read_b64_tr_b16 v[142:143], v247 offset:4096\n"
    "ds_read_b64_tr_b16 v[144:145], v244 offset:8192\n"
    "v_mfma_f32_16x16x32_bf16 v[56:59], v[168:171], a[184:187], v[56:59]\n"
    "ds_read_b64_tr_b16 v[146:147], v244 offset:12288\n"
    "ds_read_b64_tr_b16 v[148:149], v245 offset:8192\n"
    "v_mfma_f32_16x16x32_bf16 v[56:59], v[184:187], a[188:191], v[56:59]\n"
    "buffer_load_dwordx4 a[84:87], v209, s[72:75], s25 offen offset:3072 sc1\n"
    "v_mfma_f32_16x16x32_bf16 v[52:55], v[172:175], a[136:139], v[52:55]\n"
    "ds_read_b64_tr_b16 v[150:151], v245 offset:12288\n"
    "ds_read_b64_tr_b16 v[152:153], v246 offset:8192\n"
    "v_mfma_f32_16x16x32_bf16 v[52:55], v[188:191], a[140:143], v[52:55]\n"
    "ds_read_b64_tr_b16 v[154:155], v246 offset:12288\n"
    "ds_read_b64_tr_b16 v[156:157], v247 offset:8192\n"
    "v_mfma_f32_16x16x32_bf16 v[60:63], v[172:175], a[184:187], v[60:63]\n"
    "ds_read_b64_tr_b16 v[158:159], v247 offset:12288\n"
    "v_cvt_pk_bf16_f32 v1, v2, v3\n"
    "v_mfma_f32_16x16x32_bf16 v[60:63], v[188:191], a[188:191], v[60:63]\n"
    "s_waitcnt lgkmcnt(11)\n"
    "v_cvt_pk_bf16_f32 v2, v4, v5\n"
    "v_mfma_f32_16x16x32_bf16 v[32:35], v[128:131], a[144:147], v[32:35]\n"
    "s_waitcnt lgkmcnt(3)\n"
    "ds_read_b64_tr_b16 v[160:161], v244 offset:16384\n"
    "ds_read_b64_tr_b16 v[162:163], v244 offset:20480\n"
    "v_mfma_f32_16x16x32_bf16 v[32:35], v[144:147], a[148:151], v[32:35]\n"
    "ds_read_b64_tr_b16 v[164:165], v245 offset:16384\n"
    "v_mfma_f32_16x16x32_bf16 v[40:43], v[128:131], a[192:195], v[40:43]\n"
    "buffer_load_dwordx4 a[88:91], v210, s[72:75], s25 offen offset:3072 sc1\n"
    "v_mfma_f32_16x16x32_bf16 v[40:43], v[144:147], a[196:199], v[40:43]\n"
    "ds_read_b64_tr_b16 v[166:167], v245 offset:20480\n"
    "v_cvt_pk_bf16_f32 v3, v6, v7\n"
    "v_mfma_f32_16x16x32_bf16 v[36:39], v[132:135], a[144:147], v[36:39]\n"
    "ds_read_b64_tr_b16 v[168:169], v246 offset:16384\n"
    "v_mfma_f32_16x16x32_bf16 v[36:39], v[148:151], a[148:151], v[36:39]\n"
    "ds_read_b64_tr_b16 v[170:171], v246 offset:20480\n"
    "ds_read_b64_tr_b16 v[172:173], v247 offset:16384\n"
    "v_mfma_f32_16x16x32_bf16 v[44:47], v[132:135], a[192:195], v[44:47]\n"
    "ds_read_b64_tr_b16 v[174:175], v247 offset:20480\n"
    "v_mfma_f32_16x16x32_bf16 v[44:47], v[148:151], a[196:199], v[44:47]\n"
    "ds_read_b64_tr_b16 v[176:177], v244 offset:24576\n"
    "v_mfma_f32_16x16x32_bf16 v[48:51], v[136:139], a[144:147], v[48:51]\n"
    "s_waitcnt lgkmcnt(8)\n"
    "ds_read_b64_tr_b16 v[178:179], v244 offset:28672\n"
    "v_mfma_f32_16x16x32_bf16 v[48:51], v[152:155], a[148:151], v[48:51]\n"
    "buffer_load_dwordx4 a[92:95], v211, s[72:75], s25 offen offset:3072 sc1\n"
    "v_mfma_f32_16x16x32_bf16 v[56:59], v[136:139], a[192:195], v[56:59]\n"
    "ds_read_b64_tr_b16 v[180:181], v245 offset:24576\n"
    "ds_read_b64_tr_b16 v[182:183], v245 offset:28672\n"
    "v_mfma_f32_16x16x32_bf16 v[56:59], v[152:155], a[196:199], v[56:59]\n"
    "ds_read_b64_tr_b16 v[184:185], v246 offset:24576\n"
    "v_mfma_f32_16x16x32_bf16 v[52:55], v[140:143], a[144:147], v[52:55]\n"
    "ds_read_b64_tr_b16 v[186:187], v246 offset:28672\n"
    "v_mfma_f32_16x16x32_bf16 v[52:55], v[156:159], a[148:151], v[52:55]\n"
    "ds_read_b64_tr_b16 v[188:189], v247 offset:24576\n"
    "v_mfma_f32_16x16x32_bf16 v[60:63], v[140:143], a[192:195], v[60:63]\n"
    "ds_read_b64_tr_b16 v[190:191], v247 offset:28672\n"
    "v_mfma_f32_16x16x32_bf16 v[60:63], v[156:159], a[196:199], v[60:63]\n"
    "s_waitcnt lgkmcnt(11)\n"
    "buffer_store_dwordx4 v[0:3], v198, s[60:63], s27 offen offset:0 sc1\n"
    "v_mfma_f32_16x16x32_bf16 v[32:35], v[160:163], a[152:155], v[32:35]\n"
    "s_waitcnt lgkmcnt(3)\n"
    "s_add_u32 s19, s53, 98304\n"
    "s_and_b32 s19, s19, 0x1ffff\n"
    "v_add_u32 v240, s19, v192\n"
    "v_add_u32 v241, s19, v193\n"
    "s_lshl_b32 s14, s13, 2\n"
    "s_add_u32 s14, s14, s40\n"
    "s_lshl_b32 s14, s14, 6\n"
    "s_add_u32 s14, s14, 20480\n"
    "s_add_u32 s14, s14, 0x3400000\n"
    "v_mfma_f32_16x16x32_bf16 v[32:35], v[176:179], a[156:159], v[32:35]\n"
    "v_add_u32 v242, s19, v194\n"
    "v_add_u32 v243, s19, v195\n"
    "v_mfma_f32_16x16x32_bf16 v[40:43], v[160:163], a[200:203], v[40:43]\n"
    "v_add_u32 v244, s19, v196\n"
    "v_cvt_pk_bf16_f32 v8, v8, v9\n"
    "v_mfma_f32_16x16x32_bf16 v[40:43], v[176:179], a[204:207], v[40:43]\n"
    "v_add_u32 v245, s19, v197\n"
    "v_add_u32 v246, s19, v199\n"
    "v_mfma_f32_16x16x32_bf16 v[36:39], v[164:167], a[152:155], v[36:39]\n"
    "v_add_u32 v247, s19, v200\n"
    "v_cvt_pk_bf16_f32 v9, v10, v11\n"
    "v_mfma_f32_16x16x32_bf16 v[36:39], v[180:183], a[156:159], v[36:39]\n"
    "ds_read_b64_tr_b16 v[128:129], v244 offset:0\n"
    "ds_read_b64_tr_b16 v[130:131], v244 offset:4096\n"
    "v_mfma_f32_16x16x32_bf16 v[44:47], v[164:167], a[200:203], v[44:47]\n"
    "ds_read_b64_tr_b16 v[132:133], v245 offset:0\n"
    "ds_read_b64_tr_b16 v[134:135], v245 offset:4096\n"
    "v_mfma_f32_16x16x32_bf16 v[44:47], v[180:183], a[204:207], v[44:47]\n"
    "ds_read_b64_tr_b16 v[136:137], v246 offset:0\n"
    "v_cvt_pk_bf16_f32 v10, v12, v13\n"
    "v_mfma_f32_16x16x32_bf16 v[48:51], v[168:171], a[152:155], v[48:51]\n"
    "s_waitcnt lgkmcnt(4)\n"
    "ds_read_b64_tr_b16 v[138:139], v246 offset:4096\n"
    "ds_read_b64_tr_b16 v[140:141], v247 offset:0\n"
    "v_mfma_f32_16x16x32_bf16 v[48:51], v[184:187], a[156:159], v[48:51]\n"
    "ds_read_b64_tr_b16 v[142:143], v247 offset:4096\n"
    "v_cvt_pk_bf16_f32 v11, v14, v15\n"
    "v_mfma_f32_16x16x32_bf16 v[56:59], v[168:171], a[200:203], v[56:59]\n"
    "ds_read_b64_tr_b16 v[144:145], v244 offset:8192\n"
    "ds_read_b64_tr_b16 v[146:147], v244 offset:12288\n"
    "v_mfma_f32_16x16x32_bf16 v[56:59], v[184:187], a[204:207], v[56:59]\n"
    "ds_read_b64_tr_b16 v[148:149], v245 offset:8192\n"
    "ds_read_b64_tr_b16 v[150:151], v245 offset:12288\n"
    "v_mfma_f32_16x16x32_bf16 v[52:55], v[172:175], a[152:155], v[52:55]\n"
    "ds_read_b64_tr_b16 v[152:153], v246 offset:8192\n"
    "v_min_u32 v250, 7, v255\n"
    "v_mfma_f32_16x16x32_bf16 v[52:55], v[188:191], a[156:159], v[52:55]\n"
    "ds_read_b64_tr_b16 v[154:155], v246 offset:12288\n"
    "ds_read_b64_tr_b16 v[156:157], v247 offset:8192\n"
    "v_mfma_f32_16x16x32_bf16 v[60:63], v[172:175], a[200:203], v[60:63]\n"
    "ds_read_b64_tr_b16 v[158:159], v247 offset:12288\n"
    "v_lshlrev_b32 v250, 3, v250\n"
    "v_mfma_f32_16x16x32_bf16 v[60:63], v[188:191], a[204:207], v[60:63]\n"
    "s_waitcnt lgkmcnt(11)\n"
    "buffer_store_dwordx4 v[8:11], v198, s[60:63], s27 offen offset:1024 sc1\n"
    "v_mfma_f32_16x16x32_bf16 v[32:35], v[128:131], a[160:163], v[32:35]\n"
    "s_waitcnt lgkmcnt(3)\n"
    "ds_read_b64_tr_b16 v[160:161], v244 offset:16384\n"
    "ds_read_b64_tr_b16 v[162:163], v244 offset:20480\n"
    "v_mfma_f32_16x16x32_bf16 v[32:35], v[144:147], a[164:167], v[32:35]\n"
    "ds_read_b64_tr_b16 v[164:165], v245 offset:16384\n"
    "v_cvt_pk_bf16_f32 v16, v16, v17\n"
    "v_mfma_f32_16x16x32_bf16 v[40:43], v[128:131], a[208:211], v[40:43]\n"
    "ds_read_b64_tr_b16 v[166:167], v245 offset:20480\n"
    "v_cvt_pk_bf16_f32 v17, v18, v19\n"
    "v_mfma_f32_16x16x32_bf16 v[40:43], v[144:147], a[212:215], v[40:43]\n"
    "ds_read_b64_tr_b16 v[168:169], v246 offset:16384\n"
    "v_cvt_pk_bf16_f32 v18, v20, v21\n"
    "v_mfma_f32_16x16x32_bf16 v[36:39], v[132:135], a[160:163], v[36:39]\n"
    "ds_read_b64_tr_b16 v[170:171], v246 offset:20480\n"
    "v_cvt_pk_bf16_f32 v19, v22, v23\n"
    "v_mfma_f32_16x16x32_bf16 v[36:39], v[148:151], a[164:167], v[36:39]\n"
    "ds_read_b64_tr_b16 v[172:173], v247 offset:16384\n"
    "v_add_u32 v250, s14, v250\n"
    "v_mfma_f32_16x16x32_bf16 v[44:47], v[132:135], a[208:211], v[44:47]\n"
    "ds_read_b64_tr_b16 v[174:175], v247 offset:20480\n"
    "v_mfma_f32_16x16x32_bf16 v[44:47], v[148:151], a[212:215], v[44:47]\n"
    "ds_read_b64_tr_b16 v[176:177], v244 offset:24576\n"
    "v_mfma_f32_16x16x32_bf16 v[48:51], v[136:139], a[160:163], v[48:51]\n"
    "s_waitcnt lgkmcnt(8)\n"
    "ds_read_b64_tr_b16 v[178:179], v244 offset:28672\n"
    "v_mfma_f32_16x16x32_bf16 v[48:51], v[152:155], a[164:167], v[48:51]\n"
    "ds_read_b64_tr_b16 v[180:181], v245 offset:24576\n"
    "v_mfma_f32_16x16x32_bf16 v[56:59], v[136:139], a[208:211], v[56:59]\n"
    "ds_read_b64_tr_b16 v[182:183], v245 offset:28672\n"
    "v_mfma_f32_16x16x32_bf16 v[56:59], v[152:155], a[212:215], v[56:59]\n"
    "ds_read_b64_tr_b16 v[184:185], v246 offset:24576\n"
    "v_mfma_f32_16x16x32_bf16 v[52:55], v[140:143], a[160:163], v[52:55]\n"
    "ds_read_b64_tr_b16 v[186:187], v246 offset:28672\n"
    "v_mfma_f32_16x16x32_bf16 v[52:55], v[156:159], a[164:167], v[52:55]\n"
    "ds_read_b64_tr_b16 v[188:189], v247 offset:24576\n"
    "v_mfma_f32_16x16x32_bf16 v[60:63], v[140:143], a[208:211], v[60:63]\n"
    "ds_read_b64_tr_b16 v[190:191], v247 offset:28672\n"
    "v_mfma_f32_16x16x32_bf16 v[60:63], v[156:159], a[212:215], v[60:63]\n"
    "s_waitcnt lgkmcnt(11)\n"
    "buffer_store_dwordx4 v[16:19], v198, s[60:63], s27 offen offset:2048 sc1\n"
    "v_cvt_pk_bf16_f32 v24, v24, v25\n"
    "v_cvt_pk_bf16_f32 v25, v26, v27\n"
    "v_cvt_pk_bf16_f32 v26, v28, v29\n"
    "v_cvt_pk_bf16_f32 v27, v30, v31\n"
    "buffer_store_dwordx4 v[24:27], v198, s[60:63], s27 offen offset:3072 sc1\n"
    "v_mfma_f32_16x16x32_bf16 v[32:35], v[160:163], a[168:171], v[32:35]\n"
    "s_waitcnt vmcnt(4)\n"
    "ds_read_b64_tr_b16 v[128:129], v216 offset:0\n"
    "s_waitcnt lgkmcnt(4)\n"
    "v_mfma_f32_16x16x32_bf16 v[32:35], v[176:179], a[172:175], v[32:35]\n"
    "ds_read_b64_tr_b16 v[130:131], v216 offset:2048\n"
    "v_mfma_f32_16x16x32_bf16 v[40:43], v[160:163], a[216:219], v[40:43]\n"
    "v_mfma_f32_16x16x32_bf16 v[40:43], v[176:179], a[220:223], v[40:43]\n"
    "ds_read_b64_tr_b16 v[132:133], v217 offset:0\n"
    "v_mfma_f32_16x16x32_bf16 v[36:39], v[164:167], a[168:171], v[36:39]\n"
    "ds_read_b64_tr_b16 v[134:135], v217 offset:2048\n"
    "v_mfma_f32_16x16x32_bf16 v[36:39], v[180:183], a[172:175], v[36:39]\n"
    "v_mfma_f32_16x16x32_bf16 v[44:47], v[164:167], a[216:219], v[44:47]\n"
    "ds_read_b64_tr_b16 v[136:137], v218 offset:0\n"
    "v_mfma_f32_16x16x32_bf16 v[44:47], v[180:183], a[220:223], v[44:47]\n"
    "ds_read_b64_tr_b16 v[138:139], v218 offset:2048\n"
    "v_mfma_f32_16x16x32_bf16 v[48:51], v[168:171], a[168:171], v[48:51]\n"
    "s_waitcnt lgkmcnt(5)\n"
    "v_mfma_f32_16x16x32_bf16 v[48:51], v[184:187], a[172:175], v[48:51]\n"
    "ds_read_b64_tr_b16 v[140:141], v219 offset:0\n"
    "v_mfma_f32_16x16x32_bf16 v[56:59], v[168:171], a[216:219], v[56:59]\n"
    "ds_read_b64_tr_b16 v[142:143], v219 offset:2048\n"
    "v_mfma_f32_16x16x32_bf16 v[56:59], v[184:187], a[220:223], v[56:59]\n"
    "v_mfma_f32_16x16x32_bf16 v[52:55], v[172:175], a[168:171], v[52:55]\n"
    "v_mfma_f32_16x16x32_bf16 v[52:55], v[188:191], a[172:175], v[52:55]\n"
    "v_mfma_f32_16x16x32_bf16 v[60:63], v[172:175], a[216:219], v[60:63]\n"
    "v_mfma_f32_16x16x32_bf16 v[60:63], v[188:191], a[220:223], v[60:63]\n"
    "s_waitcnt lgkmcnt(3)\n"
    "v_mfma_f32_16x16x32_bf16 v[64:67], v[128:131], a[32:35], 0\n"
    "s_waitcnt vmcnt(4)\n"
    "ds_read_b64_tr_b16 v[160:161], v216 offset:4096\n"
    "v_mfma_f32_16x16x32_bf16 v[80:83], v[128:131], a[36:39], 0\n"
    "ds_read_b64_tr_b16 v[162:163], v216 offset:6144\n"
    "s_add_u32 s27, s22, 278528\n"
    "v_cvt_pk_bf16_f32 v32, v32, v33\n"
    "v_mfma_f32_16x16x32_bf16 v[68:71], v[132:135], a[32:35], 0\n"
    "v_cvt_pk_bf16_f32 v33, v34, v35\n"
    "v_cvt_pk_bf16_f32 v34, v36, v37\n"
    "v_mfma_f32_16x16x32_bf16 v[84:87], v[132:135], a[36:39], 0\n"
    "s_waitcnt lgkmcnt(1)\n"
    "ds_read_b64_tr_b16 v[164:165], v217 offset:4096\n"
    "v_cvt_pk_bf16_f32 v35, v38, v39\n"
    "v_mfma_f32_16x16x32_bf16 v[72:75], v[136:139], a[32:35], 0\n"
    "ds_read_b64_tr_b16 v[166:167], v217 offset:6144\n"
    "v_mfma_f32_16x16x32_bf16 v[88:91], v[136:139], a[36:39], 0\n"
    "buffer_store_dwordx4 v[32:35], v198, s[60:63], s27 offen offset:0 sc1\n"
    "v_mfma_f32_16x16x32_bf16 v[76:79], v[140:143], a[32:35], 0\n"
    "ds_read_b64_tr_b16 v[168:169], v218 offset:4096\n"
    "v_cvt_pk_bf16_f32 v40, v40, v41\n"
    "v_mfma_f32_16x16x32_bf16 v[92:95], v[140:143], a[36:39], 0\n"
    "ds_read_b64_tr_b16 v[170:171], v218 offset:6144\n"
    "v_cvt_pk_bf16_f32 v41, v42, v43\n"
    "v_mfma_f32_16x16x32_bf16 v[96:99], v[128:131], a[40:43], 0\n"
    "v_cvt_pk_bf16_f32 v42, v44, v45\n"
    "v_cvt_pk_bf16_f32 v43, v46, v47\n"
    "v_mfma_f32_16x16x32_bf16 v[112:115], v[128:131], a[44:47], 0\n"
    "ds_read_b64_tr_b16 v[172:173], v219 offset:4096\n"
    "v_mfma_f32_16x16x32_bf16 v[100:103], v[132:135], a[40:43], 0\n"
    "ds_read_b64_tr_b16 v[174:175], v219 offset:6144\n"
    "v_mfma_f32_16x16x32_bf16 v[116:119], v[132:135], a[44:47], 0\n"
    "buffer_store_dwordx4 v[40:43], v198, s[60:63], s27 offen offset:1024 sc1\n"
    "v_mfma_f32_16x16x32_bf16 v[104:107], v[136:139], a[40:43], 0\n"
    "v_cvt_pk_bf16_f32 v48, v48, v49\n"
    "v_cvt_pk_bf16_f32 v49, v50, v51\n"
    "v_mfma_f32_16x16x32_bf16 v[120:123], v[136:139], a[44:47], 0\n"
    "v_cvt_pk_bf16_f32 v50, v52, v53\n"
    "v_cvt_pk_bf16_f32 v51, v54, v55\n"
    "v_mfma_f32_16x16x32_bf16 v[108:111], v[140:143], a[40:43], 0\n"
    "buffer_store_dwordx4 v[48:51], v198, s[60:63], s27 offen offset:2048 sc1\n"
    "v_mfma_f32_16x16x32_bf16 v[124:127], v[140:143], a[44:47], 0\n"
    "s_waitcnt lgkmcnt(3)\n"
    "v_cvt_pk_bf16_f32 v56, v56, v57\n"
    "v_cvt_pk_bf16_f32 v57, v58, v59\n"
    "v_mfma_f32_16x16x32_bf16 v[64:67], v[160:163], a[48:51], v[64:67]\n"
    "s_waitcnt vmcnt(7)\n"
    "ds_read_b64_tr_b16 v[128:129], v216 offset:8192\n"
    "v_cvt_pk_bf16_f32 v58, v60, v61\n"
    "v_mfma_f32_16x16x32_bf16 v[80:83], v[160:163], a[52:55], v[80:83]\n"
    "ds_read_b64_tr_b16 v[130:131], v216 offset:10240\n"
    "v_cvt_pk_bf16_f32 v59, v62, v63\n"
    "v_mfma_f32_16x16x32_bf16 v[68:71], v[164:167], a[48:51], v[68:71]\n"
    "buffer_store_dwordx4 v[56:59], v198, s[60:63], s27 offen offset:3072 sc1\n"
    "v_mfma_f32_16x16x32_bf16 v[84:87], v[164:167], a[52:55], v[84:87]\n"
    "s_waitcnt lgkmcnt(1)\n"
    "ds_read_b64_tr_b16 v[132:133], v217 offset:8192\n"
    "v_mfma_f32_16x16x32_bf16 v[72:75], v[168:171], a[48:51], v[72:75]\n"
    "ds_read_b64_tr_b16 v[134:135], v217 offset:10240\n"
    "v_mfma_f32_16x16x32_bf16 v[88:91], v[168:171], a[52:55], v[88:91]\n"
    "v_mfma_f32_16x16x32_bf16 v[76:79], v[172:175], a[48:51], v[76:79]\n"
    "ds_read_b64_tr_b16 v[136:137], v218 offset:8192\n"
    "v_mfma_f32_16x16x32_bf16 v[92:95], v[172:175], a[52:55], v[92:95]\n"
    "ds_read_b64_tr_b16 v[138:139], v218 offset:10240\n"
    "v_mfma_f32_16x16x32_bf16 v[96:99], v[160:163], a[56:59], v[96:99]\n"
    "v_mfma_f32_16x16x32_bf16 v[112:115], v[160:163], a[60:63], v[112:115]\n"
    "ds_read_b64_tr_b16 v[140:141], v219 offset:8192\n"
    "v_mfma_f32_16x16x32_bf16 v[100:103], v[164:167], a[56:59], v[100:103]\n"
    "ds_read_b64_tr_b16 v[142:143], v219 offset:10240\n"
    "v_mfma_f32_16x16x32_bf16 v[116:119], v[164:167], a[60:63], v[116:119]\n"
    "v_mfma_f32_16x16x32_bf16 v[104:107], v[168:171], a[56:59], v[104:107]\n"
    "v_mfma_f32_16x16x32_bf16 v[120:123], v[168:171], a[60:63], v[120:123]\n"
    "v_mfma_f32_16x16x32_bf16 v[108:111], v[172:175], a[56:59], v[108:111]\n"
    "v_mfma_f32_16x16x32_bf16 v[124:127], v[172:175], a[60:63], v[124:127]\n"
    "s_waitcnt lgkmcnt(3)\n"
    "v_mfma_f32_16x16x32_bf16 v[64:67], v[128:131], a[64:67], v[64:67]\n"
    "s_waitcnt vmcnt(8)\n"
    "ds_read_b64_tr_b16 v[160:161], v216 offset:12288\n"
    "v_mfma_f32_16x16x32_bf16 v[80:83], v[128:131], a[68:71], v[80:83]\n"
    "ds_read_b64_tr_b16 v[162:163], v216 offset:14336\n"
    "v_mfma_f32_16x16x32_bf16 v[68:71], v[132:135], a[64:67], v[68:71]\n"
    "v_mfma_f32_16x16x32_bf16 v[84:87], v[132:135], a[68:71], v[84:87]\n"
    "s_waitcnt lgkmcnt(1)\n"
    "ds_read_b64_tr_b16 v[164:165], v217 offset:12288\n"
    "v_mfma_f32_16x16x32_bf16 v[72:75], v[136:139], a[64:67], v[72:75]\n"
    "ds_read_b64_tr_b16 v[166:167], v217 offset:14336\n"
    "v_mfma_f32_16x16x32_bf16 v[88:91], v[136:139], a[68:71], v[88:91]\n"
    "v_mfma_f32_16x16x32_bf16 v[76:79], v[140:143], a[64:67], v[76:79]\n"
    "ds_read_b64_tr_b16 v[168:169], v218 offset:12288\n"
    "v_mfma_f32_16x16x32_bf16 v[92:95], v[140:143], a[68:71], v[92:95]\n"
    "ds_read_b64_tr_b16 v[170:171], v218 offset:14336\n"
    "v_mfma_f32_16x16x32_bf16 v[96:99], v[128:131], a[72:75], v[96:99]\n"
    "v_mfma_f32_16x16x32_bf16 v[112:115], v[128:131], a[76:79], v[112:115]\n"
    "ds_read_b64_tr_b16 v[172:173], v219 offset:12288\n"
    "v_mfma_f32_16x16x32_bf16 v[100:103], v[132:135], a[72:75], v[100:103]\n"
    "ds_read_b64_tr_b16 v[174:175], v219 offset:14336\n"
    "v_mfma_f32_16x16x32_bf16 v[116:119], v[132:135], a[76:79], v[116:119]\n"
    "v_mfma_f32_16x16x32_bf16 v[104:107], v[136:139], a[72:75], v[104:107]\n"
    "v_mfma_f32_16x16x32_bf16 v[120:123], v[136:139], a[76:79], v[120:123]\n"
    "v_mfma_f32_16x16x32_bf16 v[108:111], v[140:143], a[72:75], v[108:111]\n"
    "v_mfma_f32_16x16x32_bf16 v[124:127], v[140:143], a[76:79], v[124:127]\n"
    "s_waitcnt lgkmcnt(3)\n"
    "v_mfma_f32_16x16x32_bf16 v[64:67], v[160:163], a[80:83], v[64:67]\n"
    "global_load_dwordx2 v[252:253], v250, s[58:59] sc1\n"
    "v_mfma_f32_16x16x32_bf16 v[80:83], v[160:163], a[84:87], v[80:83]\n"
    "v_mfma_f32_16x16x32_bf16 v[68:71], v[164:167], a[80:83], v[68:71]\n"
    "v_mfma_f32_16x16x32_bf16 v[84:87], v[164:167], a[84:87], v[84:87]\n"
    "s_waitcnt lgkmcnt(0)\n"
    "v_mfma_f32_16x16x32_bf16 v[72:75], v[168:171], a[80:83], v[72:75]\n"
    "v_mfma_f32_16x16x32_bf16 v[88:91], v[168:171], a[84:87], v[88:91]\n"
    "v_mfma_f32_16x16x32_bf16 v[76:79], v[172:175], a[80:83], v[76:79]\n"
    "v_mfma_f32_16x16x32_bf16 v[92:95], v[172:175], a[84:87], v[92:95]\n"
    "v_mfma_f32_16x16x32_bf16 v[96:99], v[160:163], a[88:91], v[96:99]\n"
    "v_mfma_f32_16x16x32_bf16 v[112:115], v[160:163], a[92:95], v[112:115]\n"
    "v_mfma_f32_16x16x32_bf16 v[100:103], v[164:167], a[88:91], v[100:103]\n"
    "v_mfma_f32_16x16x32_bf16 v[116:119], v[164:167], a[92:95], v[116:119]\n"
    "v_mfma_f32_16x16x32_bf16 v[104:107], v[168:171], a[88:91], v[104:107]\n"
    "v_mfma_f32_16x16x32_bf16 v[120:123], v[168:171], a[92:95], v[120:123]\n"
    "v_mfma_f32_16x16x32_bf16 v[108:111], v[172:175], a[88:91], v[108:111]\n"
    "v_mfma_f32_16x16x32_bf16 v[124:127], v[172:175], a[92:95], v[124:127]\n"
    "s_barrier\n"
    "ds_write_b128 v220, v[64:67] offset:0\n"
    "ds_write_b128 v221, v[68:71] offset:0\n"
    "ds_write_b128 v222, v[72:75] offset:0\n"
    "ds_write_b128 v223, v[76:79] offset:0\n"
    "ds_write_b128 v220, v[80:83] offset:4096\n"
    "ds_write_b128 v221, v[84:87] offset:4096\n"
    "ds_write_b128 v222, v[88:91] offset:4096\n"
    "ds_write_b128 v223, v[92:95] offset:4096\n"
    "ds_write_b128 v220, v[96:99] offset:8192\n"
    "ds_write_b128 v221, v[100:103] offset:8192\n"
    "ds_write_b128 v222, v[104:107] offset:8192\n"
    "ds_write_b128 v223, v[108:111] offset:8192\n"
    "ds_write_b128 v220, v[112:115] offset:12288\n"
    "ds_write_b128 v221, v[116:119] offset:12288\n"
    "ds_write_b128 v222, v[120:123] offset:12288\n"
    "ds_write_b128 v223, v[124:127] offset:12288\n"
    "s_waitcnt vmcnt(1) lgkmcnt(0)\n"
    "s_barrier\n"
    "ds_read_b128 v[64:67], v224 offset:0\n"
    "ds_read_b128 v[68:71], v225 offset:0\n"
    "ds_read_b128 v[72:75], v226 offset:0\n"
    "ds_read_b128 v[76:79], v227 offset:0\n"
    "ds_read_b128 v[80:83], v224 offset:16384\n"
    "ds_read_b128 v[84:87], v225 offset:16384\n"
    "ds_read_b128 v[88:91], v226 offset:16384\n"
    "ds_read_b128 v[92:95], v227 offset:16384\n"
    "ds_read_b128 v[96:99], v224 offset:32768\n"
    "ds_read_b128 v[100:103], v225 offset:32768\n"
    "ds_read_b128 v[104:107], v226 offset:32768\n"
    "ds_read_b128 v[108:111], v227 offset:32768\n"
    "ds_read_b128 v[112:115], v224 offset:49152\n"
    "ds_read_b128 v[116:119], v225 offset:49152\n"
    "ds_read_b128 v[120:123], v226 offset:49152\n"
    "ds_read_b128 v[124:127], v227 offset:49152\n"
    "s_cmp_lg_u32 s8, 0\n"
    "s_cbranch_scc1 L_pub21_%=\n"
    "s_lshl_b32 s14, s13, 2\n"
    "s_add_u32 s14, s14, s40\n"
    "s_lshl_b32 s14, s14, 6\n"
    "s_lshl_b32 s15, s10, 3\n"
    "s_add_u32 s14, s14, s15\n"
    "s_add_u32 s14, s14, 20480\n"
    "s_add_u32 s14, s14, 0x3400000\n"
    "v_accvgpr_write_b32 a252, s28\n"
    "v_accvgpr_write_b32 a253, s29\n"
    "v_mov_b32 v251, s14\n"
    "s_mov_b64 exec, 1\n"
    "global_store_dwordx2 v251, a[252:253], s[58:59] sc1\n"
    "s_mov_b64 exec, -1\n"
    "L_pub21_%=:\n"
    "s_mul_i32 s22, s10, 68608\n"
    "s_lshl_b32 s14, s8, 11\n"
    "s_add_u32 s22, s22, s14\n"
    "s_mov_b32 s27, 0xff\n"
    "s_lshl_b32 s18, 1, s10\n"
    "s_waitcnt vmcnt(0)\n"
    "v_cmp_eq_u64 vcc, s[28:29], v[252:253]\n"
    "s_nop 0\n"
    "s_or_b32 s17, vcc_lo, s18\n"
    "s_and_b32 s17, s17, s27\n"
    "s_andn2_b32 s27, s27, s17\n"
    "s_bitcmp1_b32 s17, 0\n"
    "s_cbranch_scc0 L_fs22_%=\n"
    "s_add_u32 s14, s22, 0\n"
    "buffer_load_dwordx4 a[128:131], v198, s[60:63], s14 offen offset:0 sc1\n"
    "buffer_load_dwordx4 a[132:135], v198, s[60:63], s14 offen offset:1024 sc1\n"
    "L_fs22_%=:\n"
    "s_bitcmp1_b32 s17, 1\n"
    "s_cbranch_scc0 L_fs23_%=\n"
    "s_add_u32 s14, s22, 8192\n"
    "buffer_load_dwordx4 a[136:139], v198, s[60:63], s14 offen offset:0 sc1\n"
    "buffer_load_dwordx4 a[140:143], v198, s[60:63], s14 offen offset:1024 sc1\n"
    "L_fs23_%=:\n"
    "s_bitcmp1_b32 s17, 2\n"
    "s_cbranch_scc0 L_fs24_%=\n"
    "s_add_u32 s14, s22, 16384\n"
    "buffer_load_dwordx4 a[144:147], v198, s[60:63], s14 offen offset:0 sc1\n"
    "buffer_load_dwordx4 a[148:151], v198, s[60:63], s14 offen offset:1024 sc1\n"
    "L_fs24_%=:\n"
    "s_bitcmp1_b32 s17, 3\n"
    "s_cbranch_scc0 L_fs25_%=\n"
    "s_add_u32 s14, s22, 24576\n"
    "buffer_load_dwordx4 a[152:155], v198, s[60:63], s14 offen offset:0 sc1\n"
    "buffer_load_dwordx4 a[156:159], v198, s[60:63], s14 offen offset:1024 sc1\n"
    "L_fs25_%=:\n"
    "s_bitcmp1_b32 s17, 4\n"
    "s_cbranch_scc0 L_fs26_%=\n"
    "s_add_u32 s14, s22, 32768\n"
    "buffer_load_dwordx4 a[160:163], v198, s[60:63], s14 offen offset:0 sc1\n"
    "buffer_load_dwordx4 a[164:167], v198, s[60:63], s14 offen offset:1024 sc1\n"
    "L_fs26_%=:\n"
    "s_bitcmp1_b32 s17, 5\n"
    "s_cbranch_scc0 L_fs27_%=\n"
    "s_add_u32 s14, s22, 40960\n"
    "buffer_load_dwordx4 a[168:171], v198, s[60:63], s14 offen offset:0 sc1\n"
    "buffer_load_dwordx4 a[172:175], v198, s[60:63], s14 offen offset:1024 sc1\n"
    "L_fs27_%=:\n"
    "s_bitcmp1_b32 s17, 6\n"
    "s_cbranch_scc0 L_fs28_%=\n"
    "s_add_u32 s14, s22, 49152\n"
    "buffer_load_dwordx4 a[176:179], v198, s[60:63], s14 offen offset:0 sc1\n"
    "buffer_load_dwordx4 a[180:183], v198, s[60:63], s14 offen offset:1024 sc1\n"
    "L_fs28_%=:\n"
    "s_bitcmp1_b32 s17, 7\n"
    "s_cbranch_scc0 L_fs29_%=\n"
    "s_add_u32 s14, s22, 57344\n"
    "buffer_load_dwordx4 a[184:187], v198, s[60:63], s14 offen offset:0 sc1\n"
    "buffer_load_dwordx4 a[188:191], v198, s[60:63], s14 offen offset:1024 sc1\n"
    "L_fs29_%=:\n"
    "s_waitcnt lgkmcnt(0)\n"
    "v_pk_add_f32 v[64:65], v[64:65], v[80:81]\n"
    "v_pk_add_f32 v[96:97], v[96:97], v[112:113]\n"
    "v_pk_add_f32 v[66:67], v[66:67], v[82:83]\n"
    "v_pk_add_f32 v[98:99], v[98:99], v[114:115]\n"
    "v_pk_add_f32 v[68:69], v[68:69], v[84:85]\n"
    "v_pk_add_f32 v[100:101], v[100:101], v[116:117]\n"
    "v_pk_add_f32 v[70:71], v[70:71], v[86:87]\n"
    "v_pk_add_f32 v[102:103], v[102:103], v[118:119]\n"
    "v_pk_add_f32 v[72:73], v[72:73], v[88:89]\n"
    "v_pk_add_f32 v[104:105], v[104:105], v[120:121]\n"
    "v_pk_add_f32 v[74:75], v[74:75], v[90:91]\n"
    "v_pk_add_f32 v[106:107], v[106:107], v[122:123]\n"
    "v_pk_add_f32 v[76:77], v[76:77], v[92:93]\n"
    "v_pk_add_f32 v[108:109], v[108:109], v[124:125]\n"
    "v_pk_add_f32 v[78:79], v[78:79], v[94:95]\n"
    "v_pk_add_f32 v[110:111], v[110:111], v[126:127]\n"
    "v_pk_add_f32 v[64:65], v[64:65], v[96:97]\n"
    "v_pk_add_f32 v[66:67], v[66:67], v[98:99]\n"
    "v_pk_add_f32 v[68:69], v[68:69], v[100:101]\n"
    "v_pk_add_f32 v[70:71], v[70:71], v[102:103]\n"
    "v_pk_add_f32 v[72:73], v[72:73], v[104:105]\n"
    "v_pk_add_f32 v[74:75], v[74:75], v[106:107]\n"
    "v_pk_add_f32 v[76:77], v[76:77], v[108:109]\n"
    "v_pk_add_f32 v[78:79], v[78:79], v[110:111]\n"
    "v_cvt_pk_bf16_f32 v64, v64, v65\n"
    "v_cvt_pk_bf16_f32 v65, v66, v67\n"
    "v_cvt_pk_bf16_f32 v66, v68, v69\n"
    "v_cvt_pk_bf16_f32 v67, v70, v71\n"
    "v_cvt_pk_bf16_f32 v68, v72, v73\n"
    "v_cvt_pk_bf16_f32 v69, v74, v75\n"
    "v_cvt_pk_bf16_f32 v70, v76, v77\n"
    "v_cvt_pk_bf16_f32 v71, v78, v79\n"
    "global_store_dwordx4 v228, v[64:67], s[56:57]\n"
    "global_store_dwordx4 v228, v[68:71], s[56:57] offset:16\n"
    "s_mov_b32 s26, 0\n"
    "L_fl30_%=:\n"
    "s_cmp_eq_u32 s27, 0\n"
    "s_cbranch_scc1 L_fd31_%=\n"
    "global_load_dwordx2 v[252:253], v250, s[58:59] sc1\n"
    "s_add_u32 s26, s26, 1\n"
    "s_waitcnt vmcnt(0)\n"
    "v_cmp_eq_u64 vcc, s[28:29], v[252:253]\n"
    "s_nop 0\n"
    "s_or_b32 s17, vcc_lo, s18\n"
    "s_and_b32 s17, s17, s27\n"
    "s_andn2_b32 s27, s27, s17\n"
    "s_bitcmp1_b32 s17, 0\n"
    "s_cbranch_scc0 L_fs32_%=\n"
    "s_add_u32 s14, s22, 0\n"
    "buffer_load_dwordx4 a[128:131], v198, s[60:63], s14 offen offset:0 sc1\n"
    "buffer_load_dwordx4 a[132:135], v198, s[60:63], s14 offen offset:1024 sc1\n"
    "L_fs32_%=:\n"
    "s_bitcmp1_b32 s17, 1\n"
    "s_cbranch_scc0 L_fs33_%=\n"
    "s_add_u32 s14, s22, 8192\n"
    "buffer_load_dwordx4 a[136:139], v198, s[60:63], s14 offen offset:0 sc1\n"
    "buffer_load_dwordx4 a[140:143], v198, s[60:63], s14 offen offset:1024 sc1\n"
    "L_fs33_%=:\n"
    "s_bitcmp1_b32 s17, 2\n"
    "s_cbranch_scc0 L_fs34_%=\n"
    "s_add_u32 s14, s22, 16384\n"
    "buffer_load_dwordx4 a[144:147], v198, s[60:63], s14 offen offset:0 sc1\n"
    "buffer_load_dwordx4 a[148:151], v198, s[60:63], s14 offen offset:1024 sc1\n"
    "L_fs34_%=:\n"
    "s_bitcmp1_b32 s17, 3\n"
    "s_cbranch_scc0 L_fs35_%=\n"
    "s_add_u32 s14, s22, 24576\n"
    "buffer_load_dwordx4 a[152:155], v198, s[60:63], s14 offen offset:0 sc1\n"
    "buffer_load_dwordx4 a[156:159], v198, s[60:63], s14 offen offset:1024 sc1\n"
    "L_fs35_%=:\n"
    "s_bitcmp1_b32 s17, 4\n"
    "s_cbranch_scc0 L_fs36_%=\n"
    "s_add_u32 s14, s22, 32768\n"
    "buffer_load_dwordx4 a[160:163], v198, s[60:63], s14 offen offset:0 sc1\n"
    "buffer_load_dwordx4 a[164:167], v198, s[60:63], s14 offen offset:1024 sc1\n"
    "L_fs36_%=:\n"
    "s_bitcmp1_b32 s17, 5\n"
    "s_cbranch_scc0 L_fs37_%=\n"
    "s_add_u32 s14, s22, 40960\n"
    "buffer_load_dwordx4 a[168:171], v198, s[60:63], s14 offen offset:0 sc1\n"
    "buffer_load_dwordx4 a[172:175], v198, s[60:63], s14 offen offset:1024 sc1\n"
    "L_fs37_%=:\n"
    "s_bitcmp1_b32 s17, 6\n"
    "s_cbranch_scc0 L_fs38_%=\n"
    "s_add_u32 s14, s22, 49152\n"
    "buffer_load_dwordx4 a[176:179], v198, s[60:63], s14 offen offset:0 sc1\n"
    "buffer_load_dwordx4 a[180:183], v198, s[60:63], s14 offen offset:1024 sc1\n"
    "L_fs38_%=:\n"
    "s_bitcmp1_b32 s17, 7\n"
    "s_cbranch_scc0 L_fs39_%=\n"
    "s_add_u32 s14, s22, 57344\n"
    "buffer_load_dwordx4 a[184:187], v198, s[60:63], s14 offen offset:0 sc1\n"
    "buffer_load_dwordx4 a[188:191], v198, s[60:63], s14 offen offset:1024 sc1\n"
    "L_fs39_%=:\n"
    "s_cmp_lt_u32 s26, 0x40000\n"
    "s_cbranch_scc1 L_fl30_%=\n"
    "L_fd31_%=:\n"
    "s_cmp_lg_u32 s9, 0\n"
    "s_cbranch_scc1 L_noE40_%=\n"
    "s_cmp_lg_u32 s8, 0\n"
    "s_cbranch_scc1 L_pub41_%=\n"
    "s_mov_b32 s14, 0\n"
    "s_add_u32 s14, s14, 0x3400000\n"
    "v_accvgpr_write_b32 a252, s28\n"
    "v_accvgpr_write_b32 a253, s29\n"
    "v_mov_b32 v251, s14\n"
    "s_mov_b64 exec, 1\n"
    "global_store_dwordx2 v251, a[252:253], s[58:59] sc1\n"
    "s_mov_b64 exec, -1\n"
    "L_pub41_%=:\n"
    "L_noE40_%=:\n"
    "s_waitcnt vmcnt(0)\n"
    "v_mfma_f32_16x16x32_bf16 v[0:3], v[230:233], a[128:131], 0\n"
    "v_mfma_f32_16x16x32_bf16 v[4:7], v[234:237], a[128:131], 0\n"
    "v_mfma_f32_16x16x32_bf16 v[8:11], v[230:233], a[132:135], 0\n"
    "v_mfma_f32_16x16x32_bf16 v[12:15], v[234:237], a[132:135], 0\n"
    "v_mfma_f32_16x16x32_bf16 v[0:3], v[230:233], a[136:139], v[0:3]\n"
    "v_mfma_f32_16x16x32_bf16 v[4:7], v[234:237], a[136:139], v[4:7]\n"
    "v_mfma_f32_16x16x32_bf16 v[8:11], v[230:233], a[140:143], v[8:11]\n"
    "v_mfma_f32_16x16x32_bf16 v[12:15], v[234:237], a[140:143], v[12:15]\n"
    "v_mfma_f32_16x16x32_bf16 v[0:3], v[230:233], a[144:147], v[0:3]\n"
    "v_mfma_f32_16x16x32_bf16 v[4:7], v[234:237], a[144:147], v[4:7]\n"
    "v_mfma_f32_16x16x32_bf16 v[8:11], v[230:233], a[148:151], v[8:11]\n"
    "v_mfma_f32_16x16x32_bf16 v[12:15], v[234:237], a[148:151], v[12:15]\n"
    "v_mfma_f32_16x16x32_bf16 v[0:3], v[230:233], a[152:155], v[0:3]\n"
    "v_mfma_f32_16x16x32_bf16 v[4:7], v[234:237], a[152:155], v[4:7]\n"
    "v_mfma_f32_16x16x32_bf16 v[8:11], v[230:233], a[156:159], v[8:11]\n"
    "v_mfma_f32_16x16x32_bf16 v[12:15], v[234:237], a[156:159], v[12:15]\n"
    "v_mfma_f32_16x16x32_bf16 v[0:3], v[230:233], a[160:163], v[0:3]\n"
    "v_mfma_f32_16x16x32_bf16 v[4:7], v[234:237], a[160:163], v[4:7]\n"
    "v_mfma_f32_16x16x32_bf16 v[8:11], v[230:233], a[164:167], v[8:11]\n"
    "v_mfma_f32_16x16x32_bf16 v[12:15], v[234:237], a[164:167], v[12:15]\n"
    "v_mfma_f32_16x16x32_bf16 v[0:3], v[230:233], a[168:171], v[0:3]\n"
    "v_mfma_f32_16x16x32_bf16 v[4:7], v[234:237], a[168:171], v[4:7]\n"
    "v_mfma_f32_16x16x32_bf16 v[8:11], v[230:233], a[172:175], v[8:11]\n"
    "v_mfma_f32_16x16x32_bf16 v[12:15], v[234:237], a[172:175], v[12:15]\n"
    "v_mfma_f32_16x16x32_bf16 v[0:3], v[230:233], a[176:179], v[0:3]\n"
    "v_mfma_f32_16x16x32_bf16 v[4:7], v[234:237], a[176:179], v[4:7]\n"
    "v_mfma_f32_16x16x32_bf16 v[8:11], v[230:233], a[180:183], v[8:11]\n"
    "v_mfma_f32_16x16x32_bf16 v[12:15], v[234:237], a[180:183], v[12:15]\n"
    "v_mfma_f32_16x16x32_bf16 v[0:3], v[230:233], a[184:187], v[0:3]\n"
    "v_mfma_f32_16x16x32_bf16 v[4:7], v[234:237], a[184:187], v[4:7]\n"
    "v_mfma_f32_16x16x32_bf16 v[8:11], v[230:233], a[188:191], v[8:11]\n"
    "v_mfma_f32_16x16x32_bf16 v[12:15], v[234:237], a[188:191], v[12:15]\n"
    "s_nop 4\n"
    "v_cvt_pk_bf16_f32 v0, v0, v1\n"
    "v_cvt_pk_bf16_f32 v1, v2, v3\n"
    "v_cvt_pk_bf16_f32 v2, v4, v5\n"
    "v_cvt_pk_bf16_f32 v3, v6, v7\n"
    "global_store_dwordx4 v229, v[0:3], s[56:57]\n"
    "v_cvt_pk_bf16_f32 v8, v8, v9\n"
    "v_cvt_pk_bf16_f32 v9, v10, v11\n"
    "v_cvt_pk_bf16_f32 v10, v12, v13\n"
    "v_cvt_pk_bf16_f32 v11, v14, v15\n"
    "v_add_u32 v229, s97, v229\n"
    "global_store_dwordx4 v229, v[8:11], s[56:57]\n"
    "s_waitcnt vmcnt(0) lgkmcnt(0)\n"
    "s_branch L_end_%=\n"
    "L_att_U2s_%=:\n"
    "v_mfma_f32_16x16x32_bf16 v[76:79], a[192:195], v[48:51], v[172:175]\n"
    "s_waitcnt lgkmcnt(0)\n"
    "v_exp_f32 v104, v104\n"
    "global_load_dwordx4 v[64:67], v252, s[96:97] sc1\n"
    "s_add_u32 s96, s96, 4608\n"
    "s_addc_u32 s97, s97, 0\n"
    "v_mfma_f32_16x16x32_bf16 v[76:79], a[196:199], v[52:55], v[76:79]\n"
    "s_add_u32 s16, s16, 1\n"
    "ds_read_b128 a[132:135], v199 offset:9216\n"
    "ds_read_b128 a[128:131], v199 offset:8192\n"
    "global_load_dwordx4 v[68:71], v252, s[96:97] sc1\n"
    "s_add_u32 s96, s96, 4608\n"
    "s_addc_u32 s97, s97, 0\n"
    "v_mfma_f32_16x16x32_bf16 v[76:79], a[200:203], v[56:59], v[76:79]\n"
    "s_add_u32 s21, s21, 32768\n"
    "v_exp_f32 v105, v105\n"
    "v_mfma_f32_16x16x32_bf16 v[76:79], a[204:207], v[60:63], v[76:79]\n"
    "s_and_b32 s21, s21, 0x1ffff\n"
    "global_load_dwordx4 v[80:83], v252, s[96:97] sc1\n"
    "s_add_u32 s96, s96, 4608\n"
    "s_addc_u32 s97, s97, 0\n"
    "v_exp_f32 v106, v106\n"
    "v_mfma_f32_16x16x32_bf16 v[92:95], a[208:211], v[48:51], v[188:191]\n"
    "s_lshl_b32 s14, s16, 1\n"
    "s_cmp_gt_u32 s31, s14\n"
    "s_cselect_b64 vcc, -1, 0\n"
    "s_cmp_eq_u32 s31, s14\n"
    "s_cselect_b64 s[44:45], s[72:73], vcc\n"
    "s_cselect_b64 s[46:47], s[74:75], vcc\n"
    "s_cselect_b64 s[48:49], s[76:77], vcc\n"
    "s_cselect_b64 s[50:51], s[78:79], vcc\n"
    "ds_read_b128 a[140:143], v199 offset:11264\n"
    "ds_read_b128 a[136:139], v199 offset:10240\n"
    "global_load_dwordx4 v[84:87], v252, s[96:97] sc1\n"
    "s_add_u32 s96, s96, 4608\n"
    "s_addc_u32 s97, s97, 0\n"
    "v_mfma_f32_16x16x32_bf16 v[92:95], a[212:215], v[52:55], v[92:95]\n"
    "v_exp_f32 v107, v107\n"
    "v_cvt_pk_bf16_f32 v152, v104, v105\n"
    "v_cndmask_b32 v172, v202, v195, s[44:45]\n"
    "v_mfma_f32_16x16x32_bf16 v[92:95], a[216:219], v[56:59], v[92:95]\n"
    "ds_read_b128 a[160:163], v199 offset:24576\n"
    "global_load_dwordx4 v[96:99], v252, s[96:97] sc1\n"
    "s_add_u32 s96, s96, 4608\n"
    "s_addc_u32 s97, s97, 0\n"
    "v_cvt_pk_bf16_f32 v153, v106, v107\n"
    "v_mfma_f32_16x16x32_bf16 v[92:95], a[220:223], v[60:63], v[92:95]\n"
    "v_exp_f32 v108, v108\n"
    "global_load_dwordx4 v[100:103], v252, s[96:97] sc1\n"
    "s_add_u32 s96, s96, 4608\n"
    "s_addc_u32 s97, s97, 0\n"
    "v_mfma_f32_16x16x32_bf16 a[64:67], a[224:227], v[136:139], a[64:67]\n"
    "ds_read_b128 a[164:167], v199 offset:25600\n"
    "v_exp_f32 v109, v109\n"
    "v_cndmask_b32 v173, v202, v195, s[46:47]\n"
    "v_mfma_f32_16x16x32_bf16 a[96:99], a[224:227], v[140:143], a[96:99]\n"
    "ds_read_b128 a[168:171], v199 offset:26624\n"
    "global_load_dwordx4 v[112:115], v252, s[96:97] sc1\n"
    "s_add_u32 s96, s96, 4608\n"
    "s_addc_u32 s97, s97, 0\n"
    "v_cvt_pk_bf16_f32 v156, v108, v109\n"
    "v_mfma_f32_16x16x32_bf16 a[68:71], a[228:231], v[136:139], a[68:71]\n"
    "v_exp_f32 v110, v110\n"
    "global_load_dwordx4 v[116:119], v252, s[96:97] sc1\n"
    "s_add_u32 s96, s96, 4608\n"
    "s_addc_u32 s97, s97, 0\n"
    "v_mfma_f32_16x16x32_bf16 a[100:103], a[228:231], v[140:143], a[100:103]\n"
    "ds_read_b128 a[172:175], v199 offset:27648\n"
    "v_exp_f32 v111, v111\n"
    "v_cndmask_b32 v174, v202, v195, s[48:49]\n"
    "v_mfma_f32_16x16x32_bf16 a[72:75], a[232:235], v[136:139], a[72:75]\n"
    "global_load_dwordx4 v[128:131], v252, s[96:97] sc1\n"
    "s_add_u32 s96, s96, 4608\n"
    "s_addc_u32 s97, s97, 0\n"
    "v_cvt_pk_bf16_f32 v157, v110, v111\n"
    "v_cndmask_b32 v175, v202, v195, s[50:51]\n"
    "v_mfma_f32_16x16x32_bf16 a[104:107], a[232:235], v[140:143], a[104:107]\n"
    "s_add_u32 s14, s14, 1\n"
    "s_cmp_gt_u32 s31, s14\n"
    "s_cselect_b64 vcc, -1, 0\n"
    "s_cmp_eq_u32 s31, s14\n"
    "s_cselect_b64 s[44:45], s[72:73], vcc\n"
    "s_cselect_b64 s[46:47], s[74:75], vcc\n"
    "s_cselect_b64 s[48:49], s[76:77], vcc\n"
    "s_cselect_b64 s[50:51], s[78:79], vcc\n"
    "ds_read_b128 a[144:147], v199 offset:12288\n"
    "v_exp_f32 v120, v120\n"
    "v_mfma_f32_16x16x32_bf16 a[76:79], a[236:239], v[136:139], a[76:79]\n"
    "global_load_dwordx4 v[132:135], v252, s[96:97] sc1\n"
    "s_add_u32 s96, s96, 4608\n"
    "s_addc_u32 s97, s97, 0\n"
    "v_exp_f32 v121, v121\n"
    "v_mfma_f32_16x16x32_bf16 a[108:111], a[236:239], v[140:143], a[108:111]\n"
    "ds_read_b128 a[148:151], v199 offset:13312\n"
    "global_load_dwordx4 v[144:147], v252, s[96:97] sc1\n"
    "s_add_u32 s96, s96, 4608\n"
    "s_addc_u32 s97, s97, 0\n"
    "v_cvt_pk_bf16_f32 v154, v120, v121\n"
    "v_mfma_f32_16x16x32_bf16 a[80:83], a[240:243], v[136:139], a[80:83]\n"
    "ds_read_b128 a[152:155], v199 offset:14336\n"
    "v_exp_f32 v122, v122\n"
    "v_cndmask_b32 v188, v202, v195, s[44:45]\n"
    "v_mfma_f32_16x16x32_bf16 a[112:115], a[240:243], v[140:143], a[112:115]\n"
    "global_load_dwordx4 v[148:151], v252, s[96:97] sc1\n"
    "s_add_u32 s96, s96, 4608\n"
    "s_addc_u32 s97, s97, 0\n"
    "v_exp_f32 v123, v123\n"
    "v_mfma_f32_16x16x32_bf16 a[84:87], a[244:247], v[136:139], a[84:87]\n"
    "ds_read_b128 a[156:159], v199 offset:15360\n"
    "global_load_dwordx4 v[160:163], v252, s[96:97] sc1\n"
    "s_add_u32 s96, s96, 4608\n"
    "s_addc_u32 s97, s97, 0\n"
    "v_cvt_pk_bf16_f32 v155, v122, v123\n"
    "v_mfma_f32_16x16x32_bf16 a[116:119], a[244:247], v[140:143], a[116:119]\n"
    "ds_read_b128 a[176:179], v199 offset:28672\n"
    "v_exp_f32 v124, v124\n"
    "v_cndmask_b32 v189, v202, v195, s[46:47]\n"
    "v_mfma_f32_16x16x32_bf16 a[88:91], a[248:251], v[136:139], a[88:91]\n"
    "global_load_dwordx4 v[164:167], v252, s[96:97] sc1\n"
    "s_add_u32 s96, s96, 4608\n"
    "s_addc_u32 s97, s97, 0\n"
    "v_cndmask_b32 v190, v202, v195, s[48:49]\n"
    "v_cndmask_b32 v191, v202, v195, s[50:51]\n"
    "v_mfma_f32_16x16x32_bf16 a[120:123], a[248:251], v[140:143], a[120:123]\n"
    "v_exp_f32 v125, v125\n"
    "global_load_dwordx4 v[176:179], v252, s[96:97] sc1\n"
    "s_add_u32 s96, s96, 4608\n"
    "s_addc_u32 s97, s97, 0\n"
    "v_mfma_f32_16x16x32_bf16 a[92:95], a[252:255], v[136:139], a[92:95]\n"
    "s_waitcnt vmcnt(16)\n"
    "s_barrier\n"
    "ds_read_b128 a[184:187], v199 offset:30720\n"
    "ds_read_b128 a[180:183], v199 offset:29696\n"
    "v_exp_f32 v126, v126\n"
    "v_mfma_f32_16x16x32_bf16 a[124:127], a[252:255], v[140:143], a[124:127]\n"
    "global_load_dwordx4 v[180:183], v252, s[96:97] sc1\n"
    "s_add_u32 s96, s96, 4608\n"
    "s_addc_u32 s97, s97, 0\n"
    "v_cvt_pk_bf16_f32 v158, v124, v125\n"
    "v_mfma_f32_16x16x32_bf16 v[240:243], v[248:251], v[136:139], v[240:243]\n"
    "ds_read_b128 a[188:191], v199 offset:31744\n"
    "v_exp_f32 v127, v127\n"
    "v_mfma_f32_16x16x32_bf16 v[244:247], v[248:251], v[140:143], v[244:247]\n"
    "s_waitcnt lgkmcnt(7)\n"
    "v_cvt_pk_bf16_f32 v159, v126, v127\n"
    "v_add_u32 v199, s21, v198\n"
    "v_mfma_f32_16x16x32_bf16 v[108:111], a[128:131], v[48:51], v[172:175]\n"
    "v_exp_f32 v76, v76\n"
    "buffer_store_dwordx4 v[208:211], v198, s[68:71], s25 offen offset:0 sc1\n"
    "v_mfma_f32_16x16x32_bf16 v[108:111], a[132:135], v[52:55], v[108:111]\n"
    "s_add_u32 s16, s16, 1\n"
    "ds_read_b128 a[224:227], v199 offset:16384\n"
    "buffer_store_dwordx4 v[212:215], v198, s[68:71], s25 offen offset:1024 sc1\n"
    "v_mfma_f32_16x16x32_bf16 v[108:111], a[136:139], v[56:59], v[108:111]\n"
    "buffer_store_dwordx4 v[216:219], v198, s[68:71], s25 offen offset:2048 sc1\n"
    "v_mfma_f32_16x16x32_bf16 v[108:111], a[140:143], v[60:63], v[108:111]\n"
    "buffer_store_dwordx4 v[220:223], v198, s[68:71], s25 offen offset:3072 sc1\n"
    "v_exp_f32 v77, v77\n"
    "v_mfma_f32_16x16x32_bf16 v[124:127], a[144:147], v[48:51], v[188:191]\n"
    "s_waitcnt lgkmcnt(0)\n"
    "ds_read_b128 a[228:231], v199 offset:17408\n"
    "buffer_store_dwordx4 v[224:227], v198, s[68:71], s35 offen offset:0 sc1\n"
    "v_mfma_f32_16x16x32_bf16 v[124:127], a[148:151], v[52:55], v[124:127]\n"
    "buffer_store_dwordx4 v[228:231], v198, s[68:71], s35 offen offset:1024 sc1\n"
    "v_cvt_pk_bf16_f32 v140, v76, v77\n"
    "v_mfma_f32_16x16x32_bf16 v[124:127], a[152:155], v[56:59], v[124:127]\n"
    "ds_read_b128 a[232:235], v199 offset:18432\n"
    "buffer_store_dwordx4 v[232:235], v198, s[68:71], s35 offen offset:2048 sc1\n"
    "v_mfma_f32_16x16x32_bf16 v[124:127], a[156:159], v[60:63], v[124:127]\n"
    "v_exp_f32 v78, v78\n"
    "buffer_store_dwordx4 v[236:239], v198, s[68:71], s35 offen offset:3072 sc1\n"
    "v_mfma_f32_16x16x32_bf16 a[64:67], a[160:163], v[152:155], a[64:67]\n"
    "s_add_u32 m0, s41, 0\n"
    "s_nop 0\n"
    "buffer_load_dwordx4 v204, s[36:39], s52 offen lds\n"
    "v_mfma_f32_16x16x32_bf16 a[96:99], a[160:163], v[156:159], a[96:99]\n"
    "ds_read_b128 a[236:239], v199 offset:19456\n"
    "s_add_u32 m0, s41, 1024\n"
    "s_nop 0\n"
    "buffer_load_dwordx4 v205, s[36:39], s52 offen lds\n"
    "v_mfma_f32_16x16x32_bf16 a[68:71], a[164:167], v[152:155], a[68:71]\n"
    "v_exp_f32 v79, v79\n"
    "s_add_u32 m0, s41, 2048\n"
    "s_nop 0\n"
    "buffer_load_dwordx4 v206, s[36:39], s52 offen lds\n"
    "v_mfma_f32_16x16x32_bf16 a[100:103], a[164:167], v[156:159], a[100:103]\n"
    "s_add_u32 m0, s41, 3072\n"
    "s_nop 0\n"
    "buffer_load_dwordx4 v207, s[36:39], s52 offen lds\n"
    "s_add_u32 s52, s52, 65536\n"
    "s_and_b32 s52, s52, 0x7ffff\n"
    "s_add_u32 s41, s41, 16384\n"
    "s_and_b32 s41, s41, 0x1ffff\n"
    "v_cvt_pk_bf16_f32 v141, v78, v79\n"
    "v_mfma_f32_16x16x32_bf16 a[72:75], a[168:171], v[152:155], a[72:75]\n"
    "s_add_u32 m0, s41, 0\n"
    "s_nop 0\n"
    "buffer_load_dwordx4 v204, s[36:39], s52 offen lds\n"
    "v_mfma_f32_16x16x32_bf16 a[104:107], a[168:171], v[156:159], a[104:107]\n"
    "ds_read_b128 a[240:243], v199 offset:20480\n"
    "v_exp_f32 v92, v92\n"
    "v_mfma_f32_16x16x32_bf16 a[76:79], a[172:175], v[152:155], a[76:79]\n"
    "s_add_u32 m0, s41, 1024\n"
    "s_nop 0\n"
    "buffer_load_dwordx4 v205, s[36:39], s52 offen lds\n"
    "s_add_u32 m0, s41, 2048\n"
    "s_nop 0\n"
    "buffer_load_dwordx4 v206, s[36:39], s52 offen lds\n"
    "v_mfma_f32_16x16x32_bf16 a[108:111], a[172:175], v[156:159], a[108:111]\n"
    "s_add_u32 m0, s41, 3072\n"
    "s_nop 0\n"
    "buffer_load_dwordx4 v207, s[36:39], s52 offen lds\n"
    "s_add_u32 s52, s52, 65536\n"
    "s_and_b32 s52, s52, 0x7ffff\n"
    "s_add_u32 s41, s41, 16384\n"
    "s_and_b32 s41, s41, 0x1ffff\n"
    "v_mfma_f32_16x16x32_bf16 a[80:83], a[176:179], v[152:155], a[80:83]\n"
    "ds_read_b128 a[244:247], v199 offset:21504\n"
    "v_exp_f32 v93, v93\n"
    "v_mfma_f32_16x16x32_bf16 a[112:115], a[176:179], v[156:159], a[112:115]\n"
    "s_add_u32 m0, s41, 0\n"
    "s_nop 0\n"
    "buffer_load_dwordx4 v204, s[36:39], s52 offen lds\n"
    "s_add_u32 m0, s41, 1024\n"
    "s_nop 0\n"
    "buffer_load_dwordx4 v205, s[36:39], s52 offen lds\n"
    "v_mfma_f32_16x16x32_bf16 a[84:87], a[180:183], v[152:155], a[84:87]\n"
    "s_add_u32 m0, s41, 2048\n"
    "s_nop 0\n"
    "buffer_load_dwordx4 v206, s[36:39], s52 offen lds\n"
    "v_cvt_pk_bf16_f32 v142, v92, v93\n"
    "v_mfma_f32_16x16x32_bf16 a[116:119], a[180:183], v[156:159], a[116:119]\n"
    "ds_read_b128 a[248:251], v199 offset:22528\n"
    "v_exp_f32 v94, v94\n"
    "v_mfma_f32_16x16x32_bf16 a[88:91], a[184:187], v[152:155], a[88:91]\n"
    "s_add_u32 m0, s41, 3072\n"
    "s_nop 0\n"
    "buffer_load_dwordx4 v207, s[36:39], s52 offen lds\n"
    "s_add_u32 s52, s52, 65536\n"
    "s_and_b32 s52, s52, 0x7ffff\n"
    "s_add_u32 s41, s41, 16384\n"
    "s_and_b32 s41, s41, 0x1ffff\n"
    "v_mfma_f32_16x16x32_bf16 a[120:123], a[184:187], v[156:159], a[120:123]\n"
    "s_add_u32 m0, s41, 0\n"
    "s_nop 0\n"
    "buffer_load_dwordx4 v204, s[36:39], s52 offen lds\n"
    "s_add_u32 m0, s41, 1024\n"
    "s_nop 0\n"
    "buffer_load_dwordx4 v205, s[36:39], s52 offen lds\n"
    "v_mfma_f32_16x16x32_bf16 a[92:95], a[188:191], v[152:155], a[92:95]\n"
    "ds_read_b128 a[252:255], v199 offset:23552\n"
    "v_exp_f32 v95, v95\n"
    "v_mfma_f32_16x16x32_bf16 a[124:127], a[188:191], v[156:159], a[124:127]\n"
    "s_add_u32 m0, s41, 2048\n"
    "s_nop 0\n"
    "buffer_load_dwordx4 v206, s[36:39], s52 offen lds\n"
    "v_cvt_pk_bf16_f32 v143, v94, v95\n"
    "v_mfma_f32_16x16x32_bf16 v[240:243], v[248:251], v[152:155], v[240:243]\n"
    "s_add_u32 m0, s41, 3072\n"
    "s_nop 0\n"
    "buffer_load_dwordx4 v207, s[36:39], s52 offen lds\n"
    "s_add_u32 s52, s52, 65536\n"
    "s_and_b32 s52, s52, 0x7ffff\n"
    "s_add_u32 s41, s41, 16384\n"
    "s_and_b32 s41, s41, 0x1ffff\n"
    "v_mfma_f32_16x16x32_bf16 v[244:247], v[248:251], v[156:159], v[244:247]\n"
    "s_barrier\n"
    "s_branch L_att_D_%=\n"
    "L_att_fallback_%=:\n"
    "s_waitcnt vmcnt(0) lgkmcnt(0)\n"
    "s_barrier\n"
    "s_mov_b32 s24, 1\n"
    "s_mov_b32 s16, 0\n"
    "s_mov_b32 s20, 0\n"
    "v_mov_b32 v192, v202\n"
    "v_mov_b32 v193, v202\n"
    "v_mov_b32 v194, v202\n"
    "v_mov_b32 v195, v202\n"
    "L_att_fb_loop_%=:\n"
    "buffer_load_dwordx4 a[128:131], v198, s[60:63], s20 offen offset:0 sc1\n"
    "buffer_load_dwordx4 a[132:135], v198, s[60:63], s20 offen offset:1024 sc1\n"
    "buffer_load_dwordx4 a[136:139], v198, s[60:63], s20 offen offset:2048 sc1\n"
    "buffer_load_dwordx4 a[140:143], v198, s[60:63], s20 offen offset:3072 sc1\n"
    "s_add_u32 s20, s20, 4096\n"
    "buffer_load_dwordx4 a[144:147], v198, s[60:63], s20 offen offset:0 sc1\n"
    "buffer_load_dwordx4 a[148:151], v198, s[60:63], s20 offen offset:1024 sc1\n"
    "buffer_load_dwordx4 a[152:155], v198, s[60:63], s20 offen offset:2048 sc1\n"
    "buffer_load_dwordx4 a[156:159], v198, s[60:63], s20 offen offset:3072 sc1\n"
    "s_add_u32 s20, s20, 4096\n"
    "s_bitcmp1_b32 s16, 0\n"
    "s_cselect_b32 s14, 0, 0\n"
    "s_add_u32 s20, s20, s14\n"
    "s_waitcnt vmcnt(0)\n"
    "v_mfma_f32_16x16x32_bf16 v[64:67], a[128:131], v[0:3], 0\n"
    "v_mfma_f32_16x16x32_bf16 v[68:71], a[128:131], v[16:19], 0\n"
    "v_mfma_f32_16x16x32_bf16 v[72:75], a[128:131], v[32:35], 0\n"
    "v_mfma_f32_16x16x32_bf16 v[76:79], a[128:131], v[48:51], 0\n"
    "v_mfma_f32_16x16x32_bf16 v[64:67], a[132:135], v[4:7], v[64:67]\n"
    "v_mfma_f32_16x16x32_bf16 v[68:71], a[132:135], v[20:23], v[68:71]\n"
    "v_mfma_f32_16x16x32_bf16 v[72:75], a[132:135], v[36:39], v[72:75]\n"
    "v_mfma_f32_16x16x32_bf16 v[76:79], a[132:135], v[52:55], v[76:79]\n"
    "v_mfma_f32_16x16x32_bf16 v[64:67], a[136:139], v[8:11], v[64:67]\n"
    "v_mfma_f32_16x16x32_bf16 v[68:71], a[136:139], v[24:27], v[68:71]\n"
    "v_mfma_f32_16x16x32_bf16 v[72:75], a[136:139], v[40:43], v[72:75]\n"
    "v_mfma_f32_16x16x32_bf16 v[76:79], a[136:139], v[56:59], v[76:79]\n"
    "v_mfma_f32_16x16x32_bf16 v[64:67], a[140:143], v[12:15], v[64:67]\n"
    "v_mfma_f32_16x16x32_bf16 v[68:71], a[140:143], v[28:31], v[68:71]\n"
    "v_mfma_f32_16x16x32_bf16 v[72:75], a[140:143], v[44:47], v[72:75]\n"
    "v_mfma_f32_16x16x32_bf16 v[76:79], a[140:143], v[60:63], v[76:79]\n"
    "v_mfma_f32_16x16x32_bf16 v[80:83], a[144:147], v[0:3], 0\n"
    "v_mfma_f32_16x16x32_bf16 v[84:87], a[144:147], v[16:19], 0\n"
    "v_mfma_f32_16x16x32_bf16 v[88:91], a[144:147], v[32:35], 0\n"
    "v_mfma_f32_16x16x32_bf16 v[92:95], a[144:147], v[48:51], 0\n"
    "v_mfma_f32_16x16x32_bf16 v[80:83], a[148:151], v[4:7], v[80:83]\n"
    "v_mfma_f32_16x16x32_bf16 v[84:87], a[148:151], v[20:23], v[84:87]\n"
    "v_mfma_f32_16x16x32_bf16 v[88:91], a[148:151], v[36:39], v[88:91]\n"
    "v_mfma_f32_16x16x32_bf16 v[92:95], a[148:151], v[52:55], v[92:95]\n"
    "v_mfma_f32_16x16x32_bf16 v[80:83], a[152:155], v[8:11], v[80:83]\n"
    "v_mfma_f32_16x16x32_bf16 v[84:87], a[152:155], v[24:27], v[84:87]\n"
    "v_mfma_f32_16x16x32_bf16 v[88:91], a[152:155], v[40:43], v[88:91]\n"
    "v_mfma_f32_16x16x32_bf16 v[92:95], a[152:155], v[56:59], v[92:95]\n"
    "v_mfma_f32_16x16x32_bf16 v[80:83], a[156:159], v[12:15], v[80:83]\n"
    "v_mfma_f32_16x16x32_bf16 v[84:87], a[156:159], v[28:31], v[84:87]\n"
    "v_mfma_f32_16x16x32_bf16 v[88:91], a[156:159], v[44:47], v[88:91]\n"
    "v_mfma_f32_16x16x32_bf16 v[92:95], a[156:159], v[60:63], v[92:95]\n"
    "s_lshl_b32 s14, s16, 1\n"
    "s_sub_u32 s14, s17, s14\n"
    "s_lshl_b32 s14, s14, 4\n"
    "v_add_u32 v203, s14, v201\n"
    "v_cmp_le_i32 vcc, 0, v203\n"
    "s_nop 1\n"
    "v_cndmask_b32 v64, v202, v64, vcc\n"
    "v_cmp_le_i32 vcc, 1, v203\n"
    "s_nop 1\n"
    "v_cndmask_b32 v65, v202, v65, vcc\n"
    "v_cmp_le_i32 vcc, 2, v203\n"
    "s_nop 1\n"
    "v_cndmask_b32 v66, v202, v66, vcc\n"
    "v_cmp_le_i32 vcc, 3, v203\n"
    "s_nop 1\n"
    "v_cndmask_b32 v67, v202, v67, vcc\n"
    "v_cmp_le_i32 vcc, 16, v203\n"
    "s_nop 1\n"
    "v_cndmask_b32 v80, v202, v80, vcc\n"
    "v_cmp_le_i32 vcc, 17, v203\n"
    "s_nop 1\n"
    "v_cndmask_b32 v81, v202, v81, vcc\n"
    "v_cmp_le_i32 vcc, 18, v203\n"
    "s_nop 1\n"
    "v_cndmask_b32 v82, v202, v82, vcc\n"
    "v_cmp_le_i32 vcc, 19, v203\n"
    "s_nop 1\n"
    "v_cndmask_b32 v83, v202, v83, vcc\n"
    "s_lshl_b32 s14, s16, 1\n"
    "s_sub_u32 s14, s18, s14\n"
    "s_lshl_b32 s14, s14, 4\n"
    "v_add_u32 v203, s14, v201\n"
    "v_cmp_le_i32 vcc, 0, v203\n"
    "s_nop 1\n"
    "v_cndmask_b32 v68, v202, v68, vcc\n"
    "v_cmp_le_i32 vcc, 1, v203\n"
    "s_nop 1\n"
    "v_cndmask_b32 v69, v202, v69, vcc\n"
    "v_cmp_le_i32 vcc, 2, v203\n"
    "s_nop 1\n"
    "v_cndmask_b32 v70, v202, v70, vcc\n"
    "v_cmp_le_i32 vcc, 3, v203\n"
    "s_nop 1\n"
    "v_cndmask_b32 v71, v202, v71, vcc\n"
    "v_cmp_le_i32 vcc, 16, v203\n"
    "s_nop 1\n"
    "v_cndmask_b32 v84, v202, v84, vcc\n"
    "v_cmp_le_i32 vcc, 17, v203\n"
    "s_nop 1\n"
    "v_cndmask_b32 v85, v202, v85, vcc\n"
    "v_cmp_le_i32 vcc, 18, v203\n"
    "s_nop 1\n"
    "v_cndmask_b32 v86, v202, v86, vcc\n"
    "v_cmp_le_i32 vcc, 19, v203\n"
    "s_nop 1\n"
    "v_cndmask_b32 v87, v202, v87, vcc\n"
    "s_lshl_b32 s14, s16, 1\n"
    "s_sub_u32 s14, s30, s14\n"
    "s_lshl_b32 s14, s14, 4\n"
    "v_add_u32 v203, s14, v201\n"
    "v_cmp_le_i32 vcc, 0, v203\n"
    "s_nop 1\n"
    "v_cndmask_b32 v72, v202, v72, vcc\n"
    "v_cmp_le_i32 vcc, 1, v203\n"
    "s_nop 1\n"
    "v_cndmask_b32 v73, v202, v73, vcc\n"
    "v_cmp_le_i32 vcc, 2, v203\n"
    "s_nop 1\n"
    "v_cndmask_b32 v74, v202, v74, vcc\n"
    "v_cmp_le_i32 vcc, 3, v203\n"
    "s_nop 1\n"
    "v_cndmask_b32 v75, v202, v75, vcc\n"
    "v_cmp_le_i32 vcc, 16, v203\n"
    "s_nop 1\n"
    "v_cndmask_b32 v88, v202, v88, vcc\n"
    "v_cmp_le_i32 vcc, 17, v203\n"
    "s_nop 1\n"
    "v_cndmask_b32 v89, v202, v89, vcc\n"
    "v_cmp_le_i32 vcc, 18, v203\n"
    "s_nop 1\n"
    "v_cndmask_b32 v90, v202, v90, vcc\n"
    "v_cmp_le_i32 vcc, 19, v203\n"
    "s_nop 1\n"
    "v_cndmask_b32 v91, v202, v91, vcc\n"
    "s_lshl_b32 s14, s16, 1\n"
    "s_sub_u32 s14, s31, s14\n"
    "s_lshl_b32 s14, s14, 4\n"
    "v_add_u32 v203, s14, v201\n"
    "v_cmp_le_i32 vcc, 0, v203\n"
    "s_nop 1\n"
    "v_cndmask_b32 v76, v202, v76, vcc\n"
    "v_cmp_le_i32 vcc, 1, v203\n"
    "s_nop 1\n"
    "v_cndmask_b32 v77, v202, v77, vcc\n"
    "v_cmp_le_i32 vcc, 2, v203\n"
    "s_nop 1\n"
    "v_cndmask_b32 v78, v202, v78, vcc\n"
    "v_cmp_le_i32 vcc, 3, v203\n"
    "s_nop 1\n"
    "v_cndmask_b32 v79, v202, v79, vcc\n"
    "v_cmp_le_i32 vcc, 16, v203\n"
    "s_nop 1\n"
    "v_cndmask_b32 v92, v202, v92, vcc\n"
    "v_cmp_le_i32 vcc, 17, v203\n"
    "s_nop 1\n"
    "v_cndmask_b32 v93, v202, v93, vcc\n"
    "v_cmp_le_i32 vcc, 18, v203\n"
    "s_nop 1\n"
    "v_cndmask_b32 v94, v202, v94, vcc\n"
    "v_cmp_le_i32 vcc, 19, v203\n"
    "s_nop 1\n"
    "v_cndmask_b32 v95, v202, v95, vcc\n"
    "v_max3_f32 v192, v192, v64, v65\n"
    "v_max3_f32 v192, v192, v66, v67\n"
    "v_max3_f32 v192, v192, v80, v81\n"
    "v_max3_f32 v192, v192, v82, v83\n"
    "v_max3_f32 v193, v193, v68, v69\n"
    "v_max3_f32 v193, v193, v70, v71\n"
    "v_max3_f32 v193, v193, v84, v85\n"
    "v_max3_f32 v193, v193, v86, v87\n"
    "v_max3_f32 v194, v194, v72, v73\n"
    "v_max3_f32 v194, v194, v74, v75\n"
    "v_max3_f32 v194, v194, v88, v89\n"
    "v_max3_f32 v194, v194, v90, v91\n"
    "v_max3_f32 v195, v195, v76, v77\n"
    "v_max3_f32 v195, v195, v78, v79\n"
    "v_max3_f32 v195, v195, v92, v93\n"
    "v_max3_f32 v195, v195, v94, v95\n"
    "s_add_u32 s16, s16, 1\n"
    "s_lshr_b32 s14, s31, 1\n"
    "s_cmp_le_u32 s16, s14\n"
    "s_cbranch_scc1 L_att_fb_loop_%=\n"
    "v_mov_b32 v208, v192\n"
    "v_mov_b32 v209, v193\n"
    "v_mov_b32 v210, v194\n"
    "v_mov_b32 v211, v195\n"
    "v_permlane32_swap_b32 v192, v208\n"
    "v_permlane32_swap_b32 v193, v209\n"
    "v_permlane32_swap_b32 v194, v210\n"
    "v_permlane32_swap_b32 v195, v211\n"
    "v_max_f32 v192, v192, v208\n"
    "v_max_f32 v193, v193, v209\n"
    "v_max_f32 v194, v194, v210\n"
    "v_max_f32 v195, v195, v211\n"
    "v_mov_b32 v208, v192\n"
    "v_mov_b32 v209, v193\n"
    "v_mov_b32 v210, v194\n"
    "v_mov_b32 v211, v195\n"
    "v_permlane16_swap_b32 v192, v208\n"
    "v_permlane16_swap_b32 v193, v209\n"
    "v_permlane16_swap_b32 v194, v210\n"
    "v_permlane16_swap_b32 v195, v211\n"
    "v_max_f32 v192, v192, v208\n"
    "v_max_f32 v193, v193, v209\n"
    "v_max_f32 v194, v194, v210\n"
    "v_max_f32 v195, v195, v211\n"
    "v_sub_f32 v192, 0, v192\n"
    "v_sub_f32 v193, 0, v193\n"
    "v_sub_f32 v194, 0, v194\n"
    "v_sub_f32 v195, 0, v195\n"
    "v_accvgpr_write_b32 a0, 0\n"
    "v_accvgpr_write_b32 a1, 0\n"
    "v_accvgpr_write_b32 a2, 0\n"
    "v_accvgpr_write_b32 a3, 0\n"
    "v_accvgpr_write_b32 a4, 0\n"
    "v_accvgpr_write_b32 a5, 0\n"
    "v_accvgpr_write_b32 a6, 0\n"
    "v_accvgpr_write_b32 a7, 0\n"
    "v_accvgpr_write_b32 a8, 0\n"
    "v_accvgpr_write_b32 a9, 0\n"
    "v_accvgpr_write_b32 a10, 0\n"
    "v_accvgpr_write_b32 a11, 0\n"
    "v_accvgpr_write_b32 a12, 0\n"
    "v_accvgpr_write_b32 a13, 0\n"
    "v_accvgpr_write_b32 a14, 0\n"
    "v_accvgpr_write_b32 a15, 0\n"
    "v_accvgpr_write_b32 a16, 0\n"
    "v_accvgpr_write_b32 a17, 0\n"
    "v_accvgpr_write_b32 a18, 0\n"
    "v_accvgpr_write_b32 a19, 0\n"
    "v_accvgpr_write_b32 a20, 0\n"
    "v_accvgpr_write_b32 a21, 0\n"
    "v_accvgpr_write_b32 a22, 0\n"
    "v_accvgpr_write_b32 a23, 0\n"
    "v_accvgpr_write_b32 a24, 0\n"
    "v_accvgpr_write_b32 a25, 0\n"
    "v_accvgpr_write_b32 a26, 0\n"
    "v_accvgpr_write_b32 a27, 0\n"
    "v_accvgpr_write_b32 a28, 0\n"
    "v_accvgpr_write_b32 a29, 0\n"
    "v_accvgpr_write_b32 a30, 0\n"
    "v_accvgpr_write_b32 a31, 0\n"
    "v_accvgpr_write_b32 a32, 0\n"
    "v_accvgpr_write_b32 a33, 0\n"
    "v_accvgpr_write_b32 a34, 0\n"
    "v_accvgpr_write_b32 a35, 0\n"
    "v_accvgpr_write_b32 a36, 0\n"
    "v_accvgpr_write_b32 a37, 0\n"
    "v_accvgpr_write_b32 a38, 0\n"
    "v_accvgpr_write_b32 a39, 0\n"
    "v_accvgpr_write_b32 a40, 0\n"
    "v_accvgpr_write_b32 a41, 0\n"
    "v_accvgpr_write_b32 a42, 0\n"
    "v_accvgpr_write_b32 a43, 0\n"
    "v_accvgpr_write_b32 a44, 0\n"
    "v_accvgpr_write_b32 a45, 0\n"
    "v_accvgpr_write_b32 a46, 0\n"
    "v_accvgpr_write_b32 a47, 0\n"
    "v_accvgpr_write_b32 a48, 0\n"
    "v_accvgpr_write_b32 a49, 0\n"
    "v_accvgpr_write_b32 a50, 0\n"
    "v_accvgpr_write_b32 a51, 0\n"
    "v_accvgpr_write_b32 a52, 0\n"
    "v_accvgpr_write_b32 a53, 0\n"
    "v_accvgpr_write_b32 a54, 0\n"
    "v_accvgpr_write_b32 a55, 0\n"
    "v_accvgpr_write_b32 a56, 0\n"
    "v_accvgpr_write_b32 a57, 0\n"
    "v_accvgpr_write_b32 a58, 0\n"
    "v_accvgpr_write_b32 a59, 0\n"
    "v_accvgpr_write_b32 a60, 0\n"
    "v_accvgpr_write_b32 a61, 0\n"
    "v_accvgpr_write_b32 a62, 0\n"
    "v_accvgpr_write_b32 a63, 0\n"
    "v_accvgpr_write_b32 a64, 0\n"
    "v_accvgpr_write_b32 a65, 0\n"
    "v_accvgpr_write_b32 a66, 0\n"
    "v_accvgpr_write_b32 a67, 0\n"
    "v_accvgpr_write_b32 a68, 0\n"
    "v_accvgpr_write_b32 a69, 0\n"
    "v_accvgpr_write_b32 a70, 0\n"
    "v_accvgpr_write_b32 a71, 0\n"
    "v_accvgpr_write_b32 a72, 0\n"
    "v_accvgpr_write_b32 a73, 0\n"
    "v_accvgpr_write_b32 a74, 0\n"
    "v_accvgpr_write_b32 a75, 0\n"
    "v_accvgpr_write_b32 a76, 0\n"
    "v_accvgpr_write_b32 a77, 0\n"
    "v_accvgpr_write_b32 a78, 0\n"
    "v_accvgpr_write_b32 a79, 0\n"
    "v_accvgpr_write_b32 a80, 0\n"
    "v_accvgpr_write_b32 a81, 0\n"
    "v_accvgpr_write_b32 a82, 0\n"
    "v_accvgpr_write_b32 a83, 0\n"
    "v_accvgpr_write_b32 a84, 0\n"
    "v_accvgpr_write_b32 a85, 0\n"
    "v_accvgpr_write_b32 a86, 0\n"
    "v_accvgpr_write_b32 a87, 0\n"
    "v_accvgpr_write_b32 a88, 0\n"
    "v_accvgpr_write_b32 a89, 0\n"
    "v_accvgpr_write_b32 a90, 0\n"
    "v_accvgpr_write_b32 a91, 0\n"
    "v_accvgpr_write_b32 a92, 0\n"
    "v_accvgpr_write_b32 a93, 0\n"
    "v_accvgpr_write_b32 a94, 0\n"
    "v_accvgpr_write_b32 a95, 0\n"
    "v_accvgpr_write_b32 a96, 0\n"
    "v_accvgpr_write_b32 a97, 0\n"
    "v_accvgpr_write_b32 a98, 0\n"
    "v_accvgpr_write_b32 a99, 0\n"
    "v_accvgpr_write_b32 a100, 0\n"
    "v_accvgpr_write_b32 a101, 0\n"
    "v_accvgpr_write_b32 a102, 0\n"
    "v_accvgpr_write_b32 a103, 0\n"
    "v_accvgpr_write_b32 a104, 0\n"
    "v_accvgpr_write_b32 a105, 0\n"
    "v_accvgpr_write_b32 a106, 0\n"
    "v_accvgpr_write_b32 a107, 0\n"
    "v_accvgpr_write_b32 a108, 0\n"
    "v_accvgpr_write_b32 a109, 0\n"
    "v_accvgpr_write_b32 a110, 0\n"
    "v_accvgpr_write_b32 a111, 0\n"
    "v_accvgpr_write_b32 a112, 0\n"
    "v_accvgpr_write_b32 a113, 0\n"
    "v_accvgpr_write_b32 a114, 0\n"
    "v_accvgpr_write_b32 a115, 0\n"
    "v_accvgpr_write_b32 a116, 0\n"
    "v_accvgpr_write_b32 a117, 0\n"
    "v_accvgpr_write_b32 a118, 0\n"
    "v_accvgpr_write_b32 a119, 0\n"
    "v_accvgpr_write_b32 a120, 0\n"
    "v_accvgpr_write_b32 a121, 0\n"
    "v_accvgpr_write_b32 a122, 0\n"
    "v_accvgpr_write_b32 a123, 0\n"
    "v_accvgpr_write_b32 a124, 0\n"
    "v_accvgpr_write_b32 a125, 0\n"
    "v_accvgpr_write_b32 a126, 0\n"
    "v_accvgpr_write_b32 a127, 0\n"
    "s_lshl_b32 s19, s8, 12\n"
    "s_mov_b32 s20, 0\n"
    "s_mov_b32 m0, s19\n"
    "s_nop 0\n"
    "buffer_load_dwordx4 v200, s[60:63], s20 offen offset:0 sc1 lds\n"
    "buffer_load_dwordx4 v200, s[60:63], s20 offen offset:1024 sc1 lds\n"
    "buffer_load_dwordx4 v200, s[60:63], s20 offen offset:2048 sc1 lds\n"
    "buffer_load_dwordx4 v200, s[60:63], s20 offen offset:3072 sc1 lds\n"
    "s_add_u32 s20, s20, 16384\n"
    "s_add_u32 s19, s19, 32768\n"
    "s_and_b32 s19, s19, 0x1ffff\n"
    "s_mov_b32 m0, s19\n"
    "s_nop 0\n"
    "buffer_load_dwordx4 v200, s[60:63], s20 offen offset:0 sc1 lds\n"
    "buffer_load_dwordx4 v200, s[60:63], s20 offen offset:1024 sc1 lds\n"
    "buffer_load_dwordx4 v200, s[60:63], s20 offen offset:2048 sc1 lds\n"
    "buffer_load_dwordx4 v200, s[60:63], s20 offen offset:3072 sc1 lds\n"
    "s_add_u32 m0, s19, 16384\n"
    "s_nop 0\n"
    "buffer_load_dwordx4 v200, s[64:67], s20 offen offset:0 sc1 lds\n"
    "buffer_load_dwordx4 v200, s[64:67], s20 offen offset:1024 sc1 lds\n"
    "buffer_load_dwordx4 v200, s[64:67], s20 offen offset:2048 sc1 lds\n"
    "buffer_load_dwordx4 v200, s[64:67], s20 offen offset:3072 sc1 lds\n"
    "s_add_u32 s20, s20, 16384\n"
    "s_add_u32 s19, s19, 32768\n"
    "s_and_b32 s19, s19, 0x1ffff\n"
    "s_mov_b32 s16, 0\n"
    "s_mov_b32 s21, 0\n"
    "v_mov_b32 v199, v198\n"
    "v_mov_b32 v196, 0\n"
    "v_mov_b32 v197, 0\n"
    "v_mov_b32 v164, v193\n"
    "v_mov_b32 v165, v193\n"
    "v_mov_b32 v166, v193\n"
    "v_mov_b32 v167, v193\n"
    "v_mov_b32 v168, v194\n"
    "v_mov_b32 v169, v194\n"
    "v_mov_b32 v170, v194\n"
    "v_mov_b32 v171, v194\n"
    "v_mov_b32 v172, v195\n"
    "v_mov_b32 v173, v195\n"
    "v_mov_b32 v174, v195\n"
    "v_mov_b32 v175, v195\n"
    "v_mov_b32 v180, v193\n"
    "v_mov_b32 v181, v193\n"
    "v_mov_b32 v182, v193\n"
    "v_mov_b32 v183, v193\n"
    "v_mov_b32 v184, v194\n"
    "v_mov_b32 v185, v194\n"
    "v_mov_b32 v186, v194\n"
    "v_mov_b32 v187, v194\n"
    "v_mov_b32 v188, v195\n"
    "v_mov_b32 v189, v195\n"
    "v_mov_b32 v190, v195\n"
    "v_mov_b32 v191, v195\n"
    "s_lshl_b32 s14, s16, 1\n"
    "s_cmp_gt_u32 s17, s14\n"
    "s_cselect_b64 vcc, -1, 0\n"
    "s_cmp_eq_u32 s17, s14\n"
    "s_cselect_b64 s[44:45], s[72:73], vcc\n"
    "s_cselect_b64 s[46:47], s[74:75], vcc\n"
    "s_cselect_b64 s[48:49], s[76:77], vcc\n"
    "s_cselect_b64 s[50:51], s[78:79], vcc\n"
    "v_cndmask_b32 v160, v202, v192, s[44:45]\n"
    "v_cndmask_b32 v161, v202, v192, s[46:47]\n"
    "v_cndmask_b32 v162, v202, v192, s[48:49]\n"
    "v_cndmask_b32 v163, v202, v192, s[50:51]\n"
    "s_add_u32 s14, s14, 1\n"
    "s_cmp_gt_u32 s17, s14\n"
    "s_cselect_b64 vcc, -1, 0\n"
    "s_cmp_eq_u32 s17, s14\n"
    "s_cselect_b64 s[44:45], s[72:73], vcc\n"
    "s_cselect_b64 s[46:47], s[74:75], vcc\n"
    "s_cselect_b64 s[48:49], s[76:77], vcc\n"
    "s_cselect_b64 s[50:51], s[78:79], vcc\n"
    "v_cndmask_b32 v176, v202, v192, s[44:45]\n"
    "v_cndmask_b32 v177, v202, v192, s[46:47]\n"
    "v_cndmask_b32 v178, v202, v192, s[48:49]\n"
    "v_cndmask_b32 v179, v202, v192, s[50:51]\n"
    "s_waitcnt vmcnt(0)\n"
    "s_barrier\n"
    "ds_read_b128 a[192:195], v199 offset:0\n"
    "ds_read_b128 a[196:199], v199 offset:1024\n"
    "ds_read_b128 a[200:203], v199 offset:2048\n"
    "ds_read_b128 a[204:207], v199 offset:3072\n"
    "ds_read_b128 a[208:211], v199 offset:4096\n"
    "ds_read_b128 a[212:215], v199 offset:5120\n"
    "ds_read_b128 a[216:219], v199 offset:6144\n"
    "ds_read_b128 a[220:223], v199 offset:7168\n"
    "s_branch L_att_start_%=\n"
    "L_spin6_%=:\n"
    "s_mov_b32 s96, 0\n"
    "L_lp8_%=:\n"
    "s_sleep 2\n"
    "global_load_dwordx2 v[206:207], v204, s[58:59] sc1\n"
    "s_add_u32 s96, s96, 1\n"
    "s_waitcnt vmcnt(0)\n"
    "v_cmp_ne_u64 s[98:99], s[28:29], v[206:207]\n"
    "s_nop 0\n"
    "s_cmp_lg_u64 s[98:99], 0\n"
    "s_cbranch_scc0 L_ok7_%=\n"
    "s_cmp_lt_u32 s96, 0x20000\n"
    "s_cbranch_scc1 L_lp8_%=\n"
    "s_branch L_ok7_%=\n"
    "L_spin11_%=:\n"
    "s_mov_b32 s96, 0\n"
    "L_lp13_%=:\n"
    "s_sleep 2\n"
    "global_load_dwordx2 v[194:195], v192, s[58:59] sc1\n"
    "s_add_u32 s96, s96, 1\n"
    "s_waitcnt vmcnt(0)\n"
    "v_cmp_ne_u64 s[98:99], s[28:29], v[194:195]\n"
    "s_nop 0\n"
    "s_cmp_lg_u64 s[98:99], 0\n"
    "s_cbranch_scc0 L_ok12_%=\n"
    "s_cmp_lt_u32 s96, 0x20000\n"
    "s_cbranch_scc1 L_lp13_%=\n"
    "s_branch L_ok12_%=\n"
    "L_spin14_%=:\n"
    "s_mov_b32 s96, 0\n"
    "L_lp16_%=:\n"
    "s_sleep 2\n"
    "global_load_dwordx2 v[202:203], v201, s[58:59] sc1\n"
    "s_add_u32 s96, s96, 1\n"
    "s_waitcnt vmcnt(0)\n"
    "v_cmp_ne_u64 s[98:99], s[28:29], v[202:203]\n"
    "s_nop 0\n"
    "s_cmp_lg_u64 s[98:99], 0\n"
    "s_cbranch_scc0 L_ok15_%=\n"
    "s_cmp_lt_u32 s96, 0x20000\n"
    "s_cbranch_scc1 L_lp16_%=\n"
    "s_branch L_ok15_%=\n"
    "L_spin18_%=:\n"
    "s_mov_b32 s96, 0\n"
    "L_lp20_%=:\n"
    "s_sleep 2\n"
    "global_load_dwordx2 v[252:253], v250, s[58:59] sc1\n"
    "s_add_u32 s96, s96, 1\n"
    "s_waitcnt vmcnt(0)\n"
    "v_cmp_ne_u64 s[98:99], s[28:29], v[252:253]\n"
    "s_nop 0\n"
    "s_cmp_lg_u64 s[98:99], 0\n"
    "s_cbranch_scc0 L_ok19_%=\n"
    "s_cmp_lt_u32 s96, 0x20000\n"
    "s_cbranch_scc1 L_lp20_%=\n"
    "s_branch L_ok19_%=\n"
    "L_end_%=:\n"
    :: "s"(__builtin_amdgcn_kernarg_segment_ptr()), "s"(w), "s"(blockIdx.x)
    :       "v0","v1","v2","v3","v4","v5","v6","v7","v8","v9","v10","v11","v12","v13","v14","v15","v16","v17","v18","v19","v20","v21","v22","v23","v24","v25","v26","v27","v28","v29","v30","v31",
      "v32","v33","v34","v35","v36","v37","v38","v39","v40","v41","v42","v43","v44","v45","v46","v47","v48","v49","v50","v51","v52","v53","v54","v55","v56","v57","v58","v59","v60","v61","v62","v63",
      "v64","v65","v66","v67","v68","v69","v70","v71","v72","v73","v74","v75","v76","v77","v78","v79","v80","v81","v82","v83","v84","v85","v86","v87","v88","v89","v90","v91","v92","v93","v94","v95",
      "v96","v97","v98","v99","v100","v101","v102","v103","v104","v105","v106","v107","v108","v109","v110","v111","v112","v113","v114","v115","v116","v117","v118","v119","v120","v121","v122","v123","v124","v125","v126","v127",
      "v128","v129","v130","v131","v132","v133","v134","v135","v136","v137","v138","v139","v140","v141","v142","v143","v144","v145","v146","v147","v148","v149","v150","v151","v152","v153","v154","v155","v156","v157","v158","v159",
      "v160","v161","v162","v163","v164","v165","v166","v167","v168","v169","v170","v171","v172","v173","v174","v175","v176","v177","v178","v179","v180","v181","v182","v183","v184","v185","v186","v187","v188","v189","v190","v191",
      "v192","v193","v194","v195","v196","v197","v198","v199","v200","v201","v202","v203","v204","v205","v206","v207","v208","v209","v210","v211","v212","v213","v214","v215","v216","v217","v218","v219","v220","v221","v222","v223",
      "v224","v225","v226","v227","v228","v229","v230","v231","v232","v233","v234","v235","v236","v237","v238","v239","v240","v241","v242","v243","v244","v245","v246","v247","v248","v249","v250","v251","v252","v253","v254","v255",
      "a0","a1","a2","a3","a4","a5","a6","a7","a8","a9","a10","a11","a12","a13","a14","a15","a16","a17","a18","a19","a20","a21","a22","a23","a24","a25","a26","a27","a28","a29","a30","a31",
      "a32","a33","a34","a35","a36","a37","a38","a39","a40","a41","a42","a43","a44","a45","a46","a47","a48","a49","a50","a51","a52","a53","a54","a55","a56","a57","a58","a59","a60","a61","a62","a63",
      "a64","a65","a66","a67","a68","a69","a70","a71","a72","a73","a74","a75","a76","a77","a78","a79","a80","a81","a82","a83","a84","a85","a86","a87","a88","a89","a90","a91","a92","a93","a94","a95",
      "a96","a97","a98","a99","a100","a101","a102","a103","a104","a105","a106","a107","a108","a109","a110","a111","a112","a113","a114","a115","a116","a117","a118","a119","a120","a121","a122","a123","a124","a125","a126","a127",
      "a128","a129","a130","a131","a132","a133","a134","a135","a136","a137","a138","a139","a140","a141","a142","a143","a144","a145","a146","a147","a148","a149","a150","a151","a152","a153","a154","a155","a156","a157","a158","a159",
      "a160","a161","a162","a163","a164","a165","a166","a167","a168","a169","a170","a171","a172","a173","a174","a175","a176","a177","a178","a179","a180","a181","a182","a183","a184","a185","a186","a187","a188","a189","a190","a191",
      "a192","a193","a194","a195","a196","a197","a198","a199","a200","a201","a202","a203","a204","a205","a206","a207","a208","a209","a210","a211","a212","a213","a214","a215","a216","a217","a218","a219","a220","a221","a222","a223",
      "a224","a225","a226","a227","a228","a229","a230","a231","a232","a233","a234","a235","a236","a237","a238","a239","a240","a241","a242","a243","a244","a245","a246","a247","a248","a249","a250","a251","a252","a253","a254","a255",
      "s33","s34","s35","s36","s37","s38","s39","s40","s41","s42","s43","s44","s45","s46","s47","s48","s49","s50","s51","s52","s53","s54","s55","s56","s57","s58","s59","s60","s61","s62","s63","s64",
      "s65","s66","s67","s68","s69","s70","s71","s72","s73","s74","s75","s76","s77","s78","s79","s80","s81","s82","s83","s84","s85","s86","s87","s88","s89","s90","s91","s92","s93","s94","s95","s96",
      "s97","s98","s99","vcc","scc","memory",
      "s8","s9","s10","s11","s12","s13","s14","s15","s16","s17","s18","s19","s20","s21","s22","s23","s24","s25","s26","s27","s28","s29","s30","s31");
}

extern "C" void kernel_launch(void* const* d_in, const int* in_sizes, int n_in, void* d_out, int out_size, void* d_ws, size_t ws_size, hipStream_t stream) {
  hipLaunchKernelGGL(fused_fwd, dim3(256), dim3(256), 0, stream, d_in[0], d_in[1], d_in[2], d_in[3], d_in[4], d_in[5], d_in[6], d_in[7], d_in[8], d_in[9], d_out, d_ws);
}
```
